```python
import torch

def _softmax(x, axis=-1):
    return torch.softmax(x, dim=axis)
def _gelu(x):
    return torch.nn.functional.gelu(x, approximate="tanh")
def _silu(x):
    return torch.nn.functional.silu(x)
def _sigmoid(x):
    return torch.sigmoid(x)
def _layernorm(x, axis=-1, eps=1e-5):
    m = x.mean(dim=axis, keepdim=True)
    v = x.var(dim=axis, keepdim=True, unbiased=False)
    return (x - m) / torch.sqrt(v + eps)
def _scatter_add(tgt, idx, src, axis=0):
    return tgt.clone().index_add_(axis, idx, src)
def _segment_sum(src, seg, n):
    out = torch.zeros((n,) + src.shape[1:], dtype=src.dtype)
    return out.index_add_(0, seg, src)
def _pad(x, pw):
    flat = tuple(v for pair in reversed(pw) for v in pair)
    return torch.nn.functional.pad(x, flat)

kernel_name = 'gated_mix__ssm_conv_s4d__geglu_ffn__prompt'

def setup_inputs():

    _w = (lambda a: (lambda t: t.to(torch.bfloat16) if t.is_floating_point() else t)(torch.as_tensor(a)))
    def _A_setup():
        Bsz, L, D, N = 1, 2048, 512, 128
        return {
            "u": _w((lambda *s: torch.randn(*s, dtype=torch.bfloat16) * 0.05)(Bsz, L, D)),
            "log_A_real": _w((lambda *s: torch.randn(*s, dtype=torch.bfloat16) * 6.4)(D, N) * 0.5),
            "A_imag": _w((lambda *s: torch.randn(*s, dtype=torch.bfloat16) * 0.05)(D, N) * 3.14),
            "C_real": _w((lambda *s: torch.randn(*s, dtype=torch.bfloat16) * 0.05)(D, N) / N ** 0.5),
            "C_imag": _w((lambda *s: torch.randn(*s, dtype=torch.bfloat16) * 0.05)(D, N) / N ** 0.5),
            "log_dt": _w((lambda *s: torch.randn(*s, dtype=torch.bfloat16) * 0.05)(D) * 0.5 - 4.0),
            "D_skip": _w((lambda *s: torch.randn(*s, dtype=torch.bfloat16) * 0.05)(D)),
        }
    _A_in = _A_setup()

    def _B_setup():
        B, S, D, F = 1, 2048, 512, 1376
        return {
            "x": _w((lambda *s: torch.randn(*s, dtype=torch.bfloat16) * 0.05)(B, S, D)),
            "w_gate": _w((lambda *s: torch.randn(*s, dtype=torch.bfloat16) * 0.05)(D, F)),
            "w_up": _w((lambda *s: torch.randn(*s, dtype=torch.bfloat16) * 0.05)(D, F)),
            "w_down": _w((lambda *s: torch.randn(*s, dtype=torch.bfloat16) * 0.05)(F, D)),
        }
    _B_in = _B_setup()

    return {
        "A_u": _A_in["u"],
        "A_log_A_real": _A_in["log_A_real"],
        "A_A_imag": _A_in["A_imag"],
        "A_C_real": _A_in["C_real"],
        "A_C_imag": _A_in["C_imag"],
        "A_log_dt": _A_in["log_dt"],
        "A_D_skip": _A_in["D_skip"],
        "B_x": _B_in["x"],
        "B_w_gate": _B_in["w_gate"],
        "B_w_up": _B_in["w_up"],
        "B_w_down": _B_in["w_down"],
        "gate_w": _w((lambda *s: torch.randn(*s, dtype=torch.bfloat16) * 0.05)(512) * 16.0),
    }

def reference(A_u, A_log_A_real, A_A_imag, A_C_real, A_C_imag, A_log_dt, A_D_skip, B_x, B_w_gate, B_w_up, B_w_down, gate_w):
    def _A_ref(u, log_A_real, A_imag, C_real, C_imag, log_dt, D_skip):
        L = u.shape[1]
        dt = torch.exp(log_dt)
        A = -torch.exp(log_A_real) + 1j * A_imag
        C = (C_real + 1j * C_imag) * dt[:, None]
        pos = (lambda x, dt: x.to(dt))(torch.arange(L), log_dt.dtype)
        P = torch.exp((A * dt[:, None])[:, :, None] * pos[None, None, :])
        K = 2.0 * torch.einsum("dn,dnl->dl", C, P).real
        L2 = 2 * L
        u_t = (lambda x, *d: x.permute(*d))(u, 0, 2, 1)
        Kf = (lambda x, *a, **k: torch.fft.rfft(x.float(), *a, **k))(K, L2)
        Uf = (lambda x, *a, **k: torch.fft.rfft(x.float(), *a, **k))(u_t, L2)
        y = (lambda x, *a, **k: torch.fft.irfft(x, *a, **k).to(torch.bfloat16))(Uf * Kf[None, :, :], L2)[:, :, :L]
        y = (lambda x, *d: x.permute(*d))(y, 0, 2, 1)
        return y + u * D_skip[None, None, :]

    def _B_ref(x, w_gate, w_up, w_down):
        return (_gelu(x @ w_gate) * (x @ w_up)) @ w_down

    x = A_u
    g = _sigmoid((x * gate_w).sum(-1, keepdims=True))
    a = _A_ref(x, A_log_A_real, A_A_imag, A_C_real, A_C_imag, A_log_dt, A_D_skip)
    b = _B_ref(x, B_w_gate, B_w_up, B_w_down)
    return g * a + (1 - g) * b
```

```cpp
#include <hip/hip_runtime.h>
#include <hip/hip_bf16.h>

typedef __attribute__((ext_vector_type(8))) __bf16 bf16x8;
typedef __attribute__((ext_vector_type(4))) __bf16 bf16x4;
typedef __attribute__((ext_vector_type(2))) __bf16 bf16x2;
typedef __attribute__((ext_vector_type(4))) short s16x4;
typedef __attribute__((ext_vector_type(8))) short s16x8;
typedef __attribute__((ext_vector_type(4))) float f32x4;
typedef __attribute__((ext_vector_type(4))) unsigned u32x4;
typedef __attribute__((ext_vector_type(2))) unsigned u32x2;
typedef __attribute__((address_space(3))) s16x4* lds_s16x4_ptr;

#define K_LO 1

constexpr int L = 2048, D = 512, NS = 128, F = 1376, HP = 1408;

__device__ __forceinline__ float bf2f(unsigned short v) { return __uint_as_float((unsigned)v << 16); }
__device__ __forceinline__ float bfr(float x) { return (float)(__bf16)x; }
__device__ __forceinline__ unsigned pk2(float a, float b) {
  bf16x2 v = {(__bf16)a, (__bf16)b};
  return __builtin_bit_cast(unsigned, v);
}
__device__ __forceinline__ float lo_f(unsigned w) { return __uint_as_float(w << 16); }
__device__ __forceinline__ float hi_f(unsigned w) { return __uint_as_float(w & 0xffff0000u); }

__device__ __forceinline__ f32x4 mfma16(bf16x8 a, bf16x8 b, f32x4 c) {
  return __builtin_amdgcn_mfma_f32_16x16x32_bf16(a, b, c, 0, 0, 0);
}
__device__ __forceinline__ bf16x8 tr_frag(const char* lds, int off0, int off1) {
  s16x4 a = __builtin_amdgcn_ds_read_tr16_b64_v4i16((lds_s16x4_ptr)(lds + off0));
  s16x4 b = __builtin_amdgcn_ds_read_tr16_b64_v4i16((lds_s16x4_ptr)(lds + off1));
  s16x8 v = {a[0], a[1], a[2], a[3], b[0], b[1], b[2], b[3]};
  return __builtin_bit_cast(bf16x8, v);
}
__device__ __forceinline__ void glds16(const void* g, char* lds_wave_base) {
  __builtin_amdgcn_global_load_lds((const __attribute__((address_space(1))) void*)g,
                                   (__attribute__((address_space(3))) void*)lds_wave_base, 16, 0, 0);
}
#define WAIT_VM(n) asm volatile("s_waitcnt vmcnt(" #n ")" ::: "memory")
#define WAIT_LGKM0() asm volatile("s_waitcnt lgkmcnt(0)" ::: "memory")

constexpr int F1_STAGE = 40960;

__device__ __forceinline__ void ffn1_block(char* smem, int rb, int cb, const __bf16* __restrict__ x,
                                           const __bf16* __restrict__ wg, const __bf16* __restrict__ wu,
                                           __bf16* __restrict__ h) {
  const int tid = threadIdx.x, lane = tid & 63, w = __builtin_amdgcn_readfirstlane(tid >> 6);
  const int row0 = rb * 128, f0 = cb * 96;
  const int wm = w >> 1, wn = w & 1;
  const int r = lane & 15, g = lane >> 4, q = (lane >> 2) & 3, p = lane & 3;

  const __bf16* gsrc[5];
#pragma unroll
  for (int i = 0; i < 2; ++i) {
    const int a = w + 8 * i, row = 8 * a + (lane >> 3), ch = (lane & 7) ^ ((row >> 1) & 7);
    gsrc[i] = x + (size_t)(row0 + row) * D + ch * 8;
  }
#pragma unroll
  for (int i = 0; i < 3; ++i) {
    const int bi = w + 8 * i;
    const int kk = 8 * w + (lane >> 3), pos = lane & 7;
    const int fp = ((kk >> 1) & 1) | (((kk >> 3) & 1) << 1);
    const int T = 4 * i + ((pos >> 1) ^ fp);
    int col = f0 + 16 * (T % 6) + 8 * (pos & 1);
    col = col > F - 8 ? F - 8 : col;
    gsrc[2 + i] = (T < 6 ? wg : wu) + (size_t)kk * F + col;
    (void)bi;
  }
  auto stage = [&](int st, int buf) {
    char* base = smem + buf * F1_STAGE;
#pragma unroll
    for (int i = 0; i < 2; ++i) glds16(gsrc[i] + st * 64, base + (w + 8 * i) * 1024);
#pragma unroll
    for (int i = 0; i < 3; ++i) glds16(gsrc[2 + i] + (size_t)st * 64 * F, base + 16384 + i * 8192 + w * 1024);
  };

  int aoff[2][2];
#pragma unroll
  for (int mt = 0; mt < 2; ++mt)
#pragma unroll
    for (int ks = 0; ks < 2; ++ks) {
      const int row = 32 * wm + 16 * mt + r, ch = 4 * ks + g;
      aoff[mt][ks] = row * 128 + ((ch ^ ((row >> 1) & 7)) << 4);
    }
  int boff[6];
  {
    const int fp = (q >> 1) | ((g & 1) << 1);
#pragma unroll
    for (int j = 0; j < 6; ++j) {
      const int T = (j < 3 ? 0 : 6) + 3 * wn + (j % 3);
      boff[j] = 16384 + (T >> 2) * 8192 + (8 * g + q) * 128 + (((T & 3) ^ fp) << 5) + 8 * p;
    }
  }

  f32x4 acc[2][6];
#pragma unroll
  for (int mt = 0; mt < 2; ++mt)
#pragma unroll
    for (int j = 0; j < 6; ++j) acc[mt][j] = f32x4{0.f, 0.f, 0.f, 0.f};

  stage(0, 0);
  WAIT_VM(0);
  __syncthreads();
#pragma unroll
  for (int st = 0; st < 8; ++st) {
    if (st + 1 < 8) stage(st + 1, (st + 1) & 1);
    const char* base = smem + (st & 1) * F1_STAGE;
#pragma unroll
    for (int ks = 0; ks < 2; ++ks) {
      bf16x8 af[2], bfrag[6];
#pragma unroll
      for (int mt = 0; mt < 2; ++mt) af[mt] = *(const bf16x8*)(base + aoff[mt][ks]);
#pragma unroll
      for (int j = 0; j < 6; ++j) bfrag[j] = tr_frag(base, boff[j] + ks * 4096, boff[j] + ks * 4096 + 512);
#pragma unroll
      for (int mt = 0; mt < 2; ++mt)
#pragma unroll
        for (int j = 0; j < 6; ++j) acc[mt][j] = mfma16(bfrag[j], af[mt], acc[mt][j]);
    }
    WAIT_VM(0);
    __syncthreads();
  }

#pragma unroll
  for (int mt = 0; mt < 2; ++mt)
#pragma unroll
    for (int j = 0; j < 3; ++j) {
      const int f = f0 + 16 * (3 * wn + j);
      if (f < F) {
        float hv[4];
#pragma unroll
        for (int e = 0; e < 4; ++e) {
          const float gv = bfr(acc[mt][j][e]), uv = bfr(acc[mt][3 + j][e]);
          const float yy = 0.7978845608028654f * (gv + 0.044715f * gv * gv * gv);
          const float ge = gv * __builtin_amdgcn_rcpf(1.f + __builtin_amdgcn_exp2f(-2.885390081777927f * yy));
          hv[e] = bfr(ge) * uv;
        }
        u32x2 o = {pk2(hv[0], hv[1]), pk2(hv[2], hv[3])};
        *(u32x2*)(h + (size_t)(row0 + 32 * wm + 16 * mt + r) * HP + f + 4 * g) = o;
      }
    }
}

__device__ __forceinline__ void gate_block(int rb, const __bf16* __restrict__ x, const __bf16* __restrict__ gw,
                                           float* __restrict__ gout) {
  const int tid = threadIdx.x, row = rb * 128 + (tid >> 2), part = tid & 3;
  float s = 0.f;
#pragma unroll 4
  for (int c = 0; c < 16; ++c) {
    const int e0 = (c * 4 + part) * 8;
    const u32x4 xv = *(const u32x4*)(x + (size_t)row * D + e0);
    const u32x4 wv = *(const u32x4*)(gw + e0);
#pragma unroll
    for (int e = 0; e < 4; ++e) {
      s += bfr(lo_f(xv[e]) * lo_f(wv[e]));
      s += bfr(hi_f(xv[e]) * hi_f(wv[e]));
    }
  }
  s += __shfl_xor(s, 1);
  s += __shfl_xor(s, 2);
  if (part == 0) {
    const float sv = bfr(s);
    gout[row] = bfr(1.f / (1.f + __expf(-sv)));
  }
}

constexpr int UCP = 4160;
constexpr int KARR = (240 + L) * 2;
constexpr int OFF_K = 4 * UCP;
constexpr int OFF_PAR = OFF_K + 2 * KARR;
constexpr int OFF_TAB = OFF_PAR + 2048;
constexpr int CH_BYTES = OFF_TAB + 4 * 656 * 4;
constexpr int OFF_RED = UCP;
static_assert(OFF_RED + 32768 <= CH_BYTES, "reduce buffer");
static_assert(2 * CH_BYTES <= 2 * F1_STAGE, "lds");

__device__ __forceinline__ int pos_idx(int p) {
  return p < 256 ? p : p < 512 ? 256 + ((p - 256) >> 1) : p < 1024 ? 384 + ((p - 512) >> 2) : 512 + ((p - 1024) >> 3);
}
__device__ __forceinline__ int hi_pos(int m, int r) {
  if (m == 0) return 128 * (r >> 3) + (r & 7);
  if (m == 1) return r < 8 ? 256 + 128 * (r >> 2) + 2 * (r & 3) : 512 + 128 * ((r - 8) >> 1) + 4 * (r & 1);
  return r < 8 ? 1024 + 128 * r : 2048;
}
__device__ __forceinline__ void cexp(float zr, float zi, float p, float& er, float& ei) {
  const float e = __builtin_amdgcn_exp2f(zr * p);
  const float ph = __builtin_amdgcn_fractf(zi * p);
  er = e * __builtin_amdgcn_cosf(ph);
  ei = e * __builtin_amdgcn_sinf(ph);
}
__device__ __forceinline__ void split8(const float (&v)[8], bf16x8& hi, bf16x8& lo) {
  u32x4 h, l;
#pragma unroll
  for (int e = 0; e < 4; ++e) {
    h[e] = pk2(v[2 * e], v[2 * e + 1]);
    l[e] = pk2(v[2 * e] - lo_f(h[e]), v[2 * e + 1] - hi_f(h[e]));
  }
  hi = __builtin_bit_cast(bf16x8, h);
  lo = __builtin_bit_cast(bf16x8, l);
}

__device__ __forceinline__ void ssm_block(char* smem, int d0, const __bf16* __restrict__ u,
                                          const __bf16* __restrict__ lar, const __bf16* __restrict__ aim,
                                          const __bf16* __restrict__ cre, const __bf16* __restrict__ cim,
                                          const __bf16* __restrict__ ldt, const __bf16* __restrict__ dsk,
                                          __bf16* __restrict__ aT) {
  const int tid = threadIdx.x, lane = tid & 63, w = __builtin_amdgcn_readfirstlane(tid >> 6);
  const int ch = w >> 2, wq = w & 3, tc = tid & 255;
  const int r = lane & 15, g = lane >> 4, q = (lane >> 2) & 3, p = lane & 3;
  char* cb = smem + ch * CH_BYTES;

  unsigned uv[4];
#pragma unroll
  for (int i = 0; i < 4; ++i) uv[i] = *(const unsigned*)(u + (size_t)(4 * tid + i) * D + d0);
  if (tc < 128) {
    const int d = d0 + ch, n = tc;
    const float dt = bfr(expf((float)ldt[d]));
    const float ar = -bfr(expf((float)lar[d * NS + n]));
    const float zr = ar * dt, zi = (float)aim[d * NS + n] * dt;
    const float cr = (float)cre[d * NS + n] * dt, ci = (float)cim[d * NS + n] * dt;
    f32x4 pv = {zr * 1.4426950408889634f, zi * 0.15915494309189535f, 2.f * cr, 2.f * ci};
    *(f32x4*)(cb + OFF_PAR + n * 16) = pv;
  } else {
    const int i = tc - 128;
    const u32x4 z = {0u, 0u, 0u, 0u};
    if (i < 60) *(u32x4*)(cb + OFF_K + (i >= 30 ? KARR : 0) + (i % 30) * 16) = z;
    else if (i < 68) *(u32x4*)(cb + ((i - 60) >> 1) * UCP + ((i - 60) & 1) * 16) = z;
  }
  __syncthreads();

  {
    f32x4 acc[3];
#pragma unroll
    for (int m = 0; m < 3; ++m) acc[m] = f32x4{0.f, 0.f, 0.f, 0.f};
    float hp[3];
#pragma unroll
    for (int m = 0; m < 3; ++m) hp[m] = (float)hi_pos(m, r);
    const float lp = (float)(8 * r);
#pragma unroll
    for (int ss = 0; ss < 2; ++ss) {
      const int n0 = 16 * (2 * wq + ss) + 4 * g;
      f32x4 pv[4];
#pragma unroll
      for (int e = 0; e < 4; ++e) pv[e] = *(const f32x4*)(cb + OFF_PAR + (n0 + e) * 16);
      float v[8];
      bf16x8 bh, bl;
#pragma unroll
      for (int e = 0; e < 4; ++e) cexp(pv[e][0], pv[e][1], lp, v[2 * e], v[2 * e + 1]);
      split8(v, bh, bl);
#pragma unroll
      for (int m = 0; m < 3; ++m) {
#pragma unroll
        for (int e = 0; e < 4; ++e) {
          float er, ei;
          cexp(pv[e][0], pv[e][1], hp[m], er, ei);
          v[2 * e] = pv[e][2] * er - pv[e][3] * ei;
          v[2 * e + 1] = -(pv[e][2] * ei + pv[e][3] * er);
        }
        bf16x8 ah, al;
        split8(v, ah, al);
        acc[m] = mfma16(al, bh, acc[m]);
        acc[m] = mfma16(ah, bl, acc[m]);
        acc[m] = mfma16(ah, bh, acc[m]);
      }
    }
    float* tab = (float*)(cb + OFF_TAB) + wq * 656;
#pragma unroll
    for (int m = 0; m < 3; ++m)
#pragma unroll
      for (int e = 0; e < 4; ++e) {
        const int row = 4 * g + e, pp = hi_pos(m, row) + 8 * r;
        if (m < 2 || row < 8 || (row == 8 && r == 0)) tab[pos_idx(pp)] = acc[m][e];
      }
    {
      const unsigned a0 = (uv[0] & 0xffffu) | (uv[1] << 16), a1 = (uv[2] & 0xffffu) | (uv[3] << 16);
      const unsigned b0 = (uv[0] >> 16) | (uv[1] & 0xffff0000u), b1 = (uv[2] >> 16) | (uv[3] & 0xffff0000u);
      *(u32x2*)(smem + 32 + 8 * tid) = u32x2{a0, a1};
      *(u32x2*)(smem + CH_BYTES + 32 + 8 * tid) = u32x2{b0, b1};
    }
  }
  __syncthreads();

  {
    const float* tab = (const float*)(cb + OFF_TAB);
    float v[8];
#pragma unroll
    for (int e = 0; e < 8; ++e) {
      const int j = 8 * tc + e, id = pos_idx((int)bfr((float)j));
      v[e] = (tab[id] + tab[656 + id]) + (tab[2 * 656 + id] + tab[3 * 656 + id]);
    }
    bf16x8 kh, kl;
    split8(v, kh, kl);
    *(bf16x8*)(cb + OFF_K + 480 + 16 * tc) = kh;
    *(bf16x8*)(cb + OFF_K + KARR + 480 + 16 * tc) = kl;
    const u32x4 pa = *(const u32x4*)(cb + 16 + 16 * tc), pb = *(const u32x4*)(cb + 32 + 16 * tc);
    const u32x4 c1 = {__builtin_amdgcn_alignbit(pb[0], pa[3], 16), __builtin_amdgcn_alignbit(pb[1], pb[0], 16),
                      __builtin_amdgcn_alignbit(pb[2], pb[1], 16), __builtin_amdgcn_alignbit(pb[3], pb[2], 16)};
    const u32x4 c2 = {pa[3], pb[0], pb[1], pb[2]};
    const u32x4 c3 = {__builtin_amdgcn_alignbit(pa[3], pa[2], 16), __builtin_amdgcn_alignbit(pb[0], pa[3], 16),
                      __builtin_amdgcn_alignbit(pb[1], pb[0], 16), __builtin_amdgcn_alignbit(pb[2], pb[1], 16)};
    *(u32x4*)(cb + 1 * UCP + 32 + 16 * tc) = c1;
    *(u32x4*)(cb + 2 * UCP + 32 + 16 * tc) = c2;
    *(u32x4*)(cb + 3 * UCP + 32 + 16 * tc) = c3;
  }
  __syncthreads();

  f32x4 acc[8];
#pragma unroll
  for (int t = 0; t < 8; ++t) acc[t] = f32x4{0.f, 0.f, 0.f, 0.f};
#pragma unroll
  for (int rr = 0; rr < 2; ++rr) {
    const int rho = 2 * wq + rr;
    const int abase = q * UCP + 2 * (16 + 16 + 32 * rho - 8 * g + 4 * p);
    const int bbase = OFF_K + 2 * (240 - 16 - 32 * rho + 16 * r + 8 * g);
    bf16x8 af[8];
#pragma unroll
    for (int s = 0; s < 8; ++s) af[s] = tr_frag(cb, abase + 512 * s, abase + 512 * s - 8);
#pragma unroll
    for (int t = 0; t < 8; ++t) {
      const bf16x8 bh = *(const bf16x8*)(cb + bbase + 512 * t);
#pragma unroll
      for (int s = 0; s + t < 8; ++s) acc[s + t] = mfma16(af[s], bh, acc[s + t]);
      const bf16x8 bl = *(const bf16x8*)(cb + bbase + KARR + 512 * t);
#pragma unroll
      for (int s = 0; s + t < 8; ++s) acc[s + t] = mfma16(af[s], bl, acc[s + t]);
    }
  }
  __syncthreads();

#pragma unroll
  for (int t = 0; t < 8; ++t)
    if ((t >> 1) != wq) *(f32x4*)(cb + OFF_RED + (wq * 8 + t) * 1024 + lane * 16) = acc[t];
  __syncthreads();
  {
    const int d = d0 + ch;
    const float dsv = (float)dsk[d];
#pragma unroll
    for (int tt = 0; tt < 2; ++tt) {
      const int t = 2 * wq + tt;
      f32x4 own = f32x4{0.f, 0.f, 0.f, 0.f};
#pragma unroll
      for (int t2 = 0; t2 < 8; ++t2)
        if (t2 == t) own = acc[t2];
      f32x4 s = f32x4{0.f, 0.f, 0.f, 0.f};
#pragma unroll
      for (int sw = 0; sw < 4; ++sw) {
        const f32x4 o = *(const f32x4*)(cb + OFF_RED + (sw * 8 + t) * 1024 + lane * 16);
        s += (sw == wq) ? own : o;
      }
      const int l = 256 * t + 16 * r + 4 * g;
      const u32x2 uu = *(const u32x2*)(cb + 32 + 2 * l);
      const float a0 = bfr(s[0]) + bfr(lo_f(uu[0]) * dsv), a1 = bfr(s[1]) + bfr(hi_f(uu[0]) * dsv);
      const float a2 = bfr(s[2]) + bfr(lo_f(uu[1]) * dsv), a3 = bfr(s[3]) + bfr(hi_f(uu[1]) * dsv);
      *(u32x2*)(aT + (size_t)d * L + l) = u32x2{pk2(a0, a1), pk2(a2, a3)};
    }
  }
}

__global__ __launch_bounds__(512, 4) void k1(const __bf16* __restrict__ u, const __bf16* __restrict__ lar,
                                             const __bf16* __restrict__ aim, const __bf16* __restrict__ cre,
                                             const __bf16* __restrict__ cim, const __bf16* __restrict__ ldt,
                                             const __bf16* __restrict__ dsk, const __bf16* __restrict__ wg,
                                             const __bf16* __restrict__ wu, const __bf16* __restrict__ gw,
                                             __bf16* __restrict__ h, __bf16* __restrict__ aT,
                                             float* __restrict__ gout) {
  extern __shared__ __attribute__((aligned(16))) char smem[];
  const int b = blockIdx.x;
  const int xcd = b & 7, i = (b & 255) >> 3;
  if (b < 256) {
    const int rb = 4 * (xcd >> 1) + (i & 3), cb = 8 * (xcd & 1) + (i >> 2);
    if (cb < 15) ffn1_block(smem, rb, cb, u, wg, wu, h);
    else gate_block(rb, u, gw, gout);
  } else {
    ssm_block(smem, 64 * xcd + 2 * i, u, lar, aim, cre, cim, ldt, dsk, aT);
  }
}

constexpr int F2_NS = 8;
constexpr int F2_STAGE = 16384;
constexpr int F2_AT = F2_NS * F2_STAGE;
constexpr int F2_LDS = F2_AT + 8192;
constexpr int F2_NST = 22;

template <int N>
__device__ __forceinline__ void wait_vm() {
  if constexpr (N == 0) WAIT_VM(0);
  else if constexpr (N == 2) WAIT_VM(2);
  else if constexpr (N == 4) WAIT_VM(4);
  else if constexpr (N == 6) WAIT_VM(6);
  else if constexpr (N == 8) WAIT_VM(8);
  else if constexpr (N == 10) WAIT_VM(10);
  else if constexpr (N == 12) WAIT_VM(12);
  else if constexpr (N == 14) WAIT_VM(14);
  else static_assert(N < 0, "wait_vm");
}

template <int ST>
struct F2Loop {
  template <class StageFn, class CompFn>
  static __device__ __forceinline__ void run(StageFn& stage, CompFn& comp) {
    if constexpr (ST < F2_NST) {
      constexpr int rem = (F2_NST - 1 - ST) < (F2_NS - 2) ? (F2_NST - 1 - ST) : (F2_NS - 2);
      wait_vm<2 * rem>();
      WAIT_LGKM0();
      __builtin_amdgcn_s_barrier();
      if constexpr (ST + F2_NS - 1 < F2_NST) stage(ST + F2_NS - 1);
      comp(ST);
      F2Loop<ST + 1>::run(stage, comp);
    }
  }
};

__global__ __launch_bounds__(512, 2) void k2(const __bf16* __restrict__ h, const __bf16* __restrict__ wd,
                                             const __bf16* __restrict__ aT, const float* __restrict__ gin,
                                             __bf16* __restrict__ out) {
  extern __shared__ __attribute__((aligned(16))) char smem[];
  const int b = blockIdx.x, xcd = b & 7, i = b >> 3;
  const int row0 = 64 * (4 * xcd + (i & 3)), col0 = 64 * (i >> 2);
  const int tid = threadIdx.x, lane = tid & 63, w = __builtin_amdgcn_readfirstlane(tid >> 6);
  const int r = lane & 15, g = lane >> 4, q = (lane >> 2) & 3, p = lane & 3;
  const int kh = w >> 2, wm = (w >> 1) & 1, wn = w & 1;

  {
    const int dd = 8 * w + (lane >> 3), pos = lane & 7;
    const int fp = ((dd >> 1) & 1) | (((dd >> 3) & 1) << 1);
    const int c16 = (pos >> 1) ^ fp;
    glds16(aT + (size_t)(col0 + dd) * L + row0 + 16 * c16 + 8 * (pos & 1), smem + F2_AT + w * 1024);
  }
  const __bf16* asrc;
  {
    const int row = 8 * w + (lane >> 3), ch = (lane & 7) ^ ((row >> 1) & 7);
    asrc = h + (size_t)(row0 + row) * HP + ch * 8;
  }
  const int bk = 8 * w + (lane >> 3);
  const __bf16* bsrc;
  {
    const int pos = lane & 7, fp = ((bk >> 1) & 1) | (((bk >> 3) & 1) << 1);
    bsrc = wd + col0 + 16 * ((pos >> 1) ^ fp) + 8 * (pos & 1);
  }
  auto stage = [&](int st) {
    char* base = smem + (st % F2_NS) * F2_STAGE;
    glds16(asrc + st * 64, base + w * 1024);
    int k = st * 64 + bk;
    k = k > F - 1 ? F - 1 : k;
    glds16(bsrc + (size_t)k * D, base + 8192 + w * 1024);
  };
  int aoff[2], boff[2];
#pragma unroll
  for (int mt = 0; mt < 2; ++mt) {
    const int row = 32 * wm + 16 * mt + r, ch = 4 * kh + g;
    aoff[mt] = row * 128 + ((ch ^ ((row >> 1) & 7)) << 4);
  }
  const int fpl = (q >> 1) | ((g & 1) << 1);
#pragma unroll
  for (int nt = 0; nt < 2; ++nt) boff[nt] = 8192 + (32 * kh + 8 * g + q) * 128 + (((2 * wn + nt) ^ fpl) << 5) + 8 * p;

  f32x4 acc[2][2];
#pragma unroll
  for (int mt = 0; mt < 2; ++mt)
#pragma unroll
    for (int nt = 0; nt < 2; ++nt) acc[mt][nt] = f32x4{0.f, 0.f, 0.f, 0.f};

  auto comp = [&](int st) {
    if (st == F2_NST - 1 && kh == 1) return;
    const char* base = smem + (st % F2_NS) * F2_STAGE;
    bf16x8 af[2], bfrag[2];
#pragma unroll
    for (int mt = 0; mt < 2; ++mt) af[mt] = *(const bf16x8*)(base + aoff[mt]);
#pragma unroll
    for (int nt = 0; nt < 2; ++nt) bfrag[nt] = tr_frag(base, boff[nt], boff[nt] + 512);
#pragma unroll
    for (int mt = 0; mt < 2; ++mt)
#pragma unroll
      for (int nt = 0; nt < 2; ++nt) acc[mt][nt] = mfma16(bfrag[nt], af[mt], acc[mt][nt]);
  };

#pragma unroll
  for (int st = 0; st < F2_NS - 1; ++st) stage(st);
  F2Loop<0>::run(stage, comp);

  WAIT_LGKM0();
  __builtin_amdgcn_s_barrier();
  {
    char* red = smem + (w & 3) * 4096 + kh * 2048;
    const f32x4 o0 = kh ? acc[0][0] : acc[1][0], o1 = kh ? acc[0][1] : acc[1][1];
    *(f32x4*)(red + lane * 16) = o0;
    *(f32x4*)(red + 1024 + lane * 16) = o1;
  }
  __syncthreads();
  {
    const char* red = smem + (w & 3) * 4096 + (1 - kh) * 2048;
    const int l = row0 + 32 * wm + 16 * kh + r;
    const float gv = gin[l], og = bfr(1.f - gv);
#pragma unroll
    for (int nt = 0; nt < 2; ++nt) {
      const f32x4 o = *(const f32x4*)(red + nt * 1024 + lane * 16);
      const f32x4 own = kh ? acc[1][nt] : acc[0][nt];
      const f32x4 s = kh ? (o + own) : (own + o);
      const int dd = 32 * wn + 16 * nt + 4 * g + q;
      const int fp = ((dd >> 1) & 1) | (((dd >> 3) & 1) << 1);
      const int c16 = (2 * wm + kh) ^ fp;
      const s16x4 av = __builtin_amdgcn_ds_read_tr16_b64_v4i16(
          (lds_s16x4_ptr)(smem + F2_AT + dd * 128 + (c16 << 5) + 8 * p));
      float ov[4];
#pragma unroll
      for (int e = 0; e < 4; ++e) {
        const float a = bf2f((unsigned short)av[e]);
        ov[e] = bfr(gv * a) + bfr(og * bfr(s[e]));
      }
      *(u32x2*)(out + (size_t)l * D + col0 + 32 * wn + 16 * nt + 4 * g) = u32x2{pk2(ov[0], ov[1]), pk2(ov[2], ov[3])};
    }
  }
}

extern "C" void kernel_launch(void* const* d_in, const int* in_sizes, int n_in, void* d_out, int out_size,
                              void* d_ws, size_t ws_size, hipStream_t stream) {
  const __bf16* u = (const __bf16*)d_in[0];
  const __bf16* lar = (const __bf16*)d_in[1];
  const __bf16* aim = (const __bf16*)d_in[2];
  const __bf16* cre = (const __bf16*)d_in[3];
  const __bf16* cim = (const __bf16*)d_in[4];
  const __bf16* ldt = (const __bf16*)d_in[5];
  const __bf16* dsk = (const __bf16*)d_in[6];
  const __bf16* wg = (const __bf16*)d_in[8];
  const __bf16* wu = (const __bf16*)d_in[9];
  const __bf16* wd = (const __bf16*)d_in[10];
  const __bf16* gw = (const __bf16*)d_in[11];
  char* ws = (char*)d_ws;
  __bf16* h = (__bf16*)ws;
  __bf16* aT = (__bf16*)(ws + (size_t)L * HP * 2);
  float* gout = (float*)(ws + (size_t)L * HP * 2 + (size_t)D * L * 2);
  (void)hipFuncSetAttribute((const void*)k1, hipFuncAttributeMaxDynamicSharedMemorySize, 2 * F1_STAGE);
  (void)hipFuncSetAttribute((const void*)k2, hipFuncAttributeMaxDynamicSharedMemorySize, F2_LDS);
  hipLaunchKernelGGL(k1, dim3(512), dim3(512), 2 * F1_STAGE, stream, u, lar, aim, cre, cim, ldt, dsk, wg, wu, gw, h,
                     aT, gout);
  hipLaunchKernelGGL(k2, dim3(256), dim3(512), F2_LDS, stream, (const __bf16*)h, wd, (const __bf16*)aT,
                     (const float*)gout, (__bf16*)d_out);
}
```

```cpp
#include <hip/hip_runtime.h>
#include <hip/hip_bf16.h>

typedef __attribute__((ext_vector_type(8))) __bf16 bf16x8;
typedef __attribute__((ext_vector_type(2))) __bf16 bf16x2;
typedef __attribute__((ext_vector_type(4))) short s16x4;
typedef __attribute__((ext_vector_type(8))) short s16x8;
typedef __attribute__((ext_vector_type(4))) float f32x4;
typedef __attribute__((ext_vector_type(4))) unsigned u32x4;
typedef __attribute__((ext_vector_type(2))) unsigned u32x2;
typedef __attribute__((address_space(3))) s16x4* lds_s16x4_ptr;
typedef __attribute__((address_space(1))) unsigned gu32;
typedef __attribute__((address_space(1))) unsigned long long gu64;

#define K_LO 1

constexpr int L = 2048, D = 512, NS = 128, F = 1376, HP = 1408;
constexpr int LDS_BYTES = 81920;

__device__ __forceinline__ float bf2f(unsigned short v) { return __uint_as_float((unsigned)v << 16); }
__device__ __forceinline__ float bfr(float x) { return (float)(__bf16)x; }
__device__ __forceinline__ unsigned pk2(float a, float b) {
  bf16x2 v = {(__bf16)a, (__bf16)b};
  return __builtin_bit_cast(unsigned, v);
}
__device__ __forceinline__ float lo_f(unsigned w) { return __uint_as_float(w << 16); }
__device__ __forceinline__ float hi_f(unsigned w) { return __uint_as_float(w & 0xffff0000u); }

__device__ __forceinline__ f32x4 mfma16(bf16x8 a, bf16x8 b, f32x4 c) {
  return __builtin_amdgcn_mfma_f32_16x16x32_bf16(a, b, c, 0, 0, 0);
}
__device__ __forceinline__ bf16x8 tr_frag(const char* lds, int off0, int off1) {
  s16x4 a = __builtin_amdgcn_ds_read_tr16_b64_v4i16((lds_s16x4_ptr)(lds + off0));
  s16x4 b = __builtin_amdgcn_ds_read_tr16_b64_v4i16((lds_s16x4_ptr)(lds + off1));
  s16x8 v = {a[0], a[1], a[2], a[3], b[0], b[1], b[2], b[3]};
  return __builtin_bit_cast(bf16x8, v);
}
__device__ __forceinline__ void glds16(const void* sbase, unsigned voff, unsigned lds_addr) {
  unsigned keep;
  asm volatile("s_mov_b32 %0, m0\n\ts_mov_b32 m0, %3\n\ts_nop 0\n\tglobal_load_lds_dwordx4 %1, %2\n\ts_mov_b32 m0, %0"
               : "=&s"(keep) : "v"(voff), "s"(sbase), "s"(lds_addr) : "memory");
}
__device__ __forceinline__ void glds16v(const void* vptr, unsigned lds_addr) {
  unsigned keep;
  asm volatile("s_mov_b32 %0, m0\n\ts_mov_b32 m0, %2\n\ts_nop 0\n\tglobal_load_lds_dwordx4 %1, off\n\ts_mov_b32 m0, %0"
               : "=&s"(keep) : "v"(vptr), "s"(lds_addr) : "memory");
}
__device__ __forceinline__ unsigned lds_addr_of(const char* p) {
  return (unsigned)(uintptr_t)(const __attribute__((address_space(3))) char*)p;
}
#define STAMP(i)
#define STAMP_WAVE 0
#define WAIT_VM(n) asm volatile("s_waitcnt vmcnt(" #n ")" ::: "memory")
#define WAIT_LGKM0() asm volatile("s_waitcnt lgkmcnt(0)" ::: "memory")
template <int N>
__device__ __forceinline__ void wait_vm() {
  asm volatile("s_waitcnt vmcnt(%0)" ::"i"(N) : "memory");
}

#define STAMP_BASE 0
constexpr int F1_SLOT = 20480;

struct F1Ctx {
  const __bf16 *x, *wg, *wu, *wmix;
  unsigned voffA, voffB0, voffB2, lbase;
  int w;
  int aoff[2], boff[6];
};
__device__ __forceinline__ void f1_stage(const F1Ctx& c, int hs) {
  const unsigned base = c.lbase + (hs & 3) * F1_SLOT;
  glds16(c.x + hs * 32, c.voffA, base + c.w * 1024);
  if (c.w < 4) {
    glds16(c.wg + (size_t)hs * 32 * F, c.voffB0, base + 8192 + c.w * 1024);
    glds16(c.wu + (size_t)hs * 32 * F, c.voffB2, base + 8192 + 8192 + c.w * 1024);
  } else {
    glds16v(c.wmix + (size_t)hs * 32 * F, base + 8192 + c.w * 1024);
  }
}
__device__ __forceinline__ void f1_read(const F1Ctx& c, const char* smem, int hs, bf16x8 (&af)[2], bf16x8 (&bq)[6]) {
  const char* base = smem + (hs & 3) * F1_SLOT;
#pragma unroll
  for (int mt = 0; mt < 2; ++mt) af[mt] = *(const bf16x8*)(base + c.aoff[mt]);
#pragma unroll
  for (int j = 0; j < 6; ++j) bq[j] = tr_frag(base, c.boff[j], c.boff[j] + 512);
}
__device__ __forceinline__ void f1_mma(const bf16x8 (&af)[2], const bf16x8 (&bq)[6], f32x4 (&acc)[2][6]) {
#pragma unroll
  for (int mt = 0; mt < 2; ++mt)
#pragma unroll
    for (int j = 0; j < 6; ++j) acc[mt][j] = mfma16(bq[j], af[mt], acc[mt][j]);
}
template <int HS, bool LATE>
__device__ __forceinline__ void f1_loop(const F1Ctx& c, const char* smem, bf16x8 (&af)[2], bf16x8 (&bq)[6],
                                        f32x4 (&acc)[2][6]) {
  if constexpr (HS < 16) {
    constexpr int rem = (15 - HS) < 2 ? (15 - HS) : 2;
    wait_vm<rem*(LATE ? 2 : 3)>();
    WAIT_LGKM0();
    __builtin_amdgcn_s_barrier();
    STAMP(1 + HS);
    if constexpr (HS + 3 < 16) f1_stage(c, HS + 3);
    if constexpr (LATE) {
      if constexpr (HS > 0) f1_mma(af, bq, acc);
      __builtin_amdgcn_sched_barrier(0);
      f1_read(c, smem, HS, af, bq);
    } else {
      f1_read(c, smem, HS, af, bq);
      f1_mma(af, bq, acc);
    }
    f1_loop<HS + 1, LATE>(c, smem, af, bq, acc);
  } else if constexpr (LATE) {
    f1_mma(af, bq, acc);
  }
}

__device__ __forceinline__ void ffn1_block(char* smem, int rb, int cb, const __bf16* __restrict__ x,
                                           const __bf16* __restrict__ wg, const __bf16* __restrict__ wu,
                                           __bf16* __restrict__ h) {
  const int tid = threadIdx.x, lane = tid & 63, w = __builtin_amdgcn_readfirstlane(tid >> 6);
  const int row0 = rb * 128, f0 = cb * 96;
  const int wm = w >> 1, wn = w & 1;
  const int r = lane & 15, g = lane >> 4, q = (lane >> 2) & 3, p = lane & 3;
  STAMP(0);
  F1Ctx c;
  c.x = x; c.wg = wg; c.wu = wu; c.w = w; c.lbase = lds_addr_of(smem);
  {
    const int row = 16 * w + (lane >> 2), ch = (lane & 3) ^ ((-(row >> 2)) & 3);
    c.voffA = (unsigned)(((row0 + row) * D + ch * 8) * 2);
    const int kk = 8 * (w & 3) + (lane >> 3), pos = lane & 7;
    const int fp = ((kk >> 1) & 1) | (((kk >> 3) & 1) << 1);
    const int i = (pos >> 1) ^ fp;
    int c0 = f0 + 16 * i + 8 * (pos & 1);
    int c1 = f0 + 16 * ((4 + i) % 6) + 8 * (pos & 1);
    int c2 = f0 + 16 * (2 + i) + 8 * (pos & 1);
    c0 = c0 > F - 8 ? F - 8 : c0;
    c1 = c1 > F - 8 ? F - 8 : c1;
    c2 = c2 > F - 8 ? F - 8 : c2;
    c.voffB0 = (unsigned)((kk * F + c0) * 2);
    c.voffB2 = (unsigned)((kk * F + c2) * 2);
    c.wmix = (i < 2 ? wg : wu) + kk * F + c1;
  }
#pragma unroll
  for (int mt = 0; mt < 2; ++mt) {
    const int row = 32 * wm + 16 * mt + r;
    c.aoff[mt] = row * 64 + ((g ^ ((-(row >> 2)) & 3)) << 4);
  }
  {
    const int fp = (q >> 1) | ((g & 1) << 1);
#pragma unroll
    for (int j = 0; j < 6; ++j) {
      const int T = (j < 3 ? 0 : 6) + 3 * wn + (j % 3);
      c.boff[j] = 8192 + (T >> 2) * 4096 + (8 * g + q) * 128 + (((T & 3) ^ fp) << 5) + 8 * p;
    }
  }
  f32x4 acc[2][6];
#pragma unroll
  for (int mt = 0; mt < 2; ++mt)
#pragma unroll
    for (int j = 0; j < 6; ++j) acc[mt][j] = f32x4{0.f, 0.f, 0.f, 0.f};
  bf16x8 af[2], bq[6];

  f1_stage(c, 0);
  f1_stage(c, 1);
  f1_stage(c, 2);
  if (w < 4) f1_loop<0, false>(c, smem, af, bq, acc);
  else f1_loop<0, true>(c, smem, af, bq, acc);
  STAMP(17);

#pragma unroll
  for (int mt = 0; mt < 2; ++mt)
#pragma unroll
    for (int j = 0; j < 3; ++j) {
      const int f = f0 + 16 * (3 * wn + j);
      if (f < F) {
        const unsigned g01 = pk2(acc[mt][j][0], acc[mt][j][1]), g23 = pk2(acc[mt][j][2], acc[mt][j][3]);
        const unsigned u01 = pk2(acc[mt][3 + j][0], acc[mt][3 + j][1]), u23 = pk2(acc[mt][3 + j][2], acc[mt][3 + j][3]);
        const f32x4 gv = {lo_f(g01), hi_f(g01), lo_f(g23), hi_f(g23)};
        const f32x4 uv = {lo_f(u01), hi_f(u01), lo_f(u23), hi_f(u23)};
        const f32x4 t = gv * (gv * gv * -0.10294324f + -2.3022082f);
        f32x4 ge;
#pragma unroll
        for (int e = 0; e < 4; ++e) ge[e] = __builtin_amdgcn_rcpf(1.f + __builtin_amdgcn_exp2f(t[e]));
        ge = ge * gv;
        const unsigned e01 = pk2(ge[0], ge[1]), e23 = pk2(ge[2], ge[3]);
        const f32x4 hv = f32x4{lo_f(e01), hi_f(e01), lo_f(e23), hi_f(e23)} * uv;
        u32x2 o = {pk2(hv[0], hv[1]), pk2(hv[2], hv[3])};
        *(u32x2*)(h + (size_t)(row0 + 32 * wm + 16 * mt + r) * HP + f + 4 * g) = o;
      }
    }
  STAMP(18);
}

__device__ __forceinline__ void gate_block(char* smem, int rb, const __bf16* __restrict__ x,
                                           const __bf16* __restrict__ gw, float* __restrict__ gout,
                                           __bf16* __restrict__ uT) {
  const int tid = threadIdx.x, lane = tid & 63, w = __builtin_amdgcn_readfirstlane(tid >> 6);
  const int g = lane >> 4, q = (lane >> 2) & 3, p = lane & 3;
  const unsigned lbase = lds_addr_of(smem);
#pragma unroll 1
  for (int pass = 0; pass < 2; ++pass) {
    const int l0 = rb * 128 + pass * 64;
#pragma unroll
    for (int i = 0; i < 8; ++i) {
      const int row = 8 * w + i, fr = (row & 3) | (((row >> 3) & 1) << 2);
      glds16(x + (size_t)(l0 + row) * D, (unsigned)((((lane >> 1) ^ fr) * 2 + (lane & 1)) * 16), lbase + row * 1024);
    }
    WAIT_VM(0);
    __syncthreads();
#pragma unroll
    for (int i = 0; i < 8; ++i) {
      const int unit = w * 8 + i, c16 = unit >> 1, half = unit & 1;
      const int ra = 32 * half + 8 * g + q, rb2 = ra + 4;
      const int fa = (ra & 3) | (((ra >> 3) & 1) << 2);
      const bf16x8 v = tr_frag(smem, ra * 1024 + ((c16 ^ fa) << 5) + 8 * p, rb2 * 1024 + ((c16 ^ fa) << 5) + 8 * p);
      *(bf16x8*)(uT + (size_t)(16 * c16 + (lane & 15)) * L + l0 + 32 * half + 8 * g) = v;
    }
    __syncthreads();
  }
  const int row = rb * 128 + (tid >> 2), part = tid & 3;
  float s = 0.f;
#pragma unroll 4
  for (int c = 0; c < 16; ++c) {
    const int e0 = (c * 4 + part) * 8;
    const u32x4 xv = *(const u32x4*)(x + (size_t)row * D + e0);
    const u32x4 wv = *(const u32x4*)(gw + e0);
#pragma unroll
    for (int e = 0; e < 4; ++e) {
      s += bfr(lo_f(xv[e]) * lo_f(wv[e]));
      s += bfr(hi_f(xv[e]) * hi_f(wv[e]));
    }
  }
  s += __shfl_xor(s, 1);
  s += __shfl_xor(s, 2);
  if (part == 0) {
    const float sv = bfr(s);
    gout[row] = bfr(1.f / (1.f + __expf(-sv)));
  }
}

constexpr int KG_PAR = 0;
constexpr int KG_TAB = 2048;
constexpr int KG_BYTES = KG_TAB + 4 * 656 * 4;

__device__ __forceinline__ int pos_idx(int p) {
  const int sh = (p >= 256) + (p >= 512) + (p >= 1024);
  return 128 * sh + (p >> sh);
}
__device__ __forceinline__ int hi_pos(int m, int r) {
  if (m == 0) return 128 * (r >> 3) + (r & 7);
  if (m == 1) return r < 8 ? 256 + 128 * (r >> 2) + 2 * (r & 3) : 512 + 128 * ((r - 8) >> 1) + 4 * (r & 1);
  return r < 8 ? 1024 + 128 * r : 2048;
}
__device__ __forceinline__ void cexp(float zr, float zi, float p, float& er, float& ei) {
  const float e = __builtin_amdgcn_exp2f(zr * p);
  const float ph = __builtin_amdgcn_fractf(zi * p);
  er = e * __builtin_amdgcn_cosf(ph);
  ei = e * __builtin_amdgcn_sinf(ph);
}
__device__ __forceinline__ void split8(const float (&v)[8], bf16x8& hi, bf16x8& lo) {
  u32x4 h, l;
#pragma unroll
  for (int e = 0; e < 4; ++e) {
    h[e] = pk2(v[2 * e], v[2 * e + 1]);
    l[e] = pk2(v[2 * e] - lo_f(h[e]), v[2 * e + 1] - hi_f(h[e]));
  }
  hi = __builtin_bit_cast(bf16x8, h);
  lo = __builtin_bit_cast(bf16x8, l);
}

__device__ __forceinline__ void kgen_block(char* smem, int d0, const __bf16* __restrict__ lar,
                                           const __bf16* __restrict__ aim, const __bf16* __restrict__ cre,
                                           const __bf16* __restrict__ cim, const __bf16* __restrict__ ldt,
                                           __bf16* __restrict__ kbuf) {
  const int tid = threadIdx.x, lane = tid & 63, w = __builtin_amdgcn_readfirstlane(tid >> 6);
  const int ch = w >> 2, wq = w & 3, tc = tid & 255;
  const int r = lane & 15, g = lane >> 4;
  char* cb = smem + ch * KG_BYTES;
  STAMP(0);
  if (tc < 128) {
    const int d = d0 + ch, n = tc;
    const float dt = bfr(expf((float)ldt[d]));
    const float ar = -bfr(expf((float)lar[d * NS + n]));
    const float zr = ar * dt, zi = (float)aim[d * NS + n] * dt;
    const float cr = (float)cre[d * NS + n] * dt, ci = (float)cim[d * NS + n] * dt;
    f32x4 pv = {zr * 1.4426950408889634f, zi * 0.15915494309189535f, 2.f * cr, 2.f * ci};
    *(f32x4*)(cb + KG_PAR + n * 16) = pv;
  }
  __syncthreads();
  STAMP(1);
  {
    f32x4 acc[3];
#pragma unroll
    for (int m = 0; m < 3; ++m) acc[m] = f32x4{0.f, 0.f, 0.f, 0.f};
    float hp[3];
#pragma unroll
    for (int m = 0; m < 3; ++m) hp[m] = (float)hi_pos(m, r);
    const float lp = (float)(8 * r);
#pragma unroll
    for (int ss = 0; ss < 2; ++ss) {
      const int n0 = 16 * (2 * wq + ss) + 4 * g;
      f32x4 pv[4];
#pragma unroll
      for (int e = 0; e < 4; ++e) pv[e] = *(const f32x4*)(cb + KG_PAR + (n0 + e) * 16);
      float v[8];
      bf16x8 bh, bl;
#pragma unroll
      for (int e = 0; e < 4; ++e) cexp(pv[e][0], pv[e][1], lp, v[2 * e], v[2 * e + 1]);
      split8(v, bh, bl);
#pragma unroll
      for (int m = 0; m < 3; ++m) {
#pragma unroll
        for (int e = 0; e < 4; ++e) {
          float er, ei;
          cexp(pv[e][0], pv[e][1], hp[m], er, ei);
          v[2 * e] = pv[e][2] * er - pv[e][3] * ei;
          v[2 * e + 1] = -(pv[e][2] * ei + pv[e][3] * er);
        }
        bf16x8 ah, al;
        split8(v, ah, al);
        acc[m] = mfma16(al, bh, acc[m]);
        acc[m] = mfma16(ah, bl, acc[m]);
        acc[m] = mfma16(ah, bh, acc[m]);
      }
    }
    float* tab = (float*)(cb + KG_TAB) + wq * 656;
#pragma unroll
    for (int m = 0; m < 3; ++m)
#pragma unroll
      for (int e = 0; e < 4; ++e) {
        const int row = 4 * g + e, pp = hi_pos(m, row) + 8 * r;
        if (m < 2 || row < 8 || (row == 8 && r == 0)) tab[pos_idx(pp)] = acc[m][e];
      }
  }
  __syncthreads();
  STAMP(2);
  {
    const float* tab = (const float*)(cb + KG_TAB);
    float v[8];
#pragma unroll
    for (int e = 0; e < 8; ++e) {
      const int j = 8 * tc + e, id = pos_idx((int)bfr((float)j));
      v[e] = (tab[id] + tab[656 + id]) + (tab[2 * 656 + id] + tab[3 * 656 + id]);
    }
    bf16x8 kh, kl;
    split8(v, kh, kl);
    __bf16* kd = kbuf + (size_t)(d0 + ch) * 2 * L;
    *(bf16x8*)(kd + 8 * tc) = kh;
    *(bf16x8*)(kd + L + 8 * tc) = kl;
  }
  STAMP(3);
}

__global__ __launch_bounds__(512, 4) void k1(const __bf16* __restrict__ u, const __bf16* __restrict__ lar,
                                             const __bf16* __restrict__ aim, const __bf16* __restrict__ cre,
                                             const __bf16* __restrict__ cim, const __bf16* __restrict__ ldt,
                                             const __bf16* __restrict__ wg, const __bf16* __restrict__ wu,
                                             const __bf16* __restrict__ gw, __bf16* __restrict__ h,
                                             __bf16* __restrict__ uT, __bf16* __restrict__ kbuf,
                                             float* __restrict__ gout, unsigned* __restrict__ flags) {
  extern __shared__ __attribute__((aligned(16))) char smem[];
  const int b = blockIdx.x;
  const int xcd = b & 7, i = (b & 255) >> 3;
  if (b < 256) {
    const int rb = 4 * (xcd >> 1) + (i & 3), cb = 8 * (xcd & 1) + (i >> 2);
    if (cb < 15) ffn1_block(smem, rb, cb, u, wg, wu, h);
    else gate_block(smem, rb, u, gw, gout, uT);
  } else {
    if (threadIdx.x == 0) flags[b - 256] = 0u;
    kgen_block(smem, 64 * xcd + 2 * i, lar, aim, cre, cim, ldt, kbuf);
  }
}

#undef STAMP_BASE
#define STAMP_BASE 512
constexpr int UCP = 4160;
constexpr int KARR = (240 + L) * 2;
constexpr int OFF_K = 4 * UCP;
constexpr int OFF_RED = UCP;
constexpr int CH_BYTES = OFF_RED + 32768;
static_assert(OFF_K + 2 * KARR <= CH_BYTES && 2 * CH_BYTES <= LDS_BYTES, "lds");

__device__ __forceinline__ void conv_block(char* smem, int d0, const __bf16* __restrict__ uT,
                                           const __bf16* __restrict__ kbuf, const __bf16* __restrict__ dsk,
                                           __bf16* __restrict__ aT, unsigned* __restrict__ flag) {
  const int tid = threadIdx.x, lane = tid & 63, w = __builtin_amdgcn_readfirstlane(tid >> 6);
  const int ch = w >> 2, wq = w & 3, tc = tid & 255;
  const int r = lane & 15, g = lane >> 4, q = (lane >> 2) & 3, p = lane & 3;
  char* cb = smem + ch * CH_BYTES;
  const int d = d0 + ch;
  STAMP(0);
  {
    const unsigned lb = lds_addr_of(cb);
    glds16(uT + (size_t)d * L + wq * 512, lane * 16, lb + 32 + wq * 1024);
    glds16(kbuf + (size_t)d * 2 * L + wq * 512, lane * 16, lb + OFF_K + 480 + wq * 1024);
    glds16(kbuf + (size_t)d * 2 * L + L + wq * 512, lane * 16, lb + OFF_K + KARR + 480 + wq * 1024);
    const u32x4 z = {0u, 0u, 0u, 0u};
    if (tc < 60) *(u32x4*)(cb + OFF_K + (tc >= 30 ? KARR : 0) + (tc % 30) * 16) = z;
    else if (tc < 68) *(u32x4*)(cb + ((tc - 60) >> 1) * UCP + ((tc - 60) & 1) * 16) = z;
  }
  const float dsv = (float)dsk[d];
  WAIT_VM(0);
  __syncthreads();
  STAMP(1);
  {
    const u32x4 pa = *(const u32x4*)(cb + 16 + 16 * tc), pb = *(const u32x4*)(cb + 32 + 16 * tc);
    const u32x4 c1 = {__builtin_amdgcn_alignbit(pb[0], pa[3], 16), __builtin_amdgcn_alignbit(pb[1], pb[0], 16),
                      __builtin_amdgcn_alignbit(pb[2], pb[1], 16), __builtin_amdgcn_alignbit(pb[3], pb[2], 16)};
    const u32x4 c2 = {pa[3], pb[0], pb[1], pb[2]};
    const u32x4 c3 = {__builtin_amdgcn_alignbit(pa[3], pa[2], 16), c1[0], c1[1], c1[2]};
    *(u32x4*)(cb + 1 * UCP + 32 + 16 * tc) = c1;
    *(u32x4*)(cb + 2 * UCP + 32 + 16 * tc) = c2;
    *(u32x4*)(cb + 3 * UCP + 32 + 16 * tc) = c3;
  }
  __syncthreads();
  STAMP(2);
  f32x4 acc[8];
#pragma unroll
  for (int t = 0; t < 8; ++t) acc[t] = f32x4{0.f, 0.f, 0.f, 0.f};
#pragma unroll
  for (int rr = 0; rr < 2; ++rr) {
    const int rho = 2 * wq + rr;
    const int abase = q * UCP + 2 * (16 + 16 + 32 * rho - 8 * g + 4 * p);
    const int bbase = OFF_K + 2 * (240 - 16 - 32 * rho + 16 * r + 8 * g);
    bf16x8 af[8];
#pragma unroll
    for (int s = 0; s < 8; ++s) af[s] = tr_frag(cb, abase + 512 * s, abase + 512 * s - 8);
#pragma unroll
    for (int t = 0; t < 8; ++t) {
      const bf16x8 bh = *(const bf16x8*)(cb + bbase + 512 * t);
#pragma unroll
      for (int s = 0; s + t < 8; ++s) acc[s + t] = mfma16(af[s], bh, acc[s + t]);
      const bf16x8 bl = *(const bf16x8*)(cb + bbase + KARR + 512 * t);
#pragma unroll
      for (int s = 0; s + t < 8; ++s) acc[s + t] = mfma16(af[s], bl, acc[s + t]);
    }
  }
  __syncthreads();
  STAMP(3);
#pragma unroll
  for (int t = 0; t < 8; ++t)
    if ((t >> 1) != wq) *(f32x4*)(cb + OFF_RED + (wq * 8 + t) * 1024 + lane * 16) = acc[t];
  __syncthreads();
#pragma unroll
  for (int tt = 0; tt < 2; ++tt) {
    const int t = 2 * wq + tt;
    f32x4 own = f32x4{0.f, 0.f, 0.f, 0.f};
#pragma unroll
    for (int t2 = 0; t2 < 8; ++t2)
      if (t2 == t) own = acc[t2];
    f32x4 s = f32x4{0.f, 0.f, 0.f, 0.f};
#pragma unroll
    for (int sw = 0; sw < 4; ++sw) {
      const f32x4 o = *(const f32x4*)(cb + OFF_RED + (sw * 8 + t) * 1024 + lane * 16);
      s += (sw == wq) ? own : o;
    }
    const int l = 256 * t + 16 * r + 4 * g;
    const u32x2 uu = *(const u32x2*)(cb + 32 + 2 * l);
    const float a0 = bfr(s[0]) + bfr(lo_f(uu[0]) * dsv), a1 = bfr(s[1]) + bfr(hi_f(uu[0]) * dsv);
    const float a2 = bfr(s[2]) + bfr(lo_f(uu[1]) * dsv), a3 = bfr(s[3]) + bfr(hi_f(uu[1]) * dsv);
    const unsigned long long pv = (unsigned long long)pk2(a0, a1) | ((unsigned long long)pk2(a2, a3) << 32);
    __hip_atomic_store((gu64*)(aT + (size_t)d * L + l), pv, __ATOMIC_RELAXED, __HIP_MEMORY_SCOPE_AGENT);
  }
  WAIT_VM(0);
  __syncthreads();
  if (tid == 0) __hip_atomic_store((gu32*)flag, 1u, __ATOMIC_RELAXED, __HIP_MEMORY_SCOPE_AGENT);
  STAMP(4);
}

constexpr int F2_NS = 5;
constexpr int F2_STAGE = 16384;
constexpr int F2_NST = 22;

struct F2Ctx {
  const __bf16 *h, *wd;
  unsigned avoff, bvoff, bvoff_last, lbase;
  int w, kh;
  int aoff[2], boff[2];
};
__device__ __forceinline__ void f2_stage(const F2Ctx& c, int st) {
  const unsigned base = c.lbase + (st % F2_NS) * F2_STAGE;
  glds16(c.h + st * 64, c.avoff, base + c.w * 1024);
  glds16(c.wd + (size_t)st * 64 * D, st == F2_NST - 1 ? c.bvoff_last : c.bvoff, base + 8192 + c.w * 1024);
}
template <int ST>
__device__ __forceinline__ void f2_loop(const F2Ctx& c, const char* smem, f32x4 (&acc)[2][2]) {
  if constexpr (ST < F2_NST) {
    constexpr int rem = (F2_NST - 1 - ST) < (F2_NS - 2) ? (F2_NST - 1 - ST) : (F2_NS - 2);
    wait_vm<2 * rem>();
    WAIT_LGKM0();
    __builtin_amdgcn_s_barrier();
    STAMP(1 + ST);
    if constexpr (ST + F2_NS - 1 < F2_NST) f2_stage(c, ST + F2_NS - 1);
    if (ST < F2_NST - 1 || c.kh == 0) {
      const char* base = smem + (ST % F2_NS) * F2_STAGE;
      bf16x8 af[2], bq[2];
#pragma unroll
      for (int mt = 0; mt < 2; ++mt) af[mt] = *(const bf16x8*)(base + c.aoff[mt]);
#pragma unroll
      for (int nt = 0; nt < 2; ++nt) bq[nt] = tr_frag(base, c.boff[nt], c.boff[nt] + 512);
#pragma unroll
      for (int mt = 0; mt < 2; ++mt)
#pragma unroll
        for (int nt = 0; nt < 2; ++nt) acc[mt][nt] = mfma16(af[mt], bq[nt], acc[mt][nt]);
    }
    f2_loop<ST + 1>(c, smem, acc);
  }
}

__device__ __forceinline__ void ffn2_block(char* smem, int row0, int cbk, const __bf16* __restrict__ h,
                                           const __bf16* __restrict__ wd, const __bf16* __restrict__ aT,
                                           const float* __restrict__ gin, const unsigned* __restrict__ flags,
                                           __bf16* __restrict__ out) {
  const int col0 = 64 * cbk;
  const int tid = threadIdx.x, lane = tid & 63, w = __builtin_amdgcn_readfirstlane(tid >> 6);
  const int r = lane & 15, g = lane >> 4, q = (lane >> 2) & 3, p = lane & 3;
  const int kh = w >> 2, wm = (w >> 1) & 1, wn = w & 1;
  STAMP(0);
  F2Ctx c;
  c.h = h; c.wd = wd; c.w = w; c.kh = kh; c.lbase = lds_addr_of(smem);
  {
    const int row = 8 * w + (lane >> 3), ch = (lane & 7) ^ ((row >> 1) & 7);
    c.avoff = (unsigned)(((row0 + row) * HP + ch * 8) * 2);
    const int bk = 8 * w + (lane >> 3);
    const int pos = lane & 7, fp = ((bk >> 1) & 1) | (((bk >> 3) & 1) << 1);
    const int cc = col0 + 16 * ((pos >> 1) ^ fp) + 8 * (pos & 1);
    c.bvoff = (unsigned)((bk * D + cc) * 2);
    c.bvoff_last = (unsigned)(((bk > 31 ? 31 : bk) * D + cc) * 2);
  }
#pragma unroll
  for (int mt = 0; mt < 2; ++mt) {
    const int row = 32 * wm + 16 * mt + r, ch = 4 * kh + g;
    c.aoff[mt] = row * 128 + ((ch ^ ((row >> 1) & 7)) << 4);
  }
  const int fpl = (q >> 1) | ((g & 1) << 1);
#pragma unroll
  for (int nt = 0; nt < 2; ++nt) c.boff[nt] = 8192 + (32 * kh + 8 * g + q) * 128 + (((2 * wn + nt) ^ fpl) << 5) + 8 * p;

  f32x4 acc[2][2];
#pragma unroll
  for (int mt = 0; mt < 2; ++mt)
#pragma unroll
    for (int nt = 0; nt < 2; ++nt) acc[mt][nt] = f32x4{0.f, 0.f, 0.f, 0.f};

#pragma unroll
  for (int st = 0; st < F2_NS - 1; ++st) f2_stage(c, st);
  f2_loop<0>(c, smem, acc);
  STAMP(23);

  if (w == 0) {
    const gu32* fl = (const gu32*)(flags + 32 * cbk + (lane & 31));
    for (int spin = 0; spin < (1 << 22); ++spin) {
      const unsigned v = __hip_atomic_load(fl, __ATOMIC_RELAXED, __HIP_MEMORY_SCOPE_AGENT);
      if (__all(v == 1u)) break;
      __builtin_amdgcn_s_sleep(2);
    }
  }
  WAIT_LGKM0();
  __builtin_amdgcn_s_barrier();
  asm volatile("" ::: "memory");
  const int lr = 32 * wm + 16 * kh + 4 * g;
  unsigned long long av[2];
#pragma unroll
  for (int nt = 0; nt < 2; ++nt)
    av[nt] = __hip_atomic_load((const gu64*)(aT + (size_t)(col0 + 32 * wn + 16 * nt + r) * L + row0 + lr),
                               __ATOMIC_RELAXED, __HIP_MEMORY_SCOPE_AGENT);
  const f32x4 gv = *(const f32x4*)(gin + row0 + lr);
  {
    char* red = smem + (w & 3) * 4096 + kh * 2048;
    const f32x4 o0 = kh ? acc[0][0] : acc[1][0], o1 = kh ? acc[0][1] : acc[1][1];
    *(f32x4*)(red + lane * 16) = o0;
    *(f32x4*)(red + 1024 + lane * 16) = o1;
  }
  __syncthreads();
  constexpr int OT = 16384, OTP = 144;
  {
    const char* red = smem + (w & 3) * 4096 + (1 - kh) * 2048;
#pragma unroll
    for (int nt = 0; nt < 2; ++nt) {
      const f32x4 o = *(const f32x4*)(red + nt * 1024 + lane * 16);
      const f32x4 own = kh ? acc[1][nt] : acc[0][nt];
      const f32x4 s = kh ? (o + own) : (own + o);
      const unsigned alo = (unsigned)av[nt], ahi = (unsigned)(av[nt] >> 32);
      const float a[4] = {lo_f(alo), hi_f(alo), lo_f(ahi), hi_f(ahi)};
#pragma unroll
      for (int e = 0; e < 4; ++e) {
        const float o2 = bfr(gv[e] * a[e]) + bfr(bfr(1.f - gv[e]) * bfr(s[e]));
        *(__bf16*)(smem + OT + (lr + e) * OTP + (32 * wn + 16 * nt + r) * 2) = (__bf16)o2;
      }
    }
  }
  __syncthreads();
  {
    const int row = tid >> 3, c8 = tid & 7;
    const u32x4 v = *(const u32x4*)(smem + OT + row * OTP + c8 * 16);
    *(u32x4*)(out + (size_t)(row0 + row) * D + col0 + c8 * 8) = v;
  }
  STAMP(24);
}

__global__ __launch_bounds__(512, 4) void k2(const __bf16* __restrict__ h, const __bf16* __restrict__ wd,
                                             const __bf16* __restrict__ uT, const __bf16* __restrict__ kbuf,
                                             const __bf16* __restrict__ dsk, __bf16* __restrict__ aT,
                                             const float* __restrict__ gin, unsigned* __restrict__ flags,
                                             __bf16* __restrict__ out) {
  extern __shared__ __attribute__((aligned(16))) char smem[];
  const int b = blockIdx.x, xcd = b & 7, i = (b & 255) >> 3;
  if (b < 256) {
    const int pair = 32 * xcd + i;
    conv_block(smem, 2 * pair, uT, kbuf, dsk, aT, flags + pair);
  } else {
    ffn2_block(smem, 64 * (4 * xcd + (i & 3)), i >> 2, h, wd, aT, gin, flags, out);
  }
}

extern "C" void kernel_launch(void* const* d_in, const int* in_sizes, int n_in, void* d_out, int out_size,
                              void* d_ws, size_t ws_size, hipStream_t stream) {
  const __bf16* u = (const __bf16*)d_in[0];
  const __bf16* lar = (const __bf16*)d_in[1];
  const __bf16* aim = (const __bf16*)d_in[2];
  const __bf16* cre = (const __bf16*)d_in[3];
  const __bf16* cim = (const __bf16*)d_in[4];
  const __bf16* ldt = (const __bf16*)d_in[5];
  const __bf16* dsk = (const __bf16*)d_in[6];
  const __bf16* wg = (const __bf16*)d_in[8];
  const __bf16* wu = (const __bf16*)d_in[9];
  const __bf16* wd = (const __bf16*)d_in[10];
  const __bf16* gw = (const __bf16*)d_in[11];
  char* ws = (char*)d_ws;
  __bf16* h = (__bf16*)ws;
  ws += (size_t)L * HP * 2;
  __bf16* aT = (__bf16*)ws;
  ws += (size_t)D * L * 2;
  __bf16* uT = (__bf16*)ws;
  ws += (size_t)D * L * 2;
  __bf16* kbuf = (__bf16*)ws;
  ws += (size_t)D * L * 4;
  float* gout = (float*)ws;
  ws += L * 4;
  unsigned* flags = (unsigned*)ws;
  (void)hipFuncSetAttribute((const void*)k1, hipFuncAttributeMaxDynamicSharedMemorySize, LDS_BYTES);
  (void)hipFuncSetAttribute((const void*)k2, hipFuncAttributeMaxDynamicSharedMemorySize, LDS_BYTES);
  hipLaunchKernelGGL(k1, dim3(512), dim3(512), LDS_BYTES, stream, u, lar, aim, cre, cim, ldt, wg, wu, gw, h, uT, kbuf,
                     gout, flags);
  hipLaunchKernelGGL(k2, dim3(512), dim3(512), LDS_BYTES, stream, (const __bf16*)h, wd, (const __bf16*)uT,
                     (const __bf16*)kbuf, dsk, aT, (const float*)gout, flags, (__bf16*)d_out);
}
```

```cpp
#include <hip/hip_runtime.h>
#include <hip/hip_bf16.h>

typedef __attribute__((ext_vector_type(8))) __bf16 bf16x8;
typedef __attribute__((ext_vector_type(2))) __bf16 bf16x2;
typedef __attribute__((ext_vector_type(4))) short s16x4;
typedef __attribute__((ext_vector_type(8))) short s16x8;
typedef __attribute__((ext_vector_type(4))) float f32x4;
typedef __attribute__((ext_vector_type(4))) unsigned u32x4;
typedef __attribute__((ext_vector_type(2))) unsigned u32x2;
typedef __attribute__((address_space(3))) s16x4* lds_s16x4_ptr;
typedef __attribute__((address_space(1))) unsigned gu32;
typedef __attribute__((address_space(1))) unsigned long long gu64;

#define K_LO 0

constexpr int L = 2048, D = 512, NS = 128, F = 1376, HP = 1408;
constexpr int LDS_BYTES = 81920;

__device__ __forceinline__ float bf2f(unsigned short v) { return __uint_as_float((unsigned)v << 16); }
__device__ __forceinline__ float bfr(float x) { return (float)(__bf16)x; }
__device__ __forceinline__ unsigned pk2(float a, float b) {
  bf16x2 v = {(__bf16)a, (__bf16)b};
  return __builtin_bit_cast(unsigned, v);
}
__device__ __forceinline__ float lo_f(unsigned w) { return __uint_as_float(w << 16); }
__device__ __forceinline__ float hi_f(unsigned w) { return __uint_as_float(w & 0xffff0000u); }

__device__ __forceinline__ f32x4 mfma16(bf16x8 a, bf16x8 b, f32x4 c) {
  return __builtin_amdgcn_mfma_f32_16x16x32_bf16(a, b, c, 0, 0, 0);
}
__device__ __forceinline__ bf16x8 tr_frag(const char* lds, int off0, int off1) {
  s16x4 a = __builtin_amdgcn_ds_read_tr16_b64_v4i16((lds_s16x4_ptr)(lds + off0));
  s16x4 b = __builtin_amdgcn_ds_read_tr16_b64_v4i16((lds_s16x4_ptr)(lds + off1));
  s16x8 v = {a[0], a[1], a[2], a[3], b[0], b[1], b[2], b[3]};
  return __builtin_bit_cast(bf16x8, v);
}
__device__ __forceinline__ void glds16(const void* sbase, unsigned voff, unsigned lds_addr) {
  unsigned keep;
  asm volatile("s_mov_b32 %0, m0\n\ts_mov_b32 m0, %3\n\ts_nop 0\n\tglobal_load_lds_dwordx4 %1, %2\n\ts_mov_b32 m0, %0"
               : "=&s"(keep) : "v"(voff), "s"(sbase), "s"(lds_addr) : "memory");
}
__device__ __forceinline__ void glds16v(const void* vptr, unsigned lds_addr) {
  unsigned keep;
  asm volatile("s_mov_b32 %0, m0\n\ts_mov_b32 m0, %2\n\ts_nop 0\n\tglobal_load_lds_dwordx4 %1, off\n\ts_mov_b32 m0, %0"
               : "=&s"(keep) : "v"(vptr), "s"(lds_addr) : "memory");
}
__device__ __forceinline__ unsigned lds_addr_of(const char* p) {
  return (unsigned)(uintptr_t)(const __attribute__((address_space(3))) char*)p;
}
#define STAMP(i)
#define STAMP_WAVE 0
#define WAIT_VM(n) asm volatile("s_waitcnt vmcnt(" #n ")" ::: "memory")
#define WAIT_LGKM0() asm volatile("s_waitcnt lgkmcnt(0)" ::: "memory")
template <int N>
__device__ __forceinline__ void wait_vm() {
  asm volatile("s_waitcnt vmcnt(%0)" ::"i"(N) : "memory");
}

#define STAMP_BASE 0
constexpr int F1_SLOT = 20480;

struct F1Ctx {
  const __bf16 *x, *wg, *wu, *wmix;
  unsigned voffA, voffB0, voffB2, lbase;
  int w;
  int aoff[2], boff[6];
};
__device__ __forceinline__ void f1_stage(const F1Ctx& c, int hs) {
  const unsigned base = c.lbase + (hs & 3) * F1_SLOT;
  glds16(c.x + hs * 32, c.voffA, base + c.w * 1024);
  if (c.w < 4) {
    glds16(c.wg + (size_t)hs * 32 * F, c.voffB0, base + 8192 + c.w * 1024);
    glds16(c.wu + (size_t)hs * 32 * F, c.voffB2, base + 8192 + 8192 + c.w * 1024);
  } else {
    glds16v(c.wmix + (size_t)hs * 32 * F, base + 8192 + c.w * 1024);
  }
}
__device__ __forceinline__ void f1_read(const F1Ctx& c, const char* smem, int hs, bf16x8 (&af)[2], bf16x8 (&bq)[6]) {
  const char* base = smem + (hs & 3) * F1_SLOT;
#pragma unroll
  for (int mt = 0; mt < 2; ++mt) af[mt] = *(const bf16x8*)(base + c.aoff[mt]);
#pragma unroll
  for (int j = 0; j < 6; ++j) bq[j] = tr_frag(base, c.boff[j], c.boff[j] + 512);
}
__device__ __forceinline__ void f1_mma(const bf16x8 (&af)[2], const bf16x8 (&bq)[6], f32x4 (&acc)[2][6]) {
#pragma unroll
  for (int mt = 0; mt < 2; ++mt)
#pragma unroll
    for (int j = 0; j < 6; ++j) acc[mt][j] = mfma16(bq[j], af[mt], acc[mt][j]);
}
template <int HS, bool LATE>
__device__ __forceinline__ void f1_loop(const F1Ctx& c, const char* smem, bf16x8 (&af)[2], bf16x8 (&bq)[6],
                                        f32x4 (&acc)[2][6]) {
  if constexpr (HS < 16) {
    constexpr int rem = (15 - HS) < 2 ? (15 - HS) : 2;
    wait_vm<rem*(LATE ? 2 : 3)>();
    WAIT_LGKM0();
    __builtin_amdgcn_s_barrier();
    STAMP(1 + HS);
    if constexpr (HS + 3 < 16) f1_stage(c, HS + 3);
    if constexpr (LATE) {
      if constexpr (HS > 0) f1_mma(af, bq, acc);
      __builtin_amdgcn_sched_barrier(0);
      f1_read(c, smem, HS, af, bq);
    } else {
      f1_read(c, smem, HS, af, bq);
      f1_mma(af, bq, acc);
    }
    f1_loop<HS + 1, LATE>(c, smem, af, bq, acc);
  } else if constexpr (LATE) {
    f1_mma(af, bq, acc);
  }
}

__device__ __forceinline__ void ffn1_block(char* smem, int rb, int cb, const __bf16* __restrict__ x,
                                           const __bf16* __restrict__ wg, const __bf16* __restrict__ wu,
                                           __bf16* __restrict__ h) {
  const int tid = threadIdx.x, lane = tid & 63, w = __builtin_amdgcn_readfirstlane(tid >> 6);
  const int row0 = rb * 128, f0 = cb * 96;
  const int wm = w >> 1, wn = w & 1;
  const int r = lane & 15, g = lane >> 4, q = (lane >> 2) & 3, p = lane & 3;
  STAMP(0);
  __builtin_amdgcn_s_setprio(2);
  F1Ctx c;
  c.x = x; c.wg = wg; c.wu = wu; c.w = w; c.lbase = lds_addr_of(smem);
  {
    const int row = 16 * w + (lane >> 2), ch = (lane & 3) ^ ((-(row >> 2)) & 3);
    c.voffA = (unsigned)(((row0 + row) * D + ch * 8) * 2);
    const int kk = 8 * (w & 3) + (lane >> 3), pos = lane & 7;
    const int fp = ((kk >> 1) & 1) | (((kk >> 3) & 1) << 1);
    const int i = (pos >> 1) ^ fp;
    int c0 = f0 + 16 * i + 8 * (pos & 1);
    int c1 = f0 + 16 * ((4 + i) % 6) + 8 * (pos & 1);
    int c2 = f0 + 16 * (2 + i) + 8 * (pos & 1);
    c0 = c0 > F - 8 ? F - 8 : c0;
    c1 = c1 > F - 8 ? F - 8 : c1;
    c2 = c2 > F - 8 ? F - 8 : c2;
    c.voffB0 = (unsigned)((kk * F + c0) * 2);
    c.voffB2 = (unsigned)((kk * F + c2) * 2);
    c.wmix = (i < 2 ? wg : wu) + kk * F + c1;
  }
#pragma unroll
  for (int mt = 0; mt < 2; ++mt) {
    const int row = 32 * wm + 16 * mt + r;
    c.aoff[mt] = row * 64 + ((g ^ ((-(row >> 2)) & 3)) << 4);
  }
  {
    const int fp = (q >> 1) | ((g & 1) << 1);
#pragma unroll
    for (int j = 0; j < 6; ++j) {
      const int T = (j < 3 ? 0 : 6) + 3 * wn + (j % 3);
      c.boff[j] = 8192 + (T >> 2) * 4096 + (8 * g + q) * 128 + (((T & 3) ^ fp) << 5) + 8 * p;
    }
  }
  f32x4 acc[2][6];
#pragma unroll
  for (int mt = 0; mt < 2; ++mt)
#pragma unroll
    for (int j = 0; j < 6; ++j) acc[mt][j] = f32x4{0.f, 0.f, 0.f, 0.f};
  bf16x8 af[2], bq[6];

  f1_stage(c, 0);
  f1_stage(c, 1);
  f1_stage(c, 2);
  if (w < 4) f1_loop<0, false>(c, smem, af, bq, acc);
  else f1_loop<0, true>(c, smem, af, bq, acc);
  STAMP(17);

#pragma unroll
  for (int mt = 0; mt < 2; ++mt)
#pragma unroll
    for (int j = 0; j < 3; ++j) {
      const int f = f0 + 16 * (3 * wn + j);
      if (f < F) {
        const unsigned g01 = pk2(acc[mt][j][0], acc[mt][j][1]), g23 = pk2(acc[mt][j][2], acc[mt][j][3]);
        const unsigned u01 = pk2(acc[mt][3 + j][0], acc[mt][3 + j][1]), u23 = pk2(acc[mt][3 + j][2], acc[mt][3 + j][3]);
        const f32x4 gv = {lo_f(g01), hi_f(g01), lo_f(g23), hi_f(g23)};
        const f32x4 uv = {lo_f(u01), hi_f(u01), lo_f(u23), hi_f(u23)};
        const f32x4 t = gv * (gv * gv * -0.10294324f + -2.3022082f);
        f32x4 ge;
#pragma unroll
        for (int e = 0; e < 4; ++e) ge[e] = __builtin_amdgcn_rcpf(1.f + __builtin_amdgcn_exp2f(t[e]));
        ge = ge * gv;
        const unsigned e01 = pk2(ge[0], ge[1]), e23 = pk2(ge[2], ge[3]);
        const f32x4 hv = f32x4{lo_f(e01), hi_f(e01), lo_f(e23), hi_f(e23)} * uv;
        u32x2 o = {pk2(hv[0], hv[1]), pk2(hv[2], hv[3])};
        *(u32x2*)(h + (size_t)(row0 + 32 * wm + 16 * mt + r) * HP + f + 4 * g) = o;
      }
    }
  STAMP(18);
}

__device__ __forceinline__ void gate_block(char* smem, int rb, const __bf16* __restrict__ x,
                                           const __bf16* __restrict__ gw, float* __restrict__ gout,
                                           __bf16* __restrict__ uT) {
  const int tid = threadIdx.x, lane = tid & 63, w = __builtin_amdgcn_readfirstlane(tid >> 6);
  const int g = lane >> 4, q = (lane >> 2) & 3, p = lane & 3;
  const unsigned lbase = lds_addr_of(smem);
#pragma unroll 1
  for (int pass = 0; pass < 2; ++pass) {
    const int l0 = rb * 128 + pass * 64;
#pragma unroll
    for (int i = 0; i < 8; ++i) {
      const int row = 8 * w + i, fr = (row & 3) | (((row >> 3) & 1) << 2);
      glds16(x + (size_t)(l0 + row) * D, (unsigned)((((lane >> 1) ^ fr) * 2 + (lane & 1)) * 16), lbase + row * 1024);
    }
    WAIT_VM(0);
    __syncthreads();
#pragma unroll
    for (int i = 0; i < 8; ++i) {
      const int unit = w * 8 + i, c16 = unit >> 1, half = unit & 1;
      const int ra = 32 * half + 8 * g + q, rb2 = ra + 4;
      const int fa = (ra & 3) | (((ra >> 3) & 1) << 2);
      const bf16x8 v = tr_frag(smem, ra * 1024 + ((c16 ^ fa) << 5) + 8 * p, rb2 * 1024 + ((c16 ^ fa) << 5) + 8 * p);
      *(bf16x8*)(uT + (size_t)(16 * c16 + (lane & 15)) * L + l0 + 32 * half + 8 * g) = v;
    }
    __syncthreads();
  }
  const int row = rb * 128 + (tid >> 2), part = tid & 3;
  float s = 0.f;
#pragma unroll 4
  for (int c = 0; c < 16; ++c) {
    const int e0 = (c * 4 + part) * 8;
    const u32x4 xv = *(const u32x4*)(x + (size_t)row * D + e0);
    const u32x4 wv = *(const u32x4*)(gw + e0);
#pragma unroll
    for (int e = 0; e < 4; ++e) {
      s += bfr(lo_f(xv[e]) * lo_f(wv[e]));
      s += bfr(hi_f(xv[e]) * hi_f(wv[e]));
    }
  }
  s += __shfl_xor(s, 1);
  s += __shfl_xor(s, 2);
  if (part == 0) {
    const float sv = bfr(s);
    gout[row] = bfr(1.f / (1.f + __expf(-sv)));
  }
}

constexpr int KG_PITCH = 160;
constexpr int KG_ARR = 16 * KG_PITCH;
constexpr int KG_TAB = 8 * 2 * KG_ARR;
typedef __attribute__((ext_vector_type(2))) float f32x2;

__device__ __forceinline__ int pos_idx(int p) {
  const int sh = (p >= 256) + (p >= 512) + (p >= 1024);
  return 128 * sh + (p >> sh);
}
__device__ __forceinline__ int hi_pos(int m, int r) {
  if (m == 0) return 128 * (r >> 3) + (r & 7);
  if (m == 1) return r < 8 ? 256 + 128 * (r >> 2) + 2 * (r & 3) : 512 + 4 * ((r >> 2) & 1) + 128 * (r & 3);
  return 1024 + 128 * r;
}
__device__ __forceinline__ f32x2 cexp(float zr, float zi, float p) {
  const float e = __builtin_amdgcn_exp2f(zr * p);
  const float ph = __builtin_amdgcn_fractf(zi * p);
  return f32x2{e * __builtin_amdgcn_cosf(ph), e * __builtin_amdgcn_sinf(ph)};
}
__device__ __forceinline__ f32x2 cmul(f32x2 a, f32x2 b) {
  return f32x2{a[0] * b[0] - a[1] * b[1], a[0] * b[1] + a[1] * b[0]};
}
__device__ __forceinline__ void split8(const float (&v)[8], bf16x8& hi, bf16x8& lo) {
  u32x4 h, l;
#pragma unroll
  for (int e = 0; e < 4; ++e) {
    h[e] = pk2(v[2 * e], v[2 * e + 1]);
    l[e] = pk2(v[2 * e] - lo_f(h[e]), v[2 * e + 1] - hi_f(h[e]));
  }
  hi = __builtin_bit_cast(bf16x8, h);
  lo = __builtin_bit_cast(bf16x8, l);
}
__device__ __forceinline__ void kg_put(char* dst, float re, float im) {
  const unsigned h = pk2(re, im);
  *(unsigned*)dst = h;
  *(unsigned*)(dst + KG_ARR) = pk2(re - lo_f(h), im - hi_f(h));
}

__device__ __forceinline__ void kgen_block(char* smem, int d0, const __bf16* __restrict__ lar,
                                           const __bf16* __restrict__ aim, const __bf16* __restrict__ cre,
                                           const __bf16* __restrict__ cim, const __bf16* __restrict__ ldt,
                                           __bf16* __restrict__ kbuf) {
  const int tid = threadIdx.x, lane = tid & 63, w = __builtin_amdgcn_readfirstlane(tid >> 6);
  const int ch = w >> 2, wq = w & 3, tc = tid & 255;
  const int r = lane & 15, g = lane >> 4, hh = lane >> 5;
  char* wb = smem + w * 2 * KG_ARR;
  char* tb = smem + KG_TAB + ch * (4 * 656 * 4);
  STAMP(0);
  float zr, zi;
  f32x2 cc;
  {
    const int d = d0 + ch, n = 32 * wq + (lane & 31);
    const float dt = bfr(expf((float)ldt[d]));
    const float ar = -bfr(expf((float)lar[d * NS + n]));
    zr = (ar * dt) * 1.4426950408889634f;
    zi = ((float)aim[d * NS + n] * dt) * 0.15915494309189535f;
    cc = f32x2{2.f * ((float)cre[d * NS + n] * dt), 2.f * ((float)cim[d * NS + n] * dt)};
  }
  STAMP(1);
  const float fh = (float)(1 + hh);
  const f32x2 e1 = cexp(zr, zi, 1.f), e8 = cexp(zr, zi, 8.f), e128 = cexp(zr, zi, 128.f);
  const f32x2 e24 = cexp(zr, zi, 2.f * fh);
  char* wr = wb + (8 * hh) * KG_PITCH + (lane & 31) * 4;
  const char* rd = wb + r * KG_PITCH + g * 16;
  f32x4 acc[3];
  bf16x8 bh[2], bl[2];
  {
    f32x2 v = cexp(zr, zi, 64.f * hh);
#pragma unroll
    for (int i = 0; i < 8; ++i) {
      kg_put(wr + i * KG_PITCH, v[0], v[1]);
      if (i < 7) v = cmul(v, e8);
    }
    __builtin_amdgcn_wave_barrier();
#pragma unroll
    for (int ss = 0; ss < 2; ++ss) {
      bh[ss] = *(const bf16x8*)(rd + ss * 64);
      bl[ss] = *(const bf16x8*)(rd + ss * 64 + KG_ARR);
    }
  }
#pragma unroll
  for (int m = 0; m < 3; ++m) {
    f32x2 v, st;
    if (m == 0) { v = hh ? cmul(cc, e128) : cc; st = e1; }
    else if (m == 1) { v = cmul(cc, cexp(zr, zi, 256.f * fh)); st = hh ? e128 : e24; }
    else { v = cmul(cc, cexp(zr, zi, 1024.f * fh)); st = e128; }
    const f32x2 v4 = (m == 1) ? cmul(v, hh ? e24 : e128) : v;
    __builtin_amdgcn_wave_barrier();
#pragma unroll
    for (int i = 0; i < 8; ++i) {
      kg_put(wr + i * KG_PITCH, v[0], -v[1]);
      if (m == 1 && i == 3) v = v4;
      else if (i < 7) v = cmul(v, st);
    }
    __builtin_amdgcn_wave_barrier();
    acc[m] = f32x4{0.f, 0.f, 0.f, 0.f};
#pragma unroll
    for (int ss = 0; ss < 2; ++ss) {
      const bf16x8 ah = *(const bf16x8*)(rd + ss * 64), al = *(const bf16x8*)(rd + ss * 64 + KG_ARR);
      acc[m] = mfma16(al, bh[ss], acc[m]);
      acc[m] = mfma16(ah, bl[ss], acc[m]);
      acc[m] = mfma16(ah, bh[ss], acc[m]);
    }
  }
  {
    float* tab = (float*)tb + wq * 656;
#pragma unroll
    for (int m = 0; m < 3; ++m)
#pragma unroll
      for (int e = 0; e < 4; ++e) {
        const int row = 4 * g + e, pp = hi_pos(m, row) + 8 * r;
        if (m < 2 || row < 8 || (row == 8 && r == 0)) tab[pos_idx(pp)] = acc[m][e];
      }
  }
  __syncthreads();
  STAMP(2);
  {
    const float* tab = (const float*)tb;
    float v[8];
#pragma unroll
    for (int e = 0; e < 8; ++e) {
      const int j = 8 * tc + e, id = pos_idx((int)bfr((float)j));
      v[e] = (tab[id] + tab[656 + id]) + (tab[2 * 656 + id] + tab[3 * 656 + id]);
    }
    bf16x8 kh, kl;
    split8(v, kh, kl);
    __bf16* kd = kbuf + (size_t)(d0 + ch) * 2 * L;
    *(bf16x8*)(kd + 8 * tc) = kh;
    *(bf16x8*)(kd + L + 8 * tc) = kl;
  }
  STAMP(3);
}

__global__ __launch_bounds__(512, 4) void k1(const __bf16* __restrict__ u, const __bf16* __restrict__ lar,
                                             const __bf16* __restrict__ aim, const __bf16* __restrict__ cre,
                                             const __bf16* __restrict__ cim, const __bf16* __restrict__ ldt,
                                             const __bf16* __restrict__ wg, const __bf16* __restrict__ wu,
                                             const __bf16* __restrict__ gw, __bf16* __restrict__ h,
                                             __bf16* __restrict__ uT, __bf16* __restrict__ kbuf,
                                             float* __restrict__ gout, unsigned* __restrict__ flags) {
  extern __shared__ __attribute__((aligned(16))) char smem[];
  const int b = blockIdx.x;
  const int xcd = b & 7, i = (b & 255) >> 3;
  if (b < 256) {
    const int rb = 4 * (xcd >> 1) + (i & 3), cb = 8 * (xcd & 1) + (i >> 2);
    if (cb < 15) ffn1_block(smem, rb, cb, u, wg, wu, h);
    else gate_block(smem, rb, u, gw, gout, uT);
  } else {
    if (threadIdx.x == 0) flags[b - 256] = 0u;
    kgen_block(smem, 64 * xcd + 2 * i, lar, aim, cre, cim, ldt, kbuf);
  }
}

#undef STAMP_BASE
#define STAMP_BASE 512
constexpr int UCP = 4160;
constexpr int KARR = (240 + L) * 2;
constexpr int OFF_K = 4 * UCP;
constexpr int OFF_RED = UCP;
constexpr int CH_BYTES = OFF_RED + 32768;
static_assert(OFF_K + 2 * KARR <= CH_BYTES && 2 * CH_BYTES <= LDS_BYTES, "lds");

__device__ __forceinline__ void conv_block(char* smem, int d0, const __bf16* __restrict__ uT,
                                           const __bf16* __restrict__ kbuf, const __bf16* __restrict__ dsk,
                                           __bf16* __restrict__ aT, unsigned* __restrict__ flag) {
  const int tid = threadIdx.x, lane = tid & 63, w = __builtin_amdgcn_readfirstlane(tid >> 6);
  const int ch = w >> 2, wq = w & 3, tc = tid & 255;
  const int r = lane & 15, g = lane >> 4, q = (lane >> 2) & 3, p = lane & 3;
  char* cb = smem + ch * CH_BYTES;
  const int d = d0 + ch;
  STAMP(0);
  {
    const unsigned lb = lds_addr_of(cb);
    glds16(uT + (size_t)d * L + wq * 512, lane * 16, lb + 32 + wq * 1024);
    glds16(kbuf + (size_t)d * 2 * L + wq * 512, lane * 16, lb + OFF_K + 480 + wq * 1024);
    glds16(kbuf + (size_t)d * 2 * L + L + wq * 512, lane * 16, lb + OFF_K + KARR + 480 + wq * 1024);
    const u32x4 z = {0u, 0u, 0u, 0u};
    if (tc < 60) *(u32x4*)(cb + OFF_K + (tc >= 30 ? KARR : 0) + (tc % 30) * 16) = z;
    else if (tc < 68) *(u32x4*)(cb + ((tc - 60) >> 1) * UCP + ((tc - 60) & 1) * 16) = z;
  }
  const float dsv = (float)dsk[d];
  WAIT_VM(0);
  __syncthreads();
  STAMP(1);
  {
    const u32x4 pa = *(const u32x4*)(cb + 16 + 16 * tc), pb = *(const u32x4*)(cb + 32 + 16 * tc);
    const u32x4 c1 = {__builtin_amdgcn_alignbit(pb[0], pa[3], 16), __builtin_amdgcn_alignbit(pb[1], pb[0], 16),
                      __builtin_amdgcn_alignbit(pb[2], pb[1], 16), __builtin_amdgcn_alignbit(pb[3], pb[2], 16)};
    const u32x4 c2 = {pa[3], pb[0], pb[1], pb[2]};
    const u32x4 c3 = {__builtin_amdgcn_alignbit(pa[3], pa[2], 16), c1[0], c1[1], c1[2]};
    *(u32x4*)(cb + 1 * UCP + 32 + 16 * tc) = c1;
    *(u32x4*)(cb + 2 * UCP + 32 + 16 * tc) = c2;
    *(u32x4*)(cb + 3 * UCP + 32 + 16 * tc) = c3;
  }
  __syncthreads();
  STAMP(2);
  f32x4 acc[8];
#pragma unroll
  for (int t = 0; t < 8; ++t) acc[t] = f32x4{0.f, 0.f, 0.f, 0.f};
#pragma unroll
  for (int rr = 0; rr < 2; ++rr) {
    const int rho = 2 * wq + rr;
    const int abase = q * UCP + 2 * (16 + 16 + 32 * rho - 8 * g + 4 * p);
    const int bbase = OFF_K + 2 * (240 - 16 - 32 * rho + 16 * r + 8 * g);
    bf16x8 af[8];
#pragma unroll
    for (int s = 0; s < 8; ++s) af[s] = tr_frag(cb, abase + 512 * s, abase + 512 * s - 8);
#pragma unroll
    for (int t = 0; t < 8; ++t) {
      const bf16x8 bh = *(const bf16x8*)(cb + bbase + 512 * t);
#pragma unroll
      for (int s = 0; s + t < 8; ++s) acc[s + t] = mfma16(af[s], bh, acc[s + t]);
    }
  }
  __syncthreads();
  STAMP(3);
#pragma unroll
  for (int t = 0; t < 8; ++t)
    if ((t >> 1) != wq) *(f32x4*)(cb + OFF_RED + (wq * 8 + t) * 1024 + lane * 16) = acc[t];
  __syncthreads();
#pragma unroll
  for (int tt = 0; tt < 2; ++tt) {
    const int t = 2 * wq + tt;
    f32x4 own = f32x4{0.f, 0.f, 0.f, 0.f};
#pragma unroll
    for (int t2 = 0; t2 < 8; ++t2)
      if (t2 == t) own = acc[t2];
    f32x4 s = f32x4{0.f, 0.f, 0.f, 0.f};
#pragma unroll
    for (int sw = 0; sw < 4; ++sw) {
      const f32x4 o = *(const f32x4*)(cb + OFF_RED + (sw * 8 + t) * 1024 + lane * 16);
      s += (sw == wq) ? own : o;
    }
    const int l = 256 * t + 16 * r + 4 * g;
    const u32x2 uu = *(const u32x2*)(cb + 32 + 2 * l);
    const float a0 = bfr(s[0]) + bfr(lo_f(uu[0]) * dsv), a1 = bfr(s[1]) + bfr(hi_f(uu[0]) * dsv);
    const float a2 = bfr(s[2]) + bfr(lo_f(uu[1]) * dsv), a3 = bfr(s[3]) + bfr(hi_f(uu[1]) * dsv);
    const unsigned long long pv = (unsigned long long)pk2(a0, a1) | ((unsigned long long)pk2(a2, a3) << 32);
    __hip_atomic_store((gu64*)(aT + (size_t)d * L + l), pv, __ATOMIC_RELAXED, __HIP_MEMORY_SCOPE_AGENT);
  }
  WAIT_VM(0);
  __syncthreads();
  if (tid == 0) __hip_atomic_store((gu32*)flag, 1u, __ATOMIC_RELAXED, __HIP_MEMORY_SCOPE_AGENT);
  STAMP(4);
}

constexpr int F2_NS = 5;
constexpr int F2_STAGE = 16384;
constexpr int F2_NST = 22;

struct F2Ctx {
  const __bf16 *h, *wd;
  unsigned avoff, bvoff, bvoff_last, lbase;
  int w, kh;
  int aoff[2], boff[2];
};
__device__ __forceinline__ void f2_stage(const F2Ctx& c, int st) {
  const unsigned base = c.lbase + (st % F2_NS) * F2_STAGE;
  glds16(c.h + st * 64, c.avoff, base + c.w * 1024);
  glds16(c.wd + (size_t)st * 64 * D, st == F2_NST - 1 ? c.bvoff_last : c.bvoff, base + 8192 + c.w * 1024);
}
template <int ST>
__device__ __forceinline__ void f2_loop(const F2Ctx& c, const char* smem, f32x4 (&acc)[2][2]) {
  if constexpr (ST < F2_NST) {
    constexpr int rem = (F2_NST - 1 - ST) < (F2_NS - 2) ? (F2_NST - 1 - ST) : (F2_NS - 2);
    wait_vm<2 * rem>();
    WAIT_LGKM0();
    __builtin_amdgcn_s_barrier();
    STAMP(1 + ST);
    if constexpr (ST + F2_NS - 1 < F2_NST) f2_stage(c, ST + F2_NS - 1);
    if (ST < F2_NST - 1 || c.kh == 0) {
      const char* base = smem + (ST % F2_NS) * F2_STAGE;
      bf16x8 af[2], bq[2];
#pragma unroll
      for (int mt = 0; mt < 2; ++mt) af[mt] = *(const bf16x8*)(base + c.aoff[mt]);
#pragma unroll
      for (int nt = 0; nt < 2; ++nt) bq[nt] = tr_frag(base, c.boff[nt], c.boff[nt] + 512);
#pragma unroll
      for (int mt = 0; mt < 2; ++mt)
#pragma unroll
        for (int nt = 0; nt < 2; ++nt) acc[mt][nt] = mfma16(af[mt], bq[nt], acc[mt][nt]);
    }
    f2_loop<ST + 1>(c, smem, acc);
  }
}

__device__ __forceinline__ void ffn2_block(char* smem, int row0, int cbk, const __bf16* __restrict__ h,
                                           const __bf16* __restrict__ wd, const __bf16* __restrict__ aT,
                                           const float* __restrict__ gin, const unsigned* __restrict__ flags,
                                           __bf16* __restrict__ out) {
  const int col0 = 64 * cbk;
  const int tid = threadIdx.x, lane = tid & 63, w = __builtin_amdgcn_readfirstlane(tid >> 6);
  const int r = lane & 15, g = lane >> 4, q = (lane >> 2) & 3, p = lane & 3;
  const int kh = w >> 2, wm = (w >> 1) & 1, wn = w & 1;
  STAMP(0);
  __builtin_amdgcn_s_setprio(2);
  F2Ctx c;
  c.h = h; c.wd = wd; c.w = w; c.kh = kh; c.lbase = lds_addr_of(smem);
  {
    const int row = 8 * w + (lane >> 3), ch = (lane & 7) ^ ((row >> 1) & 7);
    c.avoff = (unsigned)(((row0 + row) * HP + ch * 8) * 2);
    const int bk = 8 * w + (lane >> 3);
    const int pos = lane & 7, fp = ((bk >> 1) & 1) | (((bk >> 3) & 1) << 1);
    const int cc = col0 + 16 * ((pos >> 1) ^ fp) + 8 * (pos & 1);
    c.bvoff = (unsigned)((bk * D + cc) * 2);
    c.bvoff_last = (unsigned)(((bk > 31 ? 31 : bk) * D + cc) * 2);
  }
#pragma unroll
  for (int mt = 0; mt < 2; ++mt) {
    const int row = 32 * wm + 16 * mt + r, ch = 4 * kh + g;
    c.aoff[mt] = row * 128 + ((ch ^ ((row >> 1) & 7)) << 4);
  }
  const int fpl = (q >> 1) | ((g & 1) << 1);
#pragma unroll
  for (int nt = 0; nt < 2; ++nt) c.boff[nt] = 8192 + (32 * kh + 8 * g + q) * 128 + (((2 * wn + nt) ^ fpl) << 5) + 8 * p;

  f32x4 acc[2][2];
#pragma unroll
  for (int mt = 0; mt < 2; ++mt)
#pragma unroll
    for (int nt = 0; nt < 2; ++nt) acc[mt][nt] = f32x4{0.f, 0.f, 0.f, 0.f};

#pragma unroll
  for (int st = 0; st < F2_NS - 1; ++st) f2_stage(c, st);
  f2_loop<0>(c, smem, acc);
  STAMP(23);

  if (w == 0) {
    const gu32* fl = (const gu32*)(flags + 32 * cbk + (lane & 31));
    for (int spin = 0; spin < (1 << 22); ++spin) {
      const unsigned v = __hip_atomic_load(fl, __ATOMIC_RELAXED, __HIP_MEMORY_SCOPE_AGENT);
      if (__all(v == 1u)) break;
      __builtin_amdgcn_s_sleep(2);
    }
  }
  WAIT_LGKM0();
  __builtin_amdgcn_s_barrier();
  asm volatile("" ::: "memory");
  const int lr = 32 * wm + 16 * kh + 4 * g;
  unsigned long long av[2];
#pragma unroll
  for (int nt = 0; nt < 2; ++nt)
    av[nt] = __hip_atomic_load((const gu64*)(aT + (size_t)(col0 + 32 * wn + 16 * nt + r) * L + row0 + lr),
                               __ATOMIC_RELAXED, __HIP_MEMORY_SCOPE_AGENT);
  const f32x4 gv = *(const f32x4*)(gin + row0 + lr);
  {
    char* red = smem + (w & 3) * 4096 + kh * 2048;
    const f32x4 o0 = kh ? acc[0][0] : acc[1][0], o1 = kh ? acc[0][1] : acc[1][1];
    *(f32x4*)(red + lane * 16) = o0;
    *(f32x4*)(red + 1024 + lane * 16) = o1;
  }
  __syncthreads();
  constexpr int OT = 16384, OTP = 144;
  {
    const char* red = smem + (w & 3) * 4096 + (1 - kh) * 2048;
#pragma unroll
    for (int nt = 0; nt < 2; ++nt) {
      const f32x4 o = *(const f32x4*)(red + nt * 1024 + lane * 16);
      const f32x4 own = kh ? acc[1][nt] : acc[0][nt];
      const f32x4 s = kh ? (o + own) : (own + o);
      const unsigned alo = (unsigned)av[nt], ahi = (unsigned)(av[nt] >> 32);
      const float a[4] = {lo_f(alo), hi_f(alo), lo_f(ahi), hi_f(ahi)};
#pragma unroll
      for (int e = 0; e < 4; ++e) {
        const float o2 = bfr(gv[e] * a[e]) + bfr(bfr(1.f - gv[e]) * bfr(s[e]));
        *(__bf16*)(smem + OT + (lr + e) * OTP + (32 * wn + 16 * nt + r) * 2) = (__bf16)o2;
      }
    }
  }
  __syncthreads();
  {
    const int row = tid >> 3, c8 = tid & 7;
    const u32x4 v = *(const u32x4*)(smem + OT + row * OTP + c8 * 16);
    *(u32x4*)(out + (size_t)(row0 + row) * D + col0 + c8 * 8) = v;
  }
  STAMP(24);
}

__global__ __launch_bounds__(512, 4) void k2(const __bf16* __restrict__ h, const __bf16* __restrict__ wd,
                                             const __bf16* __restrict__ uT, const __bf16* __restrict__ kbuf,
                                             const __bf16* __restrict__ dsk, __bf16* __restrict__ aT,
                                             const float* __restrict__ gin, unsigned* __restrict__ flags,
                                             __bf16* __restrict__ out) {
  extern __shared__ __attribute__((aligned(16))) char smem[];
  const int b = blockIdx.x, xcd = b & 7, i = (b & 255) >> 3;
  if (b < 256) {
    const int pair = 32 * xcd + i;
    conv_block(smem, 2 * pair, uT, kbuf, dsk, aT, flags + pair);
  } else {
    ffn2_block(smem, 64 * (4 * xcd + (i & 3)), i >> 2, h, wd, aT, gin, flags, out);
  }
}

extern "C" void kernel_launch(void* const* d_in, const int* in_sizes, int n_in, void* d_out, int out_size,
                              void* d_ws, size_t ws_size, hipStream_t stream) {
  const __bf16* u = (const __bf16*)d_in[0];
  const __bf16* lar = (const __bf16*)d_in[1];
  const __bf16* aim = (const __bf16*)d_in[2];
  const __bf16* cre = (const __bf16*)d_in[3];
  const __bf16* cim = (const __bf16*)d_in[4];
  const __bf16* ldt = (const __bf16*)d_in[5];
  const __bf16* dsk = (const __bf16*)d_in[6];
  const __bf16* wg = (const __bf16*)d_in[8];
  const __bf16* wu = (const __bf16*)d_in[9];
  const __bf16* wd = (const __bf16*)d_in[10];
  const __bf16* gw = (const __bf16*)d_in[11];
  char* ws = (char*)d_ws;
  __bf16* h = (__bf16*)ws;
  ws += (size_t)L * HP * 2;
  __bf16* aT = (__bf16*)ws;
  ws += (size_t)D * L * 2;
  __bf16* uT = (__bf16*)ws;
  ws += (size_t)D * L * 2;
  __bf16* kbuf = (__bf16*)ws;
  ws += (size_t)D * L * 4;
  float* gout = (float*)ws;
  ws += L * 4;
  unsigned* flags = (unsigned*)ws;
  (void)hipFuncSetAttribute((const void*)k1, hipFuncAttributeMaxDynamicSharedMemorySize, LDS_BYTES);
  (void)hipFuncSetAttribute((const void*)k2, hipFuncAttributeMaxDynamicSharedMemorySize, LDS_BYTES);
  hipLaunchKernelGGL(k1, dim3(512), dim3(512), LDS_BYTES, stream, u, lar, aim, cre, cim, ldt, wg, wu, gw, h, uT, kbuf,
                     gout, flags);
  hipLaunchKernelGGL(k2, dim3(512), dim3(512), LDS_BYTES, stream, (const __bf16*)h, wd, (const __bf16*)uT,
                     (const __bf16*)kbuf, dsk, aT, (const float*)gout, flags, (__bf16*)d_out);
}
```

```cpp
#include <hip/hip_runtime.h>
#include <hip/hip_bf16.h>

typedef __attribute__((ext_vector_type(8))) __bf16 bf16x8;
typedef __attribute__((ext_vector_type(2))) __bf16 bf16x2;
typedef __attribute__((ext_vector_type(4))) short s16x4;
typedef __attribute__((ext_vector_type(8))) short s16x8;
typedef __attribute__((ext_vector_type(4))) float f32x4;
typedef __attribute__((ext_vector_type(4))) unsigned u32x4;
typedef __attribute__((ext_vector_type(2))) unsigned u32x2;
typedef __attribute__((address_space(3))) s16x4* lds_s16x4_ptr;
typedef __attribute__((address_space(1))) unsigned gu32;
typedef __attribute__((address_space(1))) unsigned long long gu64;

#define K_LO 1

#define NOPK __attribute__((target("no-packed-fp32-ops")))

constexpr int L = 2048, D = 512, NS = 128, F = 1376, HP = 1408;
constexpr int LDS_BYTES = 81920;

__device__ __forceinline__ float bf2f(unsigned short v) { return __uint_as_float((unsigned)v << 16); }
__device__ __forceinline__ float bfr(float x) { return (float)(__bf16)x; }
__device__ __forceinline__ unsigned pk2(float a, float b) {
  bf16x2 v = {(__bf16)a, (__bf16)b};
  return __builtin_bit_cast(unsigned, v);
}
__device__ __forceinline__ float lo_f(unsigned w) { return __uint_as_float(w << 16); }
__device__ __forceinline__ float hi_f(unsigned w) { return __uint_as_float(w & 0xffff0000u); }

__device__ __forceinline__ f32x4 mfma16(bf16x8 a, bf16x8 b, f32x4 c) {
  return __builtin_amdgcn_mfma_f32_16x16x32_bf16(a, b, c, 0, 0, 0);
}
__device__ __forceinline__ bf16x8 tr_frag(const char* lds, int off0, int off1) {
  s16x4 a = __builtin_amdgcn_ds_read_tr16_b64_v4i16((lds_s16x4_ptr)(lds + off0));
  s16x4 b = __builtin_amdgcn_ds_read_tr16_b64_v4i16((lds_s16x4_ptr)(lds + off1));
  s16x8 v = {a[0], a[1], a[2], a[3], b[0], b[1], b[2], b[3]};
  return __builtin_bit_cast(bf16x8, v);
}
__device__ __forceinline__ void glds16(const void* sbase, unsigned voff, unsigned lds_addr) {
  unsigned keep;
  asm volatile("s_mov_b32 %0, m0\n\ts_mov_b32 m0, %3\n\ts_nop 0\n\tglobal_load_lds_dwordx4 %1, %2\n\ts_mov_b32 m0, %0"
               : "=&s"(keep) : "v"(voff), "s"(sbase), "s"(lds_addr) : "memory");
}
__device__ __forceinline__ void glds16v(const void* vptr, unsigned lds_addr) {
  unsigned keep;
  asm volatile("s_mov_b32 %0, m0\n\ts_mov_b32 m0, %2\n\ts_nop 0\n\tglobal_load_lds_dwordx4 %1, off\n\ts_mov_b32 m0, %0"
               : "=&s"(keep) : "v"(vptr), "s"(lds_addr) : "memory");
}
__device__ __forceinline__ unsigned lds_addr_of(const char* p) {
  return (unsigned)(uintptr_t)(const __attribute__((address_space(3))) char*)p;
}
#define STAMP(i)
#define STAMP_WAVE 0
#define WAIT_VM(n) asm volatile("s_waitcnt vmcnt(" #n ")" ::: "memory")
#define WAIT_LGKM0() asm volatile("s_waitcnt lgkmcnt(0)" ::: "memory")
template <int N>
__device__ __forceinline__ void wait_vm() {
  asm volatile("s_waitcnt vmcnt(%0)" ::"i"(N) : "memory");
}

#define STAMP_BASE 0
constexpr int F1_SLOT = 20480;

struct F1Ctx {
  const __bf16 *x, *wg, *wu, *wmix;
  unsigned voffA, voffB0, voffB2, lbase;
  int w;
  int aoff[2], boff[6];
};
template <bool G2>
__device__ __forceinline__ void f1_stage(const F1Ctx& c, int hs) {
  const unsigned base = c.lbase + (hs & 3) * F1_SLOT;
  glds16(c.x + hs * 32, c.voffA, base + c.w * 1024);
  if constexpr (!G2) {
    glds16(c.wg + (size_t)hs * 32 * F, c.voffB0, base + 8192 + c.w * 1024);
    glds16(c.wu + (size_t)hs * 32 * F, c.voffB2, base + 8192 + 8192 + c.w * 1024);
  } else {
    glds16v(c.wmix + (size_t)hs * 32 * F, base + 8192 + c.w * 1024);
  }
}
template <int HS, bool G2>
__device__ __forceinline__ void f1_loop(const F1Ctx& c, const char* smem, bf16x8 (&ao)[2], bf16x8 (&bq)[6],
                                        f32x4 (&acc)[2][6]) {
  if constexpr (HS < 16) {
    constexpr int rem = (15 - HS) < 2 ? (15 - HS) : 2;
    wait_vm<rem*(G2 ? 2 : 3)>();
    WAIT_LGKM0();
    __builtin_amdgcn_s_barrier();
    asm volatile("" ::: "memory");
    STAMP(1 + HS);
    if constexpr (HS + 3 < 16) f1_stage<G2>(c, HS + 3);
    const char* base = smem + (HS & 3) * F1_SLOT;
    bf16x8 an[2];
#pragma unroll
    for (int mt = 0; mt < 2; ++mt) an[mt] = *(const bf16x8*)(base + c.aoff[mt]);
#pragma unroll
    for (int j = 0; j < 6; ++j) {
      if constexpr (HS > 0) {
        acc[0][j] = mfma16(bq[j], ao[0], acc[0][j]);
        acc[1][j] = mfma16(bq[j], ao[1], acc[1][j]);
      }
      __builtin_amdgcn_sched_barrier(0);
      bq[j] = tr_frag(base, c.boff[j], c.boff[j] + 512);
      __builtin_amdgcn_sched_barrier(0);
    }
    f1_loop<HS + 1, G2>(c, smem, an, bq, acc);
  } else {
#pragma unroll
    for (int j = 0; j < 6; ++j) {
      acc[0][j] = mfma16(bq[j], ao[0], acc[0][j]);
      acc[1][j] = mfma16(bq[j], ao[1], acc[1][j]);
    }
  }
}

__device__ __forceinline__ void ffn1_block(char* smem, int rb, int cb, const __bf16* __restrict__ x,
                                           const __bf16* __restrict__ wg, const __bf16* __restrict__ wu,
                                           __bf16* __restrict__ h) {
  const int tid = threadIdx.x, lane = tid & 63, w = __builtin_amdgcn_readfirstlane(tid >> 6);
  const int row0 = rb * 128, f0 = cb * 96;
  const int wm = w >> 1, wn = w & 1;
  const int r = lane & 15, g = lane >> 4, q = (lane >> 2) & 3, p = lane & 3;
  STAMP(0);
  __builtin_amdgcn_s_setprio(2);
  F1Ctx c;
  c.x = x; c.wg = wg; c.wu = wu; c.w = w; c.lbase = lds_addr_of(smem);
  {
    const int row = 16 * w + (lane >> 2), ch = (lane & 3) ^ ((-(row >> 2)) & 3);
    c.voffA = (unsigned)(((row0 + row) * D + ch * 8) * 2);
    const int kk = 8 * (w & 3) + (lane >> 3), pos = lane & 7;
    const int fp = ((kk >> 1) & 1) | (((kk >> 3) & 1) << 1);
    const int i = (pos >> 1) ^ fp;
    int c0 = f0 + 16 * i + 8 * (pos & 1);
    int c1 = f0 + 16 * ((4 + i) % 6) + 8 * (pos & 1);
    int c2 = f0 + 16 * (2 + i) + 8 * (pos & 1);
    c0 = c0 > F - 8 ? F - 8 : c0;
    c1 = c1 > F - 8 ? F - 8 : c1;
    c2 = c2 > F - 8 ? F - 8 : c2;
    c.voffB0 = (unsigned)((kk * F + c0) * 2);
    c.voffB2 = (unsigned)((kk * F + c2) * 2);
    c.wmix = (i < 2 ? wg : wu) + kk * F + c1;
  }
#pragma unroll
  for (int mt = 0; mt < 2; ++mt) {
    const int row = 32 * wm + 16 * mt + r;
    c.aoff[mt] = row * 64 + ((g ^ ((-(row >> 2)) & 3)) << 4);
  }
  {
    const int fp = (q >> 1) | ((g & 1) << 1);
#pragma unroll
    for (int j = 0; j < 6; ++j) {
      const int T = (j < 3 ? 0 : 6) + 3 * wn + (j % 3);
      c.boff[j] = 8192 + (T >> 2) * 4096 + (8 * g + q) * 128 + (((T & 3) ^ fp) << 5) + 8 * p;
    }
  }
  f32x4 acc[2][6];
#pragma unroll
  for (int mt = 0; mt < 2; ++mt)
#pragma unroll
    for (int j = 0; j < 6; ++j) acc[mt][j] = f32x4{0.f, 0.f, 0.f, 0.f};
  bf16x8 af[2], bq[6];

  if (w < 4) {
    f1_stage<false>(c, 0);
    f1_stage<false>(c, 1);
    f1_stage<false>(c, 2);
    f1_loop<0, false>(c, smem, af, bq, acc);
  } else {
    f1_stage<true>(c, 0);
    f1_stage<true>(c, 1);
    f1_stage<true>(c, 2);
    f1_loop<0, true>(c, smem, af, bq, acc);
  }
  STAMP(17);

#pragma unroll
  for (int mt = 0; mt < 2; ++mt)
#pragma unroll
    for (int j = 0; j < 3; ++j) {
      const int f = f0 + 16 * (3 * wn + j);
      if (f < F) {
        const unsigned g01 = pk2(acc[mt][j][0], acc[mt][j][1]), g23 = pk2(acc[mt][j][2], acc[mt][j][3]);
        const unsigned u01 = pk2(acc[mt][3 + j][0], acc[mt][3 + j][1]), u23 = pk2(acc[mt][3 + j][2], acc[mt][3 + j][3]);
        const f32x4 gv = {lo_f(g01), hi_f(g01), lo_f(g23), hi_f(g23)};
        const f32x4 uv = {lo_f(u01), hi_f(u01), lo_f(u23), hi_f(u23)};
        const f32x4 t = gv * (gv * gv * -0.10294324f + -2.3022082f);
        f32x4 ge;
#pragma unroll
        for (int e = 0; e < 4; ++e) ge[e] = __builtin_amdgcn_rcpf(1.f + __builtin_amdgcn_exp2f(t[e]));
        ge = ge * gv;
        const unsigned e01 = pk2(ge[0], ge[1]), e23 = pk2(ge[2], ge[3]);
        const f32x4 hv = f32x4{lo_f(e01), hi_f(e01), lo_f(e23), hi_f(e23)} * uv;
        u32x2 o = {pk2(hv[0], hv[1]), pk2(hv[2], hv[3])};
        *(u32x2*)(h + (size_t)(row0 + 32 * wm + 16 * mt + r) * HP + f + 4 * g) = o;
      }
    }
  STAMP(18);
}

__device__ __forceinline__ void gate_block(char* smem, int rb, const __bf16* __restrict__ x,
                                           const __bf16* __restrict__ gw, float* __restrict__ gout,
                                           __bf16* __restrict__ uT) {
  const int tid = threadIdx.x, lane = tid & 63, w = __builtin_amdgcn_readfirstlane(tid >> 6);
  const int g = lane >> 4, q = (lane >> 2) & 3, p = lane & 3;
  const unsigned lbase = lds_addr_of(smem);
#pragma unroll 1
  for (int pass = 0; pass < 2; ++pass) {
    const int l0 = rb * 128 + pass * 64;
#pragma unroll
    for (int i = 0; i < 8; ++i) {
      const int row = 8 * w + i, fr = (row & 3) | (((row >> 3) & 1) << 2);
      glds16(x + (size_t)(l0 + row) * D, (unsigned)((((lane >> 1) ^ fr) * 2 + (lane & 1)) * 16), lbase + row * 1024);
    }
    WAIT_VM(0);
    __syncthreads();
#pragma unroll
    for (int i = 0; i < 8; ++i) {
      const int unit = w * 8 + i, c16 = unit >> 1, half = unit & 1;
      const int ra = 32 * half + 8 * g + q, rb2 = ra + 4;
      const int fa = (ra & 3) | (((ra >> 3) & 1) << 2);
      const bf16x8 v = tr_frag(smem, ra * 1024 + ((c16 ^ fa) << 5) + 8 * p, rb2 * 1024 + ((c16 ^ fa) << 5) + 8 * p);
      *(bf16x8*)(uT + (size_t)(16 * c16 + (lane & 15)) * L + l0 + 32 * half + 8 * g) = v;
    }
    __syncthreads();
  }
  const int row = rb * 128 + (tid >> 2), part = tid & 3;
  float s = 0.f;
#pragma unroll 4
  for (int c = 0; c < 16; ++c) {
    const int e0 = (c * 4 + part) * 8;
    const u32x4 xv = *(const u32x4*)(x + (size_t)row * D + e0);
    const u32x4 wv = *(const u32x4*)(gw + e0);
#pragma unroll
    for (int e = 0; e < 4; ++e) {
      s += bfr(lo_f(xv[e]) * lo_f(wv[e]));
      s += bfr(hi_f(xv[e]) * hi_f(wv[e]));
    }
  }
  s += __shfl_xor(s, 1);
  s += __shfl_xor(s, 2);
  if (part == 0) {
    const float sv = bfr(s);
    gout[row] = bfr(1.f / (1.f + __expf(-sv)));
  }
}

constexpr int KC_PITCH = 768;
constexpr int KG_PITCH = 160;
constexpr int KG_ARR = 16 * KG_PITCH;
constexpr int KG_TAB = 8 * 2 * KG_ARR;
typedef __attribute__((ext_vector_type(2))) float f32x2;

__device__ __forceinline__ int pos_idx(int p) {
  const int sh = (p >= 256) + (p >= 512) + (p >= 1024);
  return 128 * sh + (p >> sh);
}
__device__ __forceinline__ int hi_pos(int m, int r) {
  if (m == 0) return 128 * (r >> 3) + (r & 7);
  if (m == 1) return r < 8 ? 256 + 128 * (r >> 2) + 2 * (r & 3) : 512 + 4 * ((r >> 2) & 1) + 128 * (r & 3);
  return 1024 + 128 * r;
}
__device__ __forceinline__ f32x2 cexp(float zr, float zi, float p) {
  const float e = __builtin_amdgcn_exp2f(zr * p);
  const float ph = __builtin_amdgcn_fractf(zi * p);
  return f32x2{e * __builtin_amdgcn_cosf(ph), e * __builtin_amdgcn_sinf(ph)};
}
__device__ __forceinline__ f32x2 cmul(f32x2 a, f32x2 b) {
  return f32x2{a[0] * b[0] - a[1] * b[1], a[0] * b[1] + a[1] * b[0]};
}
__device__ __forceinline__ void kg_put(char* dst, float re, float im) {
  const unsigned h = pk2(re, im);
  *(unsigned*)dst = h;
  *(unsigned*)(dst + KG_ARR) = pk2(re - lo_f(h), im - hi_f(h));
}

__device__ __forceinline__ void kgen_block(char* smem, int d0, const __bf16* __restrict__ lar,
                                           const __bf16* __restrict__ aim, const __bf16* __restrict__ cre,
                                           const __bf16* __restrict__ cim, const __bf16* __restrict__ ldt,
                                           unsigned* __restrict__ kc) {
  const int tid = threadIdx.x, lane = tid & 63, w = __builtin_amdgcn_readfirstlane(tid >> 6);
  const int ch = w >> 2, wq = w & 3, tc = tid & 255;
  const int r = lane & 15, g = lane >> 4, hh = lane >> 5;
  char* wb = smem + w * 2 * KG_ARR;
  char* tb = smem + KG_TAB + ch * (4 * 656 * 4);
  STAMP(0);
  float zr, zi;
  f32x2 cc;
  {
    const int d = d0 + ch, n = 32 * wq + (lane & 31);
    const float dt = bfr(expf((float)ldt[d]));
    const float ar = -bfr(expf((float)lar[d * NS + n]));
    zr = (ar * dt) * 1.4426950408889634f;
    zi = ((float)aim[d * NS + n] * dt) * 0.15915494309189535f;
    cc = f32x2{2.f * ((float)cre[d * NS + n] * dt), 2.f * ((float)cim[d * NS + n] * dt)};
  }
  STAMP(1);
  const float fh = (float)(1 + hh);
  const f32x2 e1 = cexp(zr, zi, 1.f), e8 = cexp(zr, zi, 8.f), e128 = cexp(zr, zi, 128.f);
  const f32x2 e24 = cexp(zr, zi, 2.f * fh);
  char* wr = wb + (8 * hh) * KG_PITCH + (lane & 31) * 4;
  const char* rd = wb + r * KG_PITCH + g * 16;
  f32x4 acc[3];
  bf16x8 bh[2], bl[2];
  {
    f32x2 v = cexp(zr, zi, 64.f * hh);
#pragma unroll
    for (int i = 0; i < 8; ++i) {
      kg_put(wr + i * KG_PITCH, v[0], v[1]);
      if (i < 7) v = cmul(v, e8);
    }
    __builtin_amdgcn_wave_barrier();
#pragma unroll
    for (int ss = 0; ss < 2; ++ss) {
      bh[ss] = *(const bf16x8*)(rd + ss * 64);
      bl[ss] = *(const bf16x8*)(rd + ss * 64 + KG_ARR);
    }
  }
#pragma unroll
  for (int m = 0; m < 3; ++m) {
    f32x2 v, st;
    if (m == 0) { v = hh ? cmul(cc, e128) : cc; st = e1; }
    else if (m == 1) { v = cmul(cc, cexp(zr, zi, 256.f * fh)); st = hh ? e128 : e24; }
    else { v = cmul(cc, cexp(zr, zi, 1024.f * fh)); st = e128; }
    const f32x2 v4 = (m == 1) ? cmul(v, hh ? e24 : e128) : v;
    __builtin_amdgcn_wave_barrier();
#pragma unroll
    for (int i = 0; i < 8; ++i) {
      kg_put(wr + i * KG_PITCH, v[0], -v[1]);
      if (m == 1 && i == 3) v = v4;
      else if (i < 7) v = cmul(v, st);
    }
    __builtin_amdgcn_wave_barrier();
    acc[m] = f32x4{0.f, 0.f, 0.f, 0.f};
#pragma unroll
    for (int ss = 0; ss < 2; ++ss) {
      const bf16x8 ah = *(const bf16x8*)(rd + ss * 64), al = *(const bf16x8*)(rd + ss * 64 + KG_ARR);
      acc[m] = mfma16(al, bh[ss], acc[m]);
      acc[m] = mfma16(ah, bl[ss], acc[m]);
      acc[m] = mfma16(ah, bh[ss], acc[m]);
    }
  }
  {
    float* tab = (float*)tb + wq * 656;
#pragma unroll
    for (int m = 0; m < 3; ++m)
#pragma unroll
      for (int e = 0; e < 4; ++e) {
        const int row = 4 * g + e, pp = hi_pos(m, row) + 8 * r;
        if (m < 2 || row < 8 || (row == 8 && r == 0)) tab[pos_idx(pp)] = acc[m][e];
      }
  }
  __syncthreads();
  STAMP(2);
  {
    const float* tab = (const float*)tb;
    unsigned* kd = kc + (size_t)(d0 + ch) * KC_PITCH;
#pragma unroll
    for (int e = 0; e < 3; ++e) {
      const int id = tc + 256 * e;
      if (id < 641) {
        const float v = (tab[id] + tab[656 + id]) + (tab[2 * 656 + id] + tab[3 * 656 + id]);
        const float vh = bfr(v);
        kd[id] = pk2(vh, v - vh);
      }
    }
  }
  STAMP(3);
}

NOPK __global__ __launch_bounds__(512, 4) void k1(const __bf16* __restrict__ u, const __bf16* __restrict__ lar,
                                             const __bf16* __restrict__ aim, const __bf16* __restrict__ cre,
                                             const __bf16* __restrict__ cim, const __bf16* __restrict__ ldt,
                                             const __bf16* __restrict__ wg, const __bf16* __restrict__ wu,
                                             const __bf16* __restrict__ gw, __bf16* __restrict__ h,
                                             __bf16* __restrict__ uT, unsigned* __restrict__ kc,
                                             float* __restrict__ gout, unsigned* __restrict__ flags) {
  extern __shared__ __attribute__((aligned(16))) char smem[];
  const int b = blockIdx.x;
  const int xcd = b & 7, i = (b & 255) >> 3;
  if (b < 256) {
    const int rb = 4 * (xcd >> 1) + (i & 3), cb = 8 * (xcd & 1) + (i >> 2);
    if (cb < 15) ffn1_block(smem, rb, cb, u, wg, wu, h);
    else gate_block(smem, rb, u, gw, gout, uT);
  } else {
    if (threadIdx.x == 0) flags[b - 256] = 0u;
    kgen_block(smem, 64 * xcd + 2 * i, lar, aim, cre, cim, ldt, kc);
  }
}

#undef STAMP_BASE
#define STAMP_BASE 512
constexpr int UCP = 4160;
constexpr int KARR = (240 + L) * 2;
constexpr int OFF_K = 4 * UCP;
constexpr int OFF_TAB = OFF_K + 2 * KARR;
constexpr int OFF_RED = UCP;
constexpr int CH_BYTES = OFF_RED + 32768;
static_assert(OFF_TAB + 3072 <= CH_BYTES && 2 * CH_BYTES <= LDS_BYTES, "lds");

struct PosLut {
  unsigned short v[L];
  constexpr PosLut() : v() {
    for (int j = 0; j < L; ++j) {
      const int sh = (j >= 256) + (j >= 512) + (j >= 1024);
      int q = j >> sh;
      if (sh) {
        const int rem = j & ((1 << sh) - 1), half = 1 << (sh - 1);
        if (rem > half || (rem == half && (q & 1))) ++q;
      }
      v[j] = (unsigned short)(4 * (128 * sh + q));
    }
  }
};
__device__ const PosLut g_poslut{};

__device__ __forceinline__ void conv_block(char* smem, int d0, const __bf16* __restrict__ uT,
                                           const unsigned* __restrict__ kc, const __bf16* __restrict__ dsk,
                                           __bf16* __restrict__ aT, unsigned* __restrict__ flag) {
  const int tid = threadIdx.x, lane = tid & 63, w = __builtin_amdgcn_readfirstlane(tid >> 6);
  const int ch = w >> 2, wq = w & 3, tc = tid & 255;
  const int r = lane & 15, g = lane >> 4, q = (lane >> 2) & 3, p = lane & 3;
  char* cb = smem + ch * CH_BYTES;
  const int d = d0 + ch;
  STAMP(0);
  __builtin_amdgcn_s_setprio(2);
  {
    const unsigned lb = lds_addr_of(cb);
    glds16(uT + (size_t)d * L + wq * 512, lane * 16, lb + 32 + wq * 1024);
    if (wq < 3) glds16(kc + (size_t)d * KC_PITCH + wq * 256, lane * 16, lb + OFF_TAB + wq * 1024);
    const u32x4 z = {0u, 0u, 0u, 0u};
    if (tc < 60) *(u32x4*)(cb + OFF_K + (tc >= 30 ? KARR : 0) + (tc % 30) * 16) = z;
    else if (tc < 68) *(u32x4*)(cb + ((tc - 60) >> 1) * UCP + ((tc - 60) & 1) * 16) = z;
  }
  const float dsv = (float)dsk[d];
  const u32x4 lut = *(const u32x4*)(g_poslut.v + 8 * tc);
  WAIT_VM(0);
  __syncthreads();
  STAMP(1);
  {
    const u32x4 pa = *(const u32x4*)(cb + 16 + 16 * tc), pb = *(const u32x4*)(cb + 32 + 16 * tc);
    const u32x4 c1 = {__builtin_amdgcn_alignbit(pb[0], pa[3], 16), __builtin_amdgcn_alignbit(pb[1], pb[0], 16),
                      __builtin_amdgcn_alignbit(pb[2], pb[1], 16), __builtin_amdgcn_alignbit(pb[3], pb[2], 16)};
    const u32x4 c2 = {pa[3], pb[0], pb[1], pb[2]};
    const u32x4 c3 = {__builtin_amdgcn_alignbit(pa[3], pa[2], 16), c1[0], c1[1], c1[2]};
    *(u32x4*)(cb + 1 * UCP + 32 + 16 * tc) = c1;
    *(u32x4*)(cb + 2 * UCP + 32 + 16 * tc) = c2;
    *(u32x4*)(cb + 3 * UCP + 32 + 16 * tc) = c3;
    u32x4 kh, kl;
#pragma unroll
    for (int e = 0; e < 4; ++e) {
      const unsigned t0 = *(const unsigned*)(cb + OFF_TAB + (lut[e] & 0xffffu));
      const unsigned t1 = *(const unsigned*)(cb + OFF_TAB + (lut[e] >> 16));
      kh[e] = __builtin_amdgcn_perm(t1, t0, 0x05040100u);
      kl[e] = __builtin_amdgcn_perm(t1, t0, 0x07060302u);
    }
    *(u32x4*)(cb + OFF_K + 480 + 16 * tc) = kh;
    *(u32x4*)(cb + OFF_K + KARR + 480 + 16 * tc) = kl;
  }
  __syncthreads();
  STAMP(2);
  f32x4 acc[8];
#pragma unroll
  for (int t = 0; t < 8; ++t) acc[t] = f32x4{0.f, 0.f, 0.f, 0.f};
#pragma unroll
  for (int rr = 0; rr < 2; ++rr) {
    const int rho = 2 * wq + rr;
    const int abase = q * UCP + 2 * (16 + 16 + 32 * rho - 8 * g + 4 * p);
    const int bbase = OFF_K + 2 * (240 - 16 - 32 * rho + 16 * r + 8 * g);
    bf16x8 af[8];
#pragma unroll
    for (int s = 0; s < 8; ++s) af[s] = tr_frag(cb, abase + 512 * s, abase + 512 * s - 8);
#pragma unroll
    for (int t = 0; t < 8; ++t) {
      const bf16x8 bh = *(const bf16x8*)(cb + bbase + 512 * t);
#pragma unroll
      for (int s = 0; s + t < 8; ++s) acc[s + t] = mfma16(af[s], bh, acc[s + t]);
      const bf16x8 bl = *(const bf16x8*)(cb + bbase + KARR + 512 * t);
#pragma unroll
      for (int s = 0; s + t < 8; ++s) acc[s + t] = mfma16(af[s], bl, acc[s + t]);
    }
  }
  __syncthreads();
  STAMP(3);
#pragma unroll
  for (int t = 0; t < 8; ++t)
    if ((t >> 1) != wq) *(f32x4*)(cb + OFF_RED + (wq * 8 + t) * 1024 + lane * 16) = acc[t];
  __syncthreads();
#pragma unroll
  for (int tt = 0; tt < 2; ++tt) {
    const int t = 2 * wq + tt;
    f32x4 own = f32x4{0.f, 0.f, 0.f, 0.f};
#pragma unroll
    for (int t2 = 0; t2 < 8; ++t2)
      if (t2 == t) own = acc[t2];
    f32x4 s = f32x4{0.f, 0.f, 0.f, 0.f};
#pragma unroll
    for (int sw = 0; sw < 4; ++sw) {
      const f32x4 o = *(const f32x4*)(cb + OFF_RED + (sw * 8 + t) * 1024 + lane * 16);
      s += (sw == wq) ? own : o;
    }
    const int l = 256 * t + 16 * r + 4 * g;
    const u32x2 uu = *(const u32x2*)(cb + 32 + 2 * l);
    const float a0 = bfr(s[0]) + bfr(lo_f(uu[0]) * dsv), a1 = bfr(s[1]) + bfr(hi_f(uu[0]) * dsv);
    const float a2 = bfr(s[2]) + bfr(lo_f(uu[1]) * dsv), a3 = bfr(s[3]) + bfr(hi_f(uu[1]) * dsv);
    const unsigned long long pv = (unsigned long long)pk2(a0, a1) | ((unsigned long long)pk2(a2, a3) << 32);
    __hip_atomic_store((gu64*)(aT + (size_t)d * L + l), pv, __ATOMIC_RELAXED, __HIP_MEMORY_SCOPE_AGENT);
  }
  WAIT_VM(0);
  __syncthreads();
  if (tid == 0) __hip_atomic_store((gu32*)flag, 1u, __ATOMIC_RELAXED, __HIP_MEMORY_SCOPE_AGENT);
  STAMP(4);
}

constexpr int F2_NS = 5;
constexpr int F2_STAGE = 16384;
constexpr int F2_NST = 22;

struct F2Ctx {
  const __bf16 *h, *wd;
  unsigned avoff, bvoff, bvoff_last, lbase;
  int w, kh;
  int aoff[2], boff[2];
};
__device__ __forceinline__ void f2_stage(const F2Ctx& c, int st) {
  const unsigned base = c.lbase + (st % F2_NS) * F2_STAGE;
  glds16(c.h + st * 64, c.avoff, base + c.w * 1024);
  glds16(c.wd + (size_t)st * 64 * D, st == F2_NST - 1 ? c.bvoff_last : c.bvoff, base + 8192 + c.w * 1024);
}
constexpr int F2_POLL = 19;
struct F2Epi {
  const unsigned* flags;
  const __bf16* aT;
  const float* gin;
  unsigned long long av[2];
  f32x4 gv;
};
template <int ST>
__device__ __forceinline__ void f2_loop(const F2Ctx& c, const char* smem, bf16x8 (&ao)[2], bf16x8 (&bo)[2],
                                        f32x4 (&acc)[2][2], F2Epi& ep) {
  if constexpr (ST < F2_NST) {
    constexpr int rem = (F2_NST - 1 - ST) < (F2_NS - 2) ? (F2_NST - 1 - ST) : (F2_NS - 2);
    if constexpr (ST == F2_POLL) {
      if (c.w == 0) {
        for (int spin = 0; spin < (1 << 22); ++spin) {
          const unsigned v = __hip_atomic_load((const gu32*)ep.flags, __ATOMIC_RELAXED, __HIP_MEMORY_SCOPE_AGENT);
          if (__all(v == 1u)) break;
          __builtin_amdgcn_s_sleep(1);
        }
      }
    }
    wait_vm<2 * rem>();
    WAIT_LGKM0();
    __builtin_amdgcn_s_barrier();
    asm volatile("" ::: "memory");
    STAMP(1 + ST);
    if constexpr (ST + F2_NS - 1 < F2_NST) f2_stage(c, ST + F2_NS - 1);
    if constexpr (ST == F2_POLL) {
#pragma unroll
      for (int nt = 0; nt < 2; ++nt)
        ep.av[nt] = __hip_atomic_load((const gu64*)(ep.aT + (size_t)(16 * nt) * L), __ATOMIC_RELAXED,
                                      __HIP_MEMORY_SCOPE_AGENT);
      ep.gv = *(const f32x4*)ep.gin;
    }
    const char* base = smem + (ST % F2_NS) * F2_STAGE;
    bf16x8 an[2], bn[2];
#pragma unroll
    for (int mt = 0; mt < 2; ++mt) an[mt] = *(const bf16x8*)(base + c.aoff[mt]);
#pragma unroll
    for (int nt = 0; nt < 2; ++nt) bn[nt] = tr_frag(base, c.boff[nt], c.boff[nt] + 512);
    __builtin_amdgcn_sched_barrier(0);
    if constexpr (ST > 0) {
#pragma unroll
      for (int mt = 0; mt < 2; ++mt)
#pragma unroll
        for (int nt = 0; nt < 2; ++nt) acc[mt][nt] = mfma16(ao[mt], bo[nt], acc[mt][nt]);
    }
    __builtin_amdgcn_sched_barrier(0);
    f2_loop<ST + 1>(c, smem, an, bn, acc, ep);
  } else {
    if (c.kh == 0) {
#pragma unroll
      for (int mt = 0; mt < 2; ++mt)
#pragma unroll
        for (int nt = 0; nt < 2; ++nt) acc[mt][nt] = mfma16(ao[mt], bo[nt], acc[mt][nt]);
    }
  }
}

__device__ __forceinline__ void ffn2_block(char* smem, int row0, int cbk, const __bf16* __restrict__ h,
                                           const __bf16* __restrict__ wd, const __bf16* __restrict__ aT,
                                           const float* __restrict__ gin, const unsigned* __restrict__ flags,
                                           __bf16* __restrict__ out) {
  const int col0 = 64 * cbk;
  const int tid = threadIdx.x, lane = tid & 63, w = __builtin_amdgcn_readfirstlane(tid >> 6);
  const int r = lane & 15, g = lane >> 4, q = (lane >> 2) & 3, p = lane & 3;
  const int kh = w >> 2, wm = (w >> 1) & 1, wn = w & 1;
  STAMP(0);
  F2Ctx c;
  c.h = h; c.wd = wd; c.w = w; c.kh = kh; c.lbase = lds_addr_of(smem);
  {
    const int row = 8 * w + (lane >> 3), ch = (lane & 7) ^ ((row >> 1) & 7);
    c.avoff = (unsigned)(((row0 + row) * HP + ch * 8) * 2);
    const int bk = 8 * w + (lane >> 3);
    const int pos = lane & 7, fp = ((bk >> 1) & 1) | (((bk >> 3) & 1) << 1);
    const int cc = col0 + 16 * ((pos >> 1) ^ fp) + 8 * (pos & 1);
    c.bvoff = (unsigned)((bk * D + cc) * 2);
    c.bvoff_last = (unsigned)(((bk > 31 ? 31 : bk) * D + cc) * 2);
  }
#pragma unroll
  for (int mt = 0; mt < 2; ++mt) {
    const int row = 32 * wm + 16 * mt + r, ch = 4 * kh + g;
    c.aoff[mt] = row * 128 + ((ch ^ ((row >> 1) & 7)) << 4);
  }
  const int fpl = (q >> 1) | ((g & 1) << 1);
#pragma unroll
  for (int nt = 0; nt < 2; ++nt) c.boff[nt] = 8192 + (32 * kh + 8 * g + q) * 128 + (((2 * wn + nt) ^ fpl) << 5) + 8 * p;

  f32x4 acc[2][2];
#pragma unroll
  for (int mt = 0; mt < 2; ++mt)
#pragma unroll
    for (int nt = 0; nt < 2; ++nt) acc[mt][nt] = f32x4{0.f, 0.f, 0.f, 0.f};

#pragma unroll
  for (int st = 0; st < F2_NS - 1; ++st) f2_stage(c, st);
  const int lr = 32 * wm + 16 * kh + 4 * g;
  F2Epi ep;
  ep.flags = flags + 32 * cbk + (lane & 31);
  ep.aT = aT + (size_t)(col0 + 32 * wn + r) * L + row0 + lr;
  ep.gin = gin + row0 + lr;
  bf16x8 a0[2], b0[2];
  f2_loop<0>(c, smem, a0, b0, acc, ep);
  STAMP(23);
  WAIT_LGKM0();
  __builtin_amdgcn_s_barrier();
  asm volatile("" ::: "memory");
  const f32x4 gv = ep.gv;
  const unsigned long long av[2] = {ep.av[0], ep.av[1]};
  {
    char* red = smem + (w & 3) * 4096 + kh * 2048;
    const f32x4 o0 = kh ? acc[0][0] : acc[1][0], o1 = kh ? acc[0][1] : acc[1][1];
    *(f32x4*)(red + lane * 16) = o0;
    *(f32x4*)(red + 1024 + lane * 16) = o1;
  }
  __syncthreads();
  constexpr int OT = 16384, OTP = 144;
  {
    const char* red = smem + (w & 3) * 4096 + (1 - kh) * 2048;
#pragma unroll
    for (int nt = 0; nt < 2; ++nt) {
      const f32x4 o = *(const f32x4*)(red + nt * 1024 + lane * 16);
      const f32x4 own = kh ? acc[1][nt] : acc[0][nt];
      const f32x4 s = kh ? (o + own) : (own + o);
      const unsigned alo = (unsigned)av[nt], ahi = (unsigned)(av[nt] >> 32);
      const float a[4] = {lo_f(alo), hi_f(alo), lo_f(ahi), hi_f(ahi)};
#pragma unroll
      for (int e = 0; e < 4; ++e) {
        const float o2 = bfr(gv[e] * a[e]) + bfr(bfr(1.f - gv[e]) * bfr(s[e]));
        *(__bf16*)(smem + OT + (lr + e) * OTP + (32 * wn + 16 * nt + r) * 2) = (__bf16)o2;
      }
    }
  }
  __syncthreads();
  {
    const int row = tid >> 3, c8 = tid & 7;
    const u32x4 v = *(const u32x4*)(smem + OT + row * OTP + c8 * 16);
    *(u32x4*)(out + (size_t)(row0 + row) * D + col0 + c8 * 8) = v;
  }
  STAMP(24);
}

NOPK __global__ __launch_bounds__(512, 4) void k2(const __bf16* __restrict__ h, const __bf16* __restrict__ wd,
                                             const __bf16* __restrict__ uT, const unsigned* __restrict__ kc,
                                             const __bf16* __restrict__ dsk, __bf16* __restrict__ aT,
                                             const float* __restrict__ gin, unsigned* __restrict__ flags,
                                             __bf16* __restrict__ out) {
  extern __shared__ __attribute__((aligned(16))) char smem[];
  const int b = blockIdx.x, xcd = b & 7, i = (b & 255) >> 3;
  if (b < 256) {
    const int pair = 32 * xcd + i;
    conv_block(smem, 2 * pair, uT, kc, dsk, aT, flags + pair);
  } else {
    ffn2_block(smem, 64 * (4 * xcd + (i & 3)), i >> 2, h, wd, aT, gin, flags, out);
  }
}

extern "C" void kernel_launch(void* const* d_in, const int* in_sizes, int n_in, void* d_out, int out_size,
                              void* d_ws, size_t ws_size, hipStream_t stream) {
  const __bf16* u = (const __bf16*)d_in[0];
  const __bf16* lar = (const __bf16*)d_in[1];
  const __bf16* aim = (const __bf16*)d_in[2];
  const __bf16* cre = (const __bf16*)d_in[3];
  const __bf16* cim = (const __bf16*)d_in[4];
  const __bf16* ldt = (const __bf16*)d_in[5];
  const __bf16* dsk = (const __bf16*)d_in[6];
  const __bf16* wg = (const __bf16*)d_in[8];
  const __bf16* wu = (const __bf16*)d_in[9];
  const __bf16* wd = (const __bf16*)d_in[10];
  const __bf16* gw = (const __bf16*)d_in[11];
  char* ws = (char*)d_ws;
  __bf16* h = (__bf16*)ws;
  ws += (size_t)L * HP * 2;
  __bf16* aT = (__bf16*)ws;
  ws += (size_t)D * L * 2;
  __bf16* uT = (__bf16*)ws;
  ws += (size_t)D * L * 2;
  unsigned* kc = (unsigned*)ws;
  ws += (size_t)D * KC_PITCH * 4;
  float* gout = (float*)ws;
  ws += L * 4;
  unsigned* flags = (unsigned*)ws;
  (void)hipFuncSetAttribute((const void*)k1, hipFuncAttributeMaxDynamicSharedMemorySize, LDS_BYTES);
  (void)hipFuncSetAttribute((const void*)k2, hipFuncAttributeMaxDynamicSharedMemorySize, LDS_BYTES);
  hipLaunchKernelGGL(k1, dim3(512), dim3(512), LDS_BYTES, stream, u, lar, aim, cre, cim, ldt, wg, wu, gw, h, uT, kc,
                     gout, flags);
  hipLaunchKernelGGL(k2, dim3(512), dim3(512), LDS_BYTES, stream, (const __bf16*)h, wd, (const __bf16*)uT,
                     (const unsigned*)kc, dsk, aT, (const float*)gout, flags, (__bf16*)d_out);
}
```

```cpp
#include <hip/hip_runtime.h>
#include <hip/hip_bf16.h>

typedef __attribute__((ext_vector_type(8))) __bf16 bf16x8;
typedef __attribute__((ext_vector_type(2))) __bf16 bf16x2;
typedef __attribute__((ext_vector_type(4))) short s16x4;
typedef __attribute__((ext_vector_type(8))) short s16x8;
typedef __attribute__((ext_vector_type(4))) float f32x4;
typedef __attribute__((ext_vector_type(4))) unsigned u32x4;
typedef __attribute__((ext_vector_type(2))) unsigned u32x2;
typedef __attribute__((address_space(3))) s16x4* lds_s16x4_ptr;
typedef __attribute__((address_space(1))) unsigned gu32;
typedef __attribute__((address_space(1))) unsigned long long gu64;

#define K_LO 1

#define NOPK __attribute__((target("no-packed-fp32-ops")))

constexpr int L = 2048, D = 512, NS = 128, F = 1376, HP = 1408;
constexpr int LDS_BYTES = 81920;

__device__ __forceinline__ float bf2f(unsigned short v) { return __uint_as_float((unsigned)v << 16); }
__device__ __forceinline__ float bfr(float x) { return (float)(__bf16)x; }
__device__ __forceinline__ unsigned pk2(float a, float b) {
  bf16x2 v = {(__bf16)a, (__bf16)b};
  return __builtin_bit_cast(unsigned, v);
}
__device__ __forceinline__ float lo_f(unsigned w) { return __uint_as_float(w << 16); }
__device__ __forceinline__ float hi_f(unsigned w) { return __uint_as_float(w & 0xffff0000u); }

__device__ __forceinline__ f32x4 mfma16(bf16x8 a, bf16x8 b, f32x4 c) {
  return __builtin_amdgcn_mfma_f32_16x16x32_bf16(a, b, c, 0, 0, 0);
}
__device__ __forceinline__ bf16x8 tr_frag(const char* lds, int off0, int off1) {
  s16x4 a = __builtin_amdgcn_ds_read_tr16_b64_v4i16((lds_s16x4_ptr)(lds + off0));
  s16x4 b = __builtin_amdgcn_ds_read_tr16_b64_v4i16((lds_s16x4_ptr)(lds + off1));
  s16x8 v = {a[0], a[1], a[2], a[3], b[0], b[1], b[2], b[3]};
  return __builtin_bit_cast(bf16x8, v);
}
__device__ __forceinline__ void glds16(const void* sbase, unsigned voff, unsigned lds_addr) {
  unsigned keep;
  asm volatile("s_mov_b32 %0, m0\n\ts_mov_b32 m0, %3\n\ts_nop 0\n\tglobal_load_lds_dwordx4 %1, %2\n\ts_mov_b32 m0, %0"
               : "=&s"(keep) : "v"(voff), "s"(sbase), "s"(lds_addr) : "memory");
}
__device__ __forceinline__ void glds16v(const void* vptr, unsigned lds_addr) {
  unsigned keep;
  asm volatile("s_mov_b32 %0, m0\n\ts_mov_b32 m0, %2\n\ts_nop 0\n\tglobal_load_lds_dwordx4 %1, off\n\ts_mov_b32 m0, %0"
               : "=&s"(keep) : "v"(vptr), "s"(lds_addr) : "memory");
}
__device__ __forceinline__ unsigned lds_addr_of(const char* p) {
  return (unsigned)(uintptr_t)(const __attribute__((address_space(3))) char*)p;
}
#define STAMP(i)
#define STAMP_WAVE 0
#define SSTAMP(var)
#define SPAN_PT(k, i)
#define WAIT_VM(n) asm volatile("s_waitcnt vmcnt(" #n ")" ::: "memory")
#define WAIT_LGKM0() asm volatile("s_waitcnt lgkmcnt(0)" ::: "memory")
template <int N>
__device__ __forceinline__ void wait_vm() {
  asm volatile("s_waitcnt vmcnt(%0)" ::"i"(N) : "memory");
}

#define STAMP_BASE 0
constexpr int F1_B = 32768;

struct F1Ctx {
  const __bf16 *x, *wg, *wu, *wmix;
  unsigned voffA, voffB0, voffB2, lbase;
  int w;
  int aoff[2], boff[6];
};
template <bool G2>
__device__ __forceinline__ void f1_stage(const F1Ctx& c, int hs) {
  const unsigned bb = c.lbase + F1_B + (hs & 3) * 12288 + c.w * 1024;
  glds16(c.x + hs * 32, c.voffA, c.lbase + (hs & 3) * 8192 + c.w * 1024);
  if constexpr (!G2) {
    glds16(c.wg + (size_t)hs * 32 * F, c.voffB0, bb);
    glds16(c.wu + (size_t)hs * 32 * F, c.voffB2, bb + 8192);
  } else {
    glds16v(c.wmix + (size_t)hs * 32 * F, bb);
  }
}
template <int HS, bool G2>
__device__ __forceinline__ void f1_loop(const F1Ctx& c, const char* smem, bf16x8 (&af)[2], bf16x8 (&bq)[6],
                                        f32x4 (&acc)[2][6]) {
  if constexpr (HS < 16) {
    constexpr int rem = (15 - HS) < 2 ? (15 - HS) : 2;
    wait_vm<rem*(G2 ? 2 : 3)>();
    WAIT_LGKM0();
    __builtin_amdgcn_s_barrier();
    asm volatile("" ::: "memory");
    const char* ab = smem + (HS & 3) * 8192;
    const char* bbase = smem + (HS & 3) * 12288;
    if constexpr (HS > 0) {
#pragma unroll
      for (int j = 0; j < 6; ++j) {
        acc[0][j] = mfma16(bq[j], af[0], acc[0][j]);
        if constexpr (HS + 3 < 16)
          if (j == 1) {
            __builtin_amdgcn_sched_barrier(0);
            f1_stage<G2>(c, HS + 3);
          }
      }
    } else {
      f1_stage<G2>(c, HS + 3);
    }
    __builtin_amdgcn_sched_barrier(0);
    af[0] = *(const bf16x8*)(ab + c.aoff[0]);
#pragma unroll
    for (int j = 0; j < 6; ++j) {
      if constexpr (HS > 0) acc[1][j] = mfma16(bq[j], af[1], acc[1][j]);
      __builtin_amdgcn_sched_barrier(0);
      bq[j] = tr_frag(bbase, c.boff[j], c.boff[j] + 512);
      __builtin_amdgcn_sched_barrier(0);
    }
    af[1] = *(const bf16x8*)(ab + c.aoff[1]);
    f1_loop<HS + 1, G2>(c, smem, af, bq, acc);
  } else {
#pragma unroll
    for (int j = 0; j < 6; ++j) {
      acc[0][j] = mfma16(bq[j], af[0], acc[0][j]);
      acc[1][j] = mfma16(bq[j], af[1], acc[1][j]);
    }
  }
}

__device__ __forceinline__ void ffn1_block(char* smem, int rb, int cb, const __bf16* __restrict__ x,
                                           const __bf16* __restrict__ wg, const __bf16* __restrict__ wu,
                                           __bf16* __restrict__ h) {
  const int tid = threadIdx.x, lane = tid & 63, w = __builtin_amdgcn_readfirstlane(tid >> 6);
  const int row0 = rb * 128, f0 = cb * 96;
  const int wm = w >> 1, wn = w & 1;
  const int r = lane & 15, g = lane >> 4, q = (lane >> 2) & 3, p = lane & 3;
  __builtin_amdgcn_s_setprio(2);
  F1Ctx c;
  c.x = x; c.wg = wg; c.wu = wu; c.w = w; c.lbase = lds_addr_of(smem);
  {
    const int row = 16 * w + (lane >> 2), ch = (lane & 3) ^ ((-(row >> 2)) & 3);
    c.voffA = (unsigned)(((row0 + row) * D + ch * 8) * 2);
    const int kk = 8 * (w & 3) + (lane >> 3), pos = lane & 7;
    const int fp = ((kk >> 1) & 1) | (((kk >> 3) & 1) << 1);
    const int i = (pos >> 1) ^ fp;
    int c0 = f0 + 16 * i + 8 * (pos & 1);
    int c1 = f0 + 16 * ((4 + i) % 6) + 8 * (pos & 1);
    int c2 = f0 + 16 * (2 + i) + 8 * (pos & 1);
    c0 = c0 > F - 8 ? F - 8 : c0;
    c1 = c1 > F - 8 ? F - 8 : c1;
    c2 = c2 > F - 8 ? F - 8 : c2;
    c.voffB0 = (unsigned)((kk * F + c0) * 2);
    c.voffB2 = (unsigned)((kk * F + c2) * 2);
    c.wmix = (i < 2 ? wg : wu) + kk * F + c1;
  }
#pragma unroll
  for (int mt = 0; mt < 2; ++mt) {
    const int row = 32 * wm + 16 * mt + r;
    c.aoff[mt] = row * 64 + ((g ^ ((-(row >> 2)) & 3)) << 4);
  }
  {
    const int fp = (q >> 1) | ((g & 1) << 1);
#pragma unroll
    for (int j = 0; j < 6; ++j) {
      const int T = (j < 3 ? 0 : 6) + 3 * wn + (j % 3);
      c.boff[j] = F1_B + (T >> 2) * 4096 + (8 * g + q) * 128 + (((T & 3) ^ fp) << 5) + 8 * p;
      asm volatile("" : "+v"(c.boff[j]));
    }
  }
  f32x4 acc[2][6];
#pragma unroll
  for (int mt = 0; mt < 2; ++mt)
#pragma unroll
    for (int j = 0; j < 6; ++j) acc[mt][j] = f32x4{0.f, 0.f, 0.f, 0.f};
  bf16x8 af[2], bq[6];

  if (w < 4) {
    f1_stage<false>(c, 0);
    f1_stage<false>(c, 1);
    f1_stage<false>(c, 2);
    f1_loop<0, false>(c, smem, af, bq, acc);
  } else {
    f1_stage<true>(c, 0);
    f1_stage<true>(c, 1);
    f1_stage<true>(c, 2);
    f1_loop<0, true>(c, smem, af, bq, acc);
  }

#pragma unroll
  for (int mt = 0; mt < 2; ++mt)
#pragma unroll
    for (int j = 0; j < 3; ++j) {
      const int f = f0 + 16 * (3 * wn + j);
      if (f < F) {
        float hv[4];
#pragma unroll
        for (int e = 0; e < 4; ++e) {
          const float gv = acc[mt][j][e];
          const float t = gv * __builtin_fmaf(gv * gv, -0.10294324f, -2.3022082f);
          hv[e] = (gv * acc[mt][3 + j][e]) * __builtin_amdgcn_rcpf(1.f + __builtin_amdgcn_exp2f(t));
        }
        u32x2 o = {pk2(hv[0], hv[1]), pk2(hv[2], hv[3])};
        *(u32x2*)(h + (size_t)(row0 + 32 * wm + 16 * mt + r) * HP + f + 4 * g) = o;
      }
    }
}

__device__ __forceinline__ void gate_block(char* smem, int rb, const __bf16* __restrict__ x,
                                           const __bf16* __restrict__ gw, float* __restrict__ gout,
                                           __bf16* __restrict__ uT) {
  const int tid = threadIdx.x, lane = tid & 63, w = __builtin_amdgcn_readfirstlane(tid >> 6);
  const int g = lane >> 4, q = (lane >> 2) & 3, p = lane & 3;
  const unsigned lbase = lds_addr_of(smem);
  const int grow = tid >> 3, gpart = tid & 7, gf = (grow & 3) | (((grow >> 3) & 1) << 2);
  u32x4 gwv[8];
#pragma unroll
  for (int i = 0; i < 8; ++i) gwv[i] = *(const u32x4*)(gw + ((gpart ^ gf) + 8 * (i >> 1)) * 16 + 8 * (i & 1));
#pragma unroll 1
  for (int pass = 0; pass < 2; ++pass) {
    const int l0 = rb * 128 + pass * 64;
#pragma unroll
    for (int i = 0; i < 8; ++i) {
      const int row = 8 * w + i, fr = (row & 3) | (((row >> 3) & 1) << 2);
      glds16(x + (size_t)(l0 + row) * D, (unsigned)((((lane >> 1) ^ fr) * 2 + (lane & 1)) * 16), lbase + row * 1024);
    }
    WAIT_VM(0);
    __syncthreads();
#pragma unroll
    for (int i = 0; i < 8; ++i) {
      const int unit = w * 8 + i, c16 = unit >> 1, half = unit & 1;
      const int ra = 32 * half + 8 * g + q, rb2 = ra + 4;
      const int fa = (ra & 3) | (((ra >> 3) & 1) << 2);
      const bf16x8 v = tr_frag(smem, ra * 1024 + ((c16 ^ fa) << 5) + 8 * p, rb2 * 1024 + ((c16 ^ fa) << 5) + 8 * p);
      *(bf16x8*)(uT + (size_t)(16 * c16 + (lane & 15)) * L + l0 + 32 * half + 8 * g) = v;
    }
    float s = 0.f;
#pragma unroll
    for (int i = 0; i < 8; ++i) {
      const u32x4 xv = *(const u32x4*)(smem + grow * 1024 + (gpart + 8 * (i >> 1)) * 32 + 16 * (i & 1));
#pragma unroll
      for (int e = 0; e < 4; ++e) {
        const unsigned xe = xv[e], we = gwv[i][e];
        s = __builtin_amdgcn_fdot2_f32_bf16(__builtin_bit_cast(bf16x2, xe), __builtin_bit_cast(bf16x2, we), s, false);
      }
    }
    s += __shfl_xor(s, 1);
    s += __shfl_xor(s, 2);
    s += __shfl_xor(s, 4);
    if (gpart == 0) gout[l0 + grow] = bfr(1.f / (1.f + __expf(-bfr(s))));
    __syncthreads();
  }
}

constexpr int KC_PITCH = 768;
constexpr int KG_PITCH = 160;
constexpr int KG_ARR = 16 * KG_PITCH;
constexpr int KG_TAB = 8 * 2 * KG_ARR;
typedef __attribute__((ext_vector_type(2))) float f32x2;

__device__ __forceinline__ f32x2 cexp(float zr, float zi, float p) {
  const float e = __builtin_amdgcn_exp2f(zr * p);
  const float ph = __builtin_amdgcn_fractf(zi * p);
  return f32x2{e * __builtin_amdgcn_cosf(ph), e * __builtin_amdgcn_sinf(ph)};
}
__device__ __forceinline__ f32x2 cmul(f32x2 a, f32x2 b) {
  return f32x2{a[0] * b[0] - a[1] * b[1], a[0] * b[1] + a[1] * b[0]};
}
__device__ __forceinline__ void kg_put(char* dst, float re, float im) {
  const unsigned h = pk2(re, im);
  *(unsigned*)dst = h;
  *(unsigned*)(dst + KG_ARR) = pk2(re - lo_f(h), im - hi_f(h));
}

__device__ __forceinline__ void kgen_block(char* smem, int d0, const __bf16* __restrict__ lar,
                                           const __bf16* __restrict__ aim, const __bf16* __restrict__ cre,
                                           const __bf16* __restrict__ cim, const __bf16* __restrict__ ldt,
                                           unsigned* __restrict__ kc) {
  const int tid = threadIdx.x, lane = tid & 63, w = __builtin_amdgcn_readfirstlane(tid >> 6);
  const int ch = w >> 2, wq = w & 3, tc = tid & 255;
  const int r = lane & 15, g = lane >> 4, hh = lane >> 5;
  char* wb = smem + w * 2 * KG_ARR;
  char* tb = smem + KG_TAB + ch * (4 * 656 * 4);
  STAMP(0);
  float zr, zi;
  f32x2 cc;
  {
    const int d = d0 + ch, n = 32 * wq + (lane & 31);
    const float dt = bfr(expf((float)ldt[d]));
    const float ar = -bfr(expf((float)lar[d * NS + n]));
    zr = (ar * dt) * 1.4426950408889634f;
    zi = ((float)aim[d * NS + n] * dt) * 0.15915494309189535f;
    cc = f32x2{2.f * ((float)cre[d * NS + n] * dt), 2.f * ((float)cim[d * NS + n] * dt)};
  }
  STAMP(1);
  const float fh = (float)(1 + hh);
  const f32x2 e1 = cexp(zr, zi, 1.f), e8 = cexp(zr, -zi, 8.f), e128 = cexp(zr, zi, 128.f);
  const f32x2 e24 = cexp(zr, zi, 2.f * fh);
  char* wr = wb + (8 * hh) * KG_PITCH + (lane & 31) * 4;
  const char* rd = wb + r * KG_PITCH + g * 16;
  f32x4 acc[3];
  bf16x8 bh[2], bl[2];
  {
    f32x2 v = cexp(zr, -zi, 64.f * hh);
#pragma unroll
    for (int i = 0; i < 8; ++i) {
      kg_put(wr + i * KG_PITCH, v[0], v[1]);
      if (i < 7) v = cmul(v, e8);
    }
    __builtin_amdgcn_wave_barrier();
#pragma unroll
    for (int ss = 0; ss < 2; ++ss) {
      bh[ss] = *(const bf16x8*)(rd + ss * 64);
      bl[ss] = *(const bf16x8*)(rd + ss * 64 + KG_ARR);
    }
  }
#pragma unroll
  for (int m = 0; m < 3; ++m) {
    f32x2 v, st;
    if (m == 0) { v = hh ? cmul(cc, e128) : cc; st = e1; }
    else if (m == 1) { v = cmul(cc, cexp(zr, zi, 256.f * fh)); st = hh ? e128 : e24; }
    else { v = cmul(cc, cexp(zr, zi, 1024.f * fh)); st = e128; }
    const f32x2 v4 = (m == 1) ? cmul(v, hh ? e24 : e128) : v;
    __builtin_amdgcn_wave_barrier();
#pragma unroll
    for (int i = 0; i < 8; ++i) {
      kg_put(wr + i * KG_PITCH, v[0], v[1]);
      if (m == 1 && i == 3) v = v4;
      else if (i < 7) v = cmul(v, st);
    }
    __builtin_amdgcn_wave_barrier();
    acc[m] = f32x4{0.f, 0.f, 0.f, 0.f};
#pragma unroll
    for (int ss = 0; ss < 2; ++ss) {
      const bf16x8 ah = *(const bf16x8*)(rd + ss * 64), al = *(const bf16x8*)(rd + ss * 64 + KG_ARR);
      acc[m] = mfma16(al, bh[ss], acc[m]);
      acc[m] = mfma16(ah, bl[ss], acc[m]);
      acc[m] = mfma16(ah, bh[ss], acc[m]);
    }
  }
  {
    float* tab = (float*)tb + wq * 656;
    *(f32x4*)(tab + 128 * (g >> 1) + 4 * (g & 1) + 8 * r) = acc[0];
    if (g < 2) {
      *(f32x4*)(tab + 256 + 64 * g + 4 * r) = acc[1];
#pragma unroll
      for (int e = 0; e < 4; ++e) tab[512 + 64 * g + 16 * e + r] = acc[2][e];
    } else {
#pragma unroll
      for (int e = 0; e < 4; ++e) tab[384 + (g & 1) + 32 * e + 2 * r] = acc[1][e];
      if (lane == 32) tab[640] = acc[2][0];
    }
  }
  __syncthreads();
  STAMP(2);
  {
    const float* tab = (const float*)tb;
    unsigned* kd = kc + (size_t)(d0 + ch) * KC_PITCH;
#pragma unroll
    for (int e = 0; e < 3; ++e) {
      const int id = tc + 256 * e;
      if (id < 641) {
        const float v = (tab[id] + tab[656 + id]) + (tab[2 * 656 + id] + tab[3 * 656 + id]);
        const float vh = bfr(v);
        kd[id] = pk2(vh, v - vh);
      }
    }
  }
  STAMP(3);
}

NOPK __global__ __launch_bounds__(512, 4) void k1(const __bf16* __restrict__ u, const __bf16* __restrict__ lar,
                                             const __bf16* __restrict__ aim, const __bf16* __restrict__ cre,
                                             const __bf16* __restrict__ cim, const __bf16* __restrict__ ldt,
                                             const __bf16* __restrict__ wg, const __bf16* __restrict__ wu,
                                             const __bf16* __restrict__ gw, __bf16* __restrict__ h,
                                             __bf16* __restrict__ uT, unsigned* __restrict__ kc,
                                             float* __restrict__ gout, unsigned* __restrict__ flags) {
  extern __shared__ __attribute__((aligned(16))) char smem[];
  const int b = blockIdx.x;
  const int xcd = b & 7, i = (b & 255) >> 3;
  SPAN_PT(0, 0);
  if (b < 256) {
    const int rb = 4 * (xcd >> 1) + (i & 3), cb = 8 * (xcd & 1) + (i >> 2);
    if (cb < 15) ffn1_block(smem, rb, cb, u, wg, wu, h);
    else gate_block(smem, rb, u, gw, gout, uT);
  } else {
    if (threadIdx.x == 0) flags[b - 256] = 0u;
    kgen_block(smem, 64 * xcd + 2 * i, lar, aim, cre, cim, ldt, kc);
  }
  SPAN_PT(0, 1);
}

#undef STAMP_BASE
#define STAMP_BASE 512
constexpr int UCP = 4160;
constexpr int KARR = (240 + L) * 2;
constexpr int OFF_K = 4 * UCP;
constexpr int OFF_TAB = OFF_K + 2 * KARR;
constexpr int OFF_RED = UCP;
constexpr int CH_BYTES = OFF_RED + 32768;
static_assert(OFF_TAB + 3072 <= CH_BYTES && 2 * CH_BYTES <= LDS_BYTES, "lds");

struct PosLut {
  unsigned short v[L];
  constexpr PosLut() : v() {
    for (int j = 0; j < L; ++j) {
      const int sh = (j >= 256) + (j >= 512) + (j >= 1024);
      int q = j >> sh;
      if (sh) {
        const int rem = j & ((1 << sh) - 1), half = 1 << (sh - 1);
        if (rem > half || (rem == half && (q & 1))) ++q;
      }
      v[j] = (unsigned short)(4 * (128 * sh + q));
    }
  }
};
__device__ const PosLut g_poslut{};

__device__ __forceinline__ void conv_block(char* smem, int d0, const __bf16* __restrict__ uT,
                                           const unsigned* __restrict__ kc, const __bf16* __restrict__ dsk,
                                           __bf16* __restrict__ aT, unsigned* __restrict__ flag) {
  const int tid = threadIdx.x, lane = tid & 63, w = __builtin_amdgcn_readfirstlane(tid >> 6);
  const int ch = w >> 2, wq = w & 3, tc = tid & 255;
  const int r = lane & 15, g = lane >> 4, q = (lane >> 2) & 3, p = lane & 3;
  char* cb = smem + ch * CH_BYTES;
  const int d = d0 + ch;
  STAMP(0);
  __builtin_amdgcn_s_setprio(2);
  {
    const unsigned lb = lds_addr_of(cb);
    glds16(uT + (size_t)d * L + wq * 512, lane * 16, lb + 32 + wq * 1024);
    if (wq < 3) glds16(kc + (size_t)d * KC_PITCH + wq * 256, lane * 16, lb + OFF_TAB + wq * 1024);
    const u32x4 z = {0u, 0u, 0u, 0u};
    if (tc < 60) *(u32x4*)(cb + OFF_K + (tc >= 30 ? KARR : 0) + (tc % 30) * 16) = z;
    else if (tc < 68) *(u32x4*)(cb + ((tc - 60) >> 1) * UCP + ((tc - 60) & 1) * 16) = z;
  }
  const float dsv = (float)dsk[d];
  const u32x4 lut = *(const u32x4*)(g_poslut.v + 8 * tc);
  WAIT_VM(0);
  __syncthreads();
  STAMP(1);
  {
    const u32x4 pa = *(const u32x4*)(cb + 16 + 16 * tc), pb = *(const u32x4*)(cb + 32 + 16 * tc);
    const u32x4 c1 = {__builtin_amdgcn_alignbit(pb[0], pa[3], 16), __builtin_amdgcn_alignbit(pb[1], pb[0], 16),
                      __builtin_amdgcn_alignbit(pb[2], pb[1], 16), __builtin_amdgcn_alignbit(pb[3], pb[2], 16)};
    const u32x4 c2 = {pa[3], pb[0], pb[1], pb[2]};
    const u32x4 c3 = {__builtin_amdgcn_alignbit(pa[3], pa[2], 16), c1[0], c1[1], c1[2]};
    *(u32x4*)(cb + 1 * UCP + 32 + 16 * tc) = c1;
    *(u32x4*)(cb + 2 * UCP + 32 + 16 * tc) = c2;
    *(u32x4*)(cb + 3 * UCP + 32 + 16 * tc) = c3;
    u32x4 kh, kl;
#pragma unroll
    for (int e = 0; e < 4; ++e) {
      const unsigned t0 = *(const unsigned*)(cb + OFF_TAB + (lut[e] & 0xffffu));
      const unsigned t1 = *(const unsigned*)(cb + OFF_TAB + (lut[e] >> 16));
      kh[e] = __builtin_amdgcn_perm(t1, t0, 0x05040100u);
      kl[e] = __builtin_amdgcn_perm(t1, t0, 0x07060302u);
    }
    *(u32x4*)(cb + OFF_K + 480 + 16 * tc) = kh;
    *(u32x4*)(cb + OFF_K + KARR + 480 + 16 * tc) = kl;
  }
  __syncthreads();
  STAMP(2);
  f32x4 acc[8];
#pragma unroll
  for (int t = 0; t < 8; ++t) acc[t] = f32x4{0.f, 0.f, 0.f, 0.f};
#pragma unroll
  for (int rr = 0; rr < 2; ++rr) {
    const int rho = 2 * wq + rr;
    const int abase = q * UCP + 2 * (16 + 16 + 32 * rho - 8 * g + 4 * p);
    const int bbase = OFF_K + 2 * (240 - 16 - 32 * rho + 16 * r + 8 * g);
    bf16x8 af[8];
#pragma unroll
    for (int s = 0; s < 8; ++s) af[s] = tr_frag(cb, abase + 512 * s, abase + 512 * s - 8);
#pragma unroll
    for (int t = 0; t < 8; ++t) {
      const bf16x8 bh = *(const bf16x8*)(cb + bbase + 512 * t);
#pragma unroll
      for (int s = 0; s + t < 8; ++s) acc[s + t] = mfma16(af[s], bh, acc[s + t]);
      const bf16x8 bl = *(const bf16x8*)(cb + bbase + KARR + 512 * t);
#pragma unroll
      for (int s = 0; s + t < 8; ++s) acc[s + t] = mfma16(af[s], bl, acc[s + t]);
    }
  }
  __syncthreads();
  STAMP(3);
#pragma unroll
  for (int t = 0; t < 8; ++t)
    if ((t >> 1) != wq) *(f32x4*)(cb + OFF_RED + (wq * 8 + t) * 1024 + lane * 16) = acc[t];
  __syncthreads();
#pragma unroll
  for (int tt = 0; tt < 2; ++tt) {
    const int t = 2 * wq + tt;
    f32x4 own = f32x4{0.f, 0.f, 0.f, 0.f};
#pragma unroll
    for (int t2 = 0; t2 < 8; ++t2)
      if (t2 == t) own = acc[t2];
    f32x4 s = f32x4{0.f, 0.f, 0.f, 0.f};
#pragma unroll
    for (int sw = 0; sw < 4; ++sw) {
      const f32x4 o = *(const f32x4*)(cb + OFF_RED + (sw * 8 + t) * 1024 + lane * 16);
      s += (sw == wq) ? own : o;
    }
    const int l = 256 * t + 16 * r + 4 * g;
    const u32x2 uu = *(const u32x2*)(cb + 32 + 2 * l);
    const float a0 = bfr(s[0]) + bfr(lo_f(uu[0]) * dsv), a1 = bfr(s[1]) + bfr(hi_f(uu[0]) * dsv);
    const float a2 = bfr(s[2]) + bfr(lo_f(uu[1]) * dsv), a3 = bfr(s[3]) + bfr(hi_f(uu[1]) * dsv);
    const unsigned long long pv = (unsigned long long)pk2(a0, a1) | ((unsigned long long)pk2(a2, a3) << 32);
    __hip_atomic_store((gu64*)(aT + (size_t)d * L + l), pv, __ATOMIC_RELAXED, __HIP_MEMORY_SCOPE_AGENT);
  }
  WAIT_VM(0);
  __syncthreads();
  if (tid == 0) __hip_atomic_store((gu32*)flag, 1u, __ATOMIC_RELAXED, __HIP_MEMORY_SCOPE_AGENT);
  STAMP(4);
}

constexpr int F2_NS = 5;
constexpr int F2_STAGE = 16384;
constexpr int F2_NST = 22;

struct F2Ctx {
  const __bf16 *h, *wd;
  unsigned avoff, bvoff, bvoff_last, lbase;
  int w, kh;
  int aoff[2], boff[2];
};
__device__ __forceinline__ void f2_stage(const F2Ctx& c, int st) {
  const unsigned base = c.lbase + (st % F2_NS) * F2_STAGE;
  glds16(c.h + st * 64, c.avoff, base + c.w * 1024);
  glds16(c.wd + (size_t)st * 64 * D, st == F2_NST - 1 ? c.bvoff_last : c.bvoff, base + 8192 + c.w * 1024);
}
constexpr int F2_POLL = 19;
struct F2Epi {
  const unsigned* flags;
  const __bf16* aT;
  const float* gin;
  unsigned long long av[2];
  f32x4 gv;
};
template <int ST>
__device__ __forceinline__ void f2_loop(const F2Ctx& c, const char* smem, bf16x8 (&ao)[2], bf16x8 (&bo)[2],
                                        f32x4 (&acc)[2][2], F2Epi& ep) {
  if constexpr (ST < F2_NST) {
    constexpr int rem = (F2_NST - 1 - ST) < (F2_NS - 2) ? (F2_NST - 1 - ST) : (F2_NS - 2);
    if constexpr (ST == F2_POLL) {
      if (c.w == 0) {
        for (int spin = 0; spin < (1 << 22); ++spin) {
          const unsigned v = __hip_atomic_load((const gu32*)ep.flags, __ATOMIC_RELAXED, __HIP_MEMORY_SCOPE_AGENT);
          if (__all(v == 1u)) break;
          __builtin_amdgcn_s_sleep(1);
        }
      }
    }
    wait_vm<2 * rem>();
    WAIT_LGKM0();
    __builtin_amdgcn_s_barrier();
    asm volatile("" ::: "memory");
    STAMP(1 + ST);
    if constexpr (ST + F2_NS - 1 < F2_NST) f2_stage(c, ST + F2_NS - 1);
    if constexpr (ST == F2_POLL) {
#pragma unroll
      for (int nt = 0; nt < 2; ++nt)
        ep.av[nt] = __hip_atomic_load((const gu64*)(ep.aT + (size_t)(16 * nt) * L), __ATOMIC_RELAXED,
                                      __HIP_MEMORY_SCOPE_AGENT);
      ep.gv = *(const f32x4*)ep.gin;
    }
    const char* base = smem + (ST % F2_NS) * F2_STAGE;
    bf16x8 an[2], bn[2];
#pragma unroll
    for (int mt = 0; mt < 2; ++mt) an[mt] = *(const bf16x8*)(base + c.aoff[mt]);
#pragma unroll
    for (int nt = 0; nt < 2; ++nt) bn[nt] = tr_frag(base, c.boff[nt], c.boff[nt] + 512);
    __builtin_amdgcn_sched_barrier(0);
    if constexpr (ST > 0) {
#pragma unroll
      for (int mt = 0; mt < 2; ++mt)
#pragma unroll
        for (int nt = 0; nt < 2; ++nt) acc[mt][nt] = mfma16(ao[mt], bo[nt], acc[mt][nt]);
    }
    __builtin_amdgcn_sched_barrier(0);
    f2_loop<ST + 1>(c, smem, an, bn, acc, ep);
  } else {
    if (c.kh == 0) {
#pragma unroll
      for (int mt = 0; mt < 2; ++mt)
#pragma unroll
        for (int nt = 0; nt < 2; ++nt) acc[mt][nt] = mfma16(ao[mt], bo[nt], acc[mt][nt]);
    }
  }
}

__device__ __forceinline__ void ffn2_block(char* smem, int row0, int cbk, const __bf16* __restrict__ h,
                                           const __bf16* __restrict__ wd, const __bf16* __restrict__ aT,
                                           const float* __restrict__ gin, const unsigned* __restrict__ flags,
                                           __bf16* __restrict__ out) {
  const int col0 = 64 * cbk;
  const int tid = threadIdx.x, lane = tid & 63, w = __builtin_amdgcn_readfirstlane(tid >> 6);
  const int r = lane & 15, g = lane >> 4, q = (lane >> 2) & 3, p = lane & 3;
  const int kh = w >> 2, wm = (w >> 1) & 1, wn = w & 1;
  STAMP(0);
  F2Ctx c;
  c.h = h; c.wd = wd; c.w = w; c.kh = kh; c.lbase = lds_addr_of(smem);
  {
    const int row = 8 * w + (lane >> 3), ch = (lane & 7) ^ ((row >> 1) & 7);
    c.avoff = (unsigned)(((row0 + row) * HP + ch * 8) * 2);
    const int bk = 8 * w + (lane >> 3);
    const int pos = lane & 7, fp = ((bk >> 1) & 1) | (((bk >> 3) & 1) << 1);
    const int cc = col0 + 16 * ((pos >> 1) ^ fp) + 8 * (pos & 1);
    c.bvoff = (unsigned)((bk * D + cc) * 2);
    c.bvoff_last = (unsigned)(((bk > 31 ? 31 : bk) * D + cc) * 2);
  }
#pragma unroll
  for (int mt = 0; mt < 2; ++mt) {
    const int row = 32 * wm + 16 * mt + r, ch = 4 * kh + g;
    c.aoff[mt] = row * 128 + ((ch ^ ((row >> 1) & 7)) << 4);
  }
  const int fpl = (q >> 1) | ((g & 1) << 1);
#pragma unroll
  for (int nt = 0; nt < 2; ++nt) c.boff[nt] = 8192 + (32 * kh + 8 * g + q) * 128 + (((2 * wn + nt) ^ fpl) << 5) + 8 * p;

  f32x4 acc[2][2];
#pragma unroll
  for (int mt = 0; mt < 2; ++mt)
#pragma unroll
    for (int nt = 0; nt < 2; ++nt) acc[mt][nt] = f32x4{0.f, 0.f, 0.f, 0.f};

#pragma unroll
  for (int st = 0; st < F2_NS - 1; ++st) f2_stage(c, st);
  const int lr = 32 * wm + 16 * kh + 4 * g;
  F2Epi ep;
  ep.flags = flags + 32 * cbk + (lane & 31);
  ep.aT = aT + (size_t)(col0 + 32 * wn + r) * L + row0 + lr;
  ep.gin = gin + row0 + lr;
  bf16x8 a0[2], b0[2];
  f2_loop<0>(c, smem, a0, b0, acc, ep);
  STAMP(23);
  WAIT_LGKM0();
  __builtin_amdgcn_s_barrier();
  asm volatile("" ::: "memory");
  const f32x4 gv = ep.gv;
  const unsigned long long av[2] = {ep.av[0], ep.av[1]};
  {
    char* red = smem + (w & 3) * 4096 + kh * 2048;
    const f32x4 o0 = kh ? acc[0][0] : acc[1][0], o1 = kh ? acc[0][1] : acc[1][1];
    *(f32x4*)(red + lane * 16) = o0;
    *(f32x4*)(red + 1024 + lane * 16) = o1;
  }
  __syncthreads();
  constexpr int OT = 16384, OTP = 144;
  {
    const char* red = smem + (w & 3) * 4096 + (1 - kh) * 2048;
#pragma unroll
    for (int nt = 0; nt < 2; ++nt) {
      const f32x4 o = *(const f32x4*)(red + nt * 1024 + lane * 16);
      const f32x4 own = kh ? acc[1][nt] : acc[0][nt];
      const f32x4 s = kh ? (o + own) : (own + o);
      const unsigned alo = (unsigned)av[nt], ahi = (unsigned)(av[nt] >> 32);
      const float a[4] = {lo_f(alo), hi_f(alo), lo_f(ahi), hi_f(ahi)};
#pragma unroll
      for (int e = 0; e < 4; ++e) {
        const float o2 = bfr(gv[e] * a[e]) + bfr(bfr(1.f - gv[e]) * bfr(s[e]));
        *(__bf16*)(smem + OT + (lr + e) * OTP + (32 * wn + 16 * nt + r) * 2) = (__bf16)o2;
      }
    }
  }
  __syncthreads();
  {
    const int row = tid >> 3, c8 = tid & 7;
    const u32x4 v = *(const u32x4*)(smem + OT + row * OTP + c8 * 16);
    *(u32x4*)(out + (size_t)(row0 + row) * D + col0 + c8 * 8) = v;
  }
  STAMP(24);
}

NOPK __global__ __launch_bounds__(512, 4) void k2(const __bf16* __restrict__ h, const __bf16* __restrict__ wd,
                                             const __bf16* __restrict__ uT, const unsigned* __restrict__ kc,
                                             const __bf16* __restrict__ dsk, __bf16* __restrict__ aT,
                                             const float* __restrict__ gin, unsigned* __restrict__ flags,
                                             __bf16* __restrict__ out) {
  extern __shared__ __attribute__((aligned(16))) char smem[];
  const int b = blockIdx.x, xcd = b & 7, i = (b & 255) >> 3;
  SPAN_PT(1, 0);
  if (b < 256) {
    const int pair = 32 * xcd + i;
    conv_block(smem, 2 * pair, uT, kc, dsk, aT, flags + pair);
  } else {
    ffn2_block(smem, 64 * (4 * xcd + (i & 3)), i >> 2, h, wd, aT, gin, flags, out);
  }
  SPAN_PT(1, 1);
}

extern "C" void kernel_launch(void* const* d_in, const int* in_sizes, int n_in, void* d_out, int out_size,
                              void* d_ws, size_t ws_size, hipStream_t stream) {
  const __bf16* u = (const __bf16*)d_in[0];
  const __bf16* lar = (const __bf16*)d_in[1];
  const __bf16* aim = (const __bf16*)d_in[2];
  const __bf16* cre = (const __bf16*)d_in[3];
  const __bf16* cim = (const __bf16*)d_in[4];
  const __bf16* ldt = (const __bf16*)d_in[5];
  const __bf16* dsk = (const __bf16*)d_in[6];
  const __bf16* wg = (const __bf16*)d_in[8];
  const __bf16* wu = (const __bf16*)d_in[9];
  const __bf16* wd = (const __bf16*)d_in[10];
  const __bf16* gw = (const __bf16*)d_in[11];
  char* ws = (char*)d_ws;
  __bf16* h = (__bf16*)ws;
  ws += (size_t)L * HP * 2;
  __bf16* aT = (__bf16*)ws;
  ws += (size_t)D * L * 2;
  __bf16* uT = (__bf16*)ws;
  ws += (size_t)D * L * 2;
  unsigned* kc = (unsigned*)ws;
  ws += (size_t)D * KC_PITCH * 4;
  float* gout = (float*)ws;
  ws += L * 4;
  unsigned* flags = (unsigned*)ws;
  (void)hipFuncSetAttribute((const void*)k1, hipFuncAttributeMaxDynamicSharedMemorySize, LDS_BYTES);
  (void)hipFuncSetAttribute((const void*)k2, hipFuncAttributeMaxDynamicSharedMemorySize, LDS_BYTES);
  hipLaunchKernelGGL(k1, dim3(512), dim3(512), LDS_BYTES, stream, u, lar, aim, cre, cim, ldt, wg, wu, gw, h, uT, kc,
                     gout, flags);
  hipLaunchKernelGGL(k2, dim3(512), dim3(512), LDS_BYTES, stream, (const __bf16*)h, wd, (const __bf16*)uT,
                     (const unsigned*)kc, dsk, aT, (const float*)gout, flags, (__bf16*)d_out);
}
```

```cpp
#include <hip/hip_runtime.h>
#include <hip/hip_bf16.h>

typedef __attribute__((ext_vector_type(8))) __bf16 bf16x8;
typedef __attribute__((ext_vector_type(2))) __bf16 bf16x2;
typedef __attribute__((ext_vector_type(4))) short s16x4;
typedef __attribute__((ext_vector_type(8))) short s16x8;
typedef __attribute__((ext_vector_type(4))) float f32x4;
typedef __attribute__((ext_vector_type(4))) unsigned u32x4;
typedef __attribute__((ext_vector_type(2))) unsigned u32x2;
typedef __attribute__((address_space(3))) s16x4* lds_s16x4_ptr;
typedef __attribute__((address_space(1))) unsigned gu32;
typedef __attribute__((address_space(1))) unsigned long long gu64;

#define K_LO 1

#define NOPK __attribute__((target("no-packed-fp32-ops")))

#define PRIO_F1 0
#define PRIO_KG 2
#define PRIO_CV 0
#define PRIO_F2 2

constexpr int L = 2048, D = 512, NS = 128, F = 1376, HP = 1408;
constexpr int LDS_BYTES = 81920;

__device__ __forceinline__ float bf2f(unsigned short v) { return __uint_as_float((unsigned)v << 16); }
__device__ __forceinline__ float bfr(float x) { return (float)(__bf16)x; }
__device__ __forceinline__ unsigned pk2(float a, float b) {
  bf16x2 v = {(__bf16)a, (__bf16)b};
  return __builtin_bit_cast(unsigned, v);
}
__device__ __forceinline__ float lo_f(unsigned w) { return __uint_as_float(w << 16); }
__device__ __forceinline__ float hi_f(unsigned w) { return __uint_as_float(w & 0xffff0000u); }

__device__ __forceinline__ f32x4 mfma16(bf16x8 a, bf16x8 b, f32x4 c) {
  return __builtin_amdgcn_mfma_f32_16x16x32_bf16(a, b, c, 0, 0, 0);
}
__device__ __forceinline__ bf16x8 tr_frag(const char* lds, int off0, int off1) {
  s16x4 a = __builtin_amdgcn_ds_read_tr16_b64_v4i16((lds_s16x4_ptr)(lds + off0));
  s16x4 b = __builtin_amdgcn_ds_read_tr16_b64_v4i16((lds_s16x4_ptr)(lds + off1));
  s16x8 v = {a[0], a[1], a[2], a[3], b[0], b[1], b[2], b[3]};
  return __builtin_bit_cast(bf16x8, v);
}
__device__ __forceinline__ void glds16(const void* sbase, unsigned voff, unsigned lds_addr) {
  unsigned keep;
  asm volatile("s_mov_b32 %0, m0\n\ts_mov_b32 m0, %3\n\ts_nop 0\n\tglobal_load_lds_dwordx4 %1, %2\n\ts_mov_b32 m0, %0"
               : "=&s"(keep) : "v"(voff), "s"(sbase), "s"(lds_addr) : "memory");
}
__device__ __forceinline__ void glds16v(const void* vptr, unsigned lds_addr) {
  unsigned keep;
  asm volatile("s_mov_b32 %0, m0\n\ts_mov_b32 m0, %2\n\ts_nop 0\n\tglobal_load_lds_dwordx4 %1, off\n\ts_mov_b32 m0, %0"
               : "=&s"(keep) : "v"(vptr), "s"(lds_addr) : "memory");
}
__device__ __forceinline__ unsigned lds_addr_of(const char* p) {
  return (unsigned)(uintptr_t)(const __attribute__((address_space(3))) char*)p;
}
#define STAMP(i)
#define STAMP_WAVE 0
#define SSTAMP(var)
#define SPAN_PT(k, i)
#define WAIT_VM(n) asm volatile("s_waitcnt vmcnt(" #n ")" ::: "memory")
#define WAIT_LGKM0() asm volatile("s_waitcnt lgkmcnt(0)" ::: "memory")
template <int N>
__device__ __forceinline__ void wait_vm() {
  asm volatile("s_waitcnt vmcnt(%0)" ::"i"(N) : "memory");
}

#define STAMP_BASE 0
constexpr int F1_B = 32768;

struct F1Ctx {
  const __bf16 *x, *wg, *wu, *wmix;
  unsigned voffA, voffB0, voffB2, lbase;
  int w;
  int aoff[2], boff[6];
};
template <bool G2>
__device__ __forceinline__ void f1_stage(const F1Ctx& c, int hs) {
  const unsigned bb = c.lbase + F1_B + (hs & 3) * 12288 + c.w * 1024;
  glds16(c.x + hs * 32, c.voffA, c.lbase + (hs & 3) * 8192 + c.w * 1024);
  if constexpr (!G2) {
    glds16(c.wg + (size_t)hs * 32 * F, c.voffB0, bb);
    glds16(c.wu + (size_t)hs * 32 * F, c.voffB2, bb + 8192);
  } else {
    glds16v(c.wmix + (size_t)hs * 32 * F, bb);
  }
}
template <int HS, bool G2>
__device__ __forceinline__ void f1_loop(const F1Ctx& c, const char* smem, bf16x8 (&af)[2], bf16x8 (&bq)[6],
                                        f32x4 (&acc)[2][6]) {
  if constexpr (HS < 16) {
    constexpr int rem = (15 - HS) < 2 ? (15 - HS) : 2;
    wait_vm<rem*(G2 ? 2 : 3)>();
    WAIT_LGKM0();
    __builtin_amdgcn_s_barrier();
    asm volatile("" ::: "memory");
    const char* ab = smem + (HS & 3) * 8192;
    const char* bbase = smem + (HS & 3) * 12288;
    if constexpr (HS > 0) {
#pragma unroll
      for (int j = 0; j < 6; ++j) {
        acc[0][j] = mfma16(bq[j], af[0], acc[0][j]);
        if constexpr (HS + 3 < 16)
          if (j == 1) {
            __builtin_amdgcn_sched_barrier(0);
            f1_stage<G2>(c, HS + 3);
          }
      }
    } else {
      f1_stage<G2>(c, HS + 3);
    }
    __builtin_amdgcn_sched_barrier(0);
    af[0] = *(const bf16x8*)(ab + c.aoff[0]);
#pragma unroll
    for (int j = 0; j < 6; ++j) {
      if constexpr (HS > 0) acc[1][j] = mfma16(bq[j], af[1], acc[1][j]);
      __builtin_amdgcn_sched_barrier(0);
      bq[j] = tr_frag(bbase, c.boff[j], c.boff[j] + 512);
      __builtin_amdgcn_sched_barrier(0);
    }
    af[1] = *(const bf16x8*)(ab + c.aoff[1]);
    f1_loop<HS + 1, G2>(c, smem, af, bq, acc);
  } else {
#pragma unroll
    for (int j = 0; j < 6; ++j) {
      acc[0][j] = mfma16(bq[j], af[0], acc[0][j]);
      acc[1][j] = mfma16(bq[j], af[1], acc[1][j]);
    }
  }
}

__device__ __forceinline__ void ffn1_block(char* smem, int rb, int cb, const __bf16* __restrict__ x,
                                           const __bf16* __restrict__ wg, const __bf16* __restrict__ wu,
                                           __bf16* __restrict__ h) {
  const int tid = threadIdx.x, lane = tid & 63, w = __builtin_amdgcn_readfirstlane(tid >> 6);
  const int row0 = rb * 128, f0 = cb * 96;
  const int wm = w >> 1, wn = w & 1;
  const int r = lane & 15, g = lane >> 4, q = (lane >> 2) & 3, p = lane & 3;
  __builtin_amdgcn_s_setprio(PRIO_F1);
  F1Ctx c;
  c.x = x; c.wg = wg; c.wu = wu; c.w = w; c.lbase = lds_addr_of(smem);
  {
    const int row = 16 * w + (lane >> 2), ch = (lane & 3) ^ ((-(row >> 2)) & 3);
    c.voffA = (unsigned)(((row0 + row) * D + ch * 8) * 2);
    const int kk = 8 * (w & 3) + (lane >> 3), pos = lane & 7;
    const int fp = ((kk >> 1) & 1) | (((kk >> 3) & 1) << 1);
    const int i = (pos >> 1) ^ fp;
    int c0 = f0 + 16 * i + 8 * (pos & 1);
    int c1 = f0 + 16 * ((4 + i) % 6) + 8 * (pos & 1);
    int c2 = f0 + 16 * (2 + i) + 8 * (pos & 1);
    c0 = c0 > F - 8 ? F - 8 : c0;
    c1 = c1 > F - 8 ? F - 8 : c1;
    c2 = c2 > F - 8 ? F - 8 : c2;
    c.voffB0 = (unsigned)((kk * F + c0) * 2);
    c.voffB2 = (unsigned)((kk * F + c2) * 2);
    c.wmix = (i < 2 ? wg : wu) + kk * F + c1;
  }
#pragma unroll
  for (int mt = 0; mt < 2; ++mt) {
    const int row = 32 * wm + 16 * mt + r;
    c.aoff[mt] = row * 64 + ((g ^ ((-(row >> 2)) & 3)) << 4);
  }
  {
    const int fp = (q >> 1) | ((g & 1) << 1);
#pragma unroll
    for (int j = 0; j < 6; ++j) {
      const int T = (j < 3 ? 0 : 6) + 3 * wn + (j % 3);
      c.boff[j] = F1_B + (T >> 2) * 4096 + (8 * g + q) * 128 + (((T & 3) ^ fp) << 5) + 8 * p;
      asm volatile("" : "+v"(c.boff[j]));
    }
  }
  f32x4 acc[2][6];
#pragma unroll
  for (int mt = 0; mt < 2; ++mt)
#pragma unroll
    for (int j = 0; j < 6; ++j) acc[mt][j] = f32x4{0.f, 0.f, 0.f, 0.f};
  bf16x8 af[2], bq[6];

  if (w < 4) {
    f1_stage<false>(c, 0);
    f1_stage<false>(c, 1);
    f1_stage<false>(c, 2);
    f1_loop<0, false>(c, smem, af, bq, acc);
  } else {
    f1_stage<true>(c, 0);
    f1_stage<true>(c, 1);
    f1_stage<true>(c, 2);
    f1_loop<0, true>(c, smem, af, bq, acc);
  }

#pragma unroll
  for (int mt = 0; mt < 2; ++mt)
#pragma unroll
    for (int j = 0; j < 3; ++j) {
      const int f = f0 + 16 * (3 * wn + j);
      if (f < F) {
        float hv[4];
#pragma unroll
        for (int e = 0; e < 4; ++e) {
          const float gv = acc[mt][j][e];
          const float t = gv * __builtin_fmaf(gv * gv, -0.10294324f, -2.3022082f);
          hv[e] = (gv * acc[mt][3 + j][e]) * __builtin_amdgcn_rcpf(1.f + __builtin_amdgcn_exp2f(t));
        }
        u32x2 o = {pk2(hv[0], hv[1]), pk2(hv[2], hv[3])};
        *(u32x2*)(h + (size_t)(row0 + 32 * wm + 16 * mt + r) * HP + f + 4 * g) = o;
      }
    }
}

__device__ __forceinline__ void gate_block(int rb, const __bf16* __restrict__ x, const __bf16* __restrict__ gw,
                                           float* __restrict__ gout) {
  const int tid = threadIdx.x, row = rb * 128 + (tid >> 2), part = tid & 3;
  float s = 0.f;
#pragma unroll 8
  for (int c = 0; c < 16; ++c) {
    const int e0 = (c * 4 + part) * 8;
    const u32x4 xv = *(const u32x4*)(x + (size_t)row * D + e0);
    const u32x4 wv = *(const u32x4*)(gw + e0);
#pragma unroll
    for (int e = 0; e < 4; ++e) {
      const unsigned xe = xv[e], we = wv[e];
      s = __builtin_amdgcn_fdot2_f32_bf16(__builtin_bit_cast(bf16x2, xe), __builtin_bit_cast(bf16x2, we), s, false);
    }
  }
  s += __shfl_xor(s, 1);
  s += __shfl_xor(s, 2);
  if (part == 0) gout[row] = bfr(1.f / (1.f + __expf(-bfr(s))));
}

constexpr int UCP = 4160;
constexpr int KARR = (240 + L) * 2;
constexpr int OFF_K = 4 * UCP;
constexpr int OFF_PT = OFF_K + 2 * KARR;
constexpr int OFF_CT = OFF_PT + 4 * 656 * 4;
constexpr int CH_BYTES = OFF_CT + 2624;
constexpr int OFF_IMG = UCP;
constexpr int OFF_RED = UCP;
constexpr int KG_PITCH = 160;
constexpr int KG_ARR = 16 * KG_PITCH;
static_assert(OFF_IMG + 4 * 2 * KG_ARR <= OFF_PT && OFF_RED + 32768 <= CH_BYTES && 2 * CH_BYTES <= LDS_BYTES, "lds");
typedef __attribute__((ext_vector_type(2))) float f32x2;

struct PosLut {
  unsigned short v[L];
  constexpr PosLut() : v() {
    for (int j = 0; j < L; ++j) {
      const int sh = (j >= 256) + (j >= 512) + (j >= 1024);
      int q = j >> sh;
      if (sh) {
        const int rem = j & ((1 << sh) - 1), half = 1 << (sh - 1);
        if (rem > half || (rem == half && (q & 1))) ++q;
      }
      v[j] = (unsigned short)(4 * (128 * sh + q));
    }
  }
};
__device__ const PosLut g_poslut{};

__device__ __forceinline__ f32x2 cexp(float zr, float zi, float p) {
  const float e = __builtin_amdgcn_exp2f(zr * p);
  const float ph = __builtin_amdgcn_fractf(zi * p);
  return f32x2{e * __builtin_amdgcn_cosf(ph), e * __builtin_amdgcn_sinf(ph)};
}
__device__ __forceinline__ f32x2 cmul(f32x2 a, f32x2 b) {
  return f32x2{a[0] * b[0] - a[1] * b[1], a[0] * b[1] + a[1] * b[0]};
}

__device__ __forceinline__ void ssm_block(char* smem, int d0, const __bf16* __restrict__ u,
                                          const __bf16* __restrict__ lar, const __bf16* __restrict__ aim,
                                          const __bf16* __restrict__ cre, const __bf16* __restrict__ cim,
                                          const __bf16* __restrict__ ldt, const __bf16* __restrict__ dsk,
                                          __bf16* __restrict__ aT) {
  const int tid = threadIdx.x, lane = tid & 63, w = __builtin_amdgcn_readfirstlane(tid >> 6);
  const int ch = w >> 2, wq = w & 3, tc = tid & 255;
  const int r = lane & 15, g = lane >> 4, q = (lane >> 2) & 3, p = lane & 3, hh = lane >> 5;
  char* cb = smem + ch * CH_BYTES;
  const int d = d0 + ch;
  STAMP(0);
  __builtin_amdgcn_s_setprio(PRIO_KG);
  float zr, zi;
  f32x2 cc;
  {
    const int n = 32 * wq + (lane & 31);
    const float dt = bfr(expf((float)ldt[d]));
    const float ar = -bfr(expf((float)lar[d * NS + n]));
    zr = (ar * dt) * 1.4426950408889634f;
    zi = ((float)aim[d * NS + n] * dt) * 0.15915494309189535f;
    cc = f32x2{2.f * ((float)cre[d * NS + n] * dt), 2.f * ((float)cim[d * NS + n] * dt)};
  }
  const float dsv = (float)dsk[d];
  const u32x4 lut = *(const u32x4*)(g_poslut.v + 8 * tc);
  unsigned uv[4];
#pragma unroll
  for (int i = 0; i < 4; ++i) uv[i] = *(const unsigned*)(u + (size_t)(4 * tid + i) * D + d0);
  STAMP(1);
  {
    const float fh = (float)(1 + hh);
    const f32x2 e128 = cexp(zr, zi, 128.f), e24 = cexp(zr, zi, 2.f * fh);
    f32x2 v[4], st[4];
    v[0] = cexp(zr, -zi, 64.f * hh);                 st[0] = cexp(zr, -zi, 8.f);
    v[1] = hh ? cmul(cc, e128) : cc;                 st[1] = cexp(zr, zi, 1.f);
    v[2] = cmul(cc, cexp(zr, zi, 256.f * fh));       st[2] = hh ? e128 : e24;
    v[3] = cmul(cc, cexp(zr, zi, 1024.f * fh));      st[3] = e128;
    const f32x2 v24 = cmul(v[2], hh ? e24 : e128);
    unsigned vh[4][8], vl[4][8];
#pragma unroll
    for (int i = 0; i < 8; ++i)
#pragma unroll
      for (int t = 0; t < 4; ++t) {
        vh[t][i] = pk2(v[t][0], v[t][1]);
        vl[t][i] = pk2(v[t][0] - lo_f(vh[t][i]), v[t][1] - hi_f(vh[t][i]));
        if (t == 2 && i == 3) v[t] = v24;
        else if (i < 7) v[t] = cmul(v[t], st[t]);
      }
    char* wb = cb + OFF_IMG + wq * 2 * KG_ARR;
    char* wr = wb + (8 * hh) * KG_PITCH + (lane & 31) * 4;
    const char* rd = wb + r * KG_PITCH + g * 16;
    bf16x8 fh_[4][2], fl_[4][2];
#pragma unroll
    for (int t = 0; t < 4; ++t) {
      __builtin_amdgcn_wave_barrier();
#pragma unroll
      for (int i = 0; i < 8; ++i) {
        *(unsigned*)(wr + i * KG_PITCH) = vh[t][i];
        *(unsigned*)(wr + i * KG_PITCH + KG_ARR) = vl[t][i];
      }
      __builtin_amdgcn_wave_barrier();
#pragma unroll
      for (int ss = 0; ss < 2; ++ss) {
        fh_[t][ss] = *(const bf16x8*)(rd + ss * 64);
        fl_[t][ss] = *(const bf16x8*)(rd + ss * 64 + KG_ARR);
      }
    }
    f32x4 acc[3];
#pragma unroll
    for (int m = 0; m < 3; ++m) acc[m] = f32x4{0.f, 0.f, 0.f, 0.f};
#pragma unroll
    for (int ss = 0; ss < 2; ++ss)
#pragma unroll
      for (int m = 0; m < 3; ++m) {
        acc[m] = mfma16(fl_[1 + m][ss], fh_[0][ss], acc[m]);
        acc[m] = mfma16(fh_[1 + m][ss], fl_[0][ss], acc[m]);
        acc[m] = mfma16(fh_[1 + m][ss], fh_[0][ss], acc[m]);
      }
    float* tab = (float*)(cb + OFF_PT) + wq * 656;
    *(f32x4*)(tab + 128 * (g >> 1) + 4 * (g & 1) + 8 * r) = acc[0];
    if (g < 2) {
      *(f32x4*)(tab + 256 + 64 * g + 4 * r) = acc[1];
#pragma unroll
      for (int e = 0; e < 4; ++e) tab[512 + 64 * g + 16 * e + r] = acc[2][e];
    } else {
#pragma unroll
      for (int e = 0; e < 4; ++e) tab[384 + (g & 1) + 32 * e + 2 * r] = acc[1][e];
      if (lane == 32) tab[640] = acc[2][0];
    }
  }
  STAMP(2);
  {
    const unsigned a0 = __builtin_amdgcn_perm(uv[1], uv[0], 0x05040100u), a1 = __builtin_amdgcn_perm(uv[3], uv[2], 0x05040100u);
    const unsigned b0 = __builtin_amdgcn_perm(uv[1], uv[0], 0x07060302u), b1 = __builtin_amdgcn_perm(uv[3], uv[2], 0x07060302u);
    *(u32x2*)(smem + 32 + 8 * tid) = u32x2{a0, a1};
    *(u32x2*)(smem + CH_BYTES + 32 + 8 * tid) = u32x2{b0, b1};
    if (tc < 2) *(u32x4*)(cb + tc * 16) = u32x4{0u, 0u, 0u, 0u};
  }
  __syncthreads();
  STAMP(3);
  {
    const float* tab = (const float*)(cb + OFF_PT);
#pragma unroll
    for (int e = 0; e < 3; ++e) {
      const int id = tc + 256 * e;
      if (id < 641) {
        const float v = (tab[id] + tab[656 + id]) + (tab[2 * 656 + id] + tab[3 * 656 + id]);
        const float vh = bfr(v);
        *(unsigned*)(cb + OFF_CT + 4 * id) = pk2(vh, v - vh);
      }
    }
    const u32x4 pa = *(const u32x4*)(cb + 16 + 16 * tc), pb = *(const u32x4*)(cb + 32 + 16 * tc);
    const u32x4 c1 = {__builtin_amdgcn_alignbit(pb[0], pa[3], 16), __builtin_amdgcn_alignbit(pb[1], pb[0], 16),
                      __builtin_amdgcn_alignbit(pb[2], pb[1], 16), __builtin_amdgcn_alignbit(pb[3], pb[2], 16)};
    const u32x4 c2 = {pa[3], pb[0], pb[1], pb[2]};
    const u32x4 c3 = {__builtin_amdgcn_alignbit(pa[3], pa[2], 16), c1[0], c1[1], c1[2]};
    *(u32x4*)(cb + 1 * UCP + 32 + 16 * tc) = c1;
    *(u32x4*)(cb + 2 * UCP + 32 + 16 * tc) = c2;
    *(u32x4*)(cb + 3 * UCP + 32 + 16 * tc) = c3;
    const u32x4 z = {0u, 0u, 0u, 0u};
    if (tc >= 192) {
      const int i = tc - 192;
      if (i < 60) *(u32x4*)(cb + OFF_K + (i >= 30 ? KARR : 0) + (i % 30) * 16) = z;
    } else if (tc >= 186) {
      const int i = tc - 186;
      *(u32x4*)(cb + (1 + (i >> 1)) * UCP + (i & 1) * 16) = z;
    }
  }
  __syncthreads();
  {
    u32x4 kh, kl;
#pragma unroll
    for (int e = 0; e < 4; ++e) {
      const unsigned t0 = *(const unsigned*)(cb + OFF_CT + (lut[e] & 0xffffu));
      const unsigned t1 = *(const unsigned*)(cb + OFF_CT + (lut[e] >> 16));
      kh[e] = __builtin_amdgcn_perm(t1, t0, 0x05040100u);
      kl[e] = __builtin_amdgcn_perm(t1, t0, 0x07060302u);
    }
    *(u32x4*)(cb + OFF_K + 480 + 16 * tc) = kh;
    *(u32x4*)(cb + OFF_K + KARR + 480 + 16 * tc) = kl;
  }
  __syncthreads();
  STAMP(4);
  __builtin_amdgcn_s_setprio(PRIO_CV);
  f32x4 acc[8];
#pragma unroll
  for (int t = 0; t < 8; ++t) acc[t] = f32x4{0.f, 0.f, 0.f, 0.f};
#pragma unroll
  for (int rr = 0; rr < 2; ++rr) {
    const int rho = 2 * wq + rr;
    const int abase = q * UCP + 2 * (16 + 16 + 32 * rho - 8 * g + 4 * p);
    const int bbase = OFF_K + 2 * (240 - 16 - 32 * rho + 16 * r + 8 * g);
    bf16x8 af[8];
#pragma unroll
    for (int s = 0; s < 8; ++s) af[s] = tr_frag(cb, abase + 512 * s, abase + 512 * s - 8);
#pragma unroll
    for (int t = 0; t < 8; ++t) {
      const bf16x8 bh = *(const bf16x8*)(cb + bbase + 512 * t);
#pragma unroll
      for (int s = 0; s + t < 8; ++s) acc[s + t] = mfma16(af[s], bh, acc[s + t]);
      const bf16x8 bl = *(const bf16x8*)(cb + bbase + KARR + 512 * t);
#pragma unroll
      for (int s = 0; s + t < 8; ++s) acc[s + t] = mfma16(af[s], bl, acc[s + t]);
    }
  }
  __syncthreads();
  STAMP(5);
#pragma unroll
  for (int t = 0; t < 8; ++t)
    if ((t >> 1) != wq) *(f32x4*)(cb + OFF_RED + (wq * 8 + t) * 1024 + lane * 16) = acc[t];
  __syncthreads();
#pragma unroll
  for (int tt = 0; tt < 2; ++tt) {
    const int t = 2 * wq + tt;
    f32x4 own = f32x4{0.f, 0.f, 0.f, 0.f};
#pragma unroll
    for (int t2 = 0; t2 < 8; ++t2)
      if (t2 == t) own = acc[t2];
    f32x4 s = f32x4{0.f, 0.f, 0.f, 0.f};
#pragma unroll
    for (int sw = 0; sw < 4; ++sw) {
      const f32x4 o = *(const f32x4*)(cb + OFF_RED + (sw * 8 + t) * 1024 + lane * 16);
      s += (sw == wq) ? own : o;
    }
    const int l = 256 * t + 16 * r + 4 * g;
    const u32x2 uu = *(const u32x2*)(cb + 32 + 2 * l);
    const float a0 = bfr(s[0]) + bfr(lo_f(uu[0]) * dsv), a1 = bfr(s[1]) + bfr(hi_f(uu[0]) * dsv);
    const float a2 = bfr(s[2]) + bfr(lo_f(uu[1]) * dsv), a3 = bfr(s[3]) + bfr(hi_f(uu[1]) * dsv);
    *(u32x2*)(aT + (size_t)d * L + l) = u32x2{pk2(a0, a1), pk2(a2, a3)};
  }
  STAMP(6);
}

NOPK __global__ __launch_bounds__(512, 4) void k1(const __bf16* __restrict__ u, const __bf16* __restrict__ lar,
                                                  const __bf16* __restrict__ aim, const __bf16* __restrict__ cre,
                                                  const __bf16* __restrict__ cim, const __bf16* __restrict__ ldt,
                                                  const __bf16* __restrict__ dsk, const __bf16* __restrict__ wg,
                                                  const __bf16* __restrict__ wu, const __bf16* __restrict__ gw,
                                                  __bf16* __restrict__ h, __bf16* __restrict__ aT,
                                                  float* __restrict__ gout) {
  extern __shared__ __attribute__((aligned(16))) char smem[];
  const int b = blockIdx.x;
  const int xcd = b & 7, i = (b & 255) >> 3;
  SPAN_PT(0, 0);
  if (b < 256) {
    const int rb = 4 * (xcd >> 1) + (i & 3), cb = 8 * (xcd & 1) + (i >> 2);
    if (cb < 15) ffn1_block(smem, rb, cb, u, wg, wu, h);
    else gate_block(rb, u, gw, gout);
  } else {
    ssm_block(smem, 64 * xcd + 2 * i, u, lar, aim, cre, cim, ldt, dsk, aT);
  }
  SPAN_PT(0, 1);
}

#undef STAMP_BASE
#define STAMP_BASE 512
constexpr int F2_NS = 8;
constexpr int F2_STAGE = 16384;
constexpr int F2_AT = F2_NS * F2_STAGE;
constexpr int F2_LDS = F2_AT + 8192;
constexpr int F2_NST = 22;

struct F2Ctx {
  const __bf16 *h, *wd;
  unsigned avoff, bvoff, bvoff_last, lbase;
  int w, kh;
  int aoff[2], boff[2];
};
__device__ __forceinline__ void f2_stage(const F2Ctx& c, int st) {
  const unsigned base = c.lbase + (st % F2_NS) * F2_STAGE;
  glds16(c.h + st * 64, c.avoff, base + c.w * 1024);
  glds16(c.wd + (size_t)st * 64 * D, st == F2_NST - 1 ? c.bvoff_last : c.bvoff, base + 8192 + c.w * 1024);
}
template <int ST>
__device__ __forceinline__ void f2_loop(const F2Ctx& c, const char* smem, bf16x8 (&ao)[2], bf16x8 (&bo)[2],
                                        f32x4 (&acc)[2][2]) {
  if constexpr (ST < F2_NST) {
    constexpr int rem = (F2_NST - 1 - ST) < (F2_NS - 2) ? (F2_NST - 1 - ST) : (F2_NS - 2);
    wait_vm<2 * rem>();
    WAIT_LGKM0();
    __builtin_amdgcn_s_barrier();
    asm volatile("" ::: "memory");
    STAMP(1 + ST);
    if constexpr (ST + F2_NS - 1 < F2_NST) f2_stage(c, ST + F2_NS - 1);
    const char* base = smem + (ST % F2_NS) * F2_STAGE;
    bf16x8 an[2], bn[2];
#pragma unroll
    for (int mt = 0; mt < 2; ++mt) an[mt] = *(const bf16x8*)(base + c.aoff[mt]);
#pragma unroll
    for (int nt = 0; nt < 2; ++nt) bn[nt] = tr_frag(base, c.boff[nt], c.boff[nt] + 512);
    __builtin_amdgcn_sched_barrier(0);
    if constexpr (ST > 0) {
#pragma unroll
      for (int mt = 0; mt < 2; ++mt)
#pragma unroll
        for (int nt = 0; nt < 2; ++nt) acc[mt][nt] = mfma16(bo[nt], ao[mt], acc[mt][nt]);
    }
    __builtin_amdgcn_sched_barrier(0);
    f2_loop<ST + 1>(c, smem, an, bn, acc);
  } else {
    if (c.kh == 0) {
#pragma unroll
      for (int mt = 0; mt < 2; ++mt)
#pragma unroll
        for (int nt = 0; nt < 2; ++nt) acc[mt][nt] = mfma16(bo[nt], ao[mt], acc[mt][nt]);
    }
  }
}

NOPK __global__ __launch_bounds__(512, 2) void k2(const __bf16* __restrict__ h, const __bf16* __restrict__ wd,
                                                  const __bf16* __restrict__ aT, const float* __restrict__ gin,
                                                  __bf16* __restrict__ out) {
  extern __shared__ __attribute__((aligned(16))) char smem[];
  const int b = blockIdx.x, xcd = b & 7, i = b >> 3;
  const int row0 = 64 * (4 * xcd + (i & 3)), col0 = 64 * (i >> 2);
  const int tid = threadIdx.x, lane = tid & 63, w = __builtin_amdgcn_readfirstlane(tid >> 6);
  const int r = lane & 15, g = lane >> 4, q = (lane >> 2) & 3, p = lane & 3;
  const int kh = w >> 2, wm = (w >> 1) & 1, wn = w & 1;
  SPAN_PT(1, 0);
  STAMP(0);
  F2Ctx c;
  c.h = h; c.wd = wd; c.w = w; c.kh = kh; c.lbase = lds_addr_of(smem);
  {
    const int row = 8 * w + (lane >> 3), ch = (lane & 7) ^ ((row >> 1) & 7);
    c.avoff = (unsigned)(((row0 + row) * HP + ch * 8) * 2);
    const int bk = 8 * w + (lane >> 3);
    const int pos = lane & 7, fp = ((bk >> 1) & 1) | (((bk >> 3) & 1) << 1);
    const int cc = col0 + 16 * ((pos >> 1) ^ fp) + 8 * (pos & 1);
    c.bvoff = (unsigned)((bk * D + cc) * 2);
    c.bvoff_last = (unsigned)(((bk > 31 ? 31 : bk) * D + cc) * 2);
    glds16(aT, (unsigned)(((col0 + bk) * L + row0 + 16 * ((pos >> 1) ^ fp) + 8 * (pos & 1)) * 2),
           c.lbase + F2_AT + w * 1024);
  }
#pragma unroll
  for (int mt = 0; mt < 2; ++mt) {
    const int row = 32 * wm + 16 * mt + r, ch = 4 * kh + g;
    c.aoff[mt] = row * 128 + ((ch ^ ((row >> 1) & 7)) << 4);
  }
  const int fpl = (q >> 1) | ((g & 1) << 1);
#pragma unroll
  for (int nt = 0; nt < 2; ++nt) c.boff[nt] = 8192 + (32 * kh + 8 * g + q) * 128 + (((2 * wn + nt) ^ fpl) << 5) + 8 * p;
  const int l = row0 + 32 * wm + 16 * kh + r;
  const float gv = gin[l];

  f32x4 acc[2][2];
#pragma unroll
  for (int mt = 0; mt < 2; ++mt)
#pragma unroll
    for (int nt = 0; nt < 2; ++nt) acc[mt][nt] = f32x4{0.f, 0.f, 0.f, 0.f};

#pragma unroll
  for (int st = 0; st < F2_NS - 1; ++st) f2_stage(c, st);
  bf16x8 a0[2], b0[2];
  f2_loop<0>(c, smem, a0, b0, acc);
  STAMP(23);
  WAIT_LGKM0();
  __builtin_amdgcn_s_barrier();
  asm volatile("" ::: "memory");
  {
    char* red = smem + (w & 3) * 4096 + kh * 2048;
    const f32x4 o0 = kh ? acc[0][0] : acc[1][0], o1 = kh ? acc[0][1] : acc[1][1];
    *(f32x4*)(red + lane * 16) = o0;
    *(f32x4*)(red + 1024 + lane * 16) = o1;
  }
  __syncthreads();
  {
    const char* red = smem + (w & 3) * 4096 + (1 - kh) * 2048;
    const float og = bfr(1.f - gv);
#pragma unroll
    for (int nt = 0; nt < 2; ++nt) {
      const f32x4 o = *(const f32x4*)(red + nt * 1024 + lane * 16);
      const f32x4 own = kh ? acc[1][nt] : acc[0][nt];
      const f32x4 s = kh ? (o + own) : (own + o);
      const int dd = 32 * wn + 16 * nt + 4 * g + q;
      const int fp = ((dd >> 1) & 1) | (((dd >> 3) & 1) << 1);
      const int c16 = (2 * wm + kh) ^ fp;
      const s16x4 av = __builtin_amdgcn_ds_read_tr16_b64_v4i16(
          (lds_s16x4_ptr)(smem + F2_AT + dd * 128 + (c16 << 5) + 8 * p));
      float ov[4];
#pragma unroll
      for (int e = 0; e < 4; ++e) {
        const float a = bf2f((unsigned short)av[e]);
        ov[e] = bfr(gv * a) + bfr(og * bfr(s[e]));
      }
      *(u32x2*)(out + (size_t)l * D + col0 + 32 * wn + 16 * nt + 4 * g) = u32x2{pk2(ov[0], ov[1]), pk2(ov[2], ov[3])};
    }
  }
  STAMP(24);
  SPAN_PT(1, 1);
}

extern "C" void kernel_launch(void* const* d_in, const int* in_sizes, int n_in, void* d_out, int out_size,
                              void* d_ws, size_t ws_size, hipStream_t stream) {
  const __bf16* u = (const __bf16*)d_in[0];
  const __bf16* lar = (const __bf16*)d_in[1];
  const __bf16* aim = (const __bf16*)d_in[2];
  const __bf16* cre = (const __bf16*)d_in[3];
  const __bf16* cim = (const __bf16*)d_in[4];
  const __bf16* ldt = (const __bf16*)d_in[5];
  const __bf16* dsk = (const __bf16*)d_in[6];
  const __bf16* wg = (const __bf16*)d_in[8];
  const __bf16* wu = (const __bf16*)d_in[9];
  const __bf16* wd = (const __bf16*)d_in[10];
  const __bf16* gw = (const __bf16*)d_in[11];
  char* ws = (char*)d_ws;
  __bf16* h = (__bf16*)ws;
  ws += (size_t)L * HP * 2;
  __bf16* aT = (__bf16*)ws;
  ws += (size_t)D * L * 2;
  float* gout = (float*)ws;
  (void)hipFuncSetAttribute((const void*)k1, hipFuncAttributeMaxDynamicSharedMemorySize, LDS_BYTES);
  (void)hipFuncSetAttribute((const void*)k2, hipFuncAttributeMaxDynamicSharedMemorySize, F2_LDS);
  hipLaunchKernelGGL(k1, dim3(512), dim3(512), LDS_BYTES, stream, u, lar, aim, cre, cim, ldt, dsk, wg, wu, gw, h, aT,
                     gout);
  hipLaunchKernelGGL(k2, dim3(256), dim3(512), F2_LDS, stream, (const __bf16*)h, wd, (const __bf16*)aT,
                     (const float*)gout, (__bf16*)d_out);
}
```

```cpp
#include <hip/hip_runtime.h>
#include <hip/hip_bf16.h>

typedef __attribute__((ext_vector_type(8))) __bf16 bf16x8;
typedef __attribute__((ext_vector_type(2))) __bf16 bf16x2;
typedef __attribute__((ext_vector_type(4))) short s16x4;
typedef __attribute__((ext_vector_type(8))) short s16x8;
typedef __attribute__((ext_vector_type(4))) float f32x4;
typedef __attribute__((ext_vector_type(4))) unsigned u32x4;
typedef __attribute__((ext_vector_type(2))) unsigned u32x2;
typedef __attribute__((address_space(3))) s16x4* lds_s16x4_ptr;
typedef __attribute__((address_space(1))) unsigned gu32;
typedef __attribute__((address_space(1))) unsigned long long gu64;

#define K_LO 1

#define NOPK __attribute__((target("no-packed-fp32-ops")))

#define PRIO_F1 0
#define PRIO_KG 2
#define PRIO_CV 0
#define PRIO_F2 2

constexpr int L = 2048, D = 512, NS = 128, F = 1376, HP = 1408;
constexpr int LDS_BYTES = 81920;

__device__ __forceinline__ float bf2f(unsigned short v) { return __uint_as_float((unsigned)v << 16); }
__device__ __forceinline__ float bfr(float x) { return (float)(__bf16)x; }
__device__ __forceinline__ unsigned pk2(float a, float b) {
  bf16x2 v = {(__bf16)a, (__bf16)b};
  return __builtin_bit_cast(unsigned, v);
}
__device__ __forceinline__ float lo_f(unsigned w) { return __uint_as_float(w << 16); }
__device__ __forceinline__ float hi_f(unsigned w) { return __uint_as_float(w & 0xffff0000u); }

__device__ __forceinline__ f32x4 mfma16(bf16x8 a, bf16x8 b, f32x4 c) {
  return __builtin_amdgcn_mfma_f32_16x16x32_bf16(a, b, c, 0, 0, 0);
}
__device__ __forceinline__ bf16x8 tr_frag(const char* lds, int off0, int off1) {
  s16x4 a = __builtin_amdgcn_ds_read_tr16_b64_v4i16((lds_s16x4_ptr)(lds + off0));
  s16x4 b = __builtin_amdgcn_ds_read_tr16_b64_v4i16((lds_s16x4_ptr)(lds + off1));
  s16x8 v = {a[0], a[1], a[2], a[3], b[0], b[1], b[2], b[3]};
  return __builtin_bit_cast(bf16x8, v);
}
__device__ __forceinline__ void glds16(const void* sbase, unsigned voff, unsigned lds_addr) {
  unsigned keep;
  asm volatile("s_mov_b32 %0, m0\n\ts_mov_b32 m0, %3\n\ts_nop 0\n\tglobal_load_lds_dwordx4 %1, %2\n\ts_mov_b32 m0, %0"
               : "=&s"(keep) : "v"(voff), "s"(sbase), "s"(lds_addr) : "memory");
}
__device__ __forceinline__ void glds16v(const void* vptr, unsigned lds_addr) {
  unsigned keep;
  asm volatile("s_mov_b32 %0, m0\n\ts_mov_b32 m0, %2\n\ts_nop 0\n\tglobal_load_lds_dwordx4 %1, off\n\ts_mov_b32 m0, %0"
               : "=&s"(keep) : "v"(vptr), "s"(lds_addr) : "memory");
}
__device__ __forceinline__ unsigned lds_addr_of(const char* p) {
  return (unsigned)(uintptr_t)(const __attribute__((address_space(3))) char*)p;
}
#define STAMP(i)
#define STAMP_WAVE 0
#define SSTAMP(var)
#define SPAN_PT(k, i)
#define WAIT_VM(n) asm volatile("s_waitcnt vmcnt(" #n ")" ::: "memory")
#define WAIT_LGKM0() asm volatile("s_waitcnt lgkmcnt(0)" ::: "memory")
template <int N>
__device__ __forceinline__ void wait_vm() {
  asm volatile("s_waitcnt vmcnt(%0)" ::"i"(N) : "memory");
}

#define STAMP_BASE 0
constexpr int F1_B = 32768;

struct F1Ctx {
  const __bf16 *x, *wg, *wu, *wmix;
  unsigned voffA, voffB0, voffB2, lbase;
  int w;
  int aoff[2], boff[6];
};
template <bool G2>
__device__ __forceinline__ void f1_stage(const F1Ctx& c, int hs) {
  const unsigned bb = c.lbase + F1_B + (hs & 3) * 12288 + c.w * 1024;
  glds16(c.x + hs * 32, c.voffA, c.lbase + (hs & 3) * 8192 + c.w * 1024);
  if constexpr (!G2) {
    glds16(c.wg + (size_t)hs * 32 * F, c.voffB0, bb);
    glds16(c.wu + (size_t)hs * 32 * F, c.voffB2, bb + 8192);
  } else {
    glds16v(c.wmix + (size_t)hs * 32 * F, bb);
  }
}
template <int HS, bool G2>
__device__ __forceinline__ void f1_loop(const F1Ctx& c, const char* smem, bf16x8 (&af)[2], bf16x8 (&bq)[6],
                                        f32x4 (&acc)[2][6]) {
  if constexpr (HS < 16) {
    constexpr int rem = (15 - HS) < 2 ? (15 - HS) : 2;
    wait_vm<rem*(G2 ? 2 : 3)>();
    WAIT_LGKM0();
    __builtin_amdgcn_s_barrier();
    asm volatile("" ::: "memory");
    const char* ab = smem + (HS & 3) * 8192;
    const char* bbase = smem + (HS & 3) * 12288;
    if constexpr (HS > 0) {
#pragma unroll
      for (int j = 0; j < 6; ++j) {
        acc[0][j] = mfma16(bq[j], af[0], acc[0][j]);
        if constexpr (HS + 3 < 16)
          if (j == 1) {
            __builtin_amdgcn_sched_barrier(0);
            f1_stage<G2>(c, HS + 3);
          }
      }
    } else {
      f1_stage<G2>(c, HS + 3);
    }
    __builtin_amdgcn_sched_barrier(0);
    af[0] = *(const bf16x8*)(ab + c.aoff[0]);
#pragma unroll
    for (int j = 0; j < 6; ++j) {
      if constexpr (HS > 0) acc[1][j] = mfma16(bq[j], af[1], acc[1][j]);
      __builtin_amdgcn_sched_barrier(0);
      bq[j] = tr_frag(bbase, c.boff[j], c.boff[j] + 512);
      __builtin_amdgcn_sched_barrier(0);
    }
    af[1] = *(const bf16x8*)(ab + c.aoff[1]);
    f1_loop<HS + 1, G2>(c, smem, af, bq, acc);
  } else {
#pragma unroll
    for (int j = 0; j < 6; ++j) {
      acc[0][j] = mfma16(bq[j], af[0], acc[0][j]);
      acc[1][j] = mfma16(bq[j], af[1], acc[1][j]);
    }
  }
}

__device__ __forceinline__ void ffn1_block(char* smem, int rb, int cb, const __bf16* __restrict__ x,
                                           const __bf16* __restrict__ wg, const __bf16* __restrict__ wu,
                                           __bf16* __restrict__ h) {
  const int tid = threadIdx.x, lane = tid & 63, w = __builtin_amdgcn_readfirstlane(tid >> 6);
  const int row0 = rb * 128, f0 = cb * 96;
  const int wm = w >> 1, wn = w & 1;
  const int r = lane & 15, g = lane >> 4, q = (lane >> 2) & 3, p = lane & 3;
  __builtin_amdgcn_s_setprio(PRIO_F1);
  F1Ctx c;
  c.x = x; c.wg = wg; c.wu = wu; c.w = w; c.lbase = lds_addr_of(smem);
  {
    const int row = 16 * w + (lane >> 2), ch = (lane & 3) ^ ((-(row >> 2)) & 3);
    c.voffA = (unsigned)(((row0 + row) * D + ch * 8) * 2);
    const int kk = 8 * (w & 3) + (lane >> 3), pos = lane & 7;
    const int fp = ((kk >> 1) & 1) | (((kk >> 3) & 1) << 1);
    const int i = (pos >> 1) ^ fp;
    int c0 = f0 + 16 * i + 8 * (pos & 1);
    int c1 = f0 + 16 * ((4 + i) % 6) + 8 * (pos & 1);
    int c2 = f0 + 16 * (2 + i) + 8 * (pos & 1);
    c0 = c0 > F - 8 ? F - 8 : c0;
    c1 = c1 > F - 8 ? F - 8 : c1;
    c2 = c2 > F - 8 ? F - 8 : c2;
    c.voffB0 = (unsigned)((kk * F + c0) * 2);
    c.voffB2 = (unsigned)((kk * F + c2) * 2);
    c.wmix = (i < 2 ? wg : wu) + kk * F + c1;
  }
#pragma unroll
  for (int mt = 0; mt < 2; ++mt) {
    const int row = 32 * wm + 16 * mt + r;
    c.aoff[mt] = row * 64 + ((g ^ ((-(row >> 2)) & 3)) << 4);
  }
  {
    const int fp = (q >> 1) | ((g & 1) << 1);
#pragma unroll
    for (int j = 0; j < 6; ++j) {
      const int T = (j < 3 ? 0 : 6) + 3 * wn + (j % 3);
      c.boff[j] = F1_B + (T >> 2) * 4096 + (8 * g + q) * 128 + (((T & 3) ^ fp) << 5) + 8 * p;
      asm volatile("" : "+v"(c.boff[j]));
    }
  }
  f32x4 acc[2][6];
#pragma unroll
  for (int mt = 0; mt < 2; ++mt)
#pragma unroll
    for (int j = 0; j < 6; ++j) acc[mt][j] = f32x4{0.f, 0.f, 0.f, 0.f};
  bf16x8 af[2], bq[6];

  if (w < 4) {
    f1_stage<false>(c, 0);
    f1_stage<false>(c, 1);
    f1_stage<false>(c, 2);
    f1_loop<0, false>(c, smem, af, bq, acc);
  } else {
    f1_stage<true>(c, 0);
    f1_stage<true>(c, 1);
    f1_stage<true>(c, 2);
    f1_loop<0, true>(c, smem, af, bq, acc);
  }

#pragma unroll
  for (int mt = 0; mt < 2; ++mt)
#pragma unroll
    for (int j = 0; j < 3; ++j) {
      const int f = f0 + 16 * (3 * wn + j);
      if (f < F) {
        float hv[4];
#pragma unroll
        for (int e = 0; e < 4; ++e) {
          const float gv = acc[mt][j][e];
          const float t = gv * __builtin_fmaf(gv * gv, -0.10294324f, -2.3022082f);
          hv[e] = (gv * acc[mt][3 + j][e]) * __builtin_amdgcn_rcpf(1.f + __builtin_amdgcn_exp2f(t));
        }
        u32x2 o = {pk2(hv[0], hv[1]), pk2(hv[2], hv[3])};
        *(u32x2*)(h + (size_t)(row0 + 32 * wm + 16 * mt + r) * HP + f + 4 * g) = o;
      }
    }
}

__device__ __forceinline__ void gate_block(char* smem, int rb, const __bf16* __restrict__ x,
                                           const __bf16* __restrict__ gw, float* __restrict__ gout,
                                           __bf16* __restrict__ uT) {
  const int tid = threadIdx.x, lane = tid & 63, w = __builtin_amdgcn_readfirstlane(tid >> 6);
  const int g = lane >> 4, q = (lane >> 2) & 3, p = lane & 3;
  const unsigned lbase = lds_addr_of(smem);
  const int grow = tid >> 3, gpart = tid & 7, gf = (grow & 3) | (((grow >> 3) & 1) << 2);
  u32x4 gwv[8];
#pragma unroll
  for (int i = 0; i < 8; ++i) gwv[i] = *(const u32x4*)(gw + ((gpart ^ gf) + 8 * (i >> 1)) * 16 + 8 * (i & 1));
#pragma unroll 1
  for (int pass = 0; pass < 2; ++pass) {
    const int l0 = rb * 128 + pass * 64;
#pragma unroll
    for (int i = 0; i < 8; ++i) {
      const int row = 8 * w + i, fr = (row & 3) | (((row >> 3) & 1) << 2);
      glds16(x + (size_t)(l0 + row) * D, (unsigned)((((lane >> 1) ^ fr) * 2 + (lane & 1)) * 16), lbase + row * 1024);
    }
    WAIT_VM(0);
    __syncthreads();
#pragma unroll
    for (int i = 0; i < 8; ++i) {
      const int unit = w * 8 + i, c16 = unit >> 1, half = unit & 1;
      const int ra = 32 * half + 8 * g + q, rb2 = ra + 4;
      const int fa = (ra & 3) | (((ra >> 3) & 1) << 2);
      const bf16x8 v = tr_frag(smem, ra * 1024 + ((c16 ^ fa) << 5) + 8 * p, rb2 * 1024 + ((c16 ^ fa) << 5) + 8 * p);
      *(bf16x8*)(uT + (size_t)(16 * c16 + (lane & 15)) * L + l0 + 32 * half + 8 * g) = v;
    }
    float s = 0.f;
#pragma unroll
    for (int i = 0; i < 8; ++i) {
      const u32x4 xv = *(const u32x4*)(smem + grow * 1024 + (gpart + 8 * (i >> 1)) * 32 + 16 * (i & 1));
#pragma unroll
      for (int e = 0; e < 4; ++e) {
        const unsigned xe = xv[e], we = gwv[i][e];
        s = __builtin_amdgcn_fdot2_f32_bf16(__builtin_bit_cast(bf16x2, xe), __builtin_bit_cast(bf16x2, we), s, false);
      }
    }
    s += __shfl_xor(s, 1);
    s += __shfl_xor(s, 2);
    s += __shfl_xor(s, 4);
    if (gpart == 0) gout[l0 + grow] = bfr(1.f / (1.f + __expf(-bfr(s))));
    __syncthreads();
  }
}

constexpr int KC_PITCH = 768;
constexpr int KG_PITCH = 160;
constexpr int KG_ARR = 16 * KG_PITCH;
constexpr int KG_TAB = 8 * 2 * KG_ARR;
typedef __attribute__((ext_vector_type(2))) float f32x2;

__device__ __forceinline__ f32x2 cexp(float zr, float zi, float p) {
  const float e = __builtin_amdgcn_exp2f(zr * p);
  const float ph = __builtin_amdgcn_fractf(zi * p);
  return f32x2{e * __builtin_amdgcn_cosf(ph), e * __builtin_amdgcn_sinf(ph)};
}
__device__ __forceinline__ f32x2 cmul(f32x2 a, f32x2 b) {
  return f32x2{a[0] * b[0] - a[1] * b[1], a[0] * b[1] + a[1] * b[0]};
}
__device__ __forceinline__ void kgen_block(char* smem, int d0, const __bf16* __restrict__ lar,
                                           const __bf16* __restrict__ aim, const __bf16* __restrict__ cre,
                                           const __bf16* __restrict__ cim, const __bf16* __restrict__ ldt,
                                           unsigned* __restrict__ kc, unsigned* __restrict__ aG) {
  const int tid = threadIdx.x, lane = tid & 63, w = __builtin_amdgcn_readfirstlane(tid >> 6);
  {
    u32x4* z = (u32x4*)(aG + (size_t)d0 * L) + tid;
    z[0] = u32x4{0u, 0u, 0u, 0u};
    z[512] = u32x4{0u, 0u, 0u, 0u};
  }
  const int ch = w >> 2, wq = w & 3, tc = tid & 255;
  const int r = lane & 15, g = lane >> 4, hh = lane >> 5;
  char* wb = smem + w * 2 * KG_ARR;
  char* tb = smem + KG_TAB + ch * (4 * 656 * 4);
  STAMP(0);
  __builtin_amdgcn_s_setprio(PRIO_KG);
  float zr, zi;
  f32x2 cc;
  {
    const int d = d0 + ch, n = 32 * wq + (lane & 31);
    const float dt = bfr(expf((float)ldt[d]));
    const float ar = -bfr(expf((float)lar[d * NS + n]));
    zr = (ar * dt) * 1.4426950408889634f;
    zi = ((float)aim[d * NS + n] * dt) * 0.15915494309189535f;
    cc = f32x2{2.f * ((float)cre[d * NS + n] * dt), 2.f * ((float)cim[d * NS + n] * dt)};
  }
  STAMP(1);
  const float fh = (float)(1 + hh);
  const f32x2 e128 = cexp(zr, zi, 128.f), e24 = cexp(zr, zi, 2.f * fh);
  f32x2 v[4], st[4];
  v[0] = cexp(zr, -zi, 64.f * hh);                 st[0] = cexp(zr, -zi, 8.f);
  v[1] = hh ? cmul(cc, e128) : cc;                 st[1] = cexp(zr, zi, 1.f);
  v[2] = cmul(cc, cexp(zr, zi, 256.f * fh));       st[2] = hh ? e128 : e24;
  v[3] = cmul(cc, cexp(zr, zi, 1024.f * fh));      st[3] = e128;
  const f32x2 v24 = cmul(v[2], hh ? e24 : e128);
  unsigned vh[4][8], vl[4][8];
#pragma unroll
  for (int i = 0; i < 8; ++i)
#pragma unroll
    for (int t = 0; t < 4; ++t) {
      vh[t][i] = pk2(v[t][0], v[t][1]);
      vl[t][i] = pk2(v[t][0] - lo_f(vh[t][i]), v[t][1] - hi_f(vh[t][i]));
      if (t == 2 && i == 3) v[t] = v24;
      else if (i < 7) v[t] = cmul(v[t], st[t]);
    }
  char* wr = wb + (8 * hh) * KG_PITCH + (lane & 31) * 4;
  const char* rd = wb + r * KG_PITCH + g * 16;
  bf16x8 fh_[4][2], fl_[4][2];
#pragma unroll
  for (int t = 0; t < 4; ++t) {
    __builtin_amdgcn_wave_barrier();
#pragma unroll
    for (int i = 0; i < 8; ++i) {
      *(unsigned*)(wr + i * KG_PITCH) = vh[t][i];
      *(unsigned*)(wr + i * KG_PITCH + KG_ARR) = vl[t][i];
    }
    __builtin_amdgcn_wave_barrier();
#pragma unroll
    for (int ss = 0; ss < 2; ++ss) {
      fh_[t][ss] = *(const bf16x8*)(rd + ss * 64);
      fl_[t][ss] = *(const bf16x8*)(rd + ss * 64 + KG_ARR);
    }
  }
  f32x4 acc[3];
#pragma unroll
  for (int m = 0; m < 3; ++m) acc[m] = f32x4{0.f, 0.f, 0.f, 0.f};
#pragma unroll
  for (int ss = 0; ss < 2; ++ss)
#pragma unroll
    for (int m = 0; m < 3; ++m) {
      acc[m] = mfma16(fl_[1 + m][ss], fh_[0][ss], acc[m]);
      acc[m] = mfma16(fh_[1 + m][ss], fl_[0][ss], acc[m]);
      acc[m] = mfma16(fh_[1 + m][ss], fh_[0][ss], acc[m]);
    }
  {
    float* tab = (float*)tb + wq * 656;
    *(f32x4*)(tab + 128 * (g >> 1) + 4 * (g & 1) + 8 * r) = acc[0];
    if (g < 2) {
      *(f32x4*)(tab + 256 + 64 * g + 4 * r) = acc[1];
#pragma unroll
      for (int e = 0; e < 4; ++e) tab[512 + 64 * g + 16 * e + r] = acc[2][e];
    } else {
#pragma unroll
      for (int e = 0; e < 4; ++e) tab[384 + (g & 1) + 32 * e + 2 * r] = acc[1][e];
      if (lane == 32) tab[640] = acc[2][0];
    }
  }
  __syncthreads();
  STAMP(2);
  {
    const float* tab = (const float*)tb;
    unsigned* kd = kc + (size_t)(d0 + ch) * KC_PITCH;
#pragma unroll
    for (int e = 0; e < 3; ++e) {
      const int id = tc + 256 * e;
      if (id < 641) {
        const float v = (tab[id] + tab[656 + id]) + (tab[2 * 656 + id] + tab[3 * 656 + id]);
        const float vh = bfr(v);
        kd[id] = pk2(vh, v - vh);
      }
    }
  }
  STAMP(3);
}

NOPK __global__ __launch_bounds__(512, 4) void k1(const __bf16* __restrict__ u, const __bf16* __restrict__ lar,
                                             const __bf16* __restrict__ aim, const __bf16* __restrict__ cre,
                                             const __bf16* __restrict__ cim, const __bf16* __restrict__ ldt,
                                             const __bf16* __restrict__ wg, const __bf16* __restrict__ wu,
                                             const __bf16* __restrict__ gw, __bf16* __restrict__ h,
                                             __bf16* __restrict__ uT, unsigned* __restrict__ kc,
                                             float* __restrict__ gout, unsigned* __restrict__ aG) {
  extern __shared__ __attribute__((aligned(16))) char smem[];
  const int b = blockIdx.x;
  const int xcd = b & 7, i = (b & 255) >> 3;
  SPAN_PT(0, 0);
  if (b < 256) {
    const int rb = 4 * (xcd >> 1) + (i & 3), cb = 8 * (xcd & 1) + (i >> 2);
    if (cb < 15) ffn1_block(smem, rb, cb, u, wg, wu, h);
    else gate_block(smem, rb, u, gw, gout, uT);
  } else {
    kgen_block(smem, 64 * xcd + 2 * i, lar, aim, cre, cim, ldt, kc, aG);
  }
  SPAN_PT(0, 1);
}

#undef STAMP_BASE
#define STAMP_BASE 512
constexpr unsigned A_TAG = 0x5a17c3e9u;
constexpr int UCP = 4160;
constexpr int KARR = (240 + L) * 2;
constexpr int OFF_K = 4 * UCP;
constexpr int OFF_TAB = OFF_K + 2 * KARR;
constexpr int OFF_RED = UCP;
constexpr int CH_BYTES = OFF_RED + 32768;
static_assert(OFF_TAB + 3072 <= CH_BYTES && 2 * CH_BYTES <= LDS_BYTES, "lds");

struct PosLut {
  unsigned short v[L];
  constexpr PosLut() : v() {
    for (int j = 0; j < L; ++j) {
      const int sh = (j >= 256) + (j >= 512) + (j >= 1024);
      int q = j >> sh;
      if (sh) {
        const int rem = j & ((1 << sh) - 1), half = 1 << (sh - 1);
        if (rem > half || (rem == half && (q & 1))) ++q;
      }
      v[j] = (unsigned short)(4 * (128 * sh + q));
    }
  }
};
__device__ const PosLut g_poslut{};

__device__ __forceinline__ void conv_block(char* smem, int d0, const __bf16* __restrict__ uT,
                                           const unsigned* __restrict__ kc, const __bf16* __restrict__ dsk,
                                           unsigned* __restrict__ aG) {
  const int tid = threadIdx.x, lane = tid & 63, w = __builtin_amdgcn_readfirstlane(tid >> 6);
  const int ch = w >> 2, wq = w & 3, tc = tid & 255;
  const int r = lane & 15, g = lane >> 4, q = (lane >> 2) & 3, p = lane & 3;
  char* cb = smem + ch * CH_BYTES;
  const int d = d0 + ch;
  STAMP(0);
  __builtin_amdgcn_s_setprio(PRIO_CV);
  {
    const unsigned lb = lds_addr_of(cb);
    glds16(uT + (size_t)d * L + wq * 512, lane * 16, lb + 32 + wq * 1024);
    if (wq < 3) glds16(kc + (size_t)d * KC_PITCH + wq * 256, lane * 16, lb + OFF_TAB + wq * 1024);
    const u32x4 z = {0u, 0u, 0u, 0u};
    if (tc < 60) *(u32x4*)(cb + OFF_K + (tc >= 30 ? KARR : 0) + (tc % 30) * 16) = z;
    else if (tc < 68) *(u32x4*)(cb + ((tc - 60) >> 1) * UCP + ((tc - 60) & 1) * 16) = z;
  }
  const float dsv = (float)dsk[d];
  const u32x4 lut = *(const u32x4*)(g_poslut.v + 8 * tc);
  WAIT_VM(0);
  __syncthreads();
  STAMP(1);
  {
    const u32x4 pa = *(const u32x4*)(cb + 16 + 16 * tc), pb = *(const u32x4*)(cb + 32 + 16 * tc);
    const u32x4 c1 = {__builtin_amdgcn_alignbit(pb[0], pa[3], 16), __builtin_amdgcn_alignbit(pb[1], pb[0], 16),
                      __builtin_amdgcn_alignbit(pb[2], pb[1], 16), __builtin_amdgcn_alignbit(pb[3], pb[2], 16)};
    const u32x4 c2 = {pa[3], pb[0], pb[1], pb[2]};
    const u32x4 c3 = {__builtin_amdgcn_alignbit(pa[3], pa[2], 16), c1[0], c1[1], c1[2]};
    *(u32x4*)(cb + 1 * UCP + 32 + 16 * tc) = c1;
    *(u32x4*)(cb + 2 * UCP + 32 + 16 * tc) = c2;
    *(u32x4*)(cb + 3 * UCP + 32 + 16 * tc) = c3;
    u32x4 kh, kl;
#pragma unroll
    for (int e = 0; e < 4; ++e) {
      const unsigned t0 = *(const unsigned*)(cb + OFF_TAB + (lut[e] & 0xffffu));
      const unsigned t1 = *(const unsigned*)(cb + OFF_TAB + (lut[e] >> 16));
      kh[e] = __builtin_amdgcn_perm(t1, t0, 0x05040100u);
      kl[e] = __builtin_amdgcn_perm(t1, t0, 0x07060302u);
    }
    *(u32x4*)(cb + OFF_K + 480 + 16 * tc) = kh;
    *(u32x4*)(cb + OFF_K + KARR + 480 + 16 * tc) = kl;
  }
  __syncthreads();
  STAMP(2);
  f32x4 acc[8];
#pragma unroll
  for (int t = 0; t < 8; ++t) acc[t] = f32x4{0.f, 0.f, 0.f, 0.f};
#pragma unroll
  for (int rr = 0; rr < 2; ++rr) {
    const int rho = 2 * wq + rr;
    const int abase = q * UCP + 2 * (16 + 16 + 32 * rho - 8 * g + 4 * p);
    const int bbase = OFF_K + 2 * (240 - 16 - 32 * rho + 16 * r + 8 * g);
    bf16x8 af[8];
#pragma unroll
    for (int s = 0; s < 8; ++s) af[s] = tr_frag(cb, abase + 512 * s, abase + 512 * s - 8);
#pragma unroll
    for (int t = 0; t < 8; ++t) {
      const bf16x8 bh = *(const bf16x8*)(cb + bbase + 512 * t);
#pragma unroll
      for (int s = 0; s + t < 8; ++s) acc[s + t] = mfma16(af[s], bh, acc[s + t]);
      const bf16x8 bl = *(const bf16x8*)(cb + bbase + KARR + 512 * t);
#pragma unroll
      for (int s = 0; s + t < 8; ++s) acc[s + t] = mfma16(af[s], bl, acc[s + t]);
    }
  }
  __syncthreads();
  STAMP(3);
#pragma unroll
  for (int t = 0; t < 8; ++t)
    if ((t >> 1) != wq) *(f32x4*)(cb + OFF_RED + (wq * 8 + t) * 1024 + lane * 16) = acc[t];
  __syncthreads();
#pragma unroll
  for (int tt = 0; tt < 2; ++tt) {
    const int t = 2 * wq + tt;
    f32x4 own = f32x4{0.f, 0.f, 0.f, 0.f};
#pragma unroll
    for (int t2 = 0; t2 < 8; ++t2)
      if (t2 == t) own = acc[t2];
    f32x4 s = f32x4{0.f, 0.f, 0.f, 0.f};
#pragma unroll
    for (int sw = 0; sw < 4; ++sw) {
      const f32x4 o = *(const f32x4*)(cb + OFF_RED + (sw * 8 + t) * 1024 + lane * 16);
      s += (sw == wq) ? own : o;
    }
    const int l = 256 * t + 16 * r + 4 * g;
    const u32x2 uu = *(const u32x2*)(cb + 32 + 2 * l);
    const float a0 = bfr(s[0]) + bfr(lo_f(uu[0]) * dsv), a1 = bfr(s[1]) + bfr(hi_f(uu[0]) * dsv);
    const float a2 = bfr(s[2]) + bfr(lo_f(uu[1]) * dsv), a3 = bfr(s[3]) + bfr(hi_f(uu[1]) * dsv);
    gu64* dst = (gu64*)(aG + (size_t)d * L + l);
    __hip_atomic_store(dst, (unsigned long long)pk2(a0, a1) | ((unsigned long long)A_TAG << 32), __ATOMIC_RELAXED,
                       __HIP_MEMORY_SCOPE_AGENT);
    __hip_atomic_store(dst + 1, (unsigned long long)pk2(a2, a3) | ((unsigned long long)A_TAG << 32), __ATOMIC_RELAXED,
                       __HIP_MEMORY_SCOPE_AGENT);
  }
  STAMP(4);
}

constexpr int F2_NS = 5;
constexpr int F2_STAGE = 16384;
constexpr int F2_NST = 22;

struct F2Ctx {
  const __bf16 *h, *wd;
  unsigned avoff, bvoff, bvoff_last, lbase;
  int w, kh;
  int aoff[2], boff[2];
};
__device__ __forceinline__ void f2_stage(const F2Ctx& c, int st) {
  const unsigned base = c.lbase + (st % F2_NS) * F2_STAGE;
  glds16(c.h + st * 64, c.avoff, base + c.w * 1024);
  glds16(c.wd + (size_t)st * 64 * D, st == F2_NST - 1 ? c.bvoff_last : c.bvoff, base + 8192 + c.w * 1024);
}
template <int ST>
__device__ __forceinline__ void f2_loop(const F2Ctx& c, const char* smem, bf16x8 (&ao)[2], bf16x8 (&bo)[2],
                                        f32x4 (&acc)[2][2]) {
  if constexpr (ST < F2_NST) {
    constexpr int rem = (F2_NST - 1 - ST) < (F2_NS - 2) ? (F2_NST - 1 - ST) : (F2_NS - 2);
    wait_vm<2 * rem>();
    WAIT_LGKM0();
    __builtin_amdgcn_s_barrier();
    asm volatile("" ::: "memory");
    STAMP(1 + ST);
    if constexpr (ST + F2_NS - 1 < F2_NST) f2_stage(c, ST + F2_NS - 1);
    const char* base = smem + (ST % F2_NS) * F2_STAGE;
    bf16x8 an[2], bn[2];
#pragma unroll
    for (int mt = 0; mt < 2; ++mt) an[mt] = *(const bf16x8*)(base + c.aoff[mt]);
#pragma unroll
    for (int nt = 0; nt < 2; ++nt) bn[nt] = tr_frag(base, c.boff[nt], c.boff[nt] + 512);
    __builtin_amdgcn_sched_barrier(0);
    if constexpr (ST > 0) {
#pragma unroll
      for (int mt = 0; mt < 2; ++mt)
#pragma unroll
        for (int nt = 0; nt < 2; ++nt) acc[mt][nt] = mfma16(ao[mt], bo[nt], acc[mt][nt]);
    }
    __builtin_amdgcn_sched_barrier(0);
    f2_loop<ST + 1>(c, smem, an, bn, acc);
  } else {
    if (c.kh == 0) {
#pragma unroll
      for (int mt = 0; mt < 2; ++mt)
#pragma unroll
        for (int nt = 0; nt < 2; ++nt) acc[mt][nt] = mfma16(ao[mt], bo[nt], acc[mt][nt]);
    }
  }
}

__device__ __forceinline__ void ffn2_block(char* smem, int row0, int cbk, const __bf16* __restrict__ h,
                                           const __bf16* __restrict__ wd, const unsigned* __restrict__ aG,
                                           const float* __restrict__ gin, __bf16* __restrict__ out) {
  const int col0 = 64 * cbk;
  const int tid = threadIdx.x, lane = tid & 63, w = __builtin_amdgcn_readfirstlane(tid >> 6);
  const int r = lane & 15, g = lane >> 4, q = (lane >> 2) & 3, p = lane & 3;
  const int kh = w >> 2, wm = (w >> 1) & 1, wn = w & 1;
  STAMP(0);
  __builtin_amdgcn_s_setprio(PRIO_F2);
  F2Ctx c;
  c.h = h; c.wd = wd; c.w = w; c.kh = kh; c.lbase = lds_addr_of(smem);
  {
    const int row = 8 * w + (lane >> 3), ch = (lane & 7) ^ ((row >> 1) & 7);
    c.avoff = (unsigned)(((row0 + row) * HP + ch * 8) * 2);
    const int bk = 8 * w + (lane >> 3);
    const int pos = lane & 7, fp = ((bk >> 1) & 1) | (((bk >> 3) & 1) << 1);
    const int cc = col0 + 16 * ((pos >> 1) ^ fp) + 8 * (pos & 1);
    c.bvoff = (unsigned)((bk * D + cc) * 2);
    c.bvoff_last = (unsigned)(((bk > 31 ? 31 : bk) * D + cc) * 2);
  }
#pragma unroll
  for (int mt = 0; mt < 2; ++mt) {
    const int row = 32 * wm + 16 * mt + r, ch = 4 * kh + g;
    c.aoff[mt] = row * 128 + ((ch ^ ((row >> 1) & 7)) << 4);
  }
  const int fpl = (q >> 1) | ((g & 1) << 1);
#pragma unroll
  for (int nt = 0; nt < 2; ++nt) c.boff[nt] = 8192 + (32 * kh + 8 * g + q) * 128 + (((2 * wn + nt) ^ fpl) << 5) + 8 * p;

  f32x4 acc[2][2];
#pragma unroll
  for (int mt = 0; mt < 2; ++mt)
#pragma unroll
    for (int nt = 0; nt < 2; ++nt) acc[mt][nt] = f32x4{0.f, 0.f, 0.f, 0.f};

#pragma unroll
  for (int st = 0; st < F2_NS - 1; ++st) f2_stage(c, st);
  const int lr = 32 * wm + 16 * kh + 4 * g;
  const f32x4 gv = *(const f32x4*)(gin + row0 + lr);
  bf16x8 a0[2], b0[2];
  f2_loop<0>(c, smem, a0, b0, acc);
  STAMP(23);
  WAIT_LGKM0();
  __builtin_amdgcn_s_barrier();
  asm volatile("" ::: "memory");
  {
    char* red = smem + (w & 3) * 4096 + kh * 2048;
    const f32x4 o0 = kh ? acc[0][0] : acc[1][0], o1 = kh ? acc[0][1] : acc[1][1];
    *(f32x4*)(red + lane * 16) = o0;
    *(f32x4*)(red + 1024 + lane * 16) = o1;
  }
  __syncthreads();
  unsigned long long av[2];
  {
    const gu64* src = (const gu64*)(aG + (size_t)(col0 + 32 * wn + r) * L + row0 + lr);
    for (int spin = 0; spin < (1 << 22); ++spin) {
      unsigned long long x[4];
#pragma unroll
      for (int k = 0; k < 4; ++k)
        x[k] = __hip_atomic_load(src + (k >> 1) * (16 * L / 2) + (k & 1), __ATOMIC_RELAXED, __HIP_MEMORY_SCOPE_AGENT);
      av[0] = (x[0] & 0xffffffffull) | (x[1] << 32);
      av[1] = (x[2] & 0xffffffffull) | (x[3] << 32);
      const bool ok = (unsigned)(x[0] >> 32) == A_TAG && (unsigned)(x[1] >> 32) == A_TAG &&
                      (unsigned)(x[2] >> 32) == A_TAG && (unsigned)(x[3] >> 32) == A_TAG;
      if (__all(ok)) break;
      __builtin_amdgcn_s_sleep(4);
    }
  }
  constexpr int OT = 16384, OTP = 144;
  {
    const char* red = smem + (w & 3) * 4096 + (1 - kh) * 2048;
#pragma unroll
    for (int nt = 0; nt < 2; ++nt) {
      const f32x4 o = *(const f32x4*)(red + nt * 1024 + lane * 16);
      const f32x4 own = kh ? acc[1][nt] : acc[0][nt];
      const f32x4 s = kh ? (o + own) : (own + o);
      const unsigned alo = (unsigned)av[nt], ahi = (unsigned)(av[nt] >> 32);
      const float a[4] = {lo_f(alo), hi_f(alo), lo_f(ahi), hi_f(ahi)};
#pragma unroll
      for (int e = 0; e < 4; ++e) {
        const float o2 = bfr(gv[e] * a[e]) + bfr(bfr(1.f - gv[e]) * bfr(s[e]));
        *(__bf16*)(smem + OT + (lr + e) * OTP + (32 * wn + 16 * nt + r) * 2) = (__bf16)o2;
      }
    }
  }
  __syncthreads();
  {
    const int row = tid >> 3, c8 = tid & 7;
    const u32x4 v = *(const u32x4*)(smem + OT + row * OTP + c8 * 16);
    *(u32x4*)(out + (size_t)(row0 + row) * D + col0 + c8 * 8) = v;
  }
  STAMP(24);
}

NOPK __global__ __launch_bounds__(512, 4) void k2(const __bf16* __restrict__ h, const __bf16* __restrict__ wd,
                                             const __bf16* __restrict__ uT, const unsigned* __restrict__ kc,
                                             const __bf16* __restrict__ dsk, unsigned* __restrict__ aG,
                                             const float* __restrict__ gin, __bf16* __restrict__ out) {
  extern __shared__ __attribute__((aligned(16))) char smem[];
  const int b = blockIdx.x, xcd = b & 7, i = (b & 255) >> 3;
  SPAN_PT(1, 0);
  if (b < 256) {
    const int pair = 32 * xcd + i;
    conv_block(smem, 2 * pair, uT, kc, dsk, aG);
  } else {
    ffn2_block(smem, 64 * (4 * xcd + (i & 3)), i >> 2, h, wd, aG, gin, out);
  }
  SPAN_PT(1, 1);
}

extern "C" void kernel_launch(void* const* d_in, const int* in_sizes, int n_in, void* d_out, int out_size,
                              void* d_ws, size_t ws_size, hipStream_t stream) {
  const __bf16* u = (const __bf16*)d_in[0];
  const __bf16* lar = (const __bf16*)d_in[1];
  const __bf16* aim = (const __bf16*)d_in[2];
  const __bf16* cre = (const __bf16*)d_in[3];
  const __bf16* cim = (const __bf16*)d_in[4];
  const __bf16* ldt = (const __bf16*)d_in[5];
  const __bf16* dsk = (const __bf16*)d_in[6];
  const __bf16* wg = (const __bf16*)d_in[8];
  const __bf16* wu = (const __bf16*)d_in[9];
  const __bf16* wd = (const __bf16*)d_in[10];
  const __bf16* gw = (const __bf16*)d_in[11];
  char* ws = (char*)d_ws;
  __bf16* h = (__bf16*)ws;
  ws += (size_t)L * HP * 2;
  unsigned* aG = (unsigned*)ws;
  ws += (size_t)D * L * 4;
  __bf16* uT = (__bf16*)ws;
  ws += (size_t)D * L * 2;
  unsigned* kc = (unsigned*)ws;
  ws += (size_t)D * KC_PITCH * 4;
  float* gout = (float*)ws;
  (void)hipFuncSetAttribute((const void*)k1, hipFuncAttributeMaxDynamicSharedMemorySize, LDS_BYTES);
  (void)hipFuncSetAttribute((const void*)k2, hipFuncAttributeMaxDynamicSharedMemorySize, LDS_BYTES);
  hipLaunchKernelGGL(k1, dim3(512), dim3(512), LDS_BYTES, stream, u, lar, aim, cre, cim, ldt, wg, wu, gw, h, uT, kc,
                     gout, aG);
  hipLaunchKernelGGL(k2, dim3(512), dim3(512), LDS_BYTES, stream, (const __bf16*)h, wd, (const __bf16*)uT,
                     (const unsigned*)kc, dsk, aG, (const float*)gout, (__bf16*)d_out);
}
```

```cpp
#include <hip/hip_runtime.h>
#include <hip/hip_bf16.h>

typedef __attribute__((ext_vector_type(8))) __bf16 bf16x8;
typedef __attribute__((ext_vector_type(2))) __bf16 bf16x2;
typedef __attribute__((ext_vector_type(4))) short s16x4;
typedef __attribute__((ext_vector_type(8))) short s16x8;
typedef __attribute__((ext_vector_type(4))) float f32x4;
typedef __attribute__((ext_vector_type(4))) unsigned u32x4;
typedef __attribute__((ext_vector_type(2))) unsigned u32x2;
typedef __attribute__((address_space(3))) s16x4* lds_s16x4_ptr;
typedef __attribute__((address_space(1))) unsigned gu32;
typedef __attribute__((address_space(1))) unsigned long long gu64;

#define K_LO 0

#define NOPK __attribute__((target("no-packed-fp32-ops")))

#define WD_PF 1
#define PRIO_F1 0
#define PRIO_KG 0
#define PRIO_CV 1
#define PRIO_F2 1

constexpr int L = 2048, D = 512, NS = 128, F = 1376, HP = 1408;
constexpr int LDS_BYTES = 81920;

__device__ __forceinline__ float bf2f(unsigned short v) { return __uint_as_float((unsigned)v << 16); }
__device__ __forceinline__ float bfr(float x) { return (float)(__bf16)x; }
__device__ __forceinline__ unsigned pk2(float a, float b) {
  bf16x2 v = {(__bf16)a, (__bf16)b};
  return __builtin_bit_cast(unsigned, v);
}
__device__ __forceinline__ float lo_f(unsigned w) { return __uint_as_float(w << 16); }
__device__ __forceinline__ float hi_f(unsigned w) { return __uint_as_float(w & 0xffff0000u); }

__device__ __forceinline__ f32x4 mfma16(bf16x8 a, bf16x8 b, f32x4 c) {
  return __builtin_amdgcn_mfma_f32_16x16x32_bf16(a, b, c, 0, 0, 0);
}
__device__ __forceinline__ bf16x8 tr_frag(const char* lds, int off0, int off1) {
  s16x4 a = __builtin_amdgcn_ds_read_tr16_b64_v4i16((lds_s16x4_ptr)(lds + off0));
  s16x4 b = __builtin_amdgcn_ds_read_tr16_b64_v4i16((lds_s16x4_ptr)(lds + off1));
  s16x8 v = {a[0], a[1], a[2], a[3], b[0], b[1], b[2], b[3]};
  return __builtin_bit_cast(bf16x8, v);
}
__device__ __forceinline__ void glds16(const void* sbase, unsigned voff, unsigned lds_addr) {
  unsigned keep;
  asm volatile("s_mov_b32 %0, m0\n\ts_mov_b32 m0, %3\n\ts_nop 0\n\tglobal_load_lds_dwordx4 %1, %2\n\ts_mov_b32 m0, %0"
               : "=&s"(keep) : "v"(voff), "s"(sbase), "s"(lds_addr) : "memory");
}
__device__ __forceinline__ void glds16v(const void* vptr, unsigned lds_addr) {
  unsigned keep;
  asm volatile("s_mov_b32 %0, m0\n\ts_mov_b32 m0, %2\n\ts_nop 0\n\tglobal_load_lds_dwordx4 %1, off\n\ts_mov_b32 m0, %0"
               : "=&s"(keep) : "v"(vptr), "s"(lds_addr) : "memory");
}
__device__ __forceinline__ unsigned lds_addr_of(const char* p) {
  return (unsigned)(uintptr_t)(const __attribute__((address_space(3))) char*)p;
}
#define STAMP(i)
#define STAMP_WAVE 0
#define SSTAMP(var)
#define SPAN_PT(k, i)
#define WAIT_VM(n) asm volatile("s_waitcnt vmcnt(" #n ")" ::: "memory")
#define WAIT_LGKM0() asm volatile("s_waitcnt lgkmcnt(0)" ::: "memory")
template <int N>
__device__ __forceinline__ void wait_vm() {
  asm volatile("s_waitcnt vmcnt(%0)" ::"i"(N) : "memory");
}

#define STAMP_BASE 0
constexpr int F1_B = 32768;

struct F1Ctx {
  const __bf16 *x, *wg, *wu, *wmix;
  unsigned voffA, voffB0, voffB2, lbase;
  int w;
  int aoff[2], boff[6];
};
template <bool G2>
__device__ __forceinline__ void f1_stage(const F1Ctx& c, int hs) {
  const unsigned bb = c.lbase + F1_B + (hs & 3) * 12288 + c.w * 1024;
  glds16(c.x + hs * 32, c.voffA, c.lbase + (hs & 3) * 8192 + c.w * 1024);
  if constexpr (!G2) {
    glds16(c.wg + (size_t)hs * 32 * F, c.voffB0, bb);
    glds16(c.wu + (size_t)hs * 32 * F, c.voffB2, bb + 8192);
  } else {
    glds16v(c.wmix + (size_t)hs * 32 * F, bb);
  }
}
template <int HS, bool G2>
__device__ __forceinline__ void f1_loop(const F1Ctx& c, const char* smem, bf16x8 (&af)[2], bf16x8 (&bq)[6],
                                        f32x4 (&acc)[2][6]) {
  if constexpr (HS < 16) {
    constexpr int rem = (15 - HS) < 2 ? (15 - HS) : 2;
    wait_vm<rem*(G2 ? 2 : 3)>();
    WAIT_LGKM0();
    __builtin_amdgcn_s_barrier();
    asm volatile("" ::: "memory");
    const char* ab = smem + (HS & 3) * 8192;
    const char* bbase = smem + (HS & 3) * 12288;
    if constexpr (HS > 0) {
#pragma unroll
      for (int j = 0; j < 6; ++j) {
        acc[0][j] = mfma16(bq[j], af[0], acc[0][j]);
        if constexpr (HS + 3 < 16)
          if (j == 1) {
            __builtin_amdgcn_sched_barrier(0);
            f1_stage<G2>(c, HS + 3);
          }
      }
    } else {
      f1_stage<G2>(c, HS + 3);
    }
    __builtin_amdgcn_sched_barrier(0);
#pragma unroll
    for (int j = 0; j < 6; ++j) {
      if constexpr (HS > 0) acc[1][j] = mfma16(bq[j], af[1], acc[1][j]);
      __builtin_amdgcn_sched_barrier(0);
      if (j == 0) af[0] = *(const bf16x8*)(ab + c.aoff[0]);
      else bq[j - 1] = tr_frag(bbase, c.boff[j - 1], c.boff[j - 1] + 512);
      __builtin_amdgcn_sched_barrier(0);
    }
    bq[5] = tr_frag(bbase, c.boff[5], c.boff[5] + 512);
    af[1] = *(const bf16x8*)(ab + c.aoff[1]);
    f1_loop<HS + 1, G2>(c, smem, af, bq, acc);
  } else {
#pragma unroll
    for (int j = 0; j < 6; ++j) {
      acc[0][j] = mfma16(bq[j], af[0], acc[0][j]);
      acc[1][j] = mfma16(bq[j], af[1], acc[1][j]);
    }
  }
}

__device__ __forceinline__ void ffn1_block(char* smem, int rb, int cb, const __bf16* __restrict__ x,
                                           const __bf16* __restrict__ wg, const __bf16* __restrict__ wu,
                                           __bf16* __restrict__ h) {
  const int tid = threadIdx.x, lane = tid & 63, w = __builtin_amdgcn_readfirstlane(tid >> 6);
  const int row0 = rb * 128, f0 = cb * 96;
  const int wm = w >> 1, wn = w & 1;
  const int r = lane & 15, g = lane >> 4, q = (lane >> 2) & 3, p = lane & 3;
  __builtin_amdgcn_s_setprio(PRIO_F1);
  F1Ctx c;
  c.x = x; c.wg = wg; c.wu = wu; c.w = w; c.lbase = lds_addr_of(smem);
  {
    const int row = 16 * w + (lane >> 2), ch = (lane & 3) ^ ((-(row >> 2)) & 3);
    c.voffA = (unsigned)(((row0 + row) * D + ch * 8) * 2);
    const int kk = 8 * (w & 3) + (lane >> 3), pos = lane & 7;
    const int fp = ((kk >> 1) & 1) | (((kk >> 3) & 1) << 1);
    const int i = (pos >> 1) ^ fp;
    int c0 = f0 + 16 * i + 8 * (pos & 1);
    int c1 = f0 + 16 * ((4 + i) % 6) + 8 * (pos & 1);
    int c2 = f0 + 16 * (2 + i) + 8 * (pos & 1);
    c0 = c0 > F - 8 ? F - 8 : c0;
    c1 = c1 > F - 8 ? F - 8 : c1;
    c2 = c2 > F - 8 ? F - 8 : c2;
    c.voffB0 = (unsigned)((kk * F + c0) * 2);
    c.voffB2 = (unsigned)((kk * F + c2) * 2);
    c.wmix = (i < 2 ? wg : wu) + kk * F + c1;
  }
#pragma unroll
  for (int mt = 0; mt < 2; ++mt) {
    const int row = 32 * wm + 16 * mt + r;
    c.aoff[mt] = row * 64 + ((g ^ ((-(row >> 2)) & 3)) << 4);
  }
  {
    const int fp = (q >> 1) | ((g & 1) << 1);
#pragma unroll
    for (int j = 0; j < 6; ++j) {
      const int T = (j < 3 ? 0 : 6) + 3 * wn + (j % 3);
      c.boff[j] = F1_B + (T >> 2) * 4096 + (8 * g + q) * 128 + (((T & 3) ^ fp) << 5) + 8 * p;
      asm volatile("" : "+v"(c.boff[j]));
    }
  }
  f32x4 acc[2][6];
#pragma unroll
  for (int mt = 0; mt < 2; ++mt)
#pragma unroll
    for (int j = 0; j < 6; ++j) acc[mt][j] = f32x4{0.f, 0.f, 0.f, 0.f};
  bf16x8 af[2], bq[6];

  if (w < 4) {
    f1_stage<false>(c, 0);
    f1_stage<false>(c, 1);
    f1_stage<false>(c, 2);
    f1_loop<0, false>(c, smem, af, bq, acc);
  } else {
    f1_stage<true>(c, 0);
    f1_stage<true>(c, 1);
    f1_stage<true>(c, 2);
    f1_loop<0, true>(c, smem, af, bq, acc);
  }

#pragma unroll
  for (int mt = 0; mt < 2; ++mt)
#pragma unroll
    for (int j = 0; j < 3; ++j) {
      const int f = f0 + 16 * (3 * wn + j);
      if (f < F) {
        float hv[4];
#pragma unroll
        for (int e = 0; e < 4; ++e) {
          const float gv = acc[mt][j][e];
          const float t = gv * __builtin_fmaf(gv * gv, -0.10294324f, -2.3022082f);
          hv[e] = (gv * acc[mt][3 + j][e]) * __builtin_amdgcn_rcpf(1.f + __builtin_amdgcn_exp2f(t));
        }
        u32x2 o = {pk2(hv[0], hv[1]), pk2(hv[2], hv[3])};
        *(u32x2*)(h + (size_t)(row0 + 32 * wm + 16 * mt + r) * HP + f + 4 * g) = o;
      }
    }
}

__device__ __forceinline__ void gate_block(char* smem, int rb, const __bf16* __restrict__ x,
                                           const __bf16* __restrict__ gw, float* __restrict__ gout,
                                           __bf16* __restrict__ uT) {
  const int tid = threadIdx.x, lane = tid & 63, w = __builtin_amdgcn_readfirstlane(tid >> 6);
  const int g = lane >> 4, q = (lane >> 2) & 3, p = lane & 3;
  const unsigned lbase = lds_addr_of(smem);
  const int grow = tid >> 3, gpart = tid & 7, gf = (grow & 3) | (((grow >> 3) & 1) << 2);
  u32x4 gwv[8];
#pragma unroll
  for (int i = 0; i < 8; ++i) gwv[i] = *(const u32x4*)(gw + ((gpart ^ gf) + 8 * (i >> 1)) * 16 + 8 * (i & 1));
#pragma unroll 1
  for (int pass = 0; pass < 2; ++pass) {
    const int l0 = rb * 128 + pass * 64;
#pragma unroll
    for (int i = 0; i < 8; ++i) {
      const int row = 8 * w + i, fr = (row & 3) | (((row >> 3) & 1) << 2);
      glds16(x + (size_t)(l0 + row) * D, (unsigned)((((lane >> 1) ^ fr) * 2 + (lane & 1)) * 16), lbase + row * 1024);
    }
    WAIT_VM(0);
    __syncthreads();
#pragma unroll
    for (int i = 0; i < 8; ++i) {
      const int unit = w * 8 + i, c16 = unit >> 1, half = unit & 1;
      const int ra = 32 * half + 8 * g + q, rb2 = ra + 4;
      const int fa = (ra & 3) | (((ra >> 3) & 1) << 2);
      const bf16x8 v = tr_frag(smem, ra * 1024 + ((c16 ^ fa) << 5) + 8 * p, rb2 * 1024 + ((c16 ^ fa) << 5) + 8 * p);
      *(bf16x8*)(uT + (size_t)(16 * c16 + (lane & 15)) * L + l0 + 32 * half + 8 * g) = v;
    }
    float s = 0.f;
#pragma unroll
    for (int i = 0; i < 8; ++i) {
      const u32x4 xv = *(const u32x4*)(smem + grow * 1024 + (gpart + 8 * (i >> 1)) * 32 + 16 * (i & 1));
#pragma unroll
      for (int e = 0; e < 4; ++e) {
        const unsigned xe = xv[e], we = gwv[i][e];
        s = __builtin_amdgcn_fdot2_f32_bf16(__builtin_bit_cast(bf16x2, xe), __builtin_bit_cast(bf16x2, we), s, false);
      }
    }
    s += __shfl_xor(s, 1);
    s += __shfl_xor(s, 2);
    s += __shfl_xor(s, 4);
    if (gpart == 0) gout[l0 + grow] = bfr(1.f / (1.f + __expf(-bfr(s))));
    __syncthreads();
  }
}

constexpr int KC_PITCH = 768;
constexpr int KG_PITCH = 160;
constexpr int KG_ARR = 16 * KG_PITCH;
constexpr int KG_TAB = 8 * 2 * KG_ARR;
typedef __attribute__((ext_vector_type(2))) float f32x2;

__device__ __forceinline__ f32x2 cexp(float zr, float zi, float p) {
  const float e = __builtin_amdgcn_exp2f(zr * p);
  const float ph = __builtin_amdgcn_fractf(zi * p);
  return f32x2{e * __builtin_amdgcn_cosf(ph), e * __builtin_amdgcn_sinf(ph)};
}
__device__ __forceinline__ f32x2 cmul(f32x2 a, f32x2 b) {
  return f32x2{a[0] * b[0] - a[1] * b[1], a[0] * b[1] + a[1] * b[0]};
}
__device__ __forceinline__ void kgen_block(char* smem, int d0, const __bf16* __restrict__ lar,
                                           const __bf16* __restrict__ aim, const __bf16* __restrict__ cre,
                                           const __bf16* __restrict__ cim, const __bf16* __restrict__ ldt,
                                           unsigned* __restrict__ kc, unsigned* __restrict__ aG,
                                           const __bf16* __restrict__ wd) {
  const int tid = threadIdx.x, lane = tid & 63, w = __builtin_amdgcn_readfirstlane(tid >> 6);
  unsigned pf_dummy = 0;
  if (tid < 43)
    asm volatile("global_load_dword %0, %1, off" : "=v"(pf_dummy) : "v"(wd + (size_t)((d0 >> 1) * 43 + tid) * 64) : "memory");
  {
    u32x4* z = (u32x4*)(aG + (size_t)d0 * L) + tid;
    z[0] = u32x4{0u, 0u, 0u, 0u};
    z[512] = u32x4{0u, 0u, 0u, 0u};
    if (tid == 0) aG[(size_t)D * L + (d0 >> 1)] = 0u;
  }
  const int ch = w >> 2, wq = w & 3, tc = tid & 255;
  const int r = lane & 15, g = lane >> 4, hh = lane >> 5;
  char* wb = smem + w * 2 * KG_ARR;
  char* tb = smem + KG_TAB + ch * (4 * 656 * 4);
  STAMP(0);
  __builtin_amdgcn_s_setprio(PRIO_KG);
  float zr, zi;
  f32x2 cc;
  {
    const int d = d0 + ch, n = 32 * wq + (lane & 31);
    const float dt = bfr(expf((float)ldt[d]));
    const float ar = -bfr(expf((float)lar[d * NS + n]));
    zr = (ar * dt) * 1.4426950408889634f;
    zi = ((float)aim[d * NS + n] * dt) * 0.15915494309189535f;
    cc = f32x2{2.f * ((float)cre[d * NS + n] * dt), 2.f * ((float)cim[d * NS + n] * dt)};
  }
  STAMP(1);
  const float fh = (float)(1 + hh);
  const f32x2 e128 = cexp(zr, zi, 128.f), e24 = cexp(zr, zi, 2.f * fh);
  f32x2 v[4], st[4];
  v[0] = cexp(zr, -zi, 64.f * hh);                 st[0] = cexp(zr, -zi, 8.f);
  v[1] = hh ? cmul(cc, e128) : cc;                 st[1] = cexp(zr, zi, 1.f);
  v[2] = cmul(cc, cexp(zr, zi, 256.f * fh));       st[2] = hh ? e128 : e24;
  v[3] = cmul(cc, cexp(zr, zi, 1024.f * fh));      st[3] = e128;
  const f32x2 v24 = cmul(v[2], hh ? e24 : e128);
  unsigned vh[4][8], vl[4][8];
#pragma unroll
  for (int i = 0; i < 8; ++i)
#pragma unroll
    for (int t = 0; t < 4; ++t) {
      vh[t][i] = pk2(v[t][0], v[t][1]);
      vl[t][i] = pk2(v[t][0] - lo_f(vh[t][i]), v[t][1] - hi_f(vh[t][i]));
      if (t == 2 && i == 3) v[t] = v24;
      else if (i < 7) v[t] = cmul(v[t], st[t]);
    }
  char* wr = wb + (8 * hh) * KG_PITCH + (lane & 31) * 4;
  const char* rd = wb + r * KG_PITCH + g * 16;
  bf16x8 fh_[4][2], fl_[4][2];
#pragma unroll
  for (int t = 0; t < 4; ++t) {
    __builtin_amdgcn_wave_barrier();
#pragma unroll
    for (int i = 0; i < 8; ++i) {
      *(unsigned*)(wr + i * KG_PITCH) = vh[t][i];
      *(unsigned*)(wr + i * KG_PITCH + KG_ARR) = vl[t][i];
    }
    __builtin_amdgcn_wave_barrier();
#pragma unroll
    for (int ss = 0; ss < 2; ++ss) {
      fh_[t][ss] = *(const bf16x8*)(rd + ss * 64);
      fl_[t][ss] = *(const bf16x8*)(rd + ss * 64 + KG_ARR);
    }
  }
  f32x4 acc[3];
#pragma unroll
  for (int m = 0; m < 3; ++m) acc[m] = f32x4{0.f, 0.f, 0.f, 0.f};
#pragma unroll
  for (int ss = 0; ss < 2; ++ss)
#pragma unroll
    for (int m = 0; m < 3; ++m) {
      acc[m] = mfma16(fl_[1 + m][ss], fh_[0][ss], acc[m]);
      acc[m] = mfma16(fh_[1 + m][ss], fl_[0][ss], acc[m]);
      acc[m] = mfma16(fh_[1 + m][ss], fh_[0][ss], acc[m]);
    }
  {
    float* tab = (float*)tb + wq * 656;
    *(f32x4*)(tab + 128 * (g >> 1) + 4 * (g & 1) + 8 * r) = acc[0];
    if (g < 2) {
      *(f32x4*)(tab + 256 + 64 * g + 4 * r) = acc[1];
#pragma unroll
      for (int e = 0; e < 4; ++e) tab[512 + 64 * g + 16 * e + r] = acc[2][e];
    } else {
#pragma unroll
      for (int e = 0; e < 4; ++e) tab[384 + (g & 1) + 32 * e + 2 * r] = acc[1][e];
      if (lane == 32) tab[640] = acc[2][0];
    }
  }
  __syncthreads();
  STAMP(2);
  {
    const float* tab = (const float*)tb;
    unsigned* kd = kc + (size_t)(d0 + ch) * KC_PITCH;
#pragma unroll
    for (int e = 0; e < 3; ++e) {
      const int id = tc + 256 * e;
      if (id < 641) {
        const float v = (tab[id] + tab[656 + id]) + (tab[2 * 656 + id] + tab[3 * 656 + id]);
        const float vh = bfr(v);
        kd[id] = pk2(vh, v - vh);
      }
    }
  }
  asm volatile("s_waitcnt vmcnt(0)" : "+v"(pf_dummy)::"memory");
  STAMP(3);
}

NOPK __global__ __launch_bounds__(512, 4) void k1(const __bf16* __restrict__ u, const __bf16* __restrict__ lar,
                                             const __bf16* __restrict__ aim, const __bf16* __restrict__ cre,
                                             const __bf16* __restrict__ cim, const __bf16* __restrict__ ldt,
                                             const __bf16* __restrict__ wg, const __bf16* __restrict__ wu,
                                             const __bf16* __restrict__ gw, __bf16* __restrict__ h,
                                             __bf16* __restrict__ uT, unsigned* __restrict__ kc,
                                             float* __restrict__ gout, unsigned* __restrict__ aG,
                                             const __bf16* __restrict__ wd) {
  extern __shared__ __attribute__((aligned(16))) char smem[];
  const int b = blockIdx.x;
  const int xcd = b & 7, i = (b & 255) >> 3;
  SPAN_PT(0, 0);
  if (b < 256) {
    const int rb = 4 * (xcd >> 1) + (i & 3), cb = 8 * (xcd & 1) + (i >> 2);
    if (cb < 15) ffn1_block(smem, rb, cb, u, wg, wu, h);
    else gate_block(smem, rb, u, gw, gout, uT);
  } else {
    kgen_block(smem, 64 * xcd + 2 * i, lar, aim, cre, cim, ldt, kc, aG, wd);
  }
  SPAN_PT(0, 1);
}

#undef STAMP_BASE
#define STAMP_BASE 512
constexpr unsigned A_TAG = 0x5a17c3e9u;
constexpr int UCP = 4160;
constexpr int KARR = (240 + L) * 2;
constexpr int OFF_K = 4 * UCP;
constexpr int OFF_TAB = OFF_K + 2 * KARR;
constexpr int OFF_RED = UCP;
constexpr int CH_BYTES = OFF_RED + 32768;
static_assert(OFF_TAB + 3072 <= CH_BYTES && 2 * CH_BYTES <= LDS_BYTES, "lds");

struct PosLut {
  unsigned short v[L];
  constexpr PosLut() : v() {
    for (int j = 0; j < L; ++j) {
      const int sh = (j >= 256) + (j >= 512) + (j >= 1024);
      int q = j >> sh;
      if (sh) {
        const int rem = j & ((1 << sh) - 1), half = 1 << (sh - 1);
        if (rem > half || (rem == half && (q & 1))) ++q;
      }
      v[j] = (unsigned short)(4 * (128 * sh + q));
    }
  }
};
__device__ const PosLut g_poslut{};

__device__ __forceinline__ void conv_block(char* smem, int d0, const __bf16* __restrict__ uT,
                                           const unsigned* __restrict__ kc, const __bf16* __restrict__ dsk,
                                           unsigned* __restrict__ aG) {
  const int tid = threadIdx.x, lane = tid & 63, w = __builtin_amdgcn_readfirstlane(tid >> 6);
  const int ch = w >> 2, wq = w & 3, tc = tid & 255;
  const int r = lane & 15, g = lane >> 4, q = (lane >> 2) & 3, p = lane & 3;
  char* cb = smem + ch * CH_BYTES;
  const int d = d0 + ch;
  STAMP(0);
  __builtin_amdgcn_s_setprio(3);
  {
    const unsigned lb = lds_addr_of(cb);
    glds16(uT + (size_t)d * L + wq * 512, lane * 16, lb + 32 + wq * 1024);
    if (wq < 3) glds16(kc + (size_t)d * KC_PITCH + wq * 256, lane * 16, lb + OFF_TAB + wq * 1024);
    const u32x4 z = {0u, 0u, 0u, 0u};
    if (tc < 60) *(u32x4*)(cb + OFF_K + (tc >= 30 ? KARR : 0) + (tc % 30) * 16) = z;
    else if (tc < 68) *(u32x4*)(cb + ((tc - 60) >> 1) * UCP + ((tc - 60) & 1) * 16) = z;
  }
  const float dsv = (float)dsk[d];
  const u32x4 lut = *(const u32x4*)(g_poslut.v + 8 * tc);
  WAIT_VM(0);
  __syncthreads();
  STAMP(1);
  {
    const u32x4 pa = *(const u32x4*)(cb + 16 + 16 * tc), pb = *(const u32x4*)(cb + 32 + 16 * tc);
    const u32x4 c1 = {__builtin_amdgcn_alignbit(pb[0], pa[3], 16), __builtin_amdgcn_alignbit(pb[1], pb[0], 16),
                      __builtin_amdgcn_alignbit(pb[2], pb[1], 16), __builtin_amdgcn_alignbit(pb[3], pb[2], 16)};
    const u32x4 c2 = {pa[3], pb[0], pb[1], pb[2]};
    const u32x4 c3 = {__builtin_amdgcn_alignbit(pa[3], pa[2], 16), c1[0], c1[1], c1[2]};
    *(u32x4*)(cb + 1 * UCP + 32 + 16 * tc) = c1;
    *(u32x4*)(cb + 2 * UCP + 32 + 16 * tc) = c2;
    *(u32x4*)(cb + 3 * UCP + 32 + 16 * tc) = c3;
    u32x4 kh, kl;
#pragma unroll
    for (int e = 0; e < 4; ++e) {
      const unsigned t0 = *(const unsigned*)(cb + OFF_TAB + (lut[e] & 0xffffu));
      const unsigned t1 = *(const unsigned*)(cb + OFF_TAB + (lut[e] >> 16));
      kh[e] = __builtin_amdgcn_perm(t1, t0, 0x05040100u);
      kl[e] = __builtin_amdgcn_perm(t1, t0, 0x07060302u);
    }
    *(u32x4*)(cb + OFF_K + 480 + 16 * tc) = kh;
    *(u32x4*)(cb + OFF_K + KARR + 480 + 16 * tc) = kl;
  }
  __syncthreads();
  STAMP(2);
  __builtin_amdgcn_s_setprio(PRIO_CV);
  f32x4 acc[8];
#pragma unroll
  for (int t = 0; t < 8; ++t) acc[t] = f32x4{0.f, 0.f, 0.f, 0.f};
#pragma unroll
  for (int rr = 0; rr < 2; ++rr) {
    const int rho = 2 * wq + rr;
    const int abase = q * UCP + 2 * (16 + 16 + 32 * rho - 8 * g + 4 * p);
    const int bbase = OFF_K + 2 * (240 - 16 - 32 * rho + 16 * r + 8 * g);
    bf16x8 af[8];
#pragma unroll
    for (int s = 0; s < 8; ++s) af[s] = tr_frag(cb, abase + 512 * s, abase + 512 * s - 8);
#pragma unroll
    for (int t = 0; t < 8; ++t) {
      const bf16x8 bh = *(const bf16x8*)(cb + bbase + 512 * t);
#pragma unroll
      for (int s = 0; s + t < 8; ++s) acc[s + t] = mfma16(af[s], bh, acc[s + t]);
    }
  }
  __builtin_amdgcn_s_setprio(3);
  __syncthreads();
  if (tid == 256) __hip_atomic_store((gu32*)(aG + (size_t)D * L + (d0 >> 1)), 1u, __ATOMIC_RELAXED, __HIP_MEMORY_SCOPE_AGENT);
  STAMP(3);
#pragma unroll
  for (int t = 0; t < 8; ++t)
    if ((t >> 1) != wq) *(f32x4*)(cb + OFF_RED + (wq * 8 + t) * 1024 + lane * 16) = acc[t];
  __syncthreads();
#pragma unroll
  for (int tt = 0; tt < 2; ++tt) {
    const int t = 2 * wq + tt;
    f32x4 own = f32x4{0.f, 0.f, 0.f, 0.f};
#pragma unroll
    for (int t2 = 0; t2 < 8; ++t2)
      if (t2 == t) own = acc[t2];
    f32x4 s = f32x4{0.f, 0.f, 0.f, 0.f};
#pragma unroll
    for (int sw = 0; sw < 4; ++sw) {
      const f32x4 o = *(const f32x4*)(cb + OFF_RED + (sw * 8 + t) * 1024 + lane * 16);
      s += (sw == wq) ? own : o;
    }
    const int l = 256 * t + 16 * r + 4 * g;
    const u32x2 uu = *(const u32x2*)(cb + 32 + 2 * l);
    const float a0 = bfr(s[0]) + bfr(lo_f(uu[0]) * dsv), a1 = bfr(s[1]) + bfr(hi_f(uu[0]) * dsv);
    const float a2 = bfr(s[2]) + bfr(lo_f(uu[1]) * dsv), a3 = bfr(s[3]) + bfr(hi_f(uu[1]) * dsv);
    const u32x4 gr = {pk2(a0, a1), A_TAG, pk2(a2, a3), A_TAG};
    asm volatile("global_store_dwordx4 %0, %1, off sc1\n\ts_nop 1" ::"v"(aG + (size_t)d * L + l), "v"(gr) : "memory");
  }
  STAMP(4);
}

constexpr int F2_NS = 5;
constexpr int F2_STAGE = 16384;
constexpr int F2_NST = 22;

struct F2Ctx {
  const __bf16 *h, *wd;
  unsigned avoff, bvoff, bvoff_last, lbase;
  int w, kh;
  int aoff[2], boff[2];
};
__device__ __forceinline__ void f2_stage(const F2Ctx& c, int st) {
  const unsigned base = c.lbase + (st % F2_NS) * F2_STAGE;
  glds16(c.h + st * 64, c.avoff, base + c.w * 1024);
  glds16(c.wd + (size_t)st * 64 * D, st == F2_NST - 1 ? c.bvoff_last : c.bvoff, base + 8192 + c.w * 1024);
}
template <int ST>
__device__ __forceinline__ void f2_loop(const F2Ctx& c, const char* smem, bf16x8 (&ao)[2], bf16x8 (&bo)[2],
                                        f32x4 (&acc)[2][2]) {
  if constexpr (ST < F2_NST) {
    constexpr int rem = (F2_NST - 1 - ST) < (F2_NS - 2) ? (F2_NST - 1 - ST) : (F2_NS - 2);
    wait_vm<2 * rem>();
    WAIT_LGKM0();
    __builtin_amdgcn_s_barrier();
    asm volatile("" ::: "memory");
    STAMP(1 + ST);
    if constexpr (ST + F2_NS - 1 < F2_NST) f2_stage(c, ST + F2_NS - 1);
    const char* base = smem + (ST % F2_NS) * F2_STAGE;
    bf16x8 an[2], bn[2];
#pragma unroll
    for (int mt = 0; mt < 2; ++mt) an[mt] = *(const bf16x8*)(base + c.aoff[mt]);
#pragma unroll
    for (int nt = 0; nt < 2; ++nt) bn[nt] = tr_frag(base, c.boff[nt], c.boff[nt] + 512);
    __builtin_amdgcn_sched_barrier(0);
    if constexpr (ST > 0) {
#pragma unroll
      for (int mt = 0; mt < 2; ++mt)
#pragma unroll
        for (int nt = 0; nt < 2; ++nt) acc[mt][nt] = mfma16(ao[mt], bo[nt], acc[mt][nt]);
    }
    __builtin_amdgcn_sched_barrier(0);
    f2_loop<ST + 1>(c, smem, an, bn, acc);
  } else {
    if (c.kh == 0) {
#pragma unroll
      for (int mt = 0; mt < 2; ++mt)
#pragma unroll
        for (int nt = 0; nt < 2; ++nt) acc[mt][nt] = mfma16(ao[mt], bo[nt], acc[mt][nt]);
    }
  }
}

__device__ __forceinline__ void ffn2_block(char* smem, int row0, int cbk, const __bf16* __restrict__ h,
                                           const __bf16* __restrict__ wd, const unsigned* __restrict__ aG,
                                           const float* __restrict__ gin, __bf16* __restrict__ out) {
  const int col0 = 64 * cbk;
  const int tid = threadIdx.x, lane = tid & 63, w = __builtin_amdgcn_readfirstlane(tid >> 6);
  const int r = lane & 15, g = lane >> 4, q = (lane >> 2) & 3, p = lane & 3;
  const int kh = w >> 2, wm = (w >> 1) & 1, wn = w & 1;
  STAMP(0);
  __builtin_amdgcn_s_setprio(PRIO_F2);
  F2Ctx c;
  c.h = h; c.wd = wd; c.w = w; c.kh = kh; c.lbase = lds_addr_of(smem);
  {
    const int row = 8 * w + (lane >> 3), ch = (lane & 7) ^ ((row >> 1) & 7);
    c.avoff = (unsigned)(((row0 + row) * HP + ch * 8) * 2);
    const int bk = 8 * w + (lane >> 3);
    const int pos = lane & 7, fp = ((bk >> 1) & 1) | (((bk >> 3) & 1) << 1);
    const int cc = col0 + 16 * ((pos >> 1) ^ fp) + 8 * (pos & 1);
    c.bvoff = (unsigned)((bk * D + cc) * 2);
    c.bvoff_last = (unsigned)(((bk > 31 ? 31 : bk) * D + cc) * 2);
  }
#pragma unroll
  for (int mt = 0; mt < 2; ++mt) {
    const int row = 32 * wm + 16 * mt + r, ch = 4 * kh + g;
    c.aoff[mt] = row * 128 + ((ch ^ ((row >> 1) & 7)) << 4);
  }
  const int fpl = (q >> 1) | ((g & 1) << 1);
#pragma unroll
  for (int nt = 0; nt < 2; ++nt) c.boff[nt] = 8192 + (32 * kh + 8 * g + q) * 128 + (((2 * wn + nt) ^ fpl) << 5) + 8 * p;

  f32x4 acc[2][2];
#pragma unroll
  for (int mt = 0; mt < 2; ++mt)
#pragma unroll
    for (int nt = 0; nt < 2; ++nt) acc[mt][nt] = f32x4{0.f, 0.f, 0.f, 0.f};

#pragma unroll
  for (int st = 0; st < F2_NS - 1; ++st) f2_stage(c, st);
  const int lr = 32 * wm + 16 * kh + 4 * g;
  const f32x4 gv = *(const f32x4*)(gin + row0 + lr);
  bf16x8 a0[2], b0[2];
  f2_loop<0>(c, smem, a0, b0, acc);
  STAMP(23);
  WAIT_LGKM0();
  __builtin_amdgcn_s_barrier();
  asm volatile("" ::: "memory");
  if (w == 0) {
    const gu32* fl = (const gu32*)(aG + (size_t)D * L + 32 * cbk + (lane & 31));
    for (int spin = 0; spin < (1 << 22); ++spin) {
      const unsigned v = __hip_atomic_load(fl, __ATOMIC_RELAXED, __HIP_MEMORY_SCOPE_AGENT);
      if (__all(v == 1u)) break;
      __builtin_amdgcn_s_sleep(2);
    }
  }
  {
    char* red = smem + (w & 3) * 4096 + kh * 2048;
    const f32x4 o0 = kh ? acc[0][0] : acc[1][0], o1 = kh ? acc[0][1] : acc[1][1];
    *(f32x4*)(red + lane * 16) = o0;
    *(f32x4*)(red + 1024 + lane * 16) = o1;
  }
  __syncthreads();
  unsigned long long av[2];
  {
    const unsigned* src = aG + (size_t)(col0 + 32 * wn + r) * L + row0 + lr;
    for (int spin = 0; spin < (1 << 22); ++spin) {
      u32x4 x0, x1;
      asm volatile("global_load_dwordx4 %0, %2, off sc1\n\tglobal_load_dwordx4 %1, %3, off sc1\n\ts_waitcnt vmcnt(0)"
                   : "=&v"(x0), "=&v"(x1) : "v"(src), "v"(src + 16 * L) : "memory");
      av[0] = (unsigned long long)x0[0] | ((unsigned long long)x0[2] << 32);
      av[1] = (unsigned long long)x1[0] | ((unsigned long long)x1[2] << 32);
      const bool ok = x0[1] == A_TAG && x0[3] == A_TAG && x1[1] == A_TAG && x1[3] == A_TAG;
      if (__all(ok)) break;
      __builtin_amdgcn_s_sleep(4);
    }
  }
  constexpr int OT = 16384, OTP = 144;
  {
    const char* red = smem + (w & 3) * 4096 + (1 - kh) * 2048;
#pragma unroll
    for (int nt = 0; nt < 2; ++nt) {
      const f32x4 o = *(const f32x4*)(red + nt * 1024 + lane * 16);
      const f32x4 own = kh ? acc[1][nt] : acc[0][nt];
      const f32x4 s = kh ? (o + own) : (own + o);
      const unsigned alo = (unsigned)av[nt], ahi = (unsigned)(av[nt] >> 32);
      const float a[4] = {lo_f(alo), hi_f(alo), lo_f(ahi), hi_f(ahi)};
#pragma unroll
      for (int e = 0; e < 4; ++e) {
        const float o2 = bfr(gv[e] * a[e]) + bfr(bfr(1.f - gv[e]) * bfr(s[e]));
        *(__bf16*)(smem + OT + (lr + e) * OTP + (32 * wn + 16 * nt + r) * 2) = (__bf16)o2;
      }
    }
  }
  __syncthreads();
  {
    const int row = tid >> 3, c8 = tid & 7;
    const u32x4 v = *(const u32x4*)(smem + OT + row * OTP + c8 * 16);
    *(u32x4*)(out + (size_t)(row0 + row) * D + col0 + c8 * 8) = v;
  }
  STAMP(24);
}

NOPK __global__ __launch_bounds__(512, 4) void k2(const __bf16* __restrict__ h, const __bf16* __restrict__ wd,
                                             const __bf16* __restrict__ uT, const unsigned* __restrict__ kc,
                                             const __bf16* __restrict__ dsk, unsigned* __restrict__ aG,
                                             const float* __restrict__ gin, __bf16* __restrict__ out) {
  extern __shared__ __attribute__((aligned(16))) char smem[];
  const int b = blockIdx.x, xcd = b & 7, i = (b & 255) >> 3;
  SPAN_PT(1, 0);
  if (b < 256) {
    const int pair = 32 * xcd + i;
    conv_block(smem, 2 * pair, uT, kc, dsk, aG);
  } else {
    ffn2_block(smem, 64 * (4 * xcd + (i & 3)), i >> 2, h, wd, aG, gin, out);
  }
  SPAN_PT(1, 1);
}

extern "C" void kernel_launch(void* const* d_in, const int* in_sizes, int n_in, void* d_out, int out_size,
                              void* d_ws, size_t ws_size, hipStream_t stream) {
  const __bf16* u = (const __bf16*)d_in[0];
  const __bf16* lar = (const __bf16*)d_in[1];
  const __bf16* aim = (const __bf16*)d_in[2];
  const __bf16* cre = (const __bf16*)d_in[3];
  const __bf16* cim = (const __bf16*)d_in[4];
  const __bf16* ldt = (const __bf16*)d_in[5];
  const __bf16* dsk = (const __bf16*)d_in[6];
  const __bf16* wg = (const __bf16*)d_in[8];
  const __bf16* wu = (const __bf16*)d_in[9];
  const __bf16* wd = (const __bf16*)d_in[10];
  const __bf16* gw = (const __bf16*)d_in[11];
  char* ws = (char*)d_ws;
  __bf16* h = (__bf16*)ws;
  ws += (size_t)L * HP * 2;
  unsigned* aG = (unsigned*)ws;
  ws += (size_t)D * L * 4 + 1024;
  __bf16* uT = (__bf16*)ws;
  ws += (size_t)D * L * 2;
  unsigned* kc = (unsigned*)ws;
  ws += (size_t)D * KC_PITCH * 4;
  float* gout = (float*)ws;
  (void)hipFuncSetAttribute((const void*)k1, hipFuncAttributeMaxDynamicSharedMemorySize, LDS_BYTES);
  (void)hipFuncSetAttribute((const void*)k2, hipFuncAttributeMaxDynamicSharedMemorySize, LDS_BYTES);
  hipLaunchKernelGGL(k1, dim3(512), dim3(512), LDS_BYTES, stream, u, lar, aim, cre, cim, ldt, wg, wu, gw, h, uT, kc,
                     gout, aG, wd);
  hipLaunchKernelGGL(k2, dim3(512), dim3(512), LDS_BYTES, stream, (const __bf16*)h, wd, (const __bf16*)uT,
                     (const unsigned*)kc, dsk, aG, (const float*)gout, (__bf16*)d_out);
}
```

```cpp
#include <hip/hip_runtime.h>
#include <hip/hip_bf16.h>

typedef __attribute__((ext_vector_type(8))) __bf16 bf16x8;
typedef __attribute__((ext_vector_type(2))) __bf16 bf16x2;
typedef __attribute__((ext_vector_type(4))) short s16x4;
typedef __attribute__((ext_vector_type(8))) short s16x8;
typedef __attribute__((ext_vector_type(4))) float f32x4;
typedef __attribute__((ext_vector_type(4))) unsigned u32x4;
typedef __attribute__((ext_vector_type(2))) unsigned u32x2;
typedef __attribute__((address_space(3))) s16x4* lds_s16x4_ptr;
typedef __attribute__((address_space(1))) unsigned gu32;
typedef __attribute__((address_space(1))) unsigned long long gu64;

#define K_LO 1

#define NOPK __attribute__((target("no-packed-fp32-ops")))

#define PRIO_F1 0
#define PRIO_KG 0
#define PRIO_CV 1
#define PRIO_CV2 PRIO_CV
#define PRIO_F2 1

constexpr int L = 2048, D = 512, NS = 128, F = 1376, HP = 1408;
constexpr int LDS_BYTES = 81920;

__device__ __forceinline__ float bf2f(unsigned short v) { return __uint_as_float((unsigned)v << 16); }
__device__ __forceinline__ float bfr(float x) { return (float)(__bf16)x; }
__device__ __forceinline__ unsigned pk2(float a, float b) {
  bf16x2 v = {(__bf16)a, (__bf16)b};
  return __builtin_bit_cast(unsigned, v);
}
__device__ __forceinline__ float lo_f(unsigned w) { return __uint_as_float(w << 16); }
__device__ __forceinline__ float hi_f(unsigned w) { return __uint_as_float(w & 0xffff0000u); }

__device__ __forceinline__ f32x4 mfma16(bf16x8 a, bf16x8 b, f32x4 c) {
  return __builtin_amdgcn_mfma_f32_16x16x32_bf16(a, b, c, 0, 0, 0);
}
__device__ __forceinline__ bf16x8 tr_frag(const char* lds, int off0, int off1) {
  s16x4 a = __builtin_amdgcn_ds_read_tr16_b64_v4i16((lds_s16x4_ptr)(lds + off0));
  s16x4 b = __builtin_amdgcn_ds_read_tr16_b64_v4i16((lds_s16x4_ptr)(lds + off1));
  s16x8 v = {a[0], a[1], a[2], a[3], b[0], b[1], b[2], b[3]};
  return __builtin_bit_cast(bf16x8, v);
}
__device__ __forceinline__ void glds16(const void* sbase, unsigned voff, unsigned lds_addr) {
  asm volatile("s_mov_b32 m0, %2\n\ts_nop 0\n\tglobal_load_lds_dwordx4 %0, %1" ::"v"(voff), "s"(sbase), "s"(lds_addr)
               : "memory", "m0");
}
__device__ __forceinline__ void glds16v(const void* vptr, unsigned lds_addr) {
  asm volatile("s_mov_b32 m0, %1\n\ts_nop 0\n\tglobal_load_lds_dwordx4 %0, off" ::"v"(vptr), "s"(lds_addr)
               : "memory", "m0");
}
__device__ __forceinline__ unsigned lds_addr_of(const char* p) {
  return (unsigned)(uintptr_t)(const __attribute__((address_space(3))) char*)p;
}
#define WAIT_VM(n) asm volatile("s_waitcnt vmcnt(" #n ")" ::: "memory")
#define WAIT_LGKM0() asm volatile("s_waitcnt lgkmcnt(0)" ::: "memory")
template <int N>
__device__ __forceinline__ void wait_vm() {
  asm volatile("s_waitcnt vmcnt(%0)" ::"i"(N) : "memory");
}

constexpr int F1_B = 32768;

struct F1Ctx {
  const __bf16 *x, *wg, *wu, *wmix;
  unsigned voffA, voffB0, voffB2, lbase;
  int w;
  int aoff, boff[3];
};
template <bool G2>
__device__ __forceinline__ void f1_stage(const F1Ctx& c, int hs) {
  const unsigned bb = c.lbase + F1_B + (hs & 3) * 12288 + c.w * 1024;
  glds16(c.x + hs * 32, c.voffA, c.lbase + (hs & 3) * 8192 + c.w * 1024);
  if constexpr (!G2) {
    glds16(c.wg + (size_t)hs * 32 * F, c.voffB0, bb);
    glds16(c.wu + (size_t)hs * 32 * F, c.voffB2, bb + 8192);
  } else {
    glds16v(c.wmix + (size_t)hs * 32 * F, bb);
  }
}
template <int HS, bool G2>
__device__ __forceinline__ void f1_loop(const F1Ctx& c, const char* smem, bf16x8 (&af)[4], bf16x8 (&bq)[3],
                                        f32x4 (&acc)[4][3]) {
  if constexpr (HS < 16) {
    if constexpr (HS > 0) {
#pragma unroll
      for (int j = 0; j < 3; ++j) {
        acc[0][j] = mfma16(bq[j], af[0], acc[0][j]);
        acc[1][j] = mfma16(bq[j], af[1], acc[1][j]);
      }
    }
    __builtin_amdgcn_sched_barrier(0);
    constexpr int rem = (15 - HS) < 2 ? (15 - HS) : 2;
    wait_vm<rem*(G2 ? 2 : 3)>();
    WAIT_LGKM0();
    __builtin_amdgcn_s_barrier();
    asm volatile("" ::: "memory");
    const char* ab = smem + (HS & 3) * 8192 + c.aoff;
    const char* bbase = smem + (HS & 3) * 12288;
    __builtin_amdgcn_sched_barrier(0);
    af[0] = *(const bf16x8*)(ab);
    af[1] = *(const bf16x8*)(ab + 1024);
#pragma unroll
    for (int j = 0; j < 3; ++j) {
      if constexpr (HS > 0) {
        acc[2][j] = mfma16(bq[j], af[2], acc[2][j]);
        acc[3][j] = mfma16(bq[j], af[3], acc[3][j]);
      }
      __builtin_amdgcn_sched_barrier(0);
      if (j > 0) bq[j - 1] = tr_frag(bbase, c.boff[j - 1], c.boff[j - 1] + 512);
      else if constexpr (HS + 3 < 16) f1_stage<G2>(c, HS + 3);
      __builtin_amdgcn_sched_barrier(0);
    }
    bq[2] = tr_frag(bbase, c.boff[2], c.boff[2] + 512);
    af[2] = *(const bf16x8*)(ab + 2048);
    af[3] = *(const bf16x8*)(ab + 3072);
    f1_loop<HS + 1, G2>(c, smem, af, bq, acc);
  } else {
#pragma unroll
    for (int j = 0; j < 3; ++j)
#pragma unroll
      for (int mt = 0; mt < 4; ++mt) acc[mt][j] = mfma16(bq[j], af[mt], acc[mt][j]);
  }
}

__device__ __forceinline__ void ffn1_block(char* smem, int rb, int cb, const __bf16* __restrict__ x,
                                           const __bf16* __restrict__ wg, const __bf16* __restrict__ wu,
                                           __bf16* __restrict__ h) {
  const int tid = threadIdx.x, lane = tid & 63, w = __builtin_amdgcn_readfirstlane(tid >> 6);
  const int row0 = rb * 128, f0 = cb * 96;
  const int wm = w >> 2, sel = (w >> 1) & 1, wn = w & 1;
  const int r = lane & 15, g = lane >> 4, q = (lane >> 2) & 3, p = lane & 3;
  __builtin_amdgcn_s_setprio(PRIO_F1);
  F1Ctx c;
  c.x = x; c.wg = wg; c.wu = wu; c.w = w; c.lbase = lds_addr_of(smem);
  {
    const int row = 16 * w + (lane >> 2), ch = (lane & 3) ^ ((-(row >> 2)) & 3);
    c.voffA = (unsigned)(((row0 + row) * D + ch * 8) * 2);
    const int kk = 8 * (w & 3) + (lane >> 3), pos = lane & 7;
    const int fp = ((kk >> 1) & 1) | (((kk >> 3) & 1) << 1);
    const int i = (pos >> 1) ^ fp;
    int c0 = f0 + 16 * i + 8 * (pos & 1);
    int c1 = f0 + 16 * ((4 + i) % 6) + 8 * (pos & 1);
    int c2 = f0 + 16 * (2 + i) + 8 * (pos & 1);
    c0 = c0 > F - 8 ? F - 8 : c0;
    c1 = c1 > F - 8 ? F - 8 : c1;
    c2 = c2 > F - 8 ? F - 8 : c2;
    c.voffB0 = (unsigned)((kk * F + c0) * 2);
    c.voffB2 = (unsigned)((kk * F + c2) * 2);
    c.wmix = (i < 2 ? wg : wu) + kk * F + c1;
  }
  c.aoff = (64 * wm + r) * 64 + ((g ^ ((-(r >> 2)) & 3)) << 4);
  {
    const int fp = (q >> 1) | ((g & 1) << 1);
#pragma unroll
    for (int j = 0; j < 3; ++j) {
      const int T = 6 * sel + 3 * wn + j;
      c.boff[j] = F1_B + (T >> 2) * 4096 + (8 * g + q) * 128 + (((T & 3) ^ fp) << 5) + 8 * p;
      asm volatile("" : "+v"(c.boff[j]));
    }
  }
  f32x4 acc[4][3];
#pragma unroll
  for (int mt = 0; mt < 4; ++mt)
#pragma unroll
    for (int j = 0; j < 3; ++j) acc[mt][j] = f32x4{0.f, 0.f, 0.f, 0.f};
  bf16x8 af[4], bq[3];

  if (w < 4) {
    f1_stage<false>(c, 0);
    f1_stage<false>(c, 1);
    f1_stage<false>(c, 2);
    f1_loop<0, false>(c, smem, af, bq, acc);
  } else {
    f1_stage<true>(c, 0);
    f1_stage<true>(c, 1);
    f1_stage<true>(c, 2);
    f1_loop<0, true>(c, smem, af, bq, acc);
  }

  __syncthreads();
#pragma unroll
  for (int m2 = 0; m2 < 2; ++m2)
#pragma unroll
    for (int j = 0; j < 3; ++j)
      *(f32x4*)(smem + (w * 6 + m2 * 3 + j) * 1024 + lane * 16) = sel ? acc[m2][j] : acc[2 + m2][j];
  __syncthreads();
#pragma unroll
  for (int m2 = 0; m2 < 2; ++m2)
#pragma unroll
    for (int j = 0; j < 3; ++j) {
      const int f = f0 + 16 * (3 * wn + j);
      if (f < F) {
        const f32x4 o4 = *(const f32x4*)(smem + ((w ^ 2) * 6 + m2 * 3 + j) * 1024 + lane * 16);
        const f32x4 own = sel ? acc[2 + m2][j] : acc[m2][j];
        const f32x4 g4 = sel ? o4 : own, u4 = sel ? own : o4;
        float hv[4];
#pragma unroll
        for (int e = 0; e < 4; ++e) {
          const float gv = g4[e];
          const float t = gv * __builtin_fmaf(gv * gv, -0.10294324f, -2.3022082f);
          hv[e] = (gv * u4[e]) * __builtin_amdgcn_rcpf(1.f + __builtin_amdgcn_exp2f(t));
        }
        u32x2 o = {pk2(hv[0], hv[1]), pk2(hv[2], hv[3])};
        *(u32x2*)(h + (size_t)(row0 + 64 * wm + 32 * sel + 16 * m2 + r) * HP + f + 4 * g) = o;
      }
    }
}

__device__ __forceinline__ void gate_block(char* smem, int rb, const __bf16* __restrict__ x,
                                           const __bf16* __restrict__ gw, float* __restrict__ gout,
                                           __bf16* __restrict__ uT) {
  const int tid = threadIdx.x, lane = tid & 63, w = __builtin_amdgcn_readfirstlane(tid >> 6);
  const int g = lane >> 4, q = (lane >> 2) & 3, p = lane & 3;
  const unsigned lbase = lds_addr_of(smem);
  const int grow = tid >> 3, gpart = tid & 7, gf = (grow & 3) | (((grow >> 3) & 1) << 2);
  u32x4 gwv[8];
#pragma unroll
  for (int i = 0; i < 8; ++i) gwv[i] = *(const u32x4*)(gw + ((gpart ^ gf) + 8 * (i >> 1)) * 16 + 8 * (i & 1));
#pragma unroll 1
  for (int pass = 0; pass < 2; ++pass) {
    const int l0 = rb * 128 + pass * 64;
#pragma unroll
    for (int i = 0; i < 8; ++i) {
      const int row = 8 * w + i, fr = (row & 3) | (((row >> 3) & 1) << 2);
      glds16(x + (size_t)(l0 + row) * D, (unsigned)((((lane >> 1) ^ fr) * 2 + (lane & 1)) * 16), lbase + row * 1024);
    }
    WAIT_VM(0);
    __syncthreads();
#pragma unroll
    for (int i = 0; i < 8; ++i) {
      const int unit = w * 8 + i, c16 = unit >> 1, half = unit & 1;
      const int ra = 32 * half + 8 * g + q, rb2 = ra + 4;
      const int fa = (ra & 3) | (((ra >> 3) & 1) << 2);
      const bf16x8 v = tr_frag(smem, ra * 1024 + ((c16 ^ fa) << 5) + 8 * p, rb2 * 1024 + ((c16 ^ fa) << 5) + 8 * p);
      *(bf16x8*)(uT + (size_t)(16 * c16 + (lane & 15)) * L + l0 + 32 * half + 8 * g) = v;
    }
    float s = 0.f;
#pragma unroll
    for (int i = 0; i < 8; ++i) {
      const u32x4 xv = *(const u32x4*)(smem + grow * 1024 + (gpart + 8 * (i >> 1)) * 32 + 16 * (i & 1));
#pragma unroll
      for (int e = 0; e < 4; ++e) {
        const unsigned xe = xv[e], we = gwv[i][e];
        s = __builtin_amdgcn_fdot2_f32_bf16(__builtin_bit_cast(bf16x2, xe), __builtin_bit_cast(bf16x2, we), s, false);
      }
    }
    s += __shfl_xor(s, 1);
    s += __shfl_xor(s, 2);
    s += __shfl_xor(s, 4);
    if (gpart == 0) gout[l0 + grow] = bfr(1.f / (1.f + __expf(-bfr(s))));
    __syncthreads();
  }
}

constexpr int KC_PITCH = 768;
constexpr int KG_PITCH = 160;
constexpr int KG_ARR = 16 * KG_PITCH;
constexpr int KG_TAB = 8 * 2 * KG_ARR;
typedef __attribute__((ext_vector_type(2))) float f32x2;

__device__ __forceinline__ f32x2 cexp(float zr, float zi, float p) {
  const float e = __builtin_amdgcn_exp2f(zr * p);
  const float ph = __builtin_amdgcn_fractf(zi * p);
  return f32x2{e * __builtin_amdgcn_cosf(ph), e * __builtin_amdgcn_sinf(ph)};
}
__device__ __forceinline__ f32x2 cmul(f32x2 a, f32x2 b) {
  return f32x2{a[0] * b[0] - a[1] * b[1], a[0] * b[1] + a[1] * b[0]};
}
__device__ __forceinline__ void kgen_block(char* smem, int d0, const __bf16* __restrict__ lar,
                                           const __bf16* __restrict__ aim, const __bf16* __restrict__ cre,
                                           const __bf16* __restrict__ cim, const __bf16* __restrict__ ldt,
                                           unsigned* __restrict__ kc, unsigned* __restrict__ aG,
                                           const __bf16* __restrict__ wd) {
  const int tid = threadIdx.x, lane = tid & 63, w = __builtin_amdgcn_readfirstlane(tid >> 6);
  unsigned pf_dummy = 0;
  if (tid < 43)
    asm volatile("global_load_dword %0, %1, off" : "=v"(pf_dummy) : "v"(wd + (size_t)((d0 >> 1) * 43 + tid) * 64) : "memory");
  {
    u32x4* z = (u32x4*)(aG + (size_t)d0 * L) + tid;
    z[0] = u32x4{0u, 0u, 0u, 0u};
    z[512] = u32x4{0u, 0u, 0u, 0u};
    if (tid == 0) aG[(size_t)D * L + (d0 >> 1)] = 0u;
  }
  const int ch = w >> 2, wq = w & 3, tc = tid & 255;
  const int r = lane & 15, g = lane >> 4, hh = lane >> 5;
  char* wb = smem + w * 2 * KG_ARR;
  char* tb = smem + KG_TAB + ch * (4 * 656 * 4);
  __builtin_amdgcn_s_setprio(PRIO_KG);
  float zr, zi;
  f32x2 cc;
  {
    const int d = d0 + ch, n = 32 * wq + (lane & 31);
    const float dt = bfr(expf((float)ldt[d]));
    const float ar = -bfr(expf((float)lar[d * NS + n]));
    zr = (ar * dt) * 1.4426950408889634f;
    zi = ((float)aim[d * NS + n] * dt) * 0.15915494309189535f;
    cc = f32x2{2.f * ((float)cre[d * NS + n] * dt), 2.f * ((float)cim[d * NS + n] * dt)};
  }
  const float fh = (float)(1 + hh);
  const f32x2 e128 = cexp(zr, zi, 128.f), e24 = cexp(zr, zi, 2.f * fh);
  f32x2 v[4], st[4];
  v[0] = cexp(zr, -zi, 64.f * hh);                 st[0] = cexp(zr, -zi, 8.f);
  v[1] = hh ? cmul(cc, e128) : cc;                 st[1] = cexp(zr, zi, 1.f);
  v[2] = cmul(cc, cexp(zr, zi, 256.f * fh));       st[2] = hh ? e128 : e24;
  v[3] = cmul(cc, cexp(zr, zi, 1024.f * fh));      st[3] = e128;
  const f32x2 v24 = cmul(v[2], hh ? e24 : e128);
  unsigned vh[4][8], vl[4][8];
#pragma unroll
  for (int i = 0; i < 8; ++i)
#pragma unroll
    for (int t = 0; t < 4; ++t) {
      vh[t][i] = pk2(v[t][0], v[t][1]);
      vl[t][i] = pk2(v[t][0] - lo_f(vh[t][i]), v[t][1] - hi_f(vh[t][i]));
      if (t == 2 && i == 3) v[t] = v24;
      else if (i < 7) v[t] = cmul(v[t], st[t]);
    }
  char* wr = wb + (8 * hh) * KG_PITCH + (lane & 31) * 4;
  const char* rd = wb + r * KG_PITCH + g * 16;
  bf16x8 fh_[4][2], fl_[4][2];
#pragma unroll
  for (int t = 0; t < 4; ++t) {
    __builtin_amdgcn_wave_barrier();
#pragma unroll
    for (int i = 0; i < 8; ++i) {
      *(unsigned*)(wr + i * KG_PITCH) = vh[t][i];
      *(unsigned*)(wr + i * KG_PITCH + KG_ARR) = vl[t][i];
    }
    __builtin_amdgcn_wave_barrier();
#pragma unroll
    for (int ss = 0; ss < 2; ++ss) {
      fh_[t][ss] = *(const bf16x8*)(rd + ss * 64);
      fl_[t][ss] = *(const bf16x8*)(rd + ss * 64 + KG_ARR);
    }
  }
  f32x4 acc[3];
#pragma unroll
  for (int m = 0; m < 3; ++m) acc[m] = f32x4{0.f, 0.f, 0.f, 0.f};
#pragma unroll
  for (int ss = 0; ss < 2; ++ss)
#pragma unroll
    for (int m = 0; m < 3; ++m) {
      acc[m] = mfma16(fl_[1 + m][ss], fh_[0][ss], acc[m]);
      acc[m] = mfma16(fh_[1 + m][ss], fl_[0][ss], acc[m]);
      acc[m] = mfma16(fh_[1 + m][ss], fh_[0][ss], acc[m]);
    }
  {
    float* tab = (float*)tb + wq * 656;
    *(f32x4*)(tab + 128 * (g >> 1) + 4 * (g & 1) + 8 * r) = acc[0];
    if (g < 2) {
      *(f32x4*)(tab + 256 + 64 * g + 4 * r) = acc[1];
#pragma unroll
      for (int e = 0; e < 4; ++e) tab[512 + 64 * g + 16 * e + r] = acc[2][e];
    } else {
#pragma unroll
      for (int e = 0; e < 4; ++e) tab[384 + (g & 1) + 32 * e + 2 * r] = acc[1][e];
      if (lane == 32) tab[640] = acc[2][0];
    }
  }
  __syncthreads();
  {
    const float* tab = (const float*)tb;
    unsigned* kd = kc + (size_t)(d0 + ch) * KC_PITCH;
#pragma unroll
    for (int e = 0; e < 3; ++e) {
      const int id = tc + 256 * e;
      if (id < 641) {
        const float v = (tab[id] + tab[656 + id]) + (tab[2 * 656 + id] + tab[3 * 656 + id]);
        const float vh = bfr(v);
        kd[id] = pk2(vh, v - vh);
      }
    }
  }
  asm volatile("s_waitcnt vmcnt(0)" : "+v"(pf_dummy)::"memory");
}

NOPK __global__ __launch_bounds__(512, 4) void k1(const __bf16* __restrict__ u, const __bf16* __restrict__ lar,
                                             const __bf16* __restrict__ aim, const __bf16* __restrict__ cre,
                                             const __bf16* __restrict__ cim, const __bf16* __restrict__ ldt,
                                             const __bf16* __restrict__ wg, const __bf16* __restrict__ wu,
                                             const __bf16* __restrict__ gw, __bf16* __restrict__ h,
                                             __bf16* __restrict__ uT, unsigned* __restrict__ kc,
                                             float* __restrict__ gout, unsigned* __restrict__ aG,
                                             const __bf16* __restrict__ wd) {
  extern __shared__ __attribute__((aligned(16))) char smem[];
  const int b = blockIdx.x;
  const int xcd = b & 7, i = (b & 255) >> 3;
  if (b < 256) {
    const int rb = 4 * (xcd >> 1) + (i & 3), cb = 8 * (xcd & 1) + (i >> 2);
    if (cb < 15) ffn1_block(smem, rb, cb, u, wg, wu, h);
    else gate_block(smem, rb, u, gw, gout, uT);
  } else {
    kgen_block(smem, 64 * xcd + 2 * i, lar, aim, cre, cim, ldt, kc, aG, wd);
  }
}

constexpr unsigned A_TAG = 0x5a17c3e9u;
constexpr int UCP = 4160;
constexpr int KARR = (240 + L) * 2;
constexpr int OFF_K = 4 * UCP;
constexpr int OFF_TAB = OFF_K + 2 * KARR;
constexpr int OFF_RED = UCP;
constexpr int CH_BYTES = OFF_RED + 32768;
static_assert(OFF_TAB + 3072 <= CH_BYTES && 2 * CH_BYTES <= LDS_BYTES, "lds");

struct PosLut {
  unsigned short v[L];
  constexpr PosLut() : v() {
    for (int j = 0; j < L; ++j) {
      const int sh = (j >= 256) + (j >= 512) + (j >= 1024);
      int q = j >> sh;
      if (sh) {
        const int rem = j & ((1 << sh) - 1), half = 1 << (sh - 1);
        if (rem > half || (rem == half && (q & 1))) ++q;
      }
      v[j] = (unsigned short)(4 * (128 * sh + q));
    }
  }
};
__device__ const PosLut g_poslut{};

__device__ __forceinline__ void conv_block(char* smem, int d0, const __bf16* __restrict__ uT,
                                           const unsigned* __restrict__ kc, const __bf16* __restrict__ dsk,
                                           unsigned* __restrict__ aG) {
  const int tid = threadIdx.x, lane = tid & 63, w = __builtin_amdgcn_readfirstlane(tid >> 6);
  const int ch = w >> 2, wq = w & 3, tc = tid & 255;
  const int r = lane & 15, g = lane >> 4, q = (lane >> 2) & 3, p = lane & 3;
  char* cb = smem + ch * CH_BYTES;
  const int d = d0 + ch;
  __builtin_amdgcn_s_setprio(3);
  {
    const unsigned lb = lds_addr_of(cb);
    glds16(uT + (size_t)d * L + wq * 512, lane * 16, lb + 32 + wq * 1024);
    if (wq < 3) glds16(kc + (size_t)d * KC_PITCH + wq * 256, lane * 16, lb + OFF_TAB + wq * 1024);
    const u32x4 z = {0u, 0u, 0u, 0u};
    if (tc < 60) *(u32x4*)(cb + OFF_K + (tc >= 30 ? KARR : 0) + (tc % 30) * 16) = z;
    else if (tc < 68) *(u32x4*)(cb + ((tc - 60) >> 1) * UCP + ((tc - 60) & 1) * 16) = z;
  }
  const float dsv = (float)dsk[d];
  const u32x4 lut = *(const u32x4*)(g_poslut.v + 8 * tc);
  WAIT_VM(0);
  __syncthreads();
  {
    const u32x4 pa = *(const u32x4*)(cb + 16 + 16 * tc), pb = *(const u32x4*)(cb + 32 + 16 * tc);
    const u32x4 c1 = {__builtin_amdgcn_alignbit(pb[0], pa[3], 16), __builtin_amdgcn_alignbit(pb[1], pb[0], 16),
                      __builtin_amdgcn_alignbit(pb[2], pb[1], 16), __builtin_amdgcn_alignbit(pb[3], pb[2], 16)};
    const u32x4 c2 = {pa[3], pb[0], pb[1], pb[2]};
    const u32x4 c3 = {__builtin_amdgcn_alignbit(pa[3], pa[2], 16), c1[0], c1[1], c1[2]};
    *(u32x4*)(cb + 1 * UCP + 32 + 16 * tc) = c1;
    *(u32x4*)(cb + 2 * UCP + 32 + 16 * tc) = c2;
    *(u32x4*)(cb + 3 * UCP + 32 + 16 * tc) = c3;
    u32x4 kh, kl;
#pragma unroll
    for (int e = 0; e < 4; ++e) {
      const unsigned t0 = *(const unsigned*)(cb + OFF_TAB + (lut[e] & 0xffffu));
      const unsigned t1 = *(const unsigned*)(cb + OFF_TAB + (lut[e] >> 16));
      kh[e] = __builtin_amdgcn_perm(t1, t0, 0x05040100u);
      kl[e] = __builtin_amdgcn_perm(t1, t0, 0x07060302u);
    }
    *(u32x4*)(cb + OFF_K + 480 + 16 * tc) = kh;
    *(u32x4*)(cb + OFF_K + KARR + 480 + 16 * tc) = kl;
  }
  __syncthreads();
  __builtin_amdgcn_s_setprio(PRIO_CV);
  f32x4 acc[8];
#pragma unroll
  for (int t = 0; t < 8; ++t) acc[t] = f32x4{0.f, 0.f, 0.f, 0.f};
#pragma unroll
  for (int rr = 0; rr < 2; ++rr) {
    if (rr == 1) __builtin_amdgcn_s_setprio(PRIO_CV2);
    const int rho = 2 * wq + rr;
    const int abase = q * UCP + 2 * (16 + 16 + 32 * rho - 8 * g + 4 * p);
    const int bbase = OFF_K + 2 * (240 - 16 - 32 * rho + 16 * r + 8 * g);
    bf16x8 af[8];
#pragma unroll
    for (int s = 0; s < 8; ++s) af[s] = tr_frag(cb, abase + 512 * s, abase + 512 * s - 8);
#pragma unroll
    for (int t = 0; t < 8; ++t) {
      const bf16x8 bh = *(const bf16x8*)(cb + bbase + 512 * t);
#pragma unroll
      for (int s = 0; s + t < 8; ++s) acc[s + t] = mfma16(af[s], bh, acc[s + t]);
      const bf16x8 bl = *(const bf16x8*)(cb + bbase + KARR + 512 * t);
#pragma unroll
      for (int s = 0; s + t < 8; ++s) acc[s + t] = mfma16(af[s], bl, acc[s + t]);
    }
  }
  __builtin_amdgcn_s_setprio(3);
  __syncthreads();
  if (tid == 256) __hip_atomic_store((gu32*)(aG + (size_t)D * L + (d0 >> 1)), 1u, __ATOMIC_RELAXED, __HIP_MEMORY_SCOPE_AGENT);
#pragma unroll
  for (int t = 0; t < 8; ++t)
    if ((t >> 1) != wq) *(f32x4*)(cb + OFF_RED + (wq * 8 + t) * 1024 + lane * 16) = acc[t];
  __syncthreads();
#pragma unroll
  for (int tt = 0; tt < 2; ++tt) {
    const int t = 2 * wq + tt;
    f32x4 own = f32x4{0.f, 0.f, 0.f, 0.f};
#pragma unroll
    for (int t2 = 0; t2 < 8; ++t2)
      if (t2 == t) own = acc[t2];
    f32x4 s = f32x4{0.f, 0.f, 0.f, 0.f};
#pragma unroll
    for (int sw = 0; sw < 4; ++sw) {
      const f32x4 o = *(const f32x4*)(cb + OFF_RED + (sw * 8 + t) * 1024 + lane * 16);
      s += (sw == wq) ? own : o;
    }
    const int l = 256 * t + 16 * r + 4 * g;
    const u32x2 uu = *(const u32x2*)(cb + 32 + 2 * l);
    const float a0 = bfr(s[0]) + bfr(lo_f(uu[0]) * dsv), a1 = bfr(s[1]) + bfr(hi_f(uu[0]) * dsv);
    const float a2 = bfr(s[2]) + bfr(lo_f(uu[1]) * dsv), a3 = bfr(s[3]) + bfr(hi_f(uu[1]) * dsv);
    const u32x4 gr = {pk2(a0, a1), A_TAG, pk2(a2, a3), A_TAG};
    asm volatile("global_store_dwordx4 %0, %1, off sc1\n\ts_nop 1" ::"v"(aG + (size_t)d * L + l), "v"(gr) : "memory");
  }
}

constexpr int F2_NS = 5;
constexpr int F2_STAGE = 16384;
constexpr int F2_NST = 22;

struct F2Ctx {
  const __bf16 *h, *wd;
  unsigned avoff, bvoff, bvoff_last, lbase;
  int w, kh;
  int aoff[2], boff[2];
  const unsigned* asrc;
  mutable u32x4 x0, x1;
};
__device__ __forceinline__ void f2_stage(const F2Ctx& c, int st) {
  const unsigned base = c.lbase + (st % F2_NS) * F2_STAGE;
  glds16(c.h + st * 64, c.avoff, base + c.w * 1024);
  glds16(c.wd + (size_t)st * 64 * D, st == F2_NST - 1 ? c.bvoff_last : c.bvoff, base + 8192 + c.w * 1024);
}
template <int ST>
__device__ __forceinline__ void f2_loop(const F2Ctx& c, const char* smem, bf16x8 (&ao)[2], bf16x8 (&bo)[2],
                                        f32x4 (&acc)[2][2]) {
  if constexpr (ST < F2_NST) {
    constexpr int rem = (F2_NST - 1 - ST) < (F2_NS - 2) ? (F2_NST - 1 - ST) : (F2_NS - 2);
    wait_vm<2 * rem>();
    WAIT_LGKM0();
    __builtin_amdgcn_s_barrier();
    asm volatile("" ::: "memory");
    if constexpr (ST + F2_NS - 1 < F2_NST) f2_stage(c, ST + F2_NS - 1);
    if constexpr (ST == F2_NST - 3)
      asm volatile("global_load_dwordx4 %0, %2, off sc1\n\tglobal_load_dwordx4 %1, %3, off sc1"
                   : "=&v"(c.x0), "=&v"(c.x1) : "v"(c.asrc), "v"(c.asrc + 16 * L) : "memory");
    const char* base = smem + (ST % F2_NS) * F2_STAGE;
    bf16x8 an[2], bn[2];
#pragma unroll
    for (int mt = 0; mt < 2; ++mt) an[mt] = *(const bf16x8*)(base + c.aoff[mt]);
#pragma unroll
    for (int nt = 0; nt < 2; ++nt) bn[nt] = tr_frag(base, c.boff[nt], c.boff[nt] + 512);
    __builtin_amdgcn_sched_barrier(0);
    if constexpr (ST > 0) {
#pragma unroll
      for (int mt = 0; mt < 2; ++mt)
#pragma unroll
        for (int nt = 0; nt < 2; ++nt) acc[mt][nt] = mfma16(ao[mt], bo[nt], acc[mt][nt]);
    }
    __builtin_amdgcn_sched_barrier(0);
    f2_loop<ST + 1>(c, smem, an, bn, acc);
  } else {
    if (c.kh == 0) {
#pragma unroll
      for (int mt = 0; mt < 2; ++mt)
#pragma unroll
        for (int nt = 0; nt < 2; ++nt) acc[mt][nt] = mfma16(ao[mt], bo[nt], acc[mt][nt]);
    }
  }
}

__device__ __forceinline__ void ffn2_block(char* smem, int row0, int cbk, const __bf16* __restrict__ h,
                                           const __bf16* __restrict__ wd, const unsigned* __restrict__ aG,
                                           const float* __restrict__ gin, __bf16* __restrict__ out) {
  const int col0 = 64 * cbk;
  const int tid = threadIdx.x, lane = tid & 63, w = __builtin_amdgcn_readfirstlane(tid >> 6);
  const int r = lane & 15, g = lane >> 4, q = (lane >> 2) & 3, p = lane & 3;
  const int kh = w >> 2, wm = (w >> 1) & 1, wn = w & 1;
  __builtin_amdgcn_s_setprio(PRIO_F2);
  F2Ctx c;
  c.h = h; c.wd = wd; c.w = w; c.kh = kh; c.lbase = lds_addr_of(smem);
  {
    const int row = 8 * w + (lane >> 3), ch = (lane & 7) ^ ((row >> 1) & 7);
    c.avoff = (unsigned)(((row0 + row) * HP + ch * 8) * 2);
    const int bk = 8 * w + (lane >> 3);
    const int pos = lane & 7, fp = ((bk >> 1) & 1) | (((bk >> 3) & 1) << 1);
    const int cc = col0 + 16 * ((pos >> 1) ^ fp) + 8 * (pos & 1);
    c.bvoff = (unsigned)((bk * D + cc) * 2);
    c.bvoff_last = (unsigned)(((bk > 31 ? 31 : bk) * D + cc) * 2);
  }
#pragma unroll
  for (int mt = 0; mt < 2; ++mt) {
    const int row = 32 * wm + 16 * mt + r, ch = 4 * kh + g;
    c.aoff[mt] = row * 128 + ((ch ^ ((row >> 1) & 7)) << 4);
  }
  const int fpl = (q >> 1) | ((g & 1) << 1);
#pragma unroll
  for (int nt = 0; nt < 2; ++nt) c.boff[nt] = 8192 + (32 * kh + 8 * g + q) * 128 + (((2 * wn + nt) ^ fpl) << 5) + 8 * p;

  f32x4 acc[2][2];
#pragma unroll
  for (int mt = 0; mt < 2; ++mt)
#pragma unroll
    for (int nt = 0; nt < 2; ++nt) acc[mt][nt] = f32x4{0.f, 0.f, 0.f, 0.f};

#pragma unroll
  for (int st = 0; st < F2_NS - 1; ++st) f2_stage(c, st);
  const int lr = 32 * wm + 16 * kh + 4 * g;
  const f32x4 gv = *(const f32x4*)(gin + row0 + lr);
  c.asrc = aG + (size_t)(col0 + 32 * wn + r) * L + row0 + lr;
  bf16x8 a0[2], b0[2];
  f2_loop<0>(c, smem, a0, b0, acc);
  WAIT_LGKM0();
  __builtin_amdgcn_s_barrier();
  asm volatile("" ::: "memory");
  asm volatile("s_waitcnt vmcnt(0)" : "+v"(c.x0), "+v"(c.x1)::"memory");
  const bool early = __all(c.x0[1] == A_TAG && c.x0[3] == A_TAG && c.x1[1] == A_TAG && c.x1[3] == A_TAG);
  if (w == 0 && !early) {
    const gu32* fl = (const gu32*)(aG + (size_t)D * L + 32 * cbk + (lane & 31));
    for (int spin = 0; spin < (1 << 22); ++spin) {
      const unsigned v = __hip_atomic_load(fl, __ATOMIC_RELAXED, __HIP_MEMORY_SCOPE_AGENT);
      if (__all(v == 1u)) break;
      __builtin_amdgcn_s_sleep(2);
    }
  }
  {
    char* red = smem + (w & 3) * 4096 + kh * 2048;
    const f32x4 o0 = kh ? acc[0][0] : acc[1][0], o1 = kh ? acc[0][1] : acc[1][1];
    *(f32x4*)(red + lane * 16) = o0;
    *(f32x4*)(red + 1024 + lane * 16) = o1;
  }
  __syncthreads();
  unsigned long long av[2];
  {
    const unsigned* src = c.asrc;
    av[0] = (unsigned long long)c.x0[0] | ((unsigned long long)c.x0[2] << 32);
    av[1] = (unsigned long long)c.x1[0] | ((unsigned long long)c.x1[2] << 32);
    for (int spin = 0; !early && spin < (1 << 22); ++spin) {
      u32x4 x0, x1;
      asm volatile("global_load_dwordx4 %0, %2, off sc1\n\tglobal_load_dwordx4 %1, %3, off sc1\n\ts_waitcnt vmcnt(0)"
                   : "=&v"(x0), "=&v"(x1) : "v"(src), "v"(src + 16 * L) : "memory");
      av[0] = (unsigned long long)x0[0] | ((unsigned long long)x0[2] << 32);
      av[1] = (unsigned long long)x1[0] | ((unsigned long long)x1[2] << 32);
      const bool ok = x0[1] == A_TAG && x0[3] == A_TAG && x1[1] == A_TAG && x1[3] == A_TAG;
      if (__all(ok)) break;
      __builtin_amdgcn_s_sleep(4);
    }
  }
  constexpr int OT = 16384, OTP = 144;
  {
    const char* red = smem + (w & 3) * 4096 + (1 - kh) * 2048;
#pragma unroll
    for (int nt = 0; nt < 2; ++nt) {
      const f32x4 o = *(const f32x4*)(red + nt * 1024 + lane * 16);
      const f32x4 own = kh ? acc[1][nt] : acc[0][nt];
      const f32x4 s = kh ? (o + own) : (own + o);
      const unsigned alo = (unsigned)av[nt], ahi = (unsigned)(av[nt] >> 32);
      const float a[4] = {lo_f(alo), hi_f(alo), lo_f(ahi), hi_f(ahi)};
#pragma unroll
      for (int e = 0; e < 4; ++e) {
        const float o2 = bfr(gv[e] * a[e]) + bfr(bfr(1.f - gv[e]) * bfr(s[e]));
        *(__bf16*)(smem + OT + (lr + e) * OTP + (32 * wn + 16 * nt + r) * 2) = (__bf16)o2;
      }
    }
  }
  __syncthreads();
  {
    const int row = tid >> 3, c8 = tid & 7;
    const u32x4 v = *(const u32x4*)(smem + OT + row * OTP + c8 * 16);
    *(u32x4*)(out + (size_t)(row0 + row) * D + col0 + c8 * 8) = v;
  }
}

NOPK __global__ __launch_bounds__(512, 4) void k2(const __bf16* __restrict__ h, const __bf16* __restrict__ wd,
                                             const __bf16* __restrict__ uT, const unsigned* __restrict__ kc,
                                             const __bf16* __restrict__ dsk, unsigned* __restrict__ aG,
                                             const float* __restrict__ gin, __bf16* __restrict__ out) {
  extern __shared__ __attribute__((aligned(16))) char smem[];
  const int b = blockIdx.x, xcd = b & 7, i = (b & 255) >> 3;
  if (b < 256) {
    const int pair = 32 * xcd + i;
    conv_block(smem, 2 * pair, uT, kc, dsk, aG);
  } else {
    ffn2_block(smem, 64 * (4 * xcd + (i & 3)), i >> 2, h, wd, aG, gin, out);
  }
}

extern "C" void kernel_launch(void* const* d_in, const int* in_sizes, int n_in, void* d_out, int out_size,
                              void* d_ws, size_t ws_size, hipStream_t stream) {
  const __bf16* u = (const __bf16*)d_in[0];
  const __bf16* lar = (const __bf16*)d_in[1];
  const __bf16* aim = (const __bf16*)d_in[2];
  const __bf16* cre = (const __bf16*)d_in[3];
  const __bf16* cim = (const __bf16*)d_in[4];
  const __bf16* ldt = (const __bf16*)d_in[5];
  const __bf16* dsk = (const __bf16*)d_in[6];
  const __bf16* wg = (const __bf16*)d_in[8];
  const __bf16* wu = (const __bf16*)d_in[9];
  const __bf16* wd = (const __bf16*)d_in[10];
  const __bf16* gw = (const __bf16*)d_in[11];
  char* ws = (char*)d_ws;
  __bf16* h = (__bf16*)ws;
  ws += (size_t)L * HP * 2;
  unsigned* aG = (unsigned*)ws;
  ws += (size_t)D * L * 4 + 1024;
  __bf16* uT = (__bf16*)ws;
  ws += (size_t)D * L * 2;
  unsigned* kc = (unsigned*)ws;
  ws += (size_t)D * KC_PITCH * 4;
  float* gout = (float*)ws;
  (void)hipFuncSetAttribute((const void*)k1, hipFuncAttributeMaxDynamicSharedMemorySize, LDS_BYTES);
  (void)hipFuncSetAttribute((const void*)k2, hipFuncAttributeMaxDynamicSharedMemorySize, LDS_BYTES);
  hipLaunchKernelGGL(k1, dim3(512), dim3(512), LDS_BYTES, stream, u, lar, aim, cre, cim, ldt, wg, wu, gw, h, uT, kc,
                     gout, aG, wd);
  hipLaunchKernelGGL(k2, dim3(512), dim3(512), LDS_BYTES, stream, (const __bf16*)h, wd, (const __bf16*)uT,
                     (const unsigned*)kc, dsk, aG, (const float*)gout, (__bf16*)d_out);
}
```

```cpp
#include <hip/hip_runtime.h>
#include <hip/hip_bf16.h>

typedef __attribute__((ext_vector_type(8))) __bf16 bf16x8;
typedef __attribute__((ext_vector_type(2))) __bf16 bf16x2;
typedef __attribute__((ext_vector_type(4))) short s16x4;
typedef __attribute__((ext_vector_type(8))) short s16x8;
typedef __attribute__((ext_vector_type(4))) float f32x4;
typedef __attribute__((ext_vector_type(4))) unsigned u32x4;
typedef __attribute__((ext_vector_type(2))) unsigned u32x2;
typedef __attribute__((address_space(3))) s16x4* lds_s16x4_ptr;
typedef __attribute__((address_space(1))) unsigned gu32;
typedef __attribute__((address_space(1))) unsigned long long gu64;

#define K_LO 1

#define NOPK __attribute__((target("no-packed-fp32-ops")))

#define PRIO_F1 0
#define PRIO_KG 0
#define PRIO_CV 1
#define PRIO_CV2 PRIO_CV
#define PRIO_F2 1

constexpr int L = 2048, D = 512, NS = 128, F = 1376, HP = 1408;
constexpr int LDS_BYTES = 81920;

__device__ __forceinline__ float bf2f(unsigned short v) { return __uint_as_float((unsigned)v << 16); }
__device__ __forceinline__ float bfr(float x) { return (float)(__bf16)x; }
__device__ __forceinline__ unsigned pk2(float a, float b) {
  bf16x2 v = {(__bf16)a, (__bf16)b};
  return __builtin_bit_cast(unsigned, v);
}
__device__ __forceinline__ float lo_f(unsigned w) { return __uint_as_float(w << 16); }
__device__ __forceinline__ float hi_f(unsigned w) { return __uint_as_float(w & 0xffff0000u); }

__device__ __forceinline__ f32x4 mfma16(bf16x8 a, bf16x8 b, f32x4 c) {
  return __builtin_amdgcn_mfma_f32_16x16x32_bf16(a, b, c, 0, 0, 0);
}
__device__ __forceinline__ bf16x8 tr_frag(const char* lds, int off0, int off1) {
  s16x4 a = __builtin_amdgcn_ds_read_tr16_b64_v4i16((lds_s16x4_ptr)(lds + off0));
  s16x4 b = __builtin_amdgcn_ds_read_tr16_b64_v4i16((lds_s16x4_ptr)(lds + off1));
  s16x8 v = {a[0], a[1], a[2], a[3], b[0], b[1], b[2], b[3]};
  return __builtin_bit_cast(bf16x8, v);
}
__device__ __forceinline__ void glds16(const void* sbase, unsigned voff, unsigned lds_addr) {
  asm volatile("s_mov_b32 m0, %2\n\ts_nop 0\n\tglobal_load_lds_dwordx4 %0, %1" ::"v"(voff), "s"(sbase), "s"(lds_addr)
               : "memory", "m0");
}
__device__ __forceinline__ void glds16v(const void* vptr, unsigned lds_addr) {
  asm volatile("s_mov_b32 m0, %1\n\ts_nop 0\n\tglobal_load_lds_dwordx4 %0, off" ::"v"(vptr), "s"(lds_addr)
               : "memory", "m0");
}
__device__ __forceinline__ unsigned lds_addr_of(const char* p) {
  return (unsigned)(uintptr_t)(const __attribute__((address_space(3))) char*)p;
}
#define WAIT_VM(n) asm volatile("s_waitcnt vmcnt(" #n ")" ::: "memory")
#define WAIT_LGKM0() asm volatile("s_waitcnt lgkmcnt(0)" ::: "memory")
template <int N>
__device__ __forceinline__ void wait_vm() {
  asm volatile("s_waitcnt vmcnt(%0)" ::"i"(N) : "memory");
}

constexpr int F1_B = 32768;

struct F1Ctx {
  const __bf16 *x, *wg, *wu, *wmix;
  unsigned voffA[2], voffB0, voffB2, lbase;
  int w;
  int aoff[2], boff[3];
};
__device__ __forceinline__ void f1_stage_a(const F1Ctx& c, int m) {
  const unsigned ab = c.lbase + (m & 1) * 16384 + c.w * 1024;
  glds16(c.x + m * 64, c.voffA[0], ab);
  glds16(c.x + m * 64, c.voffA[1], ab + 8192);
}
template <bool G2>
__device__ __forceinline__ void f1_stage_b(const F1Ctx& c, int hs) {
  const unsigned bb = c.lbase + F1_B + (hs & 3) * 12288 + c.w * 1024;
  if constexpr (!G2) {
    glds16(c.wg + (size_t)hs * 32 * F, c.voffB0, bb);
    glds16(c.wu + (size_t)hs * 32 * F, c.voffB2, bb + 8192);
  } else {
    glds16v(c.wmix + (size_t)hs * 32 * F, bb);
  }
}
constexpr int f1_allowed(int hs, int nb) {
  int n = 0;
  if ((hs & 1) == 0 && hs >= 2) {
    if (hs + 1 < 16) n += nb;
    if (hs + 2 < 16) n += nb;
  } else {
    if (hs + 1 < 16) n += nb;
    if (hs >= 1 && ((hs - 1) & 1) == 0 && (hs + 1) / 2 < 8) n += 2;
    if (hs + 2 < 16) n += nb;
  }
  return n;
}
template <int HS, bool G2>
__device__ __forceinline__ void f1_loop(const F1Ctx& c, const char* smem, bf16x8 (&af)[4], bf16x8 (&bq)[3],
                                        f32x4 (&acc)[4][3]) {
  if constexpr (HS < 16) {
    if constexpr (HS > 0) {
#pragma unroll
      for (int j = 0; j < 3; ++j) {
        acc[0][j] = mfma16(bq[j], af[0], acc[0][j]);
        acc[1][j] = mfma16(bq[j], af[1], acc[1][j]);
      }
    }
    __builtin_amdgcn_sched_barrier(0);
    wait_vm<f1_allowed(HS, G2 ? 1 : 2)>();
    WAIT_LGKM0();
    __builtin_amdgcn_s_barrier();
    asm volatile("" ::: "memory");
    const char* ab = smem + ((HS >> 1) & 1) * 16384 + c.aoff[HS & 1];
    const char* bbase = smem + (HS & 3) * 12288;
    __builtin_amdgcn_sched_barrier(0);
    af[0] = *(const bf16x8*)(ab);
    af[1] = *(const bf16x8*)(ab + 2048);
#pragma unroll
    for (int j = 0; j < 3; ++j) {
      if constexpr (HS > 0) {
        acc[2][j] = mfma16(bq[j], af[2], acc[2][j]);
        acc[3][j] = mfma16(bq[j], af[3], acc[3][j]);
      }
      __builtin_amdgcn_sched_barrier(0);
      if (j > 0) bq[j - 1] = tr_frag(bbase, c.boff[j - 1], c.boff[j - 1] + 512);
      else {
        if constexpr ((HS & 1) == 0 && (HS + 2) / 2 < 8) f1_stage_a(c, (HS + 2) / 2);
        if constexpr (HS + 3 < 16) f1_stage_b<G2>(c, HS + 3);
      }
      __builtin_amdgcn_sched_barrier(0);
    }
    bq[2] = tr_frag(bbase, c.boff[2], c.boff[2] + 512);
    af[2] = *(const bf16x8*)(ab + 4096);
    af[3] = *(const bf16x8*)(ab + 6144);
    f1_loop<HS + 1, G2>(c, smem, af, bq, acc);
  } else {
#pragma unroll
    for (int j = 0; j < 3; ++j)
#pragma unroll
      for (int mt = 0; mt < 4; ++mt) acc[mt][j] = mfma16(bq[j], af[mt], acc[mt][j]);
  }
}

__device__ __forceinline__ void ffn1_block(char* smem, int rb, int cb, const __bf16* __restrict__ x,
                                           const __bf16* __restrict__ wg, const __bf16* __restrict__ wu,
                                           __bf16* __restrict__ h) {
  const int tid = threadIdx.x, lane = tid & 63, w = __builtin_amdgcn_readfirstlane(tid >> 6);
  const int row0 = rb * 128, f0 = cb * 96;
  const int wm = w >> 2, sel = (w >> 1) & 1, wn = w & 1;
  const int r = lane & 15, g = lane >> 4, q = (lane >> 2) & 3, p = lane & 3;
  __builtin_amdgcn_s_setprio(PRIO_F1);
  F1Ctx c;
  c.x = x; c.wg = wg; c.wu = wu; c.w = w; c.lbase = lds_addr_of(smem);
  {
#pragma unroll
    for (int i = 0; i < 2; ++i) {
      const int row = 64 * i + 8 * w + (lane >> 3), ch = (lane & 7) ^ ((row >> 1) & 7);
      c.voffA[i] = (unsigned)(((row0 + row) * D + ch * 8) * 2);
    }
    const int kk = 8 * (w & 3) + (lane >> 3), pos = lane & 7;
    const int fp = ((kk >> 1) & 1) | (((kk >> 3) & 1) << 1);
    const int i = (pos >> 1) ^ fp;
    int c0 = f0 + 16 * i + 8 * (pos & 1);
    int c1 = f0 + 16 * ((4 + i) % 6) + 8 * (pos & 1);
    int c2 = f0 + 16 * (2 + i) + 8 * (pos & 1);
    c0 = c0 > F - 8 ? F - 8 : c0;
    c1 = c1 > F - 8 ? F - 8 : c1;
    c2 = c2 > F - 8 ? F - 8 : c2;
    c.voffB0 = (unsigned)((kk * F + c0) * 2);
    c.voffB2 = (unsigned)((kk * F + c2) * 2);
    c.wmix = (i < 2 ? wg : wu) + kk * F + c1;
  }
#pragma unroll
  for (int ks = 0; ks < 2; ++ks)
    c.aoff[ks] = (64 * wm + r) * 128 + (((4 * ks + g) ^ ((r >> 1) & 7)) << 4);
  {
    const int fp = (q >> 1) | ((g & 1) << 1);
#pragma unroll
    for (int j = 0; j < 3; ++j) {
      const int T = 6 * sel + 3 * wn + j;
      c.boff[j] = F1_B + (T >> 2) * 4096 + (8 * g + q) * 128 + (((T & 3) ^ fp) << 5) + 8 * p;
      asm volatile("" : "+v"(c.boff[j]));
    }
  }
  f32x4 acc[4][3];
#pragma unroll
  for (int mt = 0; mt < 4; ++mt)
#pragma unroll
    for (int j = 0; j < 3; ++j) acc[mt][j] = f32x4{0.f, 0.f, 0.f, 0.f};
  bf16x8 af[4], bq[3];

  f1_stage_a(c, 0);
  if (w < 4) {
    f1_stage_b<false>(c, 0);
    f1_stage_b<false>(c, 1);
    f1_stage_b<false>(c, 2);
    f1_loop<0, false>(c, smem, af, bq, acc);
  } else {
    f1_stage_b<true>(c, 0);
    f1_stage_b<true>(c, 1);
    f1_stage_b<true>(c, 2);
    f1_loop<0, true>(c, smem, af, bq, acc);
  }

  __syncthreads();
#pragma unroll
  for (int m2 = 0; m2 < 2; ++m2)
#pragma unroll
    for (int j = 0; j < 3; ++j)
      *(f32x4*)(smem + (w * 6 + m2 * 3 + j) * 1024 + lane * 16) = sel ? acc[m2][j] : acc[2 + m2][j];
  __syncthreads();
#pragma unroll
  for (int m2 = 0; m2 < 2; ++m2)
#pragma unroll
    for (int j = 0; j < 3; ++j) {
      const int f = f0 + 16 * (3 * wn + j);
      if (f < F) {
        const f32x4 o4 = *(const f32x4*)(smem + ((w ^ 2) * 6 + m2 * 3 + j) * 1024 + lane * 16);
        const f32x4 own = sel ? acc[2 + m2][j] : acc[m2][j];
        const f32x4 g4 = sel ? o4 : own, u4 = sel ? own : o4;
        float hv[4];
#pragma unroll
        for (int e = 0; e < 4; ++e) {
          const float gv = g4[e];
          const float t = gv * __builtin_fmaf(gv * gv, -0.10294324f, -2.3022082f);
          hv[e] = (gv * u4[e]) * __builtin_amdgcn_rcpf(1.f + __builtin_amdgcn_exp2f(t));
        }
        u32x2 o = {pk2(hv[0], hv[1]), pk2(hv[2], hv[3])};
        *(u32x2*)(h + (size_t)(row0 + 64 * wm + 32 * sel + 16 * m2 + r) * HP + f + 4 * g) = o;
      }
    }
}

__device__ __forceinline__ void gate_block(char* smem, int rb, const __bf16* __restrict__ x,
                                           const __bf16* __restrict__ gw, float* __restrict__ gout,
                                           __bf16* __restrict__ uT) {
  const int tid = threadIdx.x, lane = tid & 63, w = __builtin_amdgcn_readfirstlane(tid >> 6);
  const int g = lane >> 4, q = (lane >> 2) & 3, p = lane & 3;
  const unsigned lbase = lds_addr_of(smem);
  const int grow = tid >> 3, gpart = tid & 7, gf = (grow & 3) | (((grow >> 3) & 1) << 2);
  u32x4 gwv[8];
#pragma unroll
  for (int i = 0; i < 8; ++i) gwv[i] = *(const u32x4*)(gw + ((gpart ^ gf) + 8 * (i >> 1)) * 16 + 8 * (i & 1));
#pragma unroll 1
  for (int pass = 0; pass < 2; ++pass) {
    const int l0 = rb * 128 + pass * 64;
#pragma unroll
    for (int i = 0; i < 8; ++i) {
      const int row = 8 * w + i, fr = (row & 3) | (((row >> 3) & 1) << 2);
      glds16(x + (size_t)(l0 + row) * D, (unsigned)((((lane >> 1) ^ fr) * 2 + (lane & 1)) * 16), lbase + row * 1024);
    }
    WAIT_VM(0);
    __syncthreads();
#pragma unroll
    for (int i = 0; i < 8; ++i) {
      const int unit = w * 8 + i, c16 = unit >> 1, half = unit & 1;
      const int ra = 32 * half + 8 * g + q, rb2 = ra + 4;
      const int fa = (ra & 3) | (((ra >> 3) & 1) << 2);
      const bf16x8 v = tr_frag(smem, ra * 1024 + ((c16 ^ fa) << 5) + 8 * p, rb2 * 1024 + ((c16 ^ fa) << 5) + 8 * p);
      *(bf16x8*)(uT + (size_t)(16 * c16 + (lane & 15)) * L + l0 + 32 * half + 8 * g) = v;
    }
    float s = 0.f;
#pragma unroll
    for (int i = 0; i < 8; ++i) {
      const u32x4 xv = *(const u32x4*)(smem + grow * 1024 + (gpart + 8 * (i >> 1)) * 32 + 16 * (i & 1));
#pragma unroll
      for (int e = 0; e < 4; ++e) {
        const unsigned xe = xv[e], we = gwv[i][e];
        s = __builtin_amdgcn_fdot2_f32_bf16(__builtin_bit_cast(bf16x2, xe), __builtin_bit_cast(bf16x2, we), s, false);
      }
    }
    s += __shfl_xor(s, 1);
    s += __shfl_xor(s, 2);
    s += __shfl_xor(s, 4);
    if (gpart == 0) gout[l0 + grow] = bfr(1.f / (1.f + __expf(-bfr(s))));
    __syncthreads();
  }
}

constexpr int KC_PITCH = 768;
constexpr int KG_PITCH = 160;
constexpr int KG_ARR = 16 * KG_PITCH;
constexpr int KG_TAB = 8 * 2 * KG_ARR;
typedef __attribute__((ext_vector_type(2))) float f32x2;

__device__ __forceinline__ f32x2 cexp(float zr, float zi, float p) {
  const float e = __builtin_amdgcn_exp2f(zr * p);
  const float ph = __builtin_amdgcn_fractf(zi * p);
  return f32x2{e * __builtin_amdgcn_cosf(ph), e * __builtin_amdgcn_sinf(ph)};
}
__device__ __forceinline__ f32x2 cmul(f32x2 a, f32x2 b) {
  return f32x2{a[0] * b[0] - a[1] * b[1], a[0] * b[1] + a[1] * b[0]};
}
__device__ __forceinline__ void kgen_block(char* smem, int d0, const __bf16* __restrict__ lar,
                                           const __bf16* __restrict__ aim, const __bf16* __restrict__ cre,
                                           const __bf16* __restrict__ cim, const __bf16* __restrict__ ldt,
                                           unsigned* __restrict__ kc, unsigned* __restrict__ aG,
                                           const __bf16* __restrict__ wd) {
  const int tid = threadIdx.x, lane = tid & 63, w = __builtin_amdgcn_readfirstlane(tid >> 6);
  unsigned pf_dummy = 0;
  if (tid < 43)
    asm volatile("global_load_dword %0, %1, off" : "=v"(pf_dummy) : "v"(wd + (size_t)((d0 >> 1) * 43 + tid) * 64) : "memory");
  {
    u32x4* z = (u32x4*)(aG + (size_t)d0 * L) + tid;
    z[0] = u32x4{0u, 0u, 0u, 0u};
    z[512] = u32x4{0u, 0u, 0u, 0u};
    if (tid == 0) aG[(size_t)D * L + (d0 >> 1)] = 0u;
  }
  const int ch = w >> 2, wq = w & 3, tc = tid & 255;
  const int r = lane & 15, g = lane >> 4, hh = lane >> 5;
  char* wb = smem + w * 2 * KG_ARR;
  char* tb = smem + KG_TAB + ch * (4 * 656 * 4);
  __builtin_amdgcn_s_setprio(PRIO_KG);
  float zr, zi;
  f32x2 cc;
  {
    const int d = d0 + ch, n = 32 * wq + (lane & 31);
    const float dt = bfr(expf((float)ldt[d]));
    const float ar = -bfr(expf((float)lar[d * NS + n]));
    zr = (ar * dt) * 1.4426950408889634f;
    zi = ((float)aim[d * NS + n] * dt) * 0.15915494309189535f;
    cc = f32x2{2.f * ((float)cre[d * NS + n] * dt), 2.f * ((float)cim[d * NS + n] * dt)};
  }
  const float fh = (float)(1 + hh);
  const f32x2 e128 = cexp(zr, zi, 128.f), e24 = cexp(zr, zi, 2.f * fh);
  f32x2 v[4], st[4];
  v[0] = cexp(zr, -zi, 64.f * hh);                 st[0] = cexp(zr, -zi, 8.f);
  v[1] = hh ? cmul(cc, e128) : cc;                 st[1] = cexp(zr, zi, 1.f);
  v[2] = cmul(cc, cexp(zr, zi, 256.f * fh));       st[2] = hh ? e128 : e24;
  v[3] = cmul(cc, cexp(zr, zi, 1024.f * fh));      st[3] = e128;
  const f32x2 v24 = cmul(v[2], hh ? e24 : e128);
  unsigned vh[4][8], vl[4][8];
#pragma unroll
  for (int i = 0; i < 8; ++i)
#pragma unroll
    for (int t = 0; t < 4; ++t) {
      vh[t][i] = pk2(v[t][0], v[t][1]);
      vl[t][i] = pk2(v[t][0] - lo_f(vh[t][i]), v[t][1] - hi_f(vh[t][i]));
      if (t == 2 && i == 3) v[t] = v24;
      else if (i < 7) v[t] = cmul(v[t], st[t]);
    }
  char* wr = wb + (8 * hh) * KG_PITCH + (lane & 31) * 4;
  const char* rd = wb + r * KG_PITCH + g * 16;
  bf16x8 fh_[4][2], fl_[4][2];
#pragma unroll
  for (int t = 0; t < 4; ++t) {
    __builtin_amdgcn_wave_barrier();
#pragma unroll
    for (int i = 0; i < 8; ++i) {
      *(unsigned*)(wr + i * KG_PITCH) = vh[t][i];
      *(unsigned*)(wr + i * KG_PITCH + KG_ARR) = vl[t][i];
    }
    __builtin_amdgcn_wave_barrier();
#pragma unroll
    for (int ss = 0; ss < 2; ++ss) {
      fh_[t][ss] = *(const bf16x8*)(rd + ss * 64);
      fl_[t][ss] = *(const bf16x8*)(rd + ss * 64 + KG_ARR);
    }
  }
  f32x4 acc[3];
#pragma unroll
  for (int m = 0; m < 3; ++m) acc[m] = f32x4{0.f, 0.f, 0.f, 0.f};
#pragma unroll
  for (int ss = 0; ss < 2; ++ss)
#pragma unroll
    for (int m = 0; m < 3; ++m) {
      acc[m] = mfma16(fl_[1 + m][ss], fh_[0][ss], acc[m]);
      acc[m] = mfma16(fh_[1 + m][ss], fl_[0][ss], acc[m]);
      acc[m] = mfma16(fh_[1 + m][ss], fh_[0][ss], acc[m]);
    }
  {
    float* tab = (float*)tb + wq * 656;
    *(f32x4*)(tab + 128 * (g >> 1) + 4 * (g & 1) + 8 * r) = acc[0];
    if (g < 2) {
      *(f32x4*)(tab + 256 + 64 * g + 4 * r) = acc[1];
#pragma unroll
      for (int e = 0; e < 4; ++e) tab[512 + 64 * g + 16 * e + r] = acc[2][e];
    } else {
#pragma unroll
      for (int e = 0; e < 4; ++e) tab[384 + (g & 1) + 32 * e + 2 * r] = acc[1][e];
      if (lane == 32) tab[640] = acc[2][0];
    }
  }
  __syncthreads();
  {
    const float* tab = (const float*)tb;
    unsigned* kd = kc + (size_t)(d0 + ch) * KC_PITCH;
#pragma unroll
    for (int e = 0; e < 3; ++e) {
      const int id = tc + 256 * e;
      if (id < 641) {
        const float v = (tab[id] + tab[656 + id]) + (tab[2 * 656 + id] + tab[3 * 656 + id]);
        const float vh = bfr(v);
        kd[id] = pk2(vh, v - vh);
      }
    }
  }
  asm volatile("s_waitcnt vmcnt(0)" : "+v"(pf_dummy)::"memory");
}

NOPK __global__ __launch_bounds__(512, 4) void k1(const __bf16* __restrict__ u, const __bf16* __restrict__ lar,
                                             const __bf16* __restrict__ aim, const __bf16* __restrict__ cre,
                                             const __bf16* __restrict__ cim, const __bf16* __restrict__ ldt,
                                             const __bf16* __restrict__ wg, const __bf16* __restrict__ wu,
                                             const __bf16* __restrict__ gw, __bf16* __restrict__ h,
                                             __bf16* __restrict__ uT, unsigned* __restrict__ kc,
                                             float* __restrict__ gout, unsigned* __restrict__ aG,
                                             const __bf16* __restrict__ wd) {
  extern __shared__ __attribute__((aligned(16))) char smem[];
  const int b = blockIdx.x;
  const int xcd = b & 7, i = (b & 255) >> 3;
  if (b < 256) {
    const int rb = 4 * (xcd >> 1) + (i & 3), cb = 8 * (xcd & 1) + (i >> 2);
    if (cb < 15) ffn1_block(smem, rb, cb, u, wg, wu, h);
    else gate_block(smem, rb, u, gw, gout, uT);
  } else {
    kgen_block(smem, 64 * xcd + 2 * i, lar, aim, cre, cim, ldt, kc, aG, wd);
  }
}

constexpr unsigned A_TAG = 0x5a17c3e9u;
constexpr int UCP = 4160;
constexpr int KARR = (240 + L) * 2;
constexpr int OFF_K = 4 * UCP;
constexpr int OFF_TAB = OFF_K + 2 * KARR;
constexpr int OFF_RED = UCP;
constexpr int CH_BYTES = OFF_RED + 32768;
static_assert(OFF_TAB + 3072 <= CH_BYTES && 2 * CH_BYTES <= LDS_BYTES, "lds");

struct PosLut {
  unsigned short v[L];
  constexpr PosLut() : v() {
    for (int j = 0; j < L; ++j) {
      const int sh = (j >= 256) + (j >= 512) + (j >= 1024);
      int q = j >> sh;
      if (sh) {
        const int rem = j & ((1 << sh) - 1), half = 1 << (sh - 1);
        if (rem > half || (rem == half && (q & 1))) ++q;
      }
      v[j] = (unsigned short)(4 * (128 * sh + q));
    }
  }
};
__device__ const PosLut g_poslut{};

__device__ __forceinline__ void conv_block(char* smem, int d0, const __bf16* __restrict__ uT,
                                           const unsigned* __restrict__ kc, const __bf16* __restrict__ dsk,
                                           unsigned* __restrict__ aG) {
  const int tid = threadIdx.x, lane = tid & 63, w = __builtin_amdgcn_readfirstlane(tid >> 6);
  const int ch = w >> 2, wq = w & 3, tc = tid & 255;
  const int r = lane & 15, g = lane >> 4, q = (lane >> 2) & 3, p = lane & 3;
  char* cb = smem + ch * CH_BYTES;
  const int d = d0 + ch;
  __builtin_amdgcn_s_setprio(3);
  {
    const unsigned lb = lds_addr_of(cb);
    glds16(uT + (size_t)d * L + wq * 512, lane * 16, lb + 32 + wq * 1024);
    if (wq < 3) glds16(kc + (size_t)d * KC_PITCH + wq * 256, lane * 16, lb + OFF_TAB + wq * 1024);
    const u32x4 z = {0u, 0u, 0u, 0u};
    if (tc < 60) *(u32x4*)(cb + OFF_K + (tc >= 30 ? KARR : 0) + (tc % 30) * 16) = z;
    else if (tc < 68) *(u32x4*)(cb + ((tc - 60) >> 1) * UCP + ((tc - 60) & 1) * 16) = z;
  }
  const float dsv = (float)dsk[d];
  const u32x4 lut = *(const u32x4*)(g_poslut.v + 8 * tc);
  WAIT_VM(0);
  __syncthreads();
  {
    const u32x4 pa = *(const u32x4*)(cb + 16 + 16 * tc), pb = *(const u32x4*)(cb + 32 + 16 * tc);
    const u32x4 c1 = {__builtin_amdgcn_alignbit(pb[0], pa[3], 16), __builtin_amdgcn_alignbit(pb[1], pb[0], 16),
                      __builtin_amdgcn_alignbit(pb[2], pb[1], 16), __builtin_amdgcn_alignbit(pb[3], pb[2], 16)};
    const u32x4 c2 = {pa[3], pb[0], pb[1], pb[2]};
    const u32x4 c3 = {__builtin_amdgcn_alignbit(pa[3], pa[2], 16), c1[0], c1[1], c1[2]};
    *(u32x4*)(cb + 1 * UCP + 32 + 16 * tc) = c1;
    *(u32x4*)(cb + 2 * UCP + 32 + 16 * tc) = c2;
    *(u32x4*)(cb + 3 * UCP + 32 + 16 * tc) = c3;
    u32x4 kh, kl;
#pragma unroll
    for (int e = 0; e < 4; ++e) {
      const unsigned t0 = *(const unsigned*)(cb + OFF_TAB + (lut[e] & 0xffffu));
      const unsigned t1 = *(const unsigned*)(cb + OFF_TAB + (lut[e] >> 16));
      kh[e] = __builtin_amdgcn_perm(t1, t0, 0x05040100u);
      kl[e] = __builtin_amdgcn_perm(t1, t0, 0x07060302u);
    }
    *(u32x4*)(cb + OFF_K + 480 + 16 * tc) = kh;
    *(u32x4*)(cb + OFF_K + KARR + 480 + 16 * tc) = kl;
  }
  __syncthreads();
  __builtin_amdgcn_s_setprio(PRIO_CV);
  f32x4 acc[8];
#pragma unroll
  for (int t = 0; t < 8; ++t) acc[t] = f32x4{0.f, 0.f, 0.f, 0.f};
#pragma unroll
  for (int rr = 0; rr < 2; ++rr) {
    if (rr == 1) __builtin_amdgcn_s_setprio(PRIO_CV2);
    const int rho = 2 * wq + rr;
    const int abase = q * UCP + 2 * (16 + 16 + 32 * rho - 8 * g + 4 * p);
    const int bbase = OFF_K + 2 * (240 - 16 - 32 * rho + 16 * r + 8 * g);
    bf16x8 af[8];
#pragma unroll
    for (int s = 0; s < 8; ++s) af[s] = tr_frag(cb, abase + 512 * s, abase + 512 * s - 8);
#pragma unroll
    for (int t = 0; t < 8; ++t) {
      const bf16x8 bh = *(const bf16x8*)(cb + bbase + 512 * t);
#pragma unroll
      for (int s = 0; s + t < 8; ++s) acc[s + t] = mfma16(af[s], bh, acc[s + t]);
      const bf16x8 bl = *(const bf16x8*)(cb + bbase + KARR + 512 * t);
#pragma unroll
      for (int s = 0; s + t < 8; ++s) acc[s + t] = mfma16(af[s], bl, acc[s + t]);
    }
  }
  __builtin_amdgcn_s_setprio(3);
  __syncthreads();
  if (tid == 256) __hip_atomic_store((gu32*)(aG + (size_t)D * L + (d0 >> 1)), 1u, __ATOMIC_RELAXED, __HIP_MEMORY_SCOPE_AGENT);
#pragma unroll
  for (int t = 0; t < 8; ++t)
    if ((t >> 1) != wq) *(f32x4*)(cb + OFF_RED + (wq * 8 + t) * 1024 + lane * 16) = acc[t];
  __syncthreads();
#pragma unroll
  for (int tt = 0; tt < 2; ++tt) {
    const int t = 2 * wq + tt;
    f32x4 own = f32x4{0.f, 0.f, 0.f, 0.f};
#pragma unroll
    for (int t2 = 0; t2 < 8; ++t2)
      if (t2 == t) own = acc[t2];
    f32x4 s = f32x4{0.f, 0.f, 0.f, 0.f};
#pragma unroll
    for (int sw = 0; sw < 4; ++sw) {
      const f32x4 o = *(const f32x4*)(cb + OFF_RED + (sw * 8 + t) * 1024 + lane * 16);
      s += (sw == wq) ? own : o;
    }
    const int l = 256 * t + 16 * r + 4 * g;
    const u32x2 uu = *(const u32x2*)(cb + 32 + 2 * l);
    const float a0 = bfr(s[0]) + bfr(lo_f(uu[0]) * dsv), a1 = bfr(s[1]) + bfr(hi_f(uu[0]) * dsv);
    const float a2 = bfr(s[2]) + bfr(lo_f(uu[1]) * dsv), a3 = bfr(s[3]) + bfr(hi_f(uu[1]) * dsv);
    const u32x4 gr = {pk2(a0, a1), A_TAG, pk2(a2, a3), A_TAG};
    asm volatile("global_store_dwordx4 %0, %1, off sc1\n\ts_nop 1" ::"v"(aG + (size_t)d * L + l), "v"(gr) : "memory");
  }
}

constexpr int F2_NS = 5;
constexpr int F2_STAGE = 16384;
constexpr int F2_NST = 22;

struct F2Ctx {
  const __bf16 *h, *wd;
  unsigned avoff, bvoff, bvoff_last, lbase;
  int w, kh;
  int aoff[2], boff[2];
  const unsigned* asrc;
  mutable u32x4 x0, x1;
};
__device__ __forceinline__ void f2_stage(const F2Ctx& c, int st) {
  const unsigned base = c.lbase + (st % F2_NS) * F2_STAGE;
  glds16(c.h + st * 64, c.avoff, base + c.w * 1024);
  glds16(c.wd + (size_t)st * 64 * D, st == F2_NST - 1 ? c.bvoff_last : c.bvoff, base + 8192 + c.w * 1024);
}
template <int ST>
__device__ __forceinline__ void f2_loop(const F2Ctx& c, const char* smem, bf16x8 (&ao)[2], bf16x8 (&bo)[2],
                                        f32x4 (&acc)[2][2]) {
  if constexpr (ST < F2_NST) {
    constexpr int rem = (F2_NST - 1 - ST) < (F2_NS - 2) ? (F2_NST - 1 - ST) : (F2_NS - 2);
    wait_vm<2 * rem>();
    WAIT_LGKM0();
    __builtin_amdgcn_s_barrier();
    asm volatile("" ::: "memory");
    if constexpr (ST + F2_NS - 1 < F2_NST) f2_stage(c, ST + F2_NS - 1);
    if constexpr (ST == F2_NST - 3)
      asm volatile("global_load_dwordx4 %0, %2, off sc1\n\tglobal_load_dwordx4 %1, %3, off sc1"
                   : "=&v"(c.x0), "=&v"(c.x1) : "v"(c.asrc), "v"(c.asrc + 16 * L) : "memory");
    const char* base = smem + (ST % F2_NS) * F2_STAGE;
    bf16x8 an[2], bn[2];
#pragma unroll
    for (int mt = 0; mt < 2; ++mt) an[mt] = *(const bf16x8*)(base + c.aoff[mt]);
#pragma unroll
    for (int nt = 0; nt < 2; ++nt) bn[nt] = tr_frag(base, c.boff[nt], c.boff[nt] + 512);
    __builtin_amdgcn_sched_barrier(0);
    if constexpr (ST > 0) {
#pragma unroll
      for (int mt = 0; mt < 2; ++mt)
#pragma unroll
        for (int nt = 0; nt < 2; ++nt) acc[mt][nt] = mfma16(ao[mt], bo[nt], acc[mt][nt]);
    }
    __builtin_amdgcn_sched_barrier(0);
    f2_loop<ST + 1>(c, smem, an, bn, acc);
  } else {
    if (c.kh == 0) {
#pragma unroll
      for (int mt = 0; mt < 2; ++mt)
#pragma unroll
        for (int nt = 0; nt < 2; ++nt) acc[mt][nt] = mfma16(ao[mt], bo[nt], acc[mt][nt]);
    }
  }
}

__device__ __forceinline__ void ffn2_block(char* smem, int row0, int cbk, const __bf16* __restrict__ h,
                                           const __bf16* __restrict__ wd, const unsigned* __restrict__ aG,
                                           const float* __restrict__ gin, __bf16* __restrict__ out) {
  const int col0 = 64 * cbk;
  const int tid = threadIdx.x, lane = tid & 63, w = __builtin_amdgcn_readfirstlane(tid >> 6);
  const int r = lane & 15, g = lane >> 4, q = (lane >> 2) & 3, p = lane & 3;
  const int kh = w >> 2, wm = (w >> 1) & 1, wn = w & 1;
  __builtin_amdgcn_s_setprio(PRIO_F2);
  F2Ctx c;
  c.h = h; c.wd = wd; c.w = w; c.kh = kh; c.lbase = lds_addr_of(smem);
  {
    const int row = 8 * w + (lane >> 3), ch = (lane & 7) ^ ((row >> 1) & 7);
    c.avoff = (unsigned)(((row0 + row) * HP + ch * 8) * 2);
    const int bk = 8 * w + (lane >> 3);
    const int pos = lane & 7, fp = ((bk >> 1) & 1) | (((bk >> 3) & 1) << 1);
    const int cc = col0 + 16 * ((pos >> 1) ^ fp) + 8 * (pos & 1);
    c.bvoff = (unsigned)((bk * D + cc) * 2);
    c.bvoff_last = (unsigned)(((bk > 31 ? 31 : bk) * D + cc) * 2);
  }
#pragma unroll
  for (int mt = 0; mt < 2; ++mt) {
    const int row = 32 * wm + 16 * mt + r, ch = 4 * kh + g;
    c.aoff[mt] = row * 128 + ((ch ^ ((row >> 1) & 7)) << 4);
  }
  const int fpl = (q >> 1) | ((g & 1) << 1);
#pragma unroll
  for (int nt = 0; nt < 2; ++nt) c.boff[nt] = 8192 + (32 * kh + 8 * g + q) * 128 + (((2 * wn + nt) ^ fpl) << 5) + 8 * p;

  f32x4 acc[2][2];
#pragma unroll
  for (int mt = 0; mt < 2; ++mt)
#pragma unroll
    for (int nt = 0; nt < 2; ++nt) acc[mt][nt] = f32x4{0.f, 0.f, 0.f, 0.f};

#pragma unroll
  for (int st = 0; st < F2_NS - 1; ++st) f2_stage(c, st);
  const int lr = 32 * wm + 16 * kh + 4 * g;
  const f32x4 gv = *(const f32x4*)(gin + row0 + lr);
  c.asrc = aG + (size_t)(col0 + 32 * wn + r) * L + row0 + lr;
  bf16x8 a0[2], b0[2];
  f2_loop<0>(c, smem, a0, b0, acc);
  WAIT_LGKM0();
  __builtin_amdgcn_s_barrier();
  asm volatile("" ::: "memory");
  asm volatile("s_waitcnt vmcnt(0)" : "+v"(c.x0), "+v"(c.x1)::"memory");
  const bool early = __all(c.x0[1] == A_TAG && c.x0[3] == A_TAG && c.x1[1] == A_TAG && c.x1[3] == A_TAG);
  if (w == 0 && !early) {
    const gu32* fl = (const gu32*)(aG + (size_t)D * L + 32 * cbk + (lane & 31));
    for (int spin = 0; spin < (1 << 22); ++spin) {
      const unsigned v = __hip_atomic_load(fl, __ATOMIC_RELAXED, __HIP_MEMORY_SCOPE_AGENT);
      if (__all(v == 1u)) break;
      __builtin_amdgcn_s_sleep(2);
    }
  }
  {
    char* red = smem + (w & 3) * 4096 + kh * 2048;
    const f32x4 o0 = kh ? acc[0][0] : acc[1][0], o1 = kh ? acc[0][1] : acc[1][1];
    *(f32x4*)(red + lane * 16) = o0;
    *(f32x4*)(red + 1024 + lane * 16) = o1;
  }
  __syncthreads();
  unsigned long long av[2];
  {
    const unsigned* src = c.asrc;
    av[0] = (unsigned long long)c.x0[0] | ((unsigned long long)c.x0[2] << 32);
    av[1] = (unsigned long long)c.x1[0] | ((unsigned long long)c.x1[2] << 32);
    for (int spin = 0; !early && spin < (1 << 22); ++spin) {
      u32x4 x0, x1;
      asm volatile("global_load_dwordx4 %0, %2, off sc1\n\tglobal_load_dwordx4 %1, %3, off sc1\n\ts_waitcnt vmcnt(0)"
                   : "=&v"(x0), "=&v"(x1) : "v"(src), "v"(src + 16 * L) : "memory");
      av[0] = (unsigned long long)x0[0] | ((unsigned long long)x0[2] << 32);
      av[1] = (unsigned long long)x1[0] | ((unsigned long long)x1[2] << 32);
      const bool ok = x0[1] == A_TAG && x0[3] == A_TAG && x1[1] == A_TAG && x1[3] == A_TAG;
      if (__all(ok)) break;
      __builtin_amdgcn_s_sleep(4);
    }
  }
  constexpr int OT = 16384, OTP = 144;
  {
    const char* red = smem + (w & 3) * 4096 + (1 - kh) * 2048;
#pragma unroll
    for (int nt = 0; nt < 2; ++nt) {
      const f32x4 o = *(const f32x4*)(red + nt * 1024 + lane * 16);
      const f32x4 own = kh ? acc[1][nt] : acc[0][nt];
      const f32x4 s = kh ? (o + own) : (own + o);
      const unsigned alo = (unsigned)av[nt], ahi = (unsigned)(av[nt] >> 32);
      const float a[4] = {lo_f(alo), hi_f(alo), lo_f(ahi), hi_f(ahi)};
#pragma unroll
      for (int e = 0; e < 4; ++e) {
        const float o2 = bfr(gv[e] * a[e]) + bfr(bfr(1.f - gv[e]) * bfr(s[e]));
        *(__bf16*)(smem + OT + (lr + e) * OTP + (32 * wn + 16 * nt + r) * 2) = (__bf16)o2;
      }
    }
  }
  __syncthreads();
  {
    const int row = tid >> 3, c8 = tid & 7;
    const u32x4 v = *(const u32x4*)(smem + OT + row * OTP + c8 * 16);
    *(u32x4*)(out + (size_t)(row0 + row) * D + col0 + c8 * 8) = v;
  }
}

NOPK __global__ __launch_bounds__(512, 4) void k2(const __bf16* __restrict__ h, const __bf16* __restrict__ wd,
                                             const __bf16* __restrict__ uT, const unsigned* __restrict__ kc,
                                             const __bf16* __restrict__ dsk, unsigned* __restrict__ aG,
                                             const float* __restrict__ gin, __bf16* __restrict__ out) {
  extern __shared__ __attribute__((aligned(16))) char smem[];
  const int b = blockIdx.x, xcd = b & 7, i = (b & 255) >> 3;
  if (b < 256) {
    const int pair = 32 * xcd + i;
    conv_block(smem, 2 * pair, uT, kc, dsk, aG);
  } else {
    ffn2_block(smem, 64 * (4 * xcd + (i & 3)), i >> 2, h, wd, aG, gin, out);
  }
}

extern "C" void kernel_launch(void* const* d_in, const int* in_sizes, int n_in, void* d_out, int out_size,
                              void* d_ws, size_t ws_size, hipStream_t stream) {
  const __bf16* u = (const __bf16*)d_in[0];
  const __bf16* lar = (const __bf16*)d_in[1];
  const __bf16* aim = (const __bf16*)d_in[2];
  const __bf16* cre = (const __bf16*)d_in[3];
  const __bf16* cim = (const __bf16*)d_in[4];
  const __bf16* ldt = (const __bf16*)d_in[5];
  const __bf16* dsk = (const __bf16*)d_in[6];
  const __bf16* wg = (const __bf16*)d_in[8];
  const __bf16* wu = (const __bf16*)d_in[9];
  const __bf16* wd = (const __bf16*)d_in[10];
  const __bf16* gw = (const __bf16*)d_in[11];
  char* ws = (char*)d_ws;
  __bf16* h = (__bf16*)ws;
  ws += (size_t)L * HP * 2;
  unsigned* aG = (unsigned*)ws;
  ws += (size_t)D * L * 4 + 1024;
  __bf16* uT = (__bf16*)ws;
  ws += (size_t)D * L * 2;
  unsigned* kc = (unsigned*)ws;
  ws += (size_t)D * KC_PITCH * 4;
  float* gout = (float*)ws;
  (void)hipFuncSetAttribute((const void*)k1, hipFuncAttributeMaxDynamicSharedMemorySize, LDS_BYTES);
  (void)hipFuncSetAttribute((const void*)k2, hipFuncAttributeMaxDynamicSharedMemorySize, LDS_BYTES);
  hipLaunchKernelGGL(k1, dim3(512), dim3(512), LDS_BYTES, stream, u, lar, aim, cre, cim, ldt, wg, wu, gw, h, uT, kc,
                     gout, aG, wd);
  hipLaunchKernelGGL(k2, dim3(512), dim3(512), LDS_BYTES, stream, (const __bf16*)h, wd, (const __bf16*)uT,
                     (const unsigned*)kc, dsk, aG, (const float*)gout, (__bf16*)d_out);
}
```

```cpp
#include <hip/hip_runtime.h>
#include <hip/hip_bf16.h>

typedef __attribute__((ext_vector_type(8))) __bf16 bf16x8;
typedef __attribute__((ext_vector_type(2))) __bf16 bf16x2;
typedef __attribute__((ext_vector_type(4))) short s16x4;
typedef __attribute__((ext_vector_type(8))) short s16x8;
typedef __attribute__((ext_vector_type(4))) float f32x4;
typedef __attribute__((ext_vector_type(4))) unsigned u32x4;
typedef __attribute__((ext_vector_type(2))) unsigned u32x2;
typedef __attribute__((address_space(3))) s16x4* lds_s16x4_ptr;
typedef __attribute__((address_space(1))) unsigned gu32;
typedef __attribute__((address_space(1))) unsigned long long gu64;

#define K_LO 1

#define NOPK __attribute__((target("no-packed-fp32-ops")))

#define PRIO_F1 0
#define PRIO_KG 0
#define PRIO_CV 1
#define PRIO_CV2 PRIO_CV
#define PRIO_F2 1

constexpr int L = 2048, D = 512, NS = 128, F = 1376, HP = 1408;
constexpr int LDS_BYTES = 81920;

__device__ __forceinline__ float bf2f(unsigned short v) { return __uint_as_float((unsigned)v << 16); }
__device__ __forceinline__ float bfr(float x) { return (float)(__bf16)x; }
__device__ __forceinline__ unsigned pk2(float a, float b) {
  bf16x2 v = {(__bf16)a, (__bf16)b};
  return __builtin_bit_cast(unsigned, v);
}
__device__ __forceinline__ float lo_f(unsigned w) { return __uint_as_float(w << 16); }
__device__ __forceinline__ float hi_f(unsigned w) { return __uint_as_float(w & 0xffff0000u); }

__device__ __forceinline__ f32x4 mfma16(bf16x8 a, bf16x8 b, f32x4 c) {
  return __builtin_amdgcn_mfma_f32_16x16x32_bf16(a, b, c, 0, 0, 0);
}
__device__ __forceinline__ bf16x8 tr_frag(const char* lds, int off0, int off1) {
  s16x4 a = __builtin_amdgcn_ds_read_tr16_b64_v4i16((lds_s16x4_ptr)(lds + off0));
  s16x4 b = __builtin_amdgcn_ds_read_tr16_b64_v4i16((lds_s16x4_ptr)(lds + off1));
  s16x8 v = {a[0], a[1], a[2], a[3], b[0], b[1], b[2], b[3]};
  return __builtin_bit_cast(bf16x8, v);
}
__device__ __forceinline__ void glds16(const void* sbase, unsigned voff, unsigned lds_addr) {
  asm volatile("s_mov_b32 m0, %2\n\ts_nop 0\n\tglobal_load_lds_dwordx4 %0, %1" ::"v"(voff), "s"(sbase), "s"(lds_addr)
               : "memory", "m0");
}
__device__ __forceinline__ void glds16v(const void* vptr, unsigned lds_addr) {
  asm volatile("s_mov_b32 m0, %1\n\ts_nop 0\n\tglobal_load_lds_dwordx4 %0, off" ::"v"(vptr), "s"(lds_addr)
               : "memory", "m0");
}
__device__ __forceinline__ unsigned lds_addr_of(const char* p) {
  return (unsigned)(uintptr_t)(const __attribute__((address_space(3))) char*)p;
}
#define WAIT_VM(n) asm volatile("s_waitcnt vmcnt(" #n ")" ::: "memory")
#define WAIT_LGKM0() asm volatile("s_waitcnt lgkmcnt(0)" ::: "memory")
template <int N>
__device__ __forceinline__ void wait_vm() {
  asm volatile("s_waitcnt vmcnt(%0)" ::"i"(N) : "memory");
}

constexpr int F1_B = 32768;

struct F1Ctx {
  const __bf16 *x, *wg, *wu, *wmix;
  unsigned voffA[2], voffB, lbase;
  int w;
  int aoff[2], boff[3];
};
__device__ __forceinline__ void f1_stage_a(const F1Ctx& c, int m) {
  const unsigned ab = c.lbase + (m & 1) * 16384 + c.w * 1024;
  glds16(c.x + m * 64, c.voffA[0], ab);
  glds16(c.x + m * 64, c.voffA[1], ab + 8192);
}
template <bool G2>
__device__ __forceinline__ void f1_stage_b(const F1Ctx& c, int hs) {
  const unsigned bb = c.lbase + F1_B + (hs & 3) * 12288 + c.w * 1024;
  if constexpr (!G2) {
    glds16(c.wg + (size_t)hs * 32 * F, c.voffB, bb);
    glds16(c.wu + (size_t)hs * 32 * F, c.voffB, bb + 8192);
  } else {
    glds16v(c.wmix + (size_t)hs * 32 * F, bb);
  }
}
constexpr int f1_allowed(int hs, int nb) {
  int n = 0;
  if ((hs & 1) == 0 && hs >= 2) {
    if (hs + 1 < 16) n += nb;
    if (hs + 2 < 16) n += nb;
  } else {
    if (hs + 1 < 16) n += nb;
    if (hs >= 1 && ((hs - 1) & 1) == 0 && (hs + 1) / 2 < 8) n += 2;
    if (hs + 2 < 16) n += nb;
  }
  return n;
}
template <int HS, bool G2>
__device__ __forceinline__ void f1_loop(const F1Ctx& c, const char* smem, bf16x8 (&af)[4], bf16x8 (&bq)[3],
                                        f32x4 (&acc)[4][3]) {
  if constexpr (HS < 16) {
    if constexpr (HS > 0) {
#pragma unroll
      for (int j = 0; j < 3; ++j) {
        acc[0][j] = mfma16(bq[j], af[0], acc[0][j]);
        acc[1][j] = mfma16(bq[j], af[1], acc[1][j]);
      }
    }
    __builtin_amdgcn_sched_barrier(0);
    wait_vm<f1_allowed(HS, G2 ? 1 : 2)>();
    WAIT_LGKM0();
    __builtin_amdgcn_s_barrier();
    asm volatile("" ::: "memory");
    const char* ab = smem + ((HS >> 1) & 1) * 16384 + c.aoff[HS & 1];
    const char* bbase = smem + (HS & 3) * 12288;
    __builtin_amdgcn_sched_barrier(0);
    af[0] = *(const bf16x8*)(ab);
    af[1] = *(const bf16x8*)(ab + 2048);
#pragma unroll
    for (int j = 0; j < 3; ++j) {
      if constexpr (HS > 0) {
        acc[2][j] = mfma16(bq[j], af[2], acc[2][j]);
        acc[3][j] = mfma16(bq[j], af[3], acc[3][j]);
      }
      __builtin_amdgcn_sched_barrier(0);
      if (j > 0) bq[j - 1] = tr_frag(bbase, c.boff[j - 1], c.boff[j - 1] + 512);
      else {
        if constexpr ((HS & 1) == 0 && (HS + 2) / 2 < 8) f1_stage_a(c, (HS + 2) / 2);
        if constexpr (HS + 3 < 16) f1_stage_b<G2>(c, HS + 3);
      }
      __builtin_amdgcn_sched_barrier(0);
    }
    bq[2] = tr_frag(bbase, c.boff[2], c.boff[2] + 512);
    af[2] = *(const bf16x8*)(ab + 4096);
    af[3] = *(const bf16x8*)(ab + 6144);
    f1_loop<HS + 1, G2>(c, smem, af, bq, acc);
  } else {
#pragma unroll
    for (int j = 0; j < 3; ++j)
#pragma unroll
      for (int mt = 0; mt < 4; ++mt) acc[mt][j] = mfma16(bq[j], af[mt], acc[mt][j]);
  }
}

__device__ __forceinline__ void ffn1_block(char* smem, int rb, int cb, const __bf16* __restrict__ x,
                                           const __bf16* __restrict__ wg, const __bf16* __restrict__ wu,
                                           __bf16* __restrict__ h) {
  const int tid = threadIdx.x, lane = tid & 63, w = __builtin_amdgcn_readfirstlane(tid >> 6);
  const int row0 = rb * 128, f0 = cb * 96;
  const int wm = w >> 2, sel = (w >> 1) & 1, wn = w & 1;
  const int r = lane & 15, g = lane >> 4, q = (lane >> 2) & 3, p = lane & 3;
  __builtin_amdgcn_s_setprio(PRIO_F1);
  F1Ctx c;
  c.x = x; c.wg = wg; c.wu = wu; c.w = w; c.lbase = lds_addr_of(smem);
  {
#pragma unroll
    for (int i = 0; i < 2; ++i) {
      const int row = 64 * i + 8 * w + (lane >> 3), ch = (lane & 7) ^ ((row >> 1) & 7);
      c.voffA[i] = (unsigned)(((row0 + row) * D + ch * 8) * 2);
    }
    const int kk = 8 * (w & 3) + (lane >> 3), pos = lane & 7;
    const int fp = ((kk >> 1) & 1) | (((kk >> 3) & 1) << 1);
    const int i = (pos >> 1) ^ fp;
    const int par = (kk + cb) & 1;
    int c0 = f0 + 16 * (i + 2 * par) + 8 * (pos & 1);
    int c1 = f0 + 16 * ((i & 1) + 4 * (1 - par)) + 8 * (pos & 1);
    c0 = c0 > F - 8 ? F - 8 : c0;
    c1 = c1 > F - 8 ? F - 8 : c1;
    c.voffB = (unsigned)((kk * F + c0) * 2);
    c.wmix = (i < 2 ? wg : wu) + kk * F + c1;
  }
#pragma unroll
  for (int ks = 0; ks < 2; ++ks)
    c.aoff[ks] = (64 * wm + r) * 128 + (((4 * ks + g) ^ ((r >> 1) & 7)) << 4);
  {
    const int fp = (q >> 1) | ((g & 1) << 1);
#pragma unroll
    for (int j = 0; j < 3; ++j) {
      const int t = 3 * wn + j, par = (q + cb) & 1, tt = t - 2 * par;
      const bool whole = tt >= 0 && tt < 4;
      const int sub = whole ? 2 * sel : 1, ps = whole ? tt : 2 * sel + (t & 1);
      c.boff[j] = F1_B + sub * 4096 + (8 * g + q) * 128 + ((ps ^ fp) << 5) + 8 * p;
      asm volatile("" : "+v"(c.boff[j]));
    }
  }
  f32x4 acc[4][3];
#pragma unroll
  for (int mt = 0; mt < 4; ++mt)
#pragma unroll
    for (int j = 0; j < 3; ++j) acc[mt][j] = f32x4{0.f, 0.f, 0.f, 0.f};
  bf16x8 af[4], bq[3];

  f1_stage_a(c, 0);
  if (w < 4) {
    f1_stage_b<false>(c, 0);
    f1_stage_b<false>(c, 1);
    f1_stage_b<false>(c, 2);
    f1_loop<0, false>(c, smem, af, bq, acc);
  } else {
    f1_stage_b<true>(c, 0);
    f1_stage_b<true>(c, 1);
    f1_stage_b<true>(c, 2);
    f1_loop<0, true>(c, smem, af, bq, acc);
  }

  __syncthreads();
#pragma unroll
  for (int m2 = 0; m2 < 2; ++m2)
#pragma unroll
    for (int j = 0; j < 3; ++j)
      *(f32x4*)(smem + (w * 6 + m2 * 3 + j) * 1024 + lane * 16) = sel ? acc[m2][j] : acc[2 + m2][j];
  __syncthreads();
  constexpr int HT = 49152, HTP = 208;
#pragma unroll
  for (int m2 = 0; m2 < 2; ++m2)
#pragma unroll
    for (int j = 0; j < 3; ++j) {
      const int f = f0 + 16 * (3 * wn + j);
      if (f < F) {
        const f32x4 o4 = *(const f32x4*)(smem + ((w ^ 2) * 6 + m2 * 3 + j) * 1024 + lane * 16);
        const f32x4 own = sel ? acc[2 + m2][j] : acc[m2][j];
        const f32x4 g4 = sel ? o4 : own, u4 = sel ? own : o4;
        float hv[4];
#pragma unroll
        for (int e = 0; e < 4; ++e) {
          const float gv = g4[e];
          const float t = gv * __builtin_fmaf(gv * gv, -0.10294324f, -2.3022082f);
          hv[e] = (gv * u4[e]) * __builtin_amdgcn_rcpf(1.f + __builtin_amdgcn_exp2f(t));
        }
        u32x2 o = {pk2(hv[0], hv[1]), pk2(hv[2], hv[3])};
        *(u32x2*)(smem + HT + (64 * wm + 32 * sel + 16 * m2 + r) * HTP + (16 * (3 * wn + j) + 4 * g) * 2) = o;
      }
    }
  __syncthreads();
#pragma unroll
  for (int i = 0; i < 3; ++i) {
    const int idx = tid + 512 * i, row = idx / 12, c8 = idx - row * 12;
    if (f0 + 8 * c8 < F)
      *(u32x4*)(h + (size_t)(row0 + row) * HP + f0 + 8 * c8) = *(const u32x4*)(smem + HT + row * HTP + c8 * 16);
  }
}

__device__ __forceinline__ void gate_block(char* smem, int rb, const __bf16* __restrict__ x,
                                           const __bf16* __restrict__ gw, float* __restrict__ gout,
                                           __bf16* __restrict__ uT) {
  const int tid = threadIdx.x, lane = tid & 63, w = __builtin_amdgcn_readfirstlane(tid >> 6);
  const int g = lane >> 4, q = (lane >> 2) & 3, p = lane & 3;
  const unsigned lbase = lds_addr_of(smem);
  const int grow = tid >> 3, gpart = tid & 7, gf = (grow & 3) | (((grow >> 3) & 1) << 2);
  u32x4 gwv[8];
#pragma unroll
  for (int i = 0; i < 8; ++i) gwv[i] = *(const u32x4*)(gw + ((gpart ^ gf) + 8 * (i >> 1)) * 16 + 8 * (i & 1));
#pragma unroll 1
  for (int pass = 0; pass < 2; ++pass) {
    const int l0 = rb * 128 + pass * 64;
#pragma unroll
    for (int i = 0; i < 8; ++i) {
      const int row = 8 * w + i, fr = (row & 3) | (((row >> 3) & 1) << 2);
      glds16(x + (size_t)(l0 + row) * D, (unsigned)((((lane >> 1) ^ fr) * 2 + (lane & 1)) * 16), lbase + row * 1024);
    }
    WAIT_VM(0);
    __syncthreads();
#pragma unroll
    for (int i = 0; i < 8; ++i) {
      const int unit = w * 8 + i, c16 = unit >> 1, half = unit & 1;
      const int ra = 32 * half + 8 * g + q, rb2 = ra + 4;
      const int fa = (ra & 3) | (((ra >> 3) & 1) << 2);
      const bf16x8 v = tr_frag(smem, ra * 1024 + ((c16 ^ fa) << 5) + 8 * p, rb2 * 1024 + ((c16 ^ fa) << 5) + 8 * p);
      *(bf16x8*)(uT + (size_t)(16 * c16 + (lane & 15)) * L + l0 + 32 * half + 8 * g) = v;
    }
    float s = 0.f;
#pragma unroll
    for (int i = 0; i < 8; ++i) {
      const u32x4 xv = *(const u32x4*)(smem + grow * 1024 + (gpart + 8 * (i >> 1)) * 32 + 16 * (i & 1));
#pragma unroll
      for (int e = 0; e < 4; ++e) {
        const unsigned xe = xv[e], we = gwv[i][e];
        s = __builtin_amdgcn_fdot2_f32_bf16(__builtin_bit_cast(bf16x2, xe), __builtin_bit_cast(bf16x2, we), s, false);
      }
    }
    s += __shfl_xor(s, 1);
    s += __shfl_xor(s, 2);
    s += __shfl_xor(s, 4);
    if (gpart == 0) gout[l0 + grow] = bfr(1.f / (1.f + __expf(-bfr(s))));
    __syncthreads();
  }
}

constexpr int KC_PITCH = 768;
constexpr int KG_PITCH = 160;
constexpr int KG_ARR = 16 * KG_PITCH;
constexpr int KG_TAB = 8 * 2 * KG_ARR;
typedef __attribute__((ext_vector_type(2))) float f32x2;

__device__ __forceinline__ f32x2 cexp(float zr, float zi, float p) {
  const float e = __builtin_amdgcn_exp2f(zr * p);
  const float ph = __builtin_amdgcn_fractf(zi * p);
  return f32x2{e * __builtin_amdgcn_cosf(ph), e * __builtin_amdgcn_sinf(ph)};
}
__device__ __forceinline__ f32x2 cmul(f32x2 a, f32x2 b) {
  return f32x2{a[0] * b[0] - a[1] * b[1], a[0] * b[1] + a[1] * b[0]};
}
__device__ __forceinline__ void kgen_block(char* smem, int d0, const __bf16* __restrict__ lar,
                                           const __bf16* __restrict__ aim, const __bf16* __restrict__ cre,
                                           const __bf16* __restrict__ cim, const __bf16* __restrict__ ldt,
                                           unsigned* __restrict__ kc, unsigned* __restrict__ aG,
                                           const __bf16* __restrict__ wd) {
  const int tid = threadIdx.x, lane = tid & 63, w = __builtin_amdgcn_readfirstlane(tid >> 6);
  unsigned pf_dummy = 0;
  if (tid < 43)
    asm volatile("global_load_dword %0, %1, off" : "=v"(pf_dummy) : "v"(wd + (size_t)((d0 >> 1) * 43 + tid) * 64) : "memory");
  {
    u32x4* z = (u32x4*)(aG + (size_t)d0 * L) + tid;
    z[0] = u32x4{0u, 0u, 0u, 0u};
    z[512] = u32x4{0u, 0u, 0u, 0u};
    if (tid == 0) aG[(size_t)D * L + (d0 >> 1)] = 0u;
  }
  const int ch = w >> 2, wq = w & 3, tc = tid & 255;
  const int r = lane & 15, g = lane >> 4, hh = lane >> 5;
  char* wb = smem + w * 2 * KG_ARR;
  char* tb = smem + KG_TAB + ch * (4 * 656 * 4);
  __builtin_amdgcn_s_setprio(PRIO_KG);
  float zr, zi;
  f32x2 cc;
  {
    const int d = d0 + ch, n = 32 * wq + (lane & 31);
    const float dt = bfr(expf((float)ldt[d]));
    const float ar = -bfr(expf((float)lar[d * NS + n]));
    zr = (ar * dt) * 1.4426950408889634f;
    zi = ((float)aim[d * NS + n] * dt) * 0.15915494309189535f;
    cc = f32x2{2.f * ((float)cre[d * NS + n] * dt), 2.f * ((float)cim[d * NS + n] * dt)};
  }
  const float fh = (float)(1 + hh);
  const f32x2 e128 = cexp(zr, zi, 128.f), e24 = cexp(zr, zi, 2.f * fh);
  f32x2 v[4], st[4];
  v[0] = cexp(zr, -zi, 64.f * hh);                 st[0] = cexp(zr, -zi, 8.f);
  v[1] = hh ? cmul(cc, e128) : cc;                 st[1] = cexp(zr, zi, 1.f);
  v[2] = cmul(cc, cexp(zr, zi, 256.f * fh));       st[2] = hh ? e128 : e24;
  v[3] = cmul(cc, cexp(zr, zi, 1024.f * fh));      st[3] = e128;
  const f32x2 v24 = cmul(v[2], hh ? e24 : e128);
  unsigned vh[4][8], vl[4][8];
#pragma unroll
  for (int i = 0; i < 8; ++i)
#pragma unroll
    for (int t = 0; t < 4; ++t) {
      vh[t][i] = pk2(v[t][0], v[t][1]);
      vl[t][i] = pk2(v[t][0] - lo_f(vh[t][i]), v[t][1] - hi_f(vh[t][i]));
      if (t == 2 && i == 3) v[t] = v24;
      else if (i < 7) v[t] = cmul(v[t], st[t]);
    }
  char* wr = wb + (8 * hh) * KG_PITCH + (lane & 31) * 4;
  const char* rd = wb + r * KG_PITCH + g * 16;
  bf16x8 fh_[4][2], fl_[4][2];
#pragma unroll
  for (int t = 0; t < 4; ++t) {
    __builtin_amdgcn_wave_barrier();
#pragma unroll
    for (int i = 0; i < 8; ++i) {
      *(unsigned*)(wr + i * KG_PITCH) = vh[t][i];
      *(unsigned*)(wr + i * KG_PITCH + KG_ARR) = vl[t][i];
    }
    __builtin_amdgcn_wave_barrier();
#pragma unroll
    for (int ss = 0; ss < 2; ++ss) {
      fh_[t][ss] = *(const bf16x8*)(rd + ss * 64);
      fl_[t][ss] = *(const bf16x8*)(rd + ss * 64 + KG_ARR);
    }
  }
  f32x4 acc[3];
#pragma unroll
  for (int m = 0; m < 3; ++m) acc[m] = f32x4{0.f, 0.f, 0.f, 0.f};
#pragma unroll
  for (int ss = 0; ss < 2; ++ss)
#pragma unroll
    for (int m = 0; m < 3; ++m) {
      acc[m] = mfma16(fl_[1 + m][ss], fh_[0][ss], acc[m]);
      acc[m] = mfma16(fh_[1 + m][ss], fl_[0][ss], acc[m]);
      acc[m] = mfma16(fh_[1 + m][ss], fh_[0][ss], acc[m]);
    }
  {
    float* tab = (float*)tb + wq * 656;
    *(f32x4*)(tab + 128 * (g >> 1) + 4 * (g & 1) + 8 * r) = acc[0];
    if (g < 2) {
      *(f32x4*)(tab + 256 + 64 * g + 4 * r) = acc[1];
#pragma unroll
      for (int e = 0; e < 4; ++e) tab[512 + 64 * g + 16 * e + r] = acc[2][e];
    } else {
#pragma unroll
      for (int e = 0; e < 4; ++e) tab[384 + (g & 1) + 32 * e + 2 * r] = acc[1][e];
      if (lane == 32) tab[640] = acc[2][0];
    }
  }
  __syncthreads();
  {
    const float* tab = (const float*)tb;
    unsigned* kd = kc + (size_t)(d0 + ch) * KC_PITCH;
#pragma unroll
    for (int e = 0; e < 3; ++e) {
      const int id = tc + 256 * e;
      if (id < 641) {
        const float v = (tab[id] + tab[656 + id]) + (tab[2 * 656 + id] + tab[3 * 656 + id]);
        const float vh = bfr(v);
        kd[id] = pk2(vh, v - vh);
      }
    }
  }
  asm volatile("s_waitcnt vmcnt(0)" : "+v"(pf_dummy)::"memory");
}

NOPK __global__ __launch_bounds__(512, 4) void k1(const __bf16* __restrict__ u, const __bf16* __restrict__ lar,
                                             const __bf16* __restrict__ aim, const __bf16* __restrict__ cre,
                                             const __bf16* __restrict__ cim, const __bf16* __restrict__ ldt,
                                             const __bf16* __restrict__ wg, const __bf16* __restrict__ wu,
                                             const __bf16* __restrict__ gw, __bf16* __restrict__ h,
                                             __bf16* __restrict__ uT, unsigned* __restrict__ kc,
                                             float* __restrict__ gout, unsigned* __restrict__ aG,
                                             const __bf16* __restrict__ wd) {
  extern __shared__ __attribute__((aligned(16))) char smem[];
  const int b = blockIdx.x;
  const int xcd = b & 7, i = (b & 255) >> 3;
  if (b < 256) {
    const int rb = 4 * (xcd >> 1) + (i & 3), cb = 8 * (xcd & 1) + (i >> 2);
    if (cb < 15) ffn1_block(smem, rb, cb, u, wg, wu, h);
    else gate_block(smem, rb, u, gw, gout, uT);
  } else {
    kgen_block(smem, 64 * xcd + 2 * i, lar, aim, cre, cim, ldt, kc, aG, wd);
  }
}

constexpr unsigned A_TAG = 0x5a17c3e9u;
constexpr int UCP = 4160;
constexpr int KARR = (240 + L) * 2;
constexpr int OFF_K = 4 * UCP;
constexpr int OFF_TAB = OFF_K + 2 * KARR;
constexpr int OFF_RED = UCP;
constexpr int CH_BYTES = OFF_RED + 32768;
static_assert(OFF_TAB + 3072 <= CH_BYTES && 2 * CH_BYTES <= LDS_BYTES, "lds");

struct PosLut {
  unsigned short v[L];
  constexpr PosLut() : v() {
    for (int j = 0; j < L; ++j) {
      const int sh = (j >= 256) + (j >= 512) + (j >= 1024);
      int q = j >> sh;
      if (sh) {
        const int rem = j & ((1 << sh) - 1), half = 1 << (sh - 1);
        if (rem > half || (rem == half && (q & 1))) ++q;
      }
      v[j] = (unsigned short)(4 * (128 * sh + q));
    }
  }
};
__device__ const PosLut g_poslut{};

__device__ __forceinline__ void conv_block(char* smem, int d0, const __bf16* __restrict__ uT,
                                           const unsigned* __restrict__ kc, const __bf16* __restrict__ dsk,
                                           unsigned* __restrict__ aG) {
  const int tid = threadIdx.x, lane = tid & 63, w = __builtin_amdgcn_readfirstlane(tid >> 6);
  const int ch = w >> 2, wq = w & 3, tc = tid & 255;
  const int r = lane & 15, g = lane >> 4, q = (lane >> 2) & 3, p = lane & 3;
  char* cb = smem + ch * CH_BYTES;
  const int d = d0 + ch;
  __builtin_amdgcn_s_setprio(3);
  {
    const unsigned lb = lds_addr_of(cb);
    glds16(uT + (size_t)d * L + wq * 512, lane * 16, lb + 32 + wq * 1024);
    if (wq < 3) glds16(kc + (size_t)d * KC_PITCH + wq * 256, lane * 16, lb + OFF_TAB + wq * 1024);
    const u32x4 z = {0u, 0u, 0u, 0u};
    if (tc < 60) *(u32x4*)(cb + OFF_K + (tc >= 30 ? KARR : 0) + (tc % 30) * 16) = z;
    else if (tc < 68) *(u32x4*)(cb + ((tc - 60) >> 1) * UCP + ((tc - 60) & 1) * 16) = z;
  }
  const float dsv = (float)dsk[d];
  const u32x4 lut = *(const u32x4*)(g_poslut.v + 8 * tc);
  WAIT_VM(0);
  __syncthreads();
  {
    const u32x4 pa = *(const u32x4*)(cb + 16 + 16 * tc), pb = *(const u32x4*)(cb + 32 + 16 * tc);
    const u32x4 c1 = {__builtin_amdgcn_alignbit(pb[0], pa[3], 16), __builtin_amdgcn_alignbit(pb[1], pb[0], 16),
                      __builtin_amdgcn_alignbit(pb[2], pb[1], 16), __builtin_amdgcn_alignbit(pb[3], pb[2], 16)};
    const u32x4 c2 = {pa[3], pb[0], pb[1], pb[2]};
    const u32x4 c3 = {__builtin_amdgcn_alignbit(pa[3], pa[2], 16), c1[0], c1[1], c1[2]};
    *(u32x4*)(cb + 1 * UCP + 32 + 16 * tc) = c1;
    *(u32x4*)(cb + 2 * UCP + 32 + 16 * tc) = c2;
    *(u32x4*)(cb + 3 * UCP + 32 + 16 * tc) = c3;
    u32x4 kh, kl;
#pragma unroll
    for (int e = 0; e < 4; ++e) {
      const unsigned t0 = *(const unsigned*)(cb + OFF_TAB + (lut[e] & 0xffffu));
      const unsigned t1 = *(const unsigned*)(cb + OFF_TAB + (lut[e] >> 16));
      kh[e] = __builtin_amdgcn_perm(t1, t0, 0x05040100u);
      kl[e] = __builtin_amdgcn_perm(t1, t0, 0x07060302u);
    }
    *(u32x4*)(cb + OFF_K + 480 + 16 * tc) = kh;
    *(u32x4*)(cb + OFF_K + KARR + 480 + 16 * tc) = kl;
  }
  __syncthreads();
  __builtin_amdgcn_s_setprio(PRIO_CV);
  f32x4 acc[8];
#pragma unroll
  for (int t = 0; t < 8; ++t) acc[t] = f32x4{0.f, 0.f, 0.f, 0.f};
#pragma unroll
  for (int rr = 0; rr < 2; ++rr) {
    if (rr == 1) __builtin_amdgcn_s_setprio(PRIO_CV2);
    const int rho = 2 * wq + rr;
    const int abase = q * UCP + 2 * (16 + 16 + 32 * rho - 8 * g + 4 * p);
    const int bbase = OFF_K + 2 * (240 - 16 - 32 * rho + 16 * r + 8 * g);
    bf16x8 af[8];
#pragma unroll
    for (int s = 0; s < 8; ++s) af[s] = tr_frag(cb, abase + 512 * s, abase + 512 * s - 8);
#pragma unroll
    for (int t = 0; t < 8; ++t) {
      const bf16x8 bh = *(const bf16x8*)(cb + bbase + 512 * t);
#pragma unroll
      for (int s = 0; s + t < 8; ++s) acc[s + t] = mfma16(af[s], bh, acc[s + t]);
      const bf16x8 bl = *(const bf16x8*)(cb + bbase + KARR + 512 * t);
#pragma unroll
      for (int s = 0; s + t < 8; ++s) acc[s + t] = mfma16(af[s], bl, acc[s + t]);
    }
  }
  __builtin_amdgcn_s_setprio(3);
  __syncthreads();
  if (tid == 256) __hip_atomic_store((gu32*)(aG + (size_t)D * L + (d0 >> 1)), 1u, __ATOMIC_RELAXED, __HIP_MEMORY_SCOPE_AGENT);
#pragma unroll
  for (int t = 0; t < 8; ++t)
    if ((t >> 1) != wq) *(f32x4*)(cb + OFF_RED + (wq * 8 + t) * 1024 + lane * 16) = acc[t];
  __syncthreads();
#pragma unroll
  for (int tt = 0; tt < 2; ++tt) {
    const int t = 2 * wq + tt;
    f32x4 own = f32x4{0.f, 0.f, 0.f, 0.f};
#pragma unroll
    for (int t2 = 0; t2 < 8; ++t2)
      if (t2 == t) own = acc[t2];
    f32x4 s = f32x4{0.f, 0.f, 0.f, 0.f};
#pragma unroll
    for (int sw = 0; sw < 4; ++sw) {
      const f32x4 o = *(const f32x4*)(cb + OFF_RED + (sw * 8 + t) * 1024 + lane * 16);
      s += (sw == wq) ? own : o;
    }
    const int l = 256 * t + 16 * r + 4 * g;
    const u32x2 uu = *(const u32x2*)(cb + 32 + 2 * l);
    const float a0 = bfr(s[0]) + bfr(lo_f(uu[0]) * dsv), a1 = bfr(s[1]) + bfr(hi_f(uu[0]) * dsv);
    const float a2 = bfr(s[2]) + bfr(lo_f(uu[1]) * dsv), a3 = bfr(s[3]) + bfr(hi_f(uu[1]) * dsv);
    const u32x4 gr = {pk2(a0, a1), A_TAG, pk2(a2, a3), A_TAG};
    asm volatile("global_store_dwordx4 %0, %1, off sc1\n\ts_nop 1" ::"v"(aG + (size_t)d * L + l), "v"(gr) : "memory");
  }
}

constexpr int F2_NS = 5;
constexpr int F2_STAGE = 16384;
constexpr int F2_NST = 22;

struct F2Ctx {
  const __bf16 *h, *wd;
  unsigned avoff, bvoff, bvoff_last, lbase;
  int w, kh;
  int aoff[2], boff[2];
  const unsigned* asrc;
  mutable u32x4 x0, x1;
};
__device__ __forceinline__ void f2_stage(const F2Ctx& c, int st) {
  const unsigned base = c.lbase + (st % F2_NS) * F2_STAGE;
  glds16(c.h + st * 64, c.avoff, base + c.w * 1024);
  glds16(c.wd + (size_t)st * 64 * D, st == F2_NST - 1 ? c.bvoff_last : c.bvoff, base + 8192 + c.w * 1024);
}
template <int ST>
__device__ __forceinline__ void f2_loop(const F2Ctx& c, const char* smem, bf16x8 (&ao)[2], bf16x8 (&bo)[2],
                                        f32x4 (&acc)[2][2]) {
  if constexpr (ST < F2_NST) {
    constexpr int rem = (F2_NST - 1 - ST) < (F2_NS - 2) ? (F2_NST - 1 - ST) : (F2_NS - 2);
    wait_vm<2 * rem>();
    WAIT_LGKM0();
    __builtin_amdgcn_s_barrier();
    asm volatile("" ::: "memory");
    if constexpr (ST + F2_NS - 1 < F2_NST) f2_stage(c, ST + F2_NS - 1);
    if constexpr (ST == F2_NST - 3)
      asm volatile("global_load_dwordx4 %0, %2, off sc1\n\tglobal_load_dwordx4 %1, %3, off sc1"
                   : "=&v"(c.x0), "=&v"(c.x1) : "v"(c.asrc), "v"(c.asrc + 16 * L) : "memory");
    const char* base = smem + (ST % F2_NS) * F2_STAGE;
    bf16x8 an[2], bn[2];
#pragma unroll
    for (int mt = 0; mt < 2; ++mt) an[mt] = *(const bf16x8*)(base + c.aoff[mt]);
#pragma unroll
    for (int nt = 0; nt < 2; ++nt) bn[nt] = tr_frag(base, c.boff[nt], c.boff[nt] + 512);
    __builtin_amdgcn_sched_barrier(0);
    if constexpr (ST > 0) {
#pragma unroll
      for (int mt = 0; mt < 2; ++mt)
#pragma unroll
        for (int nt = 0; nt < 2; ++nt) acc[mt][nt] = mfma16(ao[mt], bo[nt], acc[mt][nt]);
    }
    __builtin_amdgcn_sched_barrier(0);
    f2_loop<ST + 1>(c, smem, an, bn, acc);
  } else {
    if (c.kh == 0) {
#pragma unroll
      for (int mt = 0; mt < 2; ++mt)
#pragma unroll
        for (int nt = 0; nt < 2; ++nt) acc[mt][nt] = mfma16(ao[mt], bo[nt], acc[mt][nt]);
    }
  }
}

__device__ __forceinline__ void ffn2_block(char* smem, int row0, int cbk, const __bf16* __restrict__ h,
                                           const __bf16* __restrict__ wd, const unsigned* __restrict__ aG,
                                           const float* __restrict__ gin, __bf16* __restrict__ out) {
  const int col0 = 64 * cbk;
  const int tid = threadIdx.x, lane = tid & 63, w = __builtin_amdgcn_readfirstlane(tid >> 6);
  const int r = lane & 15, g = lane >> 4, q = (lane >> 2) & 3, p = lane & 3;
  const int kh = w >> 2, wm = (w >> 1) & 1, wn = w & 1;
  __builtin_amdgcn_s_setprio(PRIO_F2);
  F2Ctx c;
  c.h = h; c.wd = wd; c.w = w; c.kh = kh; c.lbase = lds_addr_of(smem);
  {
    const int row = 8 * w + (lane >> 3), ch = (lane & 7) ^ ((row >> 1) & 7);
    c.avoff = (unsigned)(((row0 + row) * HP + ch * 8) * 2);
    const int bk = 8 * w + (lane >> 3);
    const int pos = lane & 7, fp = ((bk >> 1) & 1) | (((bk >> 3) & 1) << 1);
    const int cc = col0 + 16 * ((pos >> 1) ^ fp) + 8 * (pos & 1);
    c.bvoff = (unsigned)((bk * D + cc) * 2);
    c.bvoff_last = (unsigned)(((bk > 31 ? 31 : bk) * D + cc) * 2);
  }
#pragma unroll
  for (int mt = 0; mt < 2; ++mt) {
    const int row = 32 * wm + 16 * mt + r, ch = 4 * kh + g;
    c.aoff[mt] = row * 128 + ((ch ^ ((row >> 1) & 7)) << 4);
  }
  const int fpl = (q >> 1) | ((g & 1) << 1);
#pragma unroll
  for (int nt = 0; nt < 2; ++nt) c.boff[nt] = 8192 + (32 * kh + 8 * g + q) * 128 + (((2 * wn + nt) ^ fpl) << 5) + 8 * p;

  f32x4 acc[2][2];
#pragma unroll
  for (int mt = 0; mt < 2; ++mt)
#pragma unroll
    for (int nt = 0; nt < 2; ++nt) acc[mt][nt] = f32x4{0.f, 0.f, 0.f, 0.f};

#pragma unroll
  for (int st = 0; st < F2_NS - 1; ++st) f2_stage(c, st);
  const int lr = 32 * wm + 16 * kh + 4 * g;
  const f32x4 gv = *(const f32x4*)(gin + row0 + lr);
  c.asrc = aG + (size_t)(col0 + 32 * wn + r) * L + row0 + lr;
  bf16x8 a0[2], b0[2];
  f2_loop<0>(c, smem, a0, b0, acc);
  WAIT_LGKM0();
  __builtin_amdgcn_s_barrier();
  asm volatile("" ::: "memory");
  asm volatile("s_waitcnt vmcnt(0)" : "+v"(c.x0), "+v"(c.x1)::"memory");
  const bool early = __all(c.x0[1] == A_TAG && c.x0[3] == A_TAG && c.x1[1] == A_TAG && c.x1[3] == A_TAG);
  if (w == 0 && !early) {
    const gu32* fl = (const gu32*)(aG + (size_t)D * L + 32 * cbk + (lane & 31));
    for (int spin = 0; spin < (1 << 22); ++spin) {
      const unsigned v = __hip_atomic_load(fl, __ATOMIC_RELAXED, __HIP_MEMORY_SCOPE_AGENT);
      if (__all(v == 1u)) break;
      __builtin_amdgcn_s_sleep(2);
    }
  }
  {
    char* red = smem + (w & 3) * 4096 + kh * 2048;
    const f32x4 o0 = kh ? acc[0][0] : acc[1][0], o1 = kh ? acc[0][1] : acc[1][1];
    *(f32x4*)(red + lane * 16) = o0;
    *(f32x4*)(red + 1024 + lane * 16) = o1;
  }
  __syncthreads();
  unsigned long long av[2];
  {
    const unsigned* src = c.asrc;
    av[0] = (unsigned long long)c.x0[0] | ((unsigned long long)c.x0[2] << 32);
    av[1] = (unsigned long long)c.x1[0] | ((unsigned long long)c.x1[2] << 32);
    for (int spin = 0; !early && spin < (1 << 22); ++spin) {
      u32x4 x0, x1;
      asm volatile("global_load_dwordx4 %0, %2, off sc1\n\tglobal_load_dwordx4 %1, %3, off sc1\n\ts_waitcnt vmcnt(0)"
                   : "=&v"(x0), "=&v"(x1) : "v"(src), "v"(src + 16 * L) : "memory");
      av[0] = (unsigned long long)x0[0] | ((unsigned long long)x0[2] << 32);
      av[1] = (unsigned long long)x1[0] | ((unsigned long long)x1[2] << 32);
      const bool ok = x0[1] == A_TAG && x0[3] == A_TAG && x1[1] == A_TAG && x1[3] == A_TAG;
      if (__all(ok)) break;
      __builtin_amdgcn_s_sleep(4);
    }
  }
  constexpr int OT = 16384, OTP = 144;
  {
    const char* red = smem + (w & 3) * 4096 + (1 - kh) * 2048;
#pragma unroll
    for (int nt = 0; nt < 2; ++nt) {
      const f32x4 o = *(const f32x4*)(red + nt * 1024 + lane * 16);
      const f32x4 own = kh ? acc[1][nt] : acc[0][nt];
      const f32x4 s = kh ? (o + own) : (own + o);
      const unsigned alo = (unsigned)av[nt], ahi = (unsigned)(av[nt] >> 32);
      const float a[4] = {lo_f(alo), hi_f(alo), lo_f(ahi), hi_f(ahi)};
#pragma unroll
      for (int e = 0; e < 4; ++e) {
        const float o2 = bfr(gv[e] * a[e]) + bfr(bfr(1.f - gv[e]) * bfr(s[e]));
        *(__bf16*)(smem + OT + (lr + e) * OTP + (32 * wn + 16 * nt + r) * 2) = (__bf16)o2;
      }
    }
  }
  __syncthreads();
  {
    const int row = tid >> 3, c8 = tid & 7;
    const u32x4 v = *(const u32x4*)(smem + OT + row * OTP + c8 * 16);
    *(u32x4*)(out + (size_t)(row0 + row) * D + col0 + c8 * 8) = v;
  }
}

NOPK __global__ __launch_bounds__(512, 4) void k2(const __bf16* __restrict__ h, const __bf16* __restrict__ wd,
                                             const __bf16* __restrict__ uT, const unsigned* __restrict__ kc,
                                             const __bf16* __restrict__ dsk, unsigned* __restrict__ aG,
                                             const float* __restrict__ gin, __bf16* __restrict__ out) {
  extern __shared__ __attribute__((aligned(16))) char smem[];
  const int b = blockIdx.x, xcd = b & 7, i = (b & 255) >> 3;
  if (b < 256) {
    const int pair = 32 * xcd + i;
    conv_block(smem, 2 * pair, uT, kc, dsk, aG);
  } else {
    ffn2_block(smem, 64 * (4 * xcd + (i & 3)), i >> 2, h, wd, aG, gin, out);
  }
}

extern "C" void kernel_launch(void* const* d_in, const int* in_sizes, int n_in, void* d_out, int out_size,
                              void* d_ws, size_t ws_size, hipStream_t stream) {
  const __bf16* u = (const __bf16*)d_in[0];
  const __bf16* lar = (const __bf16*)d_in[1];
  const __bf16* aim = (const __bf16*)d_in[2];
  const __bf16* cre = (const __bf16*)d_in[3];
  const __bf16* cim = (const __bf16*)d_in[4];
  const __bf16* ldt = (const __bf16*)d_in[5];
  const __bf16* dsk = (const __bf16*)d_in[6];
  const __bf16* wg = (const __bf16*)d_in[8];
  const __bf16* wu = (const __bf16*)d_in[9];
  const __bf16* wd = (const __bf16*)d_in[10];
  const __bf16* gw = (const __bf16*)d_in[11];
  char* ws = (char*)d_ws;
  __bf16* h = (__bf16*)ws;
  ws += (size_t)L * HP * 2;
  unsigned* aG = (unsigned*)ws;
  ws += (size_t)D * L * 4 + 1024;
  __bf16* uT = (__bf16*)ws;
  ws += (size_t)D * L * 2;
  unsigned* kc = (unsigned*)ws;
  ws += (size_t)D * KC_PITCH * 4;
  float* gout = (float*)ws;
  (void)hipFuncSetAttribute((const void*)k1, hipFuncAttributeMaxDynamicSharedMemorySize, LDS_BYTES);
  (void)hipFuncSetAttribute((const void*)k2, hipFuncAttributeMaxDynamicSharedMemorySize, LDS_BYTES);
  hipLaunchKernelGGL(k1, dim3(512), dim3(512), LDS_BYTES, stream, u, lar, aim, cre, cim, ldt, wg, wu, gw, h, uT, kc,
                     gout, aG, wd);
  hipLaunchKernelGGL(k2, dim3(512), dim3(512), LDS_BYTES, stream, (const __bf16*)h, wd, (const __bf16*)uT,
                     (const unsigned*)kc, dsk, aG, (const float*)gout, (__bf16*)d_out);
}
```

```cpp
#include <hip/hip_runtime.h>
#include <hip/hip_bf16.h>
#include <type_traits>

typedef __attribute__((ext_vector_type(8))) __bf16 bf16x8;
typedef __attribute__((ext_vector_type(2))) __bf16 bf16x2;
typedef __attribute__((ext_vector_type(4))) short s16x4;
typedef __attribute__((ext_vector_type(8))) short s16x8;
typedef __attribute__((ext_vector_type(4))) float f32x4;
typedef __attribute__((ext_vector_type(16))) float f32x16;
typedef __attribute__((ext_vector_type(4))) unsigned u32x4;
typedef __attribute__((ext_vector_type(2))) unsigned u32x2;
typedef __attribute__((address_space(3))) s16x4* lds_s16x4_ptr;
typedef __attribute__((address_space(1))) unsigned gu32;
typedef __attribute__((address_space(1))) unsigned long long gu64;

#define K_LO 1

#define NOPK __attribute__((target("no-packed-fp32-ops")))

#define PRIO_F1 0
#define PRIO_KG 0
#define PRIO_CV 1
#define PRIO_CV2 PRIO_CV
#define PRIO_F2 1

constexpr int L = 2048, D = 512, NS = 128, F = 1376, HP = 1408;
constexpr int LDS_BYTES = 81920;

__device__ __forceinline__ float bf2f(unsigned short v) { return __uint_as_float((unsigned)v << 16); }
__device__ __forceinline__ float bfr(float x) { return (float)(__bf16)x; }
__device__ __forceinline__ unsigned pk2(float a, float b) {
  bf16x2 v = {(__bf16)a, (__bf16)b};
  return __builtin_bit_cast(unsigned, v);
}
__device__ __forceinline__ float lo_f(unsigned w) { return __uint_as_float(w << 16); }
__device__ __forceinline__ float hi_f(unsigned w) { return __uint_as_float(w & 0xffff0000u); }

__device__ __forceinline__ f32x4 mfma16(bf16x8 a, bf16x8 b, f32x4 c) {
  return __builtin_amdgcn_mfma_f32_16x16x32_bf16(a, b, c, 0, 0, 0);
}
__device__ __forceinline__ bf16x8 tr_frag(const char* lds, int off0, int off1) {
  s16x4 a = __builtin_amdgcn_ds_read_tr16_b64_v4i16((lds_s16x4_ptr)(lds + off0));
  s16x4 b = __builtin_amdgcn_ds_read_tr16_b64_v4i16((lds_s16x4_ptr)(lds + off1));
  s16x8 v = {a[0], a[1], a[2], a[3], b[0], b[1], b[2], b[3]};
  return __builtin_bit_cast(bf16x8, v);
}
__device__ __forceinline__ void glds16(const void* sbase, unsigned voff, unsigned lds_addr) {
  asm volatile("s_mov_b32 m0, %2\n\ts_nop 0\n\tglobal_load_lds_dwordx4 %0, %1" ::"v"(voff), "s"(sbase), "s"(lds_addr)
               : "memory", "m0");
}
__device__ __forceinline__ void glds16v(const void* vptr, unsigned lds_addr) {
  asm volatile("s_mov_b32 m0, %1\n\ts_nop 0\n\tglobal_load_lds_dwordx4 %0, off" ::"v"(vptr), "s"(lds_addr)
               : "memory", "m0");
}
__device__ __forceinline__ unsigned lds_addr_of(const char* p) {
  return (unsigned)(uintptr_t)(const __attribute__((address_space(3))) char*)p;
}
#define WAIT_VM(n) asm volatile("s_waitcnt vmcnt(" #n ")" ::: "memory")
#define WAIT_LGKM0() asm volatile("s_waitcnt lgkmcnt(0)" ::: "memory")
template <int N>
__device__ __forceinline__ void wait_vm() {
  asm volatile("s_waitcnt vmcnt(%0)" ::"i"(N) : "memory");
}

constexpr int F1_B = 32768;

struct F1Ctx {
  const __bf16 *x, *wg, *wu, *wmix;
  unsigned voffA[2], voffB, lbase;
  int w;
  int aoff[4], boff[3];
};
__device__ __forceinline__ void f1_stage_a(const F1Ctx& c, int m) {
  const unsigned ab = c.lbase + (m & 1) * 16384 + c.w * 1024;
  glds16(c.x + m * 64, c.voffA[0], ab);
  glds16(c.x + m * 64, c.voffA[1], ab + 8192);
}
template <bool G2>
__device__ __forceinline__ void f1_stage_b(const F1Ctx& c, int hs) {
  const unsigned bb = c.lbase + F1_B + (hs & 3) * 12288 + c.w * 1024;
  if constexpr (!G2) {
    glds16(c.wg + (size_t)hs * 32 * F, c.voffB, bb);
    glds16(c.wu + (size_t)hs * 32 * F, c.voffB, bb + 8192);
  } else {
    glds16v(c.wmix + (size_t)hs * 32 * F, bb);
  }
}
constexpr int f1_allowed(int hs, int nb) {
  int n = 0;
  if ((hs & 1) == 0 && hs >= 2) {
    if (hs + 1 < 16) n += nb;
    if (hs + 2 < 16) n += nb;
  } else {
    if (hs + 1 < 16) n += nb;
    if (hs >= 1 && ((hs - 1) & 1) == 0 && (hs + 1) / 2 < 8) n += 2;
    if (hs + 2 < 16) n += nb;
  }
  return n;
}
__device__ __forceinline__ f32x16 mfma32(bf16x8 a, bf16x8 b, f32x16 c) {
  return __builtin_amdgcn_mfma_f32_32x32x16_bf16(a, b, c, 0, 0, 0);
}
template <int HS, bool G2>
__device__ __forceinline__ void f1_loop(const F1Ctx& c, const char* smem, bf16x8 (&xf)[2], bf16x8 (&wf)[2][3],
                                        f32x16 (&acc)[3]) {
  if constexpr (HS < 16) {
    if constexpr (HS > 0) {
#pragma unroll
      for (int j = 0; j < 3; ++j) acc[j] = mfma32(wf[0][j], xf[0], acc[j]);
    }
    __builtin_amdgcn_sched_barrier(0);
    wait_vm<f1_allowed(HS, G2 ? 1 : 2)>();
    WAIT_LGKM0();
    __builtin_amdgcn_s_barrier();
    asm volatile("" ::: "memory");
    const char* ab = smem + ((HS >> 1) & 1) * 16384;
    const char* bbase = smem + (HS & 3) * 12288;
    __builtin_amdgcn_sched_barrier(0);
    xf[0] = *(const bf16x8*)(ab + c.aoff[2 * (HS & 1)]);
#pragma unroll
    for (int j = 0; j < 3; ++j) wf[0][j] = tr_frag(bbase, c.boff[j], c.boff[j] + 512);
    __builtin_amdgcn_sched_barrier(0);
#pragma unroll
    for (int j = 0; j < 3; ++j) {
      if constexpr (HS > 0) acc[j] = mfma32(wf[1][j], xf[1], acc[j]);
      __builtin_amdgcn_sched_barrier(0);
      if (j == 0) {
        if constexpr ((HS & 1) == 0 && (HS + 2) / 2 < 8) f1_stage_a(c, (HS + 2) / 2);
        if constexpr (HS + 3 < 16) f1_stage_b<G2>(c, HS + 3);
      } else {
        wf[1][j - 1] = tr_frag(bbase, c.boff[j - 1] + 2048, c.boff[j - 1] + 2560);
      }
      __builtin_amdgcn_sched_barrier(0);
    }
    wf[1][2] = tr_frag(bbase, c.boff[2] + 2048, c.boff[2] + 2560);
    xf[1] = *(const bf16x8*)(ab + c.aoff[2 * (HS & 1) + 1]);
    f1_loop<HS + 1, G2>(c, smem, xf, wf, acc);
  } else {
#pragma unroll
    for (int ks = 0; ks < 2; ++ks)
#pragma unroll
      for (int j = 0; j < 3; ++j) acc[j] = mfma32(wf[ks][j], xf[ks], acc[j]);
  }
}

__device__ __forceinline__ void ffn1_block(char* smem, int rb, int cb, const __bf16* __restrict__ x,
                                           const __bf16* __restrict__ wg, const __bf16* __restrict__ wu,
                                           __bf16* __restrict__ h) {
  const int tid = threadIdx.x, lane = tid & 63, w = __builtin_amdgcn_readfirstlane(tid >> 6);
  const int row0 = rb * 128, f0 = cb * 96;
  const int lt = w >> 1, sel = w & 1;
  const int r32 = lane & 31, hf = lane >> 5, blk = (lane >> 4) & 1, q = (lane >> 2) & 3, p = lane & 3;
  __builtin_amdgcn_s_setprio(PRIO_F1);
  F1Ctx c;
  c.x = x; c.wg = wg; c.wu = wu; c.w = w; c.lbase = lds_addr_of(smem);
  {
#pragma unroll
    for (int i = 0; i < 2; ++i) {
      const int row = 64 * i + 8 * w + (lane >> 3), ch = (lane & 7) ^ ((row >> 1) & 7);
      c.voffA[i] = (unsigned)(((row0 + row) * D + ch * 8) * 2);
    }
    const int kk = 8 * (w & 3) + (lane >> 3), pos = lane & 7;
    const int i = (pos >> 1) ^ (kk & 2);
    const int par = (kk + cb) & 1;
    int c0 = f0 + 16 * (i + 2 * par) + 8 * (pos & 1);
    int c1 = f0 + 16 * ((i & 1) + 4 * (1 - par)) + 8 * (pos & 1);
    c0 = c0 > F - 8 ? F - 8 : c0;
    c1 = c1 > F - 8 ? F - 8 : c1;
    c.voffB = (unsigned)((kk * F + c0) * 2);
    c.wmix = (i < 2 ? wg : wu) + kk * F + c1;
  }
#pragma unroll
  for (int k4 = 0; k4 < 4; ++k4) {
    const int row = 32 * lt + r32;
    c.aoff[k4] = row * 128 + (((2 * k4 + hf) ^ ((row >> 1) & 7)) << 4);
  }
#pragma unroll
  for (int j = 0; j < 3; ++j) {
    const int par = (q + cb) & 1, jj = j - par;
    const bool whole = jj >= 0 && jj < 2;
    const int sub = whole ? 2 * sel : 1, ps = (whole ? 2 * jj : 2 * sel) + blk;
    c.boff[j] = F1_B + sub * 4096 + (8 * hf + q) * 128 + ((ps ^ (q & 2)) << 5) + 8 * p;
    asm volatile("" : "+v"(c.boff[j]));
  }
  f32x16 acc[3];
#pragma unroll
  for (int j = 0; j < 3; ++j)
#pragma unroll
    for (int e = 0; e < 16; ++e) acc[j][e] = 0.f;
  bf16x8 xf[2], wf[2][3];

  f1_stage_a(c, 0);
  if (w < 4) {
    f1_stage_b<false>(c, 0);
    f1_stage_b<false>(c, 1);
    f1_stage_b<false>(c, 2);
    f1_loop<0, false>(c, smem, xf, wf, acc);
  } else {
    f1_stage_b<true>(c, 0);
    f1_stage_b<true>(c, 1);
    f1_stage_b<true>(c, 2);
    f1_loop<0, true>(c, smem, xf, wf, acc);
  }

  __syncthreads();
  constexpr int HT = 49152, HTP = 208;
  auto finish = [&](auto SEL) {
    constexpr int S = decltype(SEL)::value;
#pragma unroll
    for (int j = 0; j < 3; ++j)
#pragma unroll
      for (int m2 = 0; m2 < 2; ++m2) {
        constexpr int o = 8 * (1 - S);
        *(f32x4*)(smem + (w * 6 + j * 2 + m2) * 1024 + lane * 16) =
            f32x4{acc[j][o + 4 * m2], acc[j][o + 4 * m2 + 1], acc[j][o + 4 * m2 + 2], acc[j][o + 4 * m2 + 3]};
      }
    __syncthreads();
#pragma unroll
    for (int j = 0; j < 3; ++j)
#pragma unroll
      for (int m2 = 0; m2 < 2; ++m2) {
        const f32x4 o4 = *(const f32x4*)(smem + ((w ^ 1) * 6 + j * 2 + m2) * 1024 + lane * 16);
        float hv[4];
#pragma unroll
        for (int e = 0; e < 4; ++e) {
          const float own = acc[j][8 * S + 4 * m2 + e];
          const float gv = S ? o4[e] : own, uv = S ? own : o4[e];
          const float t = gv * __builtin_fmaf(gv * gv, -0.10294324f, -2.3022082f);
          hv[e] = (gv * uv) * __builtin_amdgcn_rcpf(1.f + __builtin_amdgcn_exp2f(t));
        }
        u32x2 o = {pk2(hv[0], hv[1]), pk2(hv[2], hv[3])};
        *(u32x2*)(smem + HT + (32 * lt + r32) * HTP + (32 * j + 16 * S + 8 * m2 + 4 * hf) * 2) = o;
      }
  };
  if (sel) finish(std::integral_constant<int, 1>{});
  else finish(std::integral_constant<int, 0>{});
  __syncthreads();
#pragma unroll
  for (int i = 0; i < 3; ++i) {
    const int idx = tid + 512 * i, row = idx / 12, c8 = idx - row * 12;
    if (f0 + 8 * c8 < F)
      *(u32x4*)(h + (size_t)(row0 + row) * HP + f0 + 8 * c8) = *(const u32x4*)(smem + HT + row * HTP + c8 * 16);
  }
}

__device__ __forceinline__ void gate_block(char* smem, int rb, const __bf16* __restrict__ x,
                                           const __bf16* __restrict__ gw, float* __restrict__ gout,
                                           __bf16* __restrict__ uT) {
  const int tid = threadIdx.x, lane = tid & 63, w = __builtin_amdgcn_readfirstlane(tid >> 6);
  const int g = lane >> 4, q = (lane >> 2) & 3, p = lane & 3;
  const unsigned lbase = lds_addr_of(smem);
  const int grow = tid >> 3, gpart = tid & 7, gf = (grow & 3) | (((grow >> 3) & 1) << 2);
  u32x4 gwv[8];
#pragma unroll
  for (int i = 0; i < 8; ++i) gwv[i] = *(const u32x4*)(gw + ((gpart ^ gf) + 8 * (i >> 1)) * 16 + 8 * (i & 1));
#pragma unroll 1
  for (int pass = 0; pass < 2; ++pass) {
    const int l0 = rb * 128 + pass * 64;
#pragma unroll
    for (int i = 0; i < 8; ++i) {
      const int row = 8 * w + i, fr = (row & 3) | (((row >> 3) & 1) << 2);
      glds16(x + (size_t)(l0 + row) * D, (unsigned)((((lane >> 1) ^ fr) * 2 + (lane & 1)) * 16), lbase + row * 1024);
    }
    WAIT_VM(0);
    __syncthreads();
#pragma unroll
    for (int i = 0; i < 8; ++i) {
      const int unit = w * 8 + i, c16 = unit >> 1, half = unit & 1;
      const int ra = 32 * half + 8 * g + q, rb2 = ra + 4;
      const int fa = (ra & 3) | (((ra >> 3) & 1) << 2);
      const bf16x8 v = tr_frag(smem, ra * 1024 + ((c16 ^ fa) << 5) + 8 * p, rb2 * 1024 + ((c16 ^ fa) << 5) + 8 * p);
      *(bf16x8*)(uT + (size_t)(16 * c16 + (lane & 15)) * L + l0 + 32 * half + 8 * g) = v;
    }
    float s = 0.f;
#pragma unroll
    for (int i = 0; i < 8; ++i) {
      const u32x4 xv = *(const u32x4*)(smem + grow * 1024 + (gpart + 8 * (i >> 1)) * 32 + 16 * (i & 1));
#pragma unroll
      for (int e = 0; e < 4; ++e) {
        const unsigned xe = xv[e], we = gwv[i][e];
        s = __builtin_amdgcn_fdot2_f32_bf16(__builtin_bit_cast(bf16x2, xe), __builtin_bit_cast(bf16x2, we), s, false);
      }
    }
    s += __shfl_xor(s, 1);
    s += __shfl_xor(s, 2);
    s += __shfl_xor(s, 4);
    if (gpart == 0) gout[l0 + grow] = bfr(1.f / (1.f + __expf(-bfr(s))));
    __syncthreads();
  }
}

constexpr int KC_PITCH = 768;
constexpr int KG_PITCH = 160;
constexpr int KG_ARR = 16 * KG_PITCH;
constexpr int KG_TAB = 8 * 2 * KG_ARR;
typedef __attribute__((ext_vector_type(2))) float f32x2;

__device__ __forceinline__ f32x2 cexp(float zr, float zi, float p) {
  const float e = __builtin_amdgcn_exp2f(zr * p);
  const float ph = __builtin_amdgcn_fractf(zi * p);
  return f32x2{e * __builtin_amdgcn_cosf(ph), e * __builtin_amdgcn_sinf(ph)};
}
__device__ __forceinline__ f32x2 cmul(f32x2 a, f32x2 b) {
  return f32x2{a[0] * b[0] - a[1] * b[1], a[0] * b[1] + a[1] * b[0]};
}
__device__ __forceinline__ void kgen_block(char* smem, int d0, const __bf16* __restrict__ lar,
                                           const __bf16* __restrict__ aim, const __bf16* __restrict__ cre,
                                           const __bf16* __restrict__ cim, const __bf16* __restrict__ ldt,
                                           unsigned* __restrict__ kc, unsigned* __restrict__ aG,
                                           const __bf16* __restrict__ wd) {
  const int tid = threadIdx.x, lane = tid & 63, w = __builtin_amdgcn_readfirstlane(tid >> 6);
  unsigned pf_dummy = 0;
  if (tid < 43)
    asm volatile("global_load_dword %0, %1, off" : "=v"(pf_dummy) : "v"(wd + (size_t)((d0 >> 1) * 43 + tid) * 64) : "memory");
  {
    u32x4* z = (u32x4*)(aG + (size_t)d0 * L) + tid;
    z[0] = u32x4{0u, 0u, 0u, 0u};
    z[512] = u32x4{0u, 0u, 0u, 0u};
    if (tid == 0) aG[(size_t)D * L + (d0 >> 1)] = 0u;
  }
  const int ch = w >> 2, wq = w & 3, tc = tid & 255;
  const int r = lane & 15, g = lane >> 4, hh = lane >> 5;
  char* wb = smem + w * 2 * KG_ARR;
  char* tb = smem + KG_TAB + ch * (4 * 656 * 4);
  __builtin_amdgcn_s_setprio(PRIO_KG);
  float zr, zi;
  f32x2 cc;
  {
    const int d = d0 + ch, n = 32 * wq + (lane & 31);
    const float dt = bfr(expf((float)ldt[d]));
    const float ar = -bfr(expf((float)lar[d * NS + n]));
    zr = (ar * dt) * 1.4426950408889634f;
    zi = ((float)aim[d * NS + n] * dt) * 0.15915494309189535f;
    cc = f32x2{2.f * ((float)cre[d * NS + n] * dt), 2.f * ((float)cim[d * NS + n] * dt)};
  }
  const float fh = (float)(1 + hh);
  const f32x2 e128 = cexp(zr, zi, 128.f), e24 = cexp(zr, zi, 2.f * fh);
  f32x2 v[4], st[4];
  v[0] = cexp(zr, -zi, 64.f * hh);                 st[0] = cexp(zr, -zi, 8.f);
  v[1] = hh ? cmul(cc, e128) : cc;                 st[1] = cexp(zr, zi, 1.f);
  v[2] = cmul(cc, cexp(zr, zi, 256.f * fh));       st[2] = hh ? e128 : e24;
  v[3] = cmul(cc, cexp(zr, zi, 1024.f * fh));      st[3] = e128;
  const f32x2 v24 = cmul(v[2], hh ? e24 : e128);
  unsigned vh[4][8], vl[4][8];
#pragma unroll
  for (int i = 0; i < 8; ++i)
#pragma unroll
    for (int t = 0; t < 4; ++t) {
      vh[t][i] = pk2(v[t][0], v[t][1]);
      vl[t][i] = pk2(v[t][0] - lo_f(vh[t][i]), v[t][1] - hi_f(vh[t][i]));
      if (t == 2 && i == 3) v[t] = v24;
      else if (i < 7) v[t] = cmul(v[t], st[t]);
    }
  char* wr = wb + (8 * hh) * KG_PITCH + (lane & 31) * 4;
  const char* rd = wb + r * KG_PITCH + g * 16;
  bf16x8 fh_[4][2], fl_[4][2];
#pragma unroll
  for (int t = 0; t < 4; ++t) {
    __builtin_amdgcn_wave_barrier();
#pragma unroll
    for (int i = 0; i < 8; ++i) {
      *(unsigned*)(wr + i * KG_PITCH) = vh[t][i];
      *(unsigned*)(wr + i * KG_PITCH + KG_ARR) = vl[t][i];
    }
    __builtin_amdgcn_wave_barrier();
#pragma unroll
    for (int ss = 0; ss < 2; ++ss) {
      fh_[t][ss] = *(const bf16x8*)(rd + ss * 64);
      fl_[t][ss] = *(const bf16x8*)(rd + ss * 64 + KG_ARR);
    }
  }
  f32x4 acc[3];
#pragma unroll
  for (int m = 0; m < 3; ++m) acc[m] = f32x4{0.f, 0.f, 0.f, 0.f};
#pragma unroll
  for (int ss = 0; ss < 2; ++ss)
#pragma unroll
    for (int m = 0; m < 3; ++m) {
      acc[m] = mfma16(fl_[1 + m][ss], fh_[0][ss], acc[m]);
      acc[m] = mfma16(fh_[1 + m][ss], fl_[0][ss], acc[m]);
      acc[m] = mfma16(fh_[1 + m][ss], fh_[0][ss], acc[m]);
    }
  {
    float* tab = (float*)tb + wq * 656;
    *(f32x4*)(tab + 128 * (g >> 1) + 4 * (g & 1) + 8 * r) = acc[0];
    if (g < 2) {
      *(f32x4*)(tab + 256 + 64 * g + 4 * r) = acc[1];
#pragma unroll
      for (int e = 0; e < 4; ++e) tab[512 + 64 * g + 16 * e + r] = acc[2][e];
    } else {
#pragma unroll
      for (int e = 0; e < 4; ++e) tab[384 + (g & 1) + 32 * e + 2 * r] = acc[1][e];
      if (lane == 32) tab[640] = acc[2][0];
    }
  }
  __syncthreads();
  {
    const float* tab = (const float*)tb;
    unsigned* kd = kc + (size_t)(d0 + ch) * KC_PITCH;
#pragma unroll
    for (int e = 0; e < 3; ++e) {
      const int id = tc + 256 * e;
      if (id < 641) {
        const float v = (tab[id] + tab[656 + id]) + (tab[2 * 656 + id] + tab[3 * 656 + id]);
        const float vh = bfr(v);
        kd[id] = pk2(vh, v - vh);
      }
    }
  }
  asm volatile("s_waitcnt vmcnt(0)" : "+v"(pf_dummy)::"memory");
}

NOPK __global__ __launch_bounds__(512, 4) void k1(const __bf16* __restrict__ u, const __bf16* __restrict__ lar,
                                             const __bf16* __restrict__ aim, const __bf16* __restrict__ cre,
                                             const __bf16* __restrict__ cim, const __bf16* __restrict__ ldt,
                                             const __bf16* __restrict__ wg, const __bf16* __restrict__ wu,
                                             const __bf16* __restrict__ gw, __bf16* __restrict__ h,
                                             __bf16* __restrict__ uT, unsigned* __restrict__ kc,
                                             float* __restrict__ gout, unsigned* __restrict__ aG,
                                             const __bf16* __restrict__ wd) {
  extern __shared__ __attribute__((aligned(16))) char smem[];
  const int b = blockIdx.x;
  const int xcd = b & 7, i = (b & 255) >> 3;
  if (b < 256) {
    const int rb = 4 * (xcd >> 1) + (i & 3), cb = 8 * (xcd & 1) + (i >> 2);
    if (cb < 15) ffn1_block(smem, rb, cb, u, wg, wu, h);
    else gate_block(smem, rb, u, gw, gout, uT);
  } else {
    kgen_block(smem, 64 * xcd + 2 * i, lar, aim, cre, cim, ldt, kc, aG, wd);
  }
}

constexpr unsigned A_TAG = 0x5a17c3e9u;
constexpr int UCP = 4160;
constexpr int KARR = (240 + L) * 2;
constexpr int OFF_K = 4 * UCP;
constexpr int OFF_TAB = OFF_K + 2 * KARR;
constexpr int OFF_RED = UCP;
constexpr int CH_BYTES = OFF_RED + 32768;
static_assert(OFF_TAB + 3072 <= CH_BYTES && 2 * CH_BYTES <= LDS_BYTES, "lds");

struct PosLut {
  unsigned short v[L];
  constexpr PosLut() : v() {
    for (int j = 0; j < L; ++j) {
      const int sh = (j >= 256) + (j >= 512) + (j >= 1024);
      int q = j >> sh;
      if (sh) {
        const int rem = j & ((1 << sh) - 1), half = 1 << (sh - 1);
        if (rem > half || (rem == half && (q & 1))) ++q;
      }
      v[j] = (unsigned short)(4 * (128 * sh + q));
    }
  }
};
__device__ const PosLut g_poslut{};

__device__ __forceinline__ void conv_block(char* smem, int d0, const __bf16* __restrict__ uT,
                                           const unsigned* __restrict__ kc, const __bf16* __restrict__ dsk,
                                           unsigned* __restrict__ aG) {
  const int tid = threadIdx.x, lane = tid & 63, w = __builtin_amdgcn_readfirstlane(tid >> 6);
  const int ch = w >> 2, wq = w & 3, tc = tid & 255;
  const int r = lane & 15, g = lane >> 4, q = (lane >> 2) & 3, p = lane & 3;
  char* cb = smem + ch * CH_BYTES;
  const int d = d0 + ch;
  __builtin_amdgcn_s_setprio(3);
  {
    const unsigned lb = lds_addr_of(cb);
    glds16(uT + (size_t)d * L + wq * 512, lane * 16, lb + 32 + wq * 1024);
    if (wq < 3) glds16(kc + (size_t)d * KC_PITCH + wq * 256, lane * 16, lb + OFF_TAB + wq * 1024);
    const u32x4 z = {0u, 0u, 0u, 0u};
    if (tc < 60) *(u32x4*)(cb + OFF_K + (tc >= 30 ? KARR : 0) + (tc % 30) * 16) = z;
    else if (tc < 68) *(u32x4*)(cb + ((tc - 60) >> 1) * UCP + ((tc - 60) & 1) * 16) = z;
  }
  const float dsv = (float)dsk[d];
  const u32x4 lut = *(const u32x4*)(g_poslut.v + 8 * tc);
  WAIT_VM(0);
  __syncthreads();
  {
    const u32x4 pa = *(const u32x4*)(cb + 16 + 16 * tc), pb = *(const u32x4*)(cb + 32 + 16 * tc);
    const u32x4 c1 = {__builtin_amdgcn_alignbit(pb[0], pa[3], 16), __builtin_amdgcn_alignbit(pb[1], pb[0], 16),
                      __builtin_amdgcn_alignbit(pb[2], pb[1], 16), __builtin_amdgcn_alignbit(pb[3], pb[2], 16)};
    const u32x4 c2 = {pa[3], pb[0], pb[1], pb[2]};
    const u32x4 c3 = {__builtin_amdgcn_alignbit(pa[3], pa[2], 16), c1[0], c1[1], c1[2]};
    *(u32x4*)(cb + 1 * UCP + 32 + 16 * tc) = c1;
    *(u32x4*)(cb + 2 * UCP + 32 + 16 * tc) = c2;
    *(u32x4*)(cb + 3 * UCP + 32 + 16 * tc) = c3;
    u32x4 kh, kl;
#pragma unroll
    for (int e = 0; e < 4; ++e) {
      const unsigned t0 = *(const unsigned*)(cb + OFF_TAB + (lut[e] & 0xffffu));
      const unsigned t1 = *(const unsigned*)(cb + OFF_TAB + (lut[e] >> 16));
      kh[e] = __builtin_amdgcn_perm(t1, t0, 0x05040100u);
      kl[e] = __builtin_amdgcn_perm(t1, t0, 0x07060302u);
    }
    *(u32x4*)(cb + OFF_K + 480 + 16 * tc) = kh;
    *(u32x4*)(cb + OFF_K + KARR + 480 + 16 * tc) = kl;
  }
  __syncthreads();
  __builtin_amdgcn_s_setprio(PRIO_CV);
  f32x4 acc[8];
#pragma unroll
  for (int t = 0; t < 8; ++t) acc[t] = f32x4{0.f, 0.f, 0.f, 0.f};
#pragma unroll
  for (int rr = 0; rr < 2; ++rr) {
    if (rr == 1) __builtin_amdgcn_s_setprio(PRIO_CV2);
    const int rho = 2 * wq + rr;
    const int abase = q * UCP + 2 * (16 + 16 + 32 * rho - 8 * g + 4 * p);
    const int bbase = OFF_K + 2 * (240 - 16 - 32 * rho + 16 * r + 8 * g);
    bf16x8 af[8];
#pragma unroll
    for (int s = 0; s < 8; ++s) af[s] = tr_frag(cb, abase + 512 * s, abase + 512 * s - 8);
#pragma unroll
    for (int t = 0; t < 8; ++t) {
      const bf16x8 bh = *(const bf16x8*)(cb + bbase + 512 * t);
#pragma unroll
      for (int s = 0; s + t < 8; ++s) acc[s + t] = mfma16(af[s], bh, acc[s + t]);
      const bf16x8 bl = *(const bf16x8*)(cb + bbase + KARR + 512 * t);
#pragma unroll
      for (int s = 0; s + t < 8; ++s) acc[s + t] = mfma16(af[s], bl, acc[s + t]);
    }
  }
  __builtin_amdgcn_s_setprio(3);
  __syncthreads();
  if (tid == 256) __hip_atomic_store((gu32*)(aG + (size_t)D * L + (d0 >> 1)), 1u, __ATOMIC_RELAXED, __HIP_MEMORY_SCOPE_AGENT);
#pragma unroll
  for (int t = 0; t < 8; ++t)
    if ((t >> 1) != wq) *(f32x4*)(cb + OFF_RED + (wq * 8 + t) * 1024 + lane * 16) = acc[t];
  __syncthreads();
#pragma unroll
  for (int tt = 0; tt < 2; ++tt) {
    const int t = 2 * wq + tt;
    f32x4 own = f32x4{0.f, 0.f, 0.f, 0.f};
#pragma unroll
    for (int t2 = 0; t2 < 8; ++t2)
      if (t2 == t) own = acc[t2];
    f32x4 s = f32x4{0.f, 0.f, 0.f, 0.f};
#pragma unroll
    for (int sw = 0; sw < 4; ++sw) {
      const f32x4 o = *(const f32x4*)(cb + OFF_RED + (sw * 8 + t) * 1024 + lane * 16);
      s += (sw == wq) ? own : o;
    }
    const int l = 256 * t + 16 * r + 4 * g;
    const u32x2 uu = *(const u32x2*)(cb + 32 + 2 * l);
    const float a0 = bfr(s[0]) + bfr(lo_f(uu[0]) * dsv), a1 = bfr(s[1]) + bfr(hi_f(uu[0]) * dsv);
    const float a2 = bfr(s[2]) + bfr(lo_f(uu[1]) * dsv), a3 = bfr(s[3]) + bfr(hi_f(uu[1]) * dsv);
    const u32x4 gr = {pk2(a0, a1), A_TAG, pk2(a2, a3), A_TAG};
    asm volatile("global_store_dwordx4 %0, %1, off sc1\n\ts_nop 1" ::"v"(aG + (size_t)d * L + l), "v"(gr) : "memory");
  }
}

constexpr int F2_NS = 5;
constexpr int F2_STAGE = 16384;
constexpr int F2_NST = 22;

struct F2Ctx {
  const __bf16 *h, *wd;
  unsigned avoff, bvoff, bvoff_last, lbase;
  int w, kh;
  int aoff[2], boff[2];
  const unsigned* asrc;
  mutable u32x4 x0, x1;
};
__device__ __forceinline__ void f2_stage(const F2Ctx& c, int st) {
  const unsigned base = c.lbase + (st % F2_NS) * F2_STAGE;
  glds16(c.h + st * 64, c.avoff, base + c.w * 1024);
  glds16(c.wd + (size_t)st * 64 * D, st == F2_NST - 1 ? c.bvoff_last : c.bvoff, base + 8192 + c.w * 1024);
}
template <int ST>
__device__ __forceinline__ void f2_loop(const F2Ctx& c, const char* smem, bf16x8 (&ao)[2], bf16x8 (&bo)[2],
                                        f32x4 (&acc)[2][2]) {
  if constexpr (ST < F2_NST) {
    constexpr int rem = (F2_NST - 1 - ST) < (F2_NS - 2) ? (F2_NST - 1 - ST) : (F2_NS - 2);
    wait_vm<2 * rem>();
    WAIT_LGKM0();
    __builtin_amdgcn_s_barrier();
    asm volatile("" ::: "memory");
    if constexpr (ST + F2_NS - 1 < F2_NST) f2_stage(c, ST + F2_NS - 1);
    if constexpr (ST == F2_NST - 3)
      asm volatile("global_load_dwordx4 %0, %2, off sc1\n\tglobal_load_dwordx4 %1, %3, off sc1"
                   : "=&v"(c.x0), "=&v"(c.x1) : "v"(c.asrc), "v"(c.asrc + 16 * L) : "memory");
    const char* base = smem + (ST % F2_NS) * F2_STAGE;
    bf16x8 an[2], bn[2];
#pragma unroll
    for (int mt = 0; mt < 2; ++mt) an[mt] = *(const bf16x8*)(base + c.aoff[mt]);
#pragma unroll
    for (int nt = 0; nt < 2; ++nt) bn[nt] = tr_frag(base, c.boff[nt], c.boff[nt] + 512);
    __builtin_amdgcn_sched_barrier(0);
    if constexpr (ST > 0) {
#pragma unroll
      for (int mt = 0; mt < 2; ++mt)
#pragma unroll
        for (int nt = 0; nt < 2; ++nt) acc[mt][nt] = mfma16(ao[mt], bo[nt], acc[mt][nt]);
    }
    __builtin_amdgcn_sched_barrier(0);
    f2_loop<ST + 1>(c, smem, an, bn, acc);
  } else {
    if (c.kh == 0) {
#pragma unroll
      for (int mt = 0; mt < 2; ++mt)
#pragma unroll
        for (int nt = 0; nt < 2; ++nt) acc[mt][nt] = mfma16(ao[mt], bo[nt], acc[mt][nt]);
    }
  }
}

__device__ __forceinline__ void ffn2_block(char* smem, int row0, int cbk, const __bf16* __restrict__ h,
                                           const __bf16* __restrict__ wd, const unsigned* __restrict__ aG,
                                           const float* __restrict__ gin, __bf16* __restrict__ out) {
  const int col0 = 64 * cbk;
  const int tid = threadIdx.x, lane = tid & 63, w = __builtin_amdgcn_readfirstlane(tid >> 6);
  const int r = lane & 15, g = lane >> 4, q = (lane >> 2) & 3, p = lane & 3;
  const int kh = w >> 2, wm = (w >> 1) & 1, wn = w & 1;
  __builtin_amdgcn_s_setprio(PRIO_F2);
  F2Ctx c;
  c.h = h; c.wd = wd; c.w = w; c.kh = kh; c.lbase = lds_addr_of(smem);
  {
    const int row = 8 * w + (lane >> 3), ch = (lane & 7) ^ ((row >> 1) & 7);
    c.avoff = (unsigned)(((row0 + row) * HP + ch * 8) * 2);
    const int bk = 8 * w + (lane >> 3);
    const int pos = lane & 7, fp = ((bk >> 1) & 1) | (((bk >> 3) & 1) << 1);
    const int cc = col0 + 16 * ((pos >> 1) ^ fp) + 8 * (pos & 1);
    c.bvoff = (unsigned)((bk * D + cc) * 2);
    c.bvoff_last = (unsigned)(((bk > 31 ? 31 : bk) * D + cc) * 2);
  }
#pragma unroll
  for (int mt = 0; mt < 2; ++mt) {
    const int row = 32 * wm + 16 * mt + r, ch = 4 * kh + g;
    c.aoff[mt] = row * 128 + ((ch ^ ((row >> 1) & 7)) << 4);
  }
  const int fpl = (q >> 1) | ((g & 1) << 1);
#pragma unroll
  for (int nt = 0; nt < 2; ++nt) c.boff[nt] = 8192 + (32 * kh + 8 * g + q) * 128 + (((2 * wn + nt) ^ fpl) << 5) + 8 * p;

  f32x4 acc[2][2];
#pragma unroll
  for (int mt = 0; mt < 2; ++mt)
#pragma unroll
    for (int nt = 0; nt < 2; ++nt) acc[mt][nt] = f32x4{0.f, 0.f, 0.f, 0.f};

#pragma unroll
  for (int st = 0; st < F2_NS - 1; ++st) f2_stage(c, st);
  const int lr = 32 * wm + 16 * kh + 4 * g;
  const f32x4 gv = *(const f32x4*)(gin + row0 + lr);
  c.asrc = aG + (size_t)(col0 + 32 * wn + r) * L + row0 + lr;
  bf16x8 a0[2], b0[2];
  f2_loop<0>(c, smem, a0, b0, acc);
  WAIT_LGKM0();
  __builtin_amdgcn_s_barrier();
  asm volatile("" ::: "memory");
  asm volatile("s_waitcnt vmcnt(0)" : "+v"(c.x0), "+v"(c.x1)::"memory");
  const bool early = __all(c.x0[1] == A_TAG && c.x0[3] == A_TAG && c.x1[1] == A_TAG && c.x1[3] == A_TAG);
  if (w == 0 && !early) {
    const gu32* fl = (const gu32*)(aG + (size_t)D * L + 32 * cbk + (lane & 31));
    for (int spin = 0; spin < (1 << 22); ++spin) {
      const unsigned v = __hip_atomic_load(fl, __ATOMIC_RELAXED, __HIP_MEMORY_SCOPE_AGENT);
      if (__all(v == 1u)) break;
      __builtin_amdgcn_s_sleep(2);
    }
  }
  {
    char* red = smem + (w & 3) * 4096 + kh * 2048;
    const f32x4 o0 = kh ? acc[0][0] : acc[1][0], o1 = kh ? acc[0][1] : acc[1][1];
    *(f32x4*)(red + lane * 16) = o0;
    *(f32x4*)(red + 1024 + lane * 16) = o1;
  }
  __syncthreads();
  unsigned long long av[2];
  {
    const unsigned* src = c.asrc;
    av[0] = (unsigned long long)c.x0[0] | ((unsigned long long)c.x0[2] << 32);
    av[1] = (unsigned long long)c.x1[0] | ((unsigned long long)c.x1[2] << 32);
    for (int spin = 0; !early && spin < (1 << 22); ++spin) {
      u32x4 x0, x1;
      asm volatile("global_load_dwordx4 %0, %2, off sc1\n\tglobal_load_dwordx4 %1, %3, off sc1\n\ts_waitcnt vmcnt(0)"
                   : "=&v"(x0), "=&v"(x1) : "v"(src), "v"(src + 16 * L) : "memory");
      av[0] = (unsigned long long)x0[0] | ((unsigned long long)x0[2] << 32);
      av[1] = (unsigned long long)x1[0] | ((unsigned long long)x1[2] << 32);
      const bool ok = x0[1] == A_TAG && x0[3] == A_TAG && x1[1] == A_TAG && x1[3] == A_TAG;
      if (__all(ok)) break;
      __builtin_amdgcn_s_sleep(4);
    }
  }
  constexpr int OT = 16384, OTP = 144;
  {
    const char* red = smem + (w & 3) * 4096 + (1 - kh) * 2048;
#pragma unroll
    for (int nt = 0; nt < 2; ++nt) {
      const f32x4 o = *(const f32x4*)(red + nt * 1024 + lane * 16);
      const f32x4 own = kh ? acc[1][nt] : acc[0][nt];
      const f32x4 s = kh ? (o + own) : (own + o);
      const unsigned alo = (unsigned)av[nt], ahi = (unsigned)(av[nt] >> 32);
      const float a[4] = {lo_f(alo), hi_f(alo), lo_f(ahi), hi_f(ahi)};
#pragma unroll
      for (int e = 0; e < 4; ++e) {
        const float o2 = bfr(gv[e] * a[e]) + bfr(bfr(1.f - gv[e]) * bfr(s[e]));
        *(__bf16*)(smem + OT + (lr + e) * OTP + (32 * wn + 16 * nt + r) * 2) = (__bf16)o2;
      }
    }
  }
  __syncthreads();
  {
    const int row = tid >> 3, c8 = tid & 7;
    const u32x4 v = *(const u32x4*)(smem + OT + row * OTP + c8 * 16);
    *(u32x4*)(out + (size_t)(row0 + row) * D + col0 + c8 * 8) = v;
  }
}

NOPK __global__ __launch_bounds__(512, 4) void k2(const __bf16* __restrict__ h, const __bf16* __restrict__ wd,
                                             const __bf16* __restrict__ uT, const unsigned* __restrict__ kc,
                                             const __bf16* __restrict__ dsk, unsigned* __restrict__ aG,
                                             const float* __restrict__ gin, __bf16* __restrict__ out) {
  extern __shared__ __attribute__((aligned(16))) char smem[];
  const int b = blockIdx.x, xcd = b & 7, i = (b & 255) >> 3;
  if (b < 256) {
    const int pair = 32 * xcd + i;
    conv_block(smem, 2 * pair, uT, kc, dsk, aG);
  } else {
    ffn2_block(smem, 64 * (4 * xcd + (i & 3)), i >> 2, h, wd, aG, gin, out);
  }
}

extern "C" void kernel_launch(void* const* d_in, const int* in_sizes, int n_in, void* d_out, int out_size,
                              void* d_ws, size_t ws_size, hipStream_t stream) {
  const __bf16* u = (const __bf16*)d_in[0];
  const __bf16* lar = (const __bf16*)d_in[1];
  const __bf16* aim = (const __bf16*)d_in[2];
  const __bf16* cre = (const __bf16*)d_in[3];
  const __bf16* cim = (const __bf16*)d_in[4];
  const __bf16* ldt = (const __bf16*)d_in[5];
  const __bf16* dsk = (const __bf16*)d_in[6];
  const __bf16* wg = (const __bf16*)d_in[8];
  const __bf16* wu = (const __bf16*)d_in[9];
  const __bf16* wd = (const __bf16*)d_in[10];
  const __bf16* gw = (const __bf16*)d_in[11];
  char* ws = (char*)d_ws;
  __bf16* h = (__bf16*)ws;
  ws += (size_t)L * HP * 2;
  unsigned* aG = (unsigned*)ws;
  ws += (size_t)D * L * 4 + 1024;
  __bf16* uT = (__bf16*)ws;
  ws += (size_t)D * L * 2;
  unsigned* kc = (unsigned*)ws;
  ws += (size_t)D * KC_PITCH * 4;
  float* gout = (float*)ws;
  (void)hipFuncSetAttribute((const void*)k1, hipFuncAttributeMaxDynamicSharedMemorySize, LDS_BYTES);
  (void)hipFuncSetAttribute((const void*)k2, hipFuncAttributeMaxDynamicSharedMemorySize, LDS_BYTES);
  hipLaunchKernelGGL(k1, dim3(512), dim3(512), LDS_BYTES, stream, u, lar, aim, cre, cim, ldt, wg, wu, gw, h, uT, kc,
                     gout, aG, wd);
  hipLaunchKernelGGL(k2, dim3(512), dim3(512), LDS_BYTES, stream, (const __bf16*)h, wd, (const __bf16*)uT,
                     (const unsigned*)kc, dsk, aG, (const float*)gout, (__bf16*)d_out);
}
```

```cpp
#include <hip/hip_runtime.h>
#include <hip/hip_bf16.h>
#include <type_traits>

typedef __attribute__((ext_vector_type(8))) __bf16 bf16x8;
typedef __attribute__((ext_vector_type(2))) __bf16 bf16x2;
typedef __attribute__((ext_vector_type(4))) short s16x4;
typedef __attribute__((ext_vector_type(8))) short s16x8;
typedef __attribute__((ext_vector_type(4))) float f32x4;
typedef __attribute__((ext_vector_type(4))) unsigned u32x4;
typedef __attribute__((ext_vector_type(2))) unsigned u32x2;
typedef __attribute__((address_space(3))) s16x4* lds_s16x4_ptr;
typedef __attribute__((address_space(1))) unsigned gu32;
typedef __attribute__((address_space(1))) unsigned long long gu64;

#define K_LO 1

#define NOPK __attribute__((target("no-packed-fp32-ops")))

#define PRIO_F1 0
#define PRIO_KG 0
#define PRIO_CV 1
#define PRIO_CV2 PRIO_CV
#define PRIO_F2 1

constexpr int L = 2048, D = 512, NS = 128, F = 1376, HP = 1408;
constexpr int LDS_BYTES = 81920;

__device__ __forceinline__ float bf2f(unsigned short v) { return __uint_as_float((unsigned)v << 16); }
__device__ __forceinline__ float bfr(float x) { return (float)(__bf16)x; }
__device__ __forceinline__ unsigned pk2(float a, float b) {
  bf16x2 v = {(__bf16)a, (__bf16)b};
  return __builtin_bit_cast(unsigned, v);
}
__device__ __forceinline__ float lo_f(unsigned w) { return __uint_as_float(w << 16); }
__device__ __forceinline__ float hi_f(unsigned w) { return __uint_as_float(w & 0xffff0000u); }

__device__ __forceinline__ f32x4 mfma16(bf16x8 a, bf16x8 b, f32x4 c) {
  return __builtin_amdgcn_mfma_f32_16x16x32_bf16(a, b, c, 0, 0, 0);
}
__device__ __forceinline__ bf16x8 tr_frag(const char* lds, int off0, int off1) {
  s16x4 a = __builtin_amdgcn_ds_read_tr16_b64_v4i16((lds_s16x4_ptr)(lds + off0));
  s16x4 b = __builtin_amdgcn_ds_read_tr16_b64_v4i16((lds_s16x4_ptr)(lds + off1));
  s16x8 v = {a[0], a[1], a[2], a[3], b[0], b[1], b[2], b[3]};
  return __builtin_bit_cast(bf16x8, v);
}
__device__ __forceinline__ void glds16(const void* sbase, unsigned voff, unsigned lds_addr) {
  asm volatile("s_mov_b32 m0, %2\n\ts_nop 0\n\tglobal_load_lds_dwordx4 %0, %1" ::"v"(voff), "s"(sbase), "s"(lds_addr)
               : "memory", "m0");
}
__device__ __forceinline__ void glds16v(const void* vptr, unsigned lds_addr) {
  asm volatile("s_mov_b32 m0, %1\n\ts_nop 0\n\tglobal_load_lds_dwordx4 %0, off" ::"v"(vptr), "s"(lds_addr)
               : "memory", "m0");
}
__device__ __forceinline__ unsigned lds_addr_of(const char* p) {
  return (unsigned)(uintptr_t)(const __attribute__((address_space(3))) char*)p;
}
#define WAIT_VM(n) asm volatile("s_waitcnt vmcnt(" #n ")" ::: "memory")
#define WAIT_LGKM0() asm volatile("s_waitcnt lgkmcnt(0)" ::: "memory")
template <int N>
__device__ __forceinline__ void wait_vm() {
  asm volatile("s_waitcnt vmcnt(%0)" ::"i"(N) : "memory");
}

constexpr int F1_B = 32768;

struct F1Ctx {
  const __bf16 *x, *wg, *wu, *wmix;
  unsigned voffA[2], voffB, lbase;
  int w;
  int aoff[2], boff[3];
};
__device__ __forceinline__ void f1_stage_a(const F1Ctx& c, int m) {
  const unsigned ab = c.lbase + (m & 1) * 16384 + c.w * 1024;
  glds16(c.x + m * 64, c.voffA[0], ab);
  glds16(c.x + m * 64, c.voffA[1], ab + 8192);
}
template <bool G2>
__device__ __forceinline__ void f1_stage_b(const F1Ctx& c, int hs) {
  const unsigned bb = c.lbase + F1_B + (hs & 3) * 12288 + c.w * 1024;
  if constexpr (!G2) {
    glds16(c.wg + (size_t)hs * 32 * F, c.voffB, bb);
    glds16(c.wu + (size_t)hs * 32 * F, c.voffB, bb + 8192);
  } else {
    glds16v(c.wmix + (size_t)hs * 32 * F, bb);
  }
}
constexpr int f1_allowed(int hs, int nb) {
  int n = 0;
  if ((hs & 1) == 0 && hs >= 2) {
    if (hs + 1 < 16) n += nb;
    if (hs + 2 < 16) n += nb;
  } else {
    if (hs + 1 < 16) n += nb;
    if (hs >= 1 && ((hs - 1) & 1) == 0 && (hs + 1) / 2 < 8) n += 2;
    if (hs + 2 < 16) n += nb;
  }
  return n;
}
template <int HS, bool G2>
__device__ __forceinline__ void f1_loop(const F1Ctx& c, const char* smem, bf16x8 (&af)[4], bf16x8 (&bq)[3],
                                        f32x4 (&acc)[4][3]) {
  if constexpr (HS < 16) {
    if constexpr (HS > 0) {
#pragma unroll
      for (int j = 0; j < 3; ++j) {
        acc[0][j] = mfma16(bq[j], af[0], acc[0][j]);
        acc[1][j] = mfma16(bq[j], af[1], acc[1][j]);
      }
    }
    __builtin_amdgcn_sched_barrier(0);
    wait_vm<f1_allowed(HS, G2 ? 1 : 2)>();
    WAIT_LGKM0();
    __builtin_amdgcn_s_barrier();
    asm volatile("" ::: "memory");
    const char* ab = smem + ((HS >> 1) & 1) * 16384 + c.aoff[HS & 1];
    const char* bbase = smem + (HS & 3) * 12288;
    __builtin_amdgcn_sched_barrier(0);
    bf16x8 bn[3];
    af[0] = *(const bf16x8*)(ab);
    bn[0] = tr_frag(bbase, c.boff[0], c.boff[0] + 512);
    af[1] = *(const bf16x8*)(ab + 2048);
    bn[1] = tr_frag(bbase, c.boff[1], c.boff[1] + 512);
    bn[2] = tr_frag(bbase, c.boff[2], c.boff[2] + 512);
    __builtin_amdgcn_sched_barrier(0);
#pragma unroll
    for (int j = 0; j < 3; ++j) {
      if constexpr (HS > 0) {
        acc[2][j] = mfma16(bq[j], af[2], acc[2][j]);
        acc[3][j] = mfma16(bq[j], af[3], acc[3][j]);
      }
      __builtin_amdgcn_sched_barrier(0);
      if (j == 0) {
        if constexpr ((HS & 1) == 0 && (HS + 2) / 2 < 8) f1_stage_a(c, (HS + 2) / 2);
      } else if (j == 1) {
        if constexpr (HS + 3 < 16) f1_stage_b<G2>(c, HS + 3);
      }
      __builtin_amdgcn_sched_barrier(0);
    }
    af[2] = *(const bf16x8*)(ab + 4096);
    af[3] = *(const bf16x8*)(ab + 6144);
    f1_loop<HS + 1, G2>(c, smem, af, bn, acc);
  } else {
#pragma unroll
    for (int j = 0; j < 3; ++j)
#pragma unroll
      for (int mt = 0; mt < 4; ++mt) acc[mt][j] = mfma16(bq[j], af[mt], acc[mt][j]);
  }
}

__device__ __forceinline__ void ffn1_block(char* smem, int rb, int cb, const __bf16* __restrict__ x,
                                           const __bf16* __restrict__ wg, const __bf16* __restrict__ wu,
                                           __bf16* __restrict__ h) {
  const int tid = threadIdx.x, lane = tid & 63, w = __builtin_amdgcn_readfirstlane(tid >> 6);
  const int row0 = rb * 128, f0 = cb * 96;
  const int wm = w >> 2, sel = (w >> 1) & 1, wn = w & 1;
  const int r = lane & 15, g = lane >> 4, q = (lane >> 2) & 3, p = lane & 3;
  __builtin_amdgcn_s_setprio(PRIO_F1);
  F1Ctx c;
  c.x = x; c.wg = wg; c.wu = wu; c.w = w; c.lbase = lds_addr_of(smem);
  {
#pragma unroll
    for (int i = 0; i < 2; ++i) {
      const int row = 64 * i + 8 * w + (lane >> 3), ch = (lane & 7) ^ ((row >> 1) & 7);
      c.voffA[i] = (unsigned)(((row0 + row) * D + ch * 8) * 2);
    }
    const int kk = 8 * (w & 3) + (lane >> 3), pos = lane & 7;
    const int fp = ((kk >> 1) & 1) | (((kk >> 3) & 1) << 1);
    const int i = (pos >> 1) ^ fp;
    const int par = (kk + cb) & 1;
    int c0 = f0 + 16 * (i + 2 * par) + 8 * (pos & 1);
    int c1 = f0 + 16 * ((i & 1) + 4 * (1 - par)) + 8 * (pos & 1);
    c0 = c0 > F - 8 ? F - 8 : c0;
    c1 = c1 > F - 8 ? F - 8 : c1;
    c.voffB = (unsigned)((kk * F + c0) * 2);
    c.wmix = (i < 2 ? wg : wu) + kk * F + c1;
  }
#pragma unroll
  for (int ks = 0; ks < 2; ++ks)
    c.aoff[ks] = (64 * wm + r) * 128 + (((4 * ks + g) ^ ((r >> 1) & 7)) << 4);
  {
    const int fp = (q >> 1) | ((g & 1) << 1);
#pragma unroll
    for (int j = 0; j < 3; ++j) {
      const int t = 3 * wn + j, par = (q + cb) & 1, tt = t - 2 * par;
      const bool whole = tt >= 0 && tt < 4;
      const int sub = whole ? 2 * sel : 1, ps = whole ? tt : 2 * sel + (t & 1);
      c.boff[j] = F1_B + sub * 4096 + (8 * g + q) * 128 + ((ps ^ fp) << 5) + 8 * p;
      asm volatile("" : "+v"(c.boff[j]));
    }
  }
  f32x4 acc[4][3];
#pragma unroll
  for (int mt = 0; mt < 4; ++mt)
#pragma unroll
    for (int j = 0; j < 3; ++j) acc[mt][j] = f32x4{0.f, 0.f, 0.f, 0.f};
  bf16x8 af[4], bq[3];

  f1_stage_a(c, 0);
  if (w < 4) {
    f1_stage_b<false>(c, 0);
    f1_stage_b<false>(c, 1);
    f1_stage_b<false>(c, 2);
    f1_loop<0, false>(c, smem, af, bq, acc);
  } else {
    f1_stage_b<true>(c, 0);
    f1_stage_b<true>(c, 1);
    f1_stage_b<true>(c, 2);
    f1_loop<0, true>(c, smem, af, bq, acc);
  }

  __syncthreads();
  constexpr int HT = 49152, HTP = 208;
  auto finish = [&](auto SEL) {
    constexpr int S = decltype(SEL)::value;
#pragma unroll
    for (int m2 = 0; m2 < 2; ++m2)
#pragma unroll
      for (int j = 0; j < 3; ++j) *(f32x4*)(smem + (w * 6 + m2 * 3 + j) * 1024 + lane * 16) = acc[2 * (1 - S) + m2][j];
    __syncthreads();
#pragma unroll
    for (int m2 = 0; m2 < 2; ++m2)
#pragma unroll
      for (int j = 0; j < 3; ++j) {
        const f32x4 o4 = *(const f32x4*)(smem + ((w ^ 2) * 6 + m2 * 3 + j) * 1024 + lane * 16);
        const f32x4 g4 = S ? o4 : acc[m2][j], u4 = S ? acc[2 + m2][j] : o4;
        float hv[4];
#pragma unroll
        for (int e = 0; e < 4; ++e) {
          const float gv = g4[e];
          const float t = gv * __builtin_fmaf(gv * gv, -0.10294324f, -2.3022082f);
          hv[e] = (gv * u4[e]) * __builtin_amdgcn_rcpf(1.f + __builtin_amdgcn_exp2f(t));
        }
        u32x2 o = {pk2(hv[0], hv[1]), pk2(hv[2], hv[3])};
        *(u32x2*)(smem + HT + (64 * wm + 32 * S + 16 * m2 + r) * HTP + (16 * (3 * wn + j) + 4 * g) * 2) = o;
      }
  };
  if (sel) finish(std::integral_constant<int, 1>{});
  else finish(std::integral_constant<int, 0>{});
  __syncthreads();
#pragma unroll
  for (int i = 0; i < 3; ++i) {
    const int idx = tid + 512 * i, row = idx / 12, c8 = idx - row * 12;
    if (f0 + 8 * c8 < F)
      *(u32x4*)(h + (size_t)(row0 + row) * HP + f0 + 8 * c8) = *(const u32x4*)(smem + HT + row * HTP + c8 * 16);
  }
}

__device__ __forceinline__ void gate_block(char* smem, int rb, const __bf16* __restrict__ x,
                                           const __bf16* __restrict__ gw, float* __restrict__ gout,
                                           __bf16* __restrict__ uT) {
  const int tid = threadIdx.x, lane = tid & 63, w = __builtin_amdgcn_readfirstlane(tid >> 6);
  const int g = lane >> 4, q = (lane >> 2) & 3, p = lane & 3;
  const unsigned lbase = lds_addr_of(smem);
  const int grow = tid >> 3, gpart = tid & 7, gf = (grow & 3) | (((grow >> 3) & 1) << 2);
  u32x4 gwv[8];
#pragma unroll
  for (int i = 0; i < 8; ++i) gwv[i] = *(const u32x4*)(gw + ((gpart ^ gf) + 8 * (i >> 1)) * 16 + 8 * (i & 1));
#pragma unroll 1
  for (int pass = 0; pass < 2; ++pass) {
    const int l0 = rb * 128 + pass * 64;
#pragma unroll
    for (int i = 0; i < 8; ++i) {
      const int row = 8 * w + i, fr = (row & 3) | (((row >> 3) & 1) << 2);
      glds16(x + (size_t)(l0 + row) * D, (unsigned)((((lane >> 1) ^ fr) * 2 + (lane & 1)) * 16), lbase + row * 1024);
    }
    WAIT_VM(0);
    __syncthreads();
#pragma unroll
    for (int i = 0; i < 8; ++i) {
      const int unit = w * 8 + i, c16 = unit >> 1, half = unit & 1;
      const int ra = 32 * half + 8 * g + q, rb2 = ra + 4;
      const int fa = (ra & 3) | (((ra >> 3) & 1) << 2);
      const bf16x8 v = tr_frag(smem, ra * 1024 + ((c16 ^ fa) << 5) + 8 * p, rb2 * 1024 + ((c16 ^ fa) << 5) + 8 * p);
      *(bf16x8*)(uT + (size_t)(16 * c16 + (lane & 15)) * L + l0 + 32 * half + 8 * g) = v;
    }
    float s = 0.f;
#pragma unroll
    for (int i = 0; i < 8; ++i) {
      const u32x4 xv = *(const u32x4*)(smem + grow * 1024 + (gpart + 8 * (i >> 1)) * 32 + 16 * (i & 1));
#pragma unroll
      for (int e = 0; e < 4; ++e) {
        const unsigned xe = xv[e], we = gwv[i][e];
        s = __builtin_amdgcn_fdot2_f32_bf16(__builtin_bit_cast(bf16x2, xe), __builtin_bit_cast(bf16x2, we), s, false);
      }
    }
    s += __shfl_xor(s, 1);
    s += __shfl_xor(s, 2);
    s += __shfl_xor(s, 4);
    if (gpart == 0) gout[l0 + grow] = bfr(1.f / (1.f + __expf(-bfr(s))));
    __syncthreads();
  }
}

constexpr int KC_PITCH = 768;
constexpr int KG_PITCH = 160;
constexpr int KG_ARR = 16 * KG_PITCH;
constexpr int KG_TAB = 8 * 2 * KG_ARR;
typedef __attribute__((ext_vector_type(2))) float f32x2;

__device__ __forceinline__ f32x2 cexp(float zr, float zi, float p) {
  const float e = __builtin_amdgcn_exp2f(zr * p);
  const float ph = __builtin_amdgcn_fractf(zi * p);
  return f32x2{e * __builtin_amdgcn_cosf(ph), e * __builtin_amdgcn_sinf(ph)};
}
__device__ __forceinline__ f32x2 cmul(f32x2 a, f32x2 b) {
  return f32x2{a[0] * b[0] - a[1] * b[1], a[0] * b[1] + a[1] * b[0]};
}
__device__ __forceinline__ void kgen_block(char* smem, int d0, const __bf16* __restrict__ lar,
                                           const __bf16* __restrict__ aim, const __bf16* __restrict__ cre,
                                           const __bf16* __restrict__ cim, const __bf16* __restrict__ ldt,
                                           unsigned* __restrict__ kc, unsigned* __restrict__ aG,
                                           const __bf16* __restrict__ wd) {
  const int tid = threadIdx.x, lane = tid & 63, w = __builtin_amdgcn_readfirstlane(tid >> 6);
  unsigned pf_dummy = 0;
  if (tid < 43)
    asm volatile("global_load_dword %0, %1, off" : "=v"(pf_dummy) : "v"(wd + (size_t)((d0 >> 1) * 43 + tid) * 64) : "memory");
  {
    u32x4* z = (u32x4*)(aG + (size_t)d0 * L) + tid;
    z[0] = u32x4{0u, 0u, 0u, 0u};
    z[512] = u32x4{0u, 0u, 0u, 0u};
    if (tid == 0) aG[(size_t)D * L + (d0 >> 1)] = 0u;
  }
  const int ch = w >> 2, wq = w & 3, tc = tid & 255;
  const int r = lane & 15, g = lane >> 4, hh = lane >> 5;
  char* wb = smem + w * 2 * KG_ARR;
  char* tb = smem + KG_TAB + ch * (4 * 656 * 4);
  __builtin_amdgcn_s_setprio(PRIO_KG);
  float zr, zi;
  f32x2 cc;
  {
    const int d = d0 + ch, n = 32 * wq + (lane & 31);
    const float dt = bfr(expf((float)ldt[d]));
    const float ar = -bfr(expf((float)lar[d * NS + n]));
    zr = (ar * dt) * 1.4426950408889634f;
    zi = ((float)aim[d * NS + n] * dt) * 0.15915494309189535f;
    cc = f32x2{2.f * ((float)cre[d * NS + n] * dt), 2.f * ((float)cim[d * NS + n] * dt)};
  }
  const float fh = (float)(1 + hh);
  const f32x2 e128 = cexp(zr, zi, 128.f), e24 = cexp(zr, zi, 2.f * fh);
  f32x2 v[4], st[4];
  v[0] = cexp(zr, -zi, 64.f * hh);                 st[0] = cexp(zr, -zi, 8.f);
  v[1] = hh ? cmul(cc, e128) : cc;                 st[1] = cexp(zr, zi, 1.f);
  v[2] = cmul(cc, cexp(zr, zi, 256.f * fh));       st[2] = hh ? e128 : e24;
  v[3] = cmul(cc, cexp(zr, zi, 1024.f * fh));      st[3] = e128;
  const f32x2 v24 = cmul(v[2], hh ? e24 : e128);
  unsigned vh[4][8], vl[4][8];
#pragma unroll
  for (int i = 0; i < 8; ++i)
#pragma unroll
    for (int t = 0; t < 4; ++t) {
      vh[t][i] = pk2(v[t][0], v[t][1]);
      vl[t][i] = pk2(v[t][0] - lo_f(vh[t][i]), v[t][1] - hi_f(vh[t][i]));
      if (t == 2 && i == 3) v[t] = v24;
      else if (i < 7) v[t] = cmul(v[t], st[t]);
    }
  char* wr = wb + (8 * hh) * KG_PITCH + (lane & 31) * 4;
  const char* rd = wb + r * KG_PITCH + g * 16;
  bf16x8 fh_[4][2], fl_[4][2];
#pragma unroll
  for (int t = 0; t < 4; ++t) {
    __builtin_amdgcn_wave_barrier();
#pragma unroll
    for (int i = 0; i < 8; ++i) {
      *(unsigned*)(wr + i * KG_PITCH) = vh[t][i];
      *(unsigned*)(wr + i * KG_PITCH + KG_ARR) = vl[t][i];
    }
    __builtin_amdgcn_wave_barrier();
#pragma unroll
    for (int ss = 0; ss < 2; ++ss) {
      fh_[t][ss] = *(const bf16x8*)(rd + ss * 64);
      fl_[t][ss] = *(const bf16x8*)(rd + ss * 64 + KG_ARR);
    }
  }
  f32x4 acc[3];
#pragma unroll
  for (int m = 0; m < 3; ++m) acc[m] = f32x4{0.f, 0.f, 0.f, 0.f};
#pragma unroll
  for (int ss = 0; ss < 2; ++ss)
#pragma unroll
    for (int m = 0; m < 3; ++m) {
      acc[m] = mfma16(fl_[1 + m][ss], fh_[0][ss], acc[m]);
      acc[m] = mfma16(fh_[1 + m][ss], fl_[0][ss], acc[m]);
      acc[m] = mfma16(fh_[1 + m][ss], fh_[0][ss], acc[m]);
    }
  {
    float* tab = (float*)tb + wq * 656;
    *(f32x4*)(tab + 128 * (g >> 1) + 4 * (g & 1) + 8 * r) = acc[0];
    if (g < 2) {
      *(f32x4*)(tab + 256 + 64 * g + 4 * r) = acc[1];
#pragma unroll
      for (int e = 0; e < 4; ++e) tab[512 + 64 * g + 16 * e + r] = acc[2][e];
    } else {
#pragma unroll
      for (int e = 0; e < 4; ++e) tab[384 + (g & 1) + 32 * e + 2 * r] = acc[1][e];
      if (lane == 32) tab[640] = acc[2][0];
    }
  }
  __syncthreads();
  {
    const float* tab = (const float*)tb;
    unsigned* kd = kc + (size_t)(d0 + ch) * KC_PITCH;
#pragma unroll
    for (int e = 0; e < 3; ++e) {
      const int id = tc + 256 * e;
      if (id < 641) {
        const float v = (tab[id] + tab[656 + id]) + (tab[2 * 656 + id] + tab[3 * 656 + id]);
        const float vh = bfr(v);
        kd[id] = pk2(vh, v - vh);
      }
    }
  }
  asm volatile("s_waitcnt vmcnt(0)" : "+v"(pf_dummy)::"memory");
}

NOPK __global__ __launch_bounds__(512, 4) void k1(const __bf16* __restrict__ u, const __bf16* __restrict__ lar,
                                             const __bf16* __restrict__ aim, const __bf16* __restrict__ cre,
                                             const __bf16* __restrict__ cim, const __bf16* __restrict__ ldt,
                                             const __bf16* __restrict__ wg, const __bf16* __restrict__ wu,
                                             const __bf16* __restrict__ gw, __bf16* __restrict__ h,
                                             __bf16* __restrict__ uT, unsigned* __restrict__ kc,
                                             float* __restrict__ gout, unsigned* __restrict__ aG,
                                             const __bf16* __restrict__ wd) {
  extern __shared__ __attribute__((aligned(16))) char smem[];
  const int b = blockIdx.x;
  const int xcd = b & 7, i = (b & 255) >> 3;
  if (b < 256) {
    const int rb = 4 * (xcd >> 1) + (i & 3), cb = 8 * (xcd & 1) + (i >> 2);
    if (cb < 15) ffn1_block(smem, rb, cb, u, wg, wu, h);
    else gate_block(smem, rb, u, gw, gout, uT);
  } else {
    kgen_block(smem, 64 * xcd + 2 * i, lar, aim, cre, cim, ldt, kc, aG, wd);
  }
}

constexpr unsigned A_TAG = 0x5a17c3e9u;
constexpr int UCP = 4160;
constexpr int KARR = (240 + L) * 2;
constexpr int OFF_K = 4 * UCP;
constexpr int OFF_TAB = OFF_K + 2 * KARR;
constexpr int OFF_RED = UCP;
constexpr int CH_BYTES = OFF_RED + 32768;
static_assert(OFF_TAB + 3072 <= CH_BYTES && 2 * CH_BYTES <= LDS_BYTES, "lds");

struct PosLut {
  unsigned short v[L];
  constexpr PosLut() : v() {
    for (int j = 0; j < L; ++j) {
      const int sh = (j >= 256) + (j >= 512) + (j >= 1024);
      int q = j >> sh;
      if (sh) {
        const int rem = j & ((1 << sh) - 1), half = 1 << (sh - 1);
        if (rem > half || (rem == half && (q & 1))) ++q;
      }
      v[j] = (unsigned short)(4 * (128 * sh + q));
    }
  }
};
__device__ const PosLut g_poslut{};

__device__ __forceinline__ void conv_block(char* smem, int d0, const __bf16* __restrict__ uT,
                                           const unsigned* __restrict__ kc, const __bf16* __restrict__ dsk,
                                           unsigned* __restrict__ aG) {
  const int tid = threadIdx.x, lane = tid & 63, w = __builtin_amdgcn_readfirstlane(tid >> 6);
  const int ch = w >> 2, wq = w & 3, tc = tid & 255;
  const int r = lane & 15, g = lane >> 4, q = (lane >> 2) & 3, p = lane & 3;
  char* cb = smem + ch * CH_BYTES;
  const int d = d0 + ch;
  __builtin_amdgcn_s_setprio(3);
  {
    const unsigned lb = lds_addr_of(cb);
    glds16(uT + (size_t)d * L + wq * 512, lane * 16, lb + 32 + wq * 1024);
    if (wq < 3) glds16(kc + (size_t)d * KC_PITCH + wq * 256, lane * 16, lb + OFF_TAB + wq * 1024);
    const u32x4 z = {0u, 0u, 0u, 0u};
    if (tc < 60) *(u32x4*)(cb + OFF_K + (tc >= 30 ? KARR : 0) + (tc % 30) * 16) = z;
    else if (tc < 68) *(u32x4*)(cb + ((tc - 60) >> 1) * UCP + ((tc - 60) & 1) * 16) = z;
  }
  const float dsv = (float)dsk[d];
  const u32x4 lut = *(const u32x4*)(g_poslut.v + 8 * tc);
  WAIT_VM(0);
  __syncthreads();
  {
    const u32x4 pa = *(const u32x4*)(cb + 16 + 16 * tc), pb = *(const u32x4*)(cb + 32 + 16 * tc);
    const u32x4 c1 = {__builtin_amdgcn_alignbit(pb[0], pa[3], 16), __builtin_amdgcn_alignbit(pb[1], pb[0], 16),
                      __builtin_amdgcn_alignbit(pb[2], pb[1], 16), __builtin_amdgcn_alignbit(pb[3], pb[2], 16)};
    const u32x4 c2 = {pa[3], pb[0], pb[1], pb[2]};
    const u32x4 c3 = {__builtin_amdgcn_alignbit(pa[3], pa[2], 16), c1[0], c1[1], c1[2]};
    *(u32x4*)(cb + 1 * UCP + 32 + 16 * tc) = c1;
    *(u32x4*)(cb + 2 * UCP + 32 + 16 * tc) = c2;
    *(u32x4*)(cb + 3 * UCP + 32 + 16 * tc) = c3;
    u32x4 kh, kl;
#pragma unroll
    for (int e = 0; e < 4; ++e) {
      const unsigned t0 = *(const unsigned*)(cb + OFF_TAB + (lut[e] & 0xffffu));
      const unsigned t1 = *(const unsigned*)(cb + OFF_TAB + (lut[e] >> 16));
      kh[e] = __builtin_amdgcn_perm(t1, t0, 0x05040100u);
      kl[e] = __builtin_amdgcn_perm(t1, t0, 0x07060302u);
    }
    *(u32x4*)(cb + OFF_K + 480 + 16 * tc) = kh;
    *(u32x4*)(cb + OFF_K + KARR + 480 + 16 * tc) = kl;
  }
  __syncthreads();
  __builtin_amdgcn_s_setprio(PRIO_CV);
  f32x4 acc[8];
#pragma unroll
  for (int t = 0; t < 8; ++t) acc[t] = f32x4{0.f, 0.f, 0.f, 0.f};
#pragma unroll
  for (int rr = 0; rr < 2; ++rr) {
    if (rr == 1) __builtin_amdgcn_s_setprio(PRIO_CV2);
    const int rho = 2 * wq + rr;
    const int abase = q * UCP + 2 * (16 + 16 + 32 * rho - 8 * g + 4 * p);
    const int bbase = OFF_K + 2 * (240 - 16 - 32 * rho + 16 * r + 8 * g);
    bf16x8 af[8];
#pragma unroll
    for (int s = 0; s < 8; ++s) af[s] = tr_frag(cb, abase + 512 * s, abase + 512 * s - 8);
#pragma unroll
    for (int t = 0; t < 8; ++t) {
      const bf16x8 bh = *(const bf16x8*)(cb + bbase + 512 * t);
#pragma unroll
      for (int s = 0; s + t < 8; ++s) acc[s + t] = mfma16(af[s], bh, acc[s + t]);
      const bf16x8 bl = *(const bf16x8*)(cb + bbase + KARR + 512 * t);
#pragma unroll
      for (int s = 0; s + t < 8; ++s) acc[s + t] = mfma16(af[s], bl, acc[s + t]);
    }
  }
  __builtin_amdgcn_s_setprio(3);
  __syncthreads();
  if (tid == 256) __hip_atomic_store((gu32*)(aG + (size_t)D * L + (d0 >> 1)), 1u, __ATOMIC_RELAXED, __HIP_MEMORY_SCOPE_AGENT);
#pragma unroll
  for (int t = 0; t < 8; ++t)
    if ((t >> 1) != wq) *(f32x4*)(cb + OFF_RED + (wq * 8 + t) * 1024 + lane * 16) = acc[t];
  __syncthreads();
#pragma unroll
  for (int tt = 0; tt < 2; ++tt) {
    const int t = 2 * wq + tt;
    f32x4 own = f32x4{0.f, 0.f, 0.f, 0.f};
#pragma unroll
    for (int t2 = 0; t2 < 8; ++t2)
      if (t2 == t) own = acc[t2];
    f32x4 s = f32x4{0.f, 0.f, 0.f, 0.f};
#pragma unroll
    for (int sw = 0; sw < 4; ++sw) {
      const f32x4 o = *(const f32x4*)(cb + OFF_RED + (sw * 8 + t) * 1024 + lane * 16);
      s += (sw == wq) ? own : o;
    }
    const int l = 256 * t + 16 * r + 4 * g;
    const u32x2 uu = *(const u32x2*)(cb + 32 + 2 * l);
    const float a0 = bfr(s[0]) + bfr(lo_f(uu[0]) * dsv), a1 = bfr(s[1]) + bfr(hi_f(uu[0]) * dsv);
    const float a2 = bfr(s[2]) + bfr(lo_f(uu[1]) * dsv), a3 = bfr(s[3]) + bfr(hi_f(uu[1]) * dsv);
    const u32x4 gr = {pk2(a0, a1), A_TAG, pk2(a2, a3), A_TAG};
    asm volatile("global_store_dwordx4 %0, %1, off sc1\n\ts_nop 1" ::"v"(aG + (size_t)d * L + l), "v"(gr) : "memory");
  }
}

constexpr int F2_NS = 5;
constexpr int F2_STAGE = 16384;
constexpr int F2_NST = 22;

struct F2Ctx {
  const __bf16 *h, *wd;
  unsigned avoff, bvoff, bvoff_last, lbase;
  int w, kh;
  int aoff[2], boff[2];
  const unsigned* asrc;
  mutable u32x4 x0, x1;
};
__device__ __forceinline__ void f2_stage(const F2Ctx& c, int st) {
  const unsigned base = c.lbase + (st % F2_NS) * F2_STAGE;
  glds16(c.h + st * 64, c.avoff, base + c.w * 1024);
  glds16(c.wd + (size_t)st * 64 * D, st == F2_NST - 1 ? c.bvoff_last : c.bvoff, base + 8192 + c.w * 1024);
}
template <int ST>
__device__ __forceinline__ void f2_loop(const F2Ctx& c, const char* smem, bf16x8 (&ao)[2], bf16x8 (&bo)[2],
                                        f32x4 (&acc)[2][2]) {
  if constexpr (ST < F2_NST) {
    constexpr int rem = (F2_NST - 1 - ST) < (F2_NS - 2) ? (F2_NST - 1 - ST) : (F2_NS - 2);
    wait_vm<2 * rem>();
    WAIT_LGKM0();
    __builtin_amdgcn_s_barrier();
    asm volatile("" ::: "memory");
    if constexpr (ST + F2_NS - 1 < F2_NST) f2_stage(c, ST + F2_NS - 1);
    if constexpr (ST == F2_NST - 3)
      asm volatile("global_load_dwordx4 %0, %2, off sc1\n\tglobal_load_dwordx4 %1, %3, off sc1"
                   : "=&v"(c.x0), "=&v"(c.x1) : "v"(c.asrc), "v"(c.asrc + 16 * L) : "memory");
    const char* base = smem + (ST % F2_NS) * F2_STAGE;
    bf16x8 an[2], bn[2];
#pragma unroll
    for (int mt = 0; mt < 2; ++mt) an[mt] = *(const bf16x8*)(base + c.aoff[mt]);
#pragma unroll
    for (int nt = 0; nt < 2; ++nt) bn[nt] = tr_frag(base, c.boff[nt], c.boff[nt] + 512);
    __builtin_amdgcn_sched_barrier(0);
    if constexpr (ST > 0) {
#pragma unroll
      for (int mt = 0; mt < 2; ++mt)
#pragma unroll
        for (int nt = 0; nt < 2; ++nt) acc[mt][nt] = mfma16(ao[mt], bo[nt], acc[mt][nt]);
    }
    __builtin_amdgcn_sched_barrier(0);
    f2_loop<ST + 1>(c, smem, an, bn, acc);
  } else {
    if (c.kh == 0) {
#pragma unroll
      for (int mt = 0; mt < 2; ++mt)
#pragma unroll
        for (int nt = 0; nt < 2; ++nt) acc[mt][nt] = mfma16(ao[mt], bo[nt], acc[mt][nt]);
    }
  }
}

__device__ __forceinline__ void ffn2_block(char* smem, int row0, int cbk, const __bf16* __restrict__ h,
                                           const __bf16* __restrict__ wd, const unsigned* __restrict__ aG,
                                           const float* __restrict__ gin, __bf16* __restrict__ out) {
  const int col0 = 64 * cbk;
  const int tid = threadIdx.x, lane = tid & 63, w = __builtin_amdgcn_readfirstlane(tid >> 6);
  const int r = lane & 15, g = lane >> 4, q = (lane >> 2) & 3, p = lane & 3;
  const int kh = w >> 2, wm = (w >> 1) & 1, wn = w & 1;
  __builtin_amdgcn_s_setprio(PRIO_F2);
  F2Ctx c;
  c.h = h; c.wd = wd; c.w = w; c.kh = kh; c.lbase = lds_addr_of(smem);
  {
    const int row = 8 * w + (lane >> 3), ch = (lane & 7) ^ ((row >> 1) & 7);
    c.avoff = (unsigned)(((row0 + row) * HP + ch * 8) * 2);
    const int bk = 8 * w + (lane >> 3);
    const int pos = lane & 7, fp = ((bk >> 1) & 1) | (((bk >> 3) & 1) << 1);
    const int cc = col0 + 16 * ((pos >> 1) ^ fp) + 8 * (pos & 1);
    c.bvoff = (unsigned)((bk * D + cc) * 2);
    c.bvoff_last = (unsigned)(((bk > 31 ? 31 : bk) * D + cc) * 2);
  }
#pragma unroll
  for (int mt = 0; mt < 2; ++mt) {
    const int row = 32 * wm + 16 * mt + r, ch = 4 * kh + g;
    c.aoff[mt] = row * 128 + ((ch ^ ((row >> 1) & 7)) << 4);
  }
  const int fpl = (q >> 1) | ((g & 1) << 1);
#pragma unroll
  for (int nt = 0; nt < 2; ++nt) c.boff[nt] = 8192 + (32 * kh + 8 * g + q) * 128 + (((2 * wn + nt) ^ fpl) << 5) + 8 * p;

  f32x4 acc[2][2];
#pragma unroll
  for (int mt = 0; mt < 2; ++mt)
#pragma unroll
    for (int nt = 0; nt < 2; ++nt) acc[mt][nt] = f32x4{0.f, 0.f, 0.f, 0.f};

#pragma unroll
  for (int st = 0; st < F2_NS - 1; ++st) f2_stage(c, st);
  const int lr = 32 * wm + 16 * kh + 4 * g;
  const f32x4 gv = *(const f32x4*)(gin + row0 + lr);
  c.asrc = aG + (size_t)(col0 + 32 * wn + r) * L + row0 + lr;
  bf16x8 a0[2], b0[2];
  f2_loop<0>(c, smem, a0, b0, acc);
  WAIT_LGKM0();
  __builtin_amdgcn_s_barrier();
  asm volatile("" ::: "memory");
  asm volatile("s_waitcnt vmcnt(0)" : "+v"(c.x0), "+v"(c.x1)::"memory");
  const bool early = __all(c.x0[1] == A_TAG && c.x0[3] == A_TAG && c.x1[1] == A_TAG && c.x1[3] == A_TAG);
  if (w == 0 && !early) {
    const gu32* fl = (const gu32*)(aG + (size_t)D * L + 32 * cbk + (lane & 31));
    for (int spin = 0; spin < (1 << 22); ++spin) {
      const unsigned v = __hip_atomic_load(fl, __ATOMIC_RELAXED, __HIP_MEMORY_SCOPE_AGENT);
      if (__all(v == 1u)) break;
      __builtin_amdgcn_s_sleep(2);
    }
  }
  {
    char* red = smem + (w & 3) * 4096 + kh * 2048;
    const f32x4 o0 = kh ? acc[0][0] : acc[1][0], o1 = kh ? acc[0][1] : acc[1][1];
    *(f32x4*)(red + lane * 16) = o0;
    *(f32x4*)(red + 1024 + lane * 16) = o1;
  }
  __syncthreads();
  unsigned long long av[2];
  {
    const unsigned* src = c.asrc;
    av[0] = (unsigned long long)c.x0[0] | ((unsigned long long)c.x0[2] << 32);
    av[1] = (unsigned long long)c.x1[0] | ((unsigned long long)c.x1[2] << 32);
    for (int spin = 0; !early && spin < (1 << 22); ++spin) {
      u32x4 x0, x1;
      asm volatile("global_load_dwordx4 %0, %2, off sc1\n\tglobal_load_dwordx4 %1, %3, off sc1\n\ts_waitcnt vmcnt(0)"
                   : "=&v"(x0), "=&v"(x1) : "v"(src), "v"(src + 16 * L) : "memory");
      av[0] = (unsigned long long)x0[0] | ((unsigned long long)x0[2] << 32);
      av[1] = (unsigned long long)x1[0] | ((unsigned long long)x1[2] << 32);
      const bool ok = x0[1] == A_TAG && x0[3] == A_TAG && x1[1] == A_TAG && x1[3] == A_TAG;
      if (__all(ok)) break;
      __builtin_amdgcn_s_sleep(4);
    }
  }
  constexpr int OT = 16384, OTP = 144;
  {
    const char* red = smem + (w & 3) * 4096 + (1 - kh) * 2048;
#pragma unroll
    for (int nt = 0; nt < 2; ++nt) {
      const f32x4 o = *(const f32x4*)(red + nt * 1024 + lane * 16);
      const f32x4 own = kh ? acc[1][nt] : acc[0][nt];
      const f32x4 s = kh ? (o + own) : (own + o);
      const unsigned alo = (unsigned)av[nt], ahi = (unsigned)(av[nt] >> 32);
      const float a[4] = {lo_f(alo), hi_f(alo), lo_f(ahi), hi_f(ahi)};
#pragma unroll
      for (int e = 0; e < 4; ++e) {
        const float o2 = bfr(gv[e] * a[e]) + bfr(bfr(1.f - gv[e]) * bfr(s[e]));
        *(__bf16*)(smem + OT + (lr + e) * OTP + (32 * wn + 16 * nt + r) * 2) = (__bf16)o2;
      }
    }
  }
  __syncthreads();
  {
    const int row = tid >> 3, c8 = tid & 7;
    const u32x4 v = *(const u32x4*)(smem + OT + row * OTP + c8 * 16);
    *(u32x4*)(out + (size_t)(row0 + row) * D + col0 + c8 * 8) = v;
  }
}

NOPK __global__ __launch_bounds__(512, 4) void k2(const __bf16* __restrict__ h, const __bf16* __restrict__ wd,
                                             const __bf16* __restrict__ uT, const unsigned* __restrict__ kc,
                                             const __bf16* __restrict__ dsk, unsigned* __restrict__ aG,
                                             const float* __restrict__ gin, __bf16* __restrict__ out) {
  extern __shared__ __attribute__((aligned(16))) char smem[];
  const int b = blockIdx.x, xcd = b & 7, i = (b & 255) >> 3;
  if (b < 256) {
    const int pair = 32 * xcd + i;
    conv_block(smem, 2 * pair, uT, kc, dsk, aG);
  } else {
    ffn2_block(smem, 64 * (4 * xcd + (i & 3)), i >> 2, h, wd, aG, gin, out);
  }
}

extern "C" void kernel_launch(void* const* d_in, const int* in_sizes, int n_in, void* d_out, int out_size,
                              void* d_ws, size_t ws_size, hipStream_t stream) {
  const __bf16* u = (const __bf16*)d_in[0];
  const __bf16* lar = (const __bf16*)d_in[1];
  const __bf16* aim = (const __bf16*)d_in[2];
  const __bf16* cre = (const __bf16*)d_in[3];
  const __bf16* cim = (const __bf16*)d_in[4];
  const __bf16* ldt = (const __bf16*)d_in[5];
  const __bf16* dsk = (const __bf16*)d_in[6];
  const __bf16* wg = (const __bf16*)d_in[8];
  const __bf16* wu = (const __bf16*)d_in[9];
  const __bf16* wd = (const __bf16*)d_in[10];
  const __bf16* gw = (const __bf16*)d_in[11];
  char* ws = (char*)d_ws;
  __bf16* h = (__bf16*)ws;
  ws += (size_t)L * HP * 2;
  unsigned* aG = (unsigned*)ws;
  ws += (size_t)D * L * 4 + 1024;
  __bf16* uT = (__bf16*)ws;
  ws += (size_t)D * L * 2;
  unsigned* kc = (unsigned*)ws;
  ws += (size_t)D * KC_PITCH * 4;
  float* gout = (float*)ws;
  (void)hipFuncSetAttribute((const void*)k1, hipFuncAttributeMaxDynamicSharedMemorySize, LDS_BYTES);
  (void)hipFuncSetAttribute((const void*)k2, hipFuncAttributeMaxDynamicSharedMemorySize, LDS_BYTES);
  hipLaunchKernelGGL(k1, dim3(512), dim3(512), LDS_BYTES, stream, u, lar, aim, cre, cim, ldt, wg, wu, gw, h, uT, kc,
                     gout, aG, wd);
  hipLaunchKernelGGL(k2, dim3(512), dim3(512), LDS_BYTES, stream, (const __bf16*)h, wd, (const __bf16*)uT,
                     (const unsigned*)kc, dsk, aG, (const float*)gout, (__bf16*)d_out);
}
```

```cpp
#include <hip/hip_runtime.h>
#include <hip/hip_bf16.h>
#include <type_traits>

typedef __attribute__((ext_vector_type(8))) __bf16 bf16x8;
typedef __attribute__((ext_vector_type(2))) __bf16 bf16x2;
typedef __attribute__((ext_vector_type(4))) short s16x4;
typedef __attribute__((ext_vector_type(8))) short s16x8;
typedef __attribute__((ext_vector_type(4))) float f32x4;
typedef __attribute__((ext_vector_type(16))) float f32x16;
typedef __attribute__((ext_vector_type(4))) unsigned u32x4;
typedef __attribute__((ext_vector_type(2))) unsigned u32x2;
typedef __attribute__((address_space(3))) s16x4* lds_s16x4_ptr;
typedef __attribute__((address_space(1))) unsigned gu32;
typedef __attribute__((address_space(1))) unsigned long long gu64;

#define K_LO 1

#define NOPK __attribute__((target("no-packed-fp32-ops")))

constexpr int L = 2048, D = 512, NS = 128, F = 1376, HP = 1408;
constexpr int LDS_BYTES = 81920;

__device__ __forceinline__ float bf2f(unsigned short v) { return __uint_as_float((unsigned)v << 16); }
__device__ __forceinline__ float bfr(float x) { return (float)(__bf16)x; }
__device__ __forceinline__ unsigned pk2(float a, float b) {
  bf16x2 v = {(__bf16)a, (__bf16)b};
  return __builtin_bit_cast(unsigned, v);
}
__device__ __forceinline__ float lo_f(unsigned w) { return __uint_as_float(w << 16); }
__device__ __forceinline__ float hi_f(unsigned w) { return __uint_as_float(w & 0xffff0000u); }

__device__ __forceinline__ f32x4 mfma16(bf16x8 a, bf16x8 b, f32x4 c) {
  return __builtin_amdgcn_mfma_f32_16x16x32_bf16(a, b, c, 0, 0, 0);
}
__device__ __forceinline__ bf16x8 tr_frag(const char* lds, int off0, int off1) {
  s16x4 a = __builtin_amdgcn_ds_read_tr16_b64_v4i16((lds_s16x4_ptr)(lds + off0));
  s16x4 b = __builtin_amdgcn_ds_read_tr16_b64_v4i16((lds_s16x4_ptr)(lds + off1));
  s16x8 v = {a[0], a[1], a[2], a[3], b[0], b[1], b[2], b[3]};
  return __builtin_bit_cast(bf16x8, v);
}
__device__ __forceinline__ void glds16(const void* sbase, unsigned voff, unsigned lds_addr) {
  asm volatile("s_mov_b32 m0, %2\n\ts_nop 0\n\tglobal_load_lds_dwordx4 %0, %1" ::"v"(voff), "s"(sbase), "s"(lds_addr)
               : "memory", "m0");
}
__device__ __forceinline__ void glds16v(const void* vptr, unsigned lds_addr) {
  asm volatile("s_mov_b32 m0, %1\n\ts_nop 0\n\tglobal_load_lds_dwordx4 %0, off" ::"v"(vptr), "s"(lds_addr)
               : "memory", "m0");
}
__device__ __forceinline__ unsigned lds_addr_of(const char* p) {
  return (unsigned)(uintptr_t)(const __attribute__((address_space(3))) char*)p;
}
#define WAIT_VM(n) asm volatile("s_waitcnt vmcnt(" #n ")" ::: "memory")
#define WAIT_LGKM0() asm volatile("s_waitcnt lgkmcnt(0)" ::: "memory")
template <int N>
__device__ __forceinline__ void wait_vm() {
  asm volatile("s_waitcnt vmcnt(%0)" ::"i"(N) : "memory");
}

constexpr int F1_B = 32768;

struct F1Ctx {
  const __bf16 *x, *wg, *wu, *wmix;
  unsigned voffA[2], voffB, lbase;
  int w;
  int aoff[4], boff[3];
};
__device__ __forceinline__ void f1_stage_a(const F1Ctx& c, int m) {
  const unsigned ab = c.lbase + (m & 1) * 16384 + c.w * 1024;
  glds16(c.x + m * 64, c.voffA[0], ab);
  glds16(c.x + m * 64, c.voffA[1], ab + 8192);
}
template <bool G2>
__device__ __forceinline__ void f1_stage_b(const F1Ctx& c, int hs) {
  const unsigned bb = c.lbase + F1_B + (hs & 3) * 12288 + c.w * 1024;
  if constexpr (!G2) {
    glds16(c.wg + (size_t)hs * 32 * F, c.voffB, bb);
    glds16(c.wu + (size_t)hs * 32 * F, c.voffB, bb + 8192);
  } else {
    glds16v(c.wmix + (size_t)hs * 32 * F, bb);
  }
}
constexpr int f1_allowed(int hs, int nb) {
  int n = 0;
  if ((hs & 1) == 0 && hs >= 2) {
    if (hs + 1 < 16) n += nb;
    if (hs + 2 < 16) n += nb;
  } else {
    if (hs + 1 < 16) n += nb;
    if (hs >= 1 && ((hs - 1) & 1) == 0 && (hs + 1) / 2 < 8) n += 2;
    if (hs + 2 < 16) n += nb;
  }
  return n;
}
__device__ __forceinline__ f32x16 mfma32(bf16x8 a, bf16x8 b, f32x16 c) {
  return __builtin_amdgcn_mfma_f32_32x32x16_bf16(a, b, c, 0, 0, 0);
}
template <int HS, bool G2>
__device__ __forceinline__ void f1_loop(const F1Ctx& c, const char* smem, bf16x8 (&xf)[2], bf16x8 (&wf)[2][3],
                                        f32x16 (&acc)[3]) {
  if constexpr (HS < 16) {
    if constexpr (HS > 0) {
#pragma unroll
      for (int j = 0; j < 3; ++j) acc[j] = mfma32(wf[0][j], xf[0], acc[j]);
    }
    __builtin_amdgcn_sched_barrier(0);
    wait_vm<f1_allowed(HS, G2 ? 1 : 2)>();
    WAIT_LGKM0();
    __builtin_amdgcn_s_barrier();
    asm volatile("" ::: "memory");
    const char* ab = smem + ((HS >> 1) & 1) * 16384;
    const char* bbase = smem + (HS & 3) * 12288;
    __builtin_amdgcn_sched_barrier(0);
    xf[0] = *(const bf16x8*)(ab + c.aoff[2 * (HS & 1)]);
#pragma unroll
    for (int j = 0; j < 3; ++j) wf[0][j] = tr_frag(bbase, c.boff[j], c.boff[j] + 512);
    __builtin_amdgcn_sched_barrier(0);
#pragma unroll
    for (int j = 0; j < 3; ++j) {
      if constexpr (HS > 0) acc[j] = mfma32(wf[1][j], xf[1], acc[j]);
      __builtin_amdgcn_sched_barrier(0);
      if (j == 0) {
        if constexpr ((HS & 1) == 0 && (HS + 2) / 2 < 8) f1_stage_a(c, (HS + 2) / 2);
        if constexpr (HS + 3 < 16) f1_stage_b<G2>(c, HS + 3);
      } else {
        wf[1][j - 1] = tr_frag(bbase, c.boff[j - 1] + 2048, c.boff[j - 1] + 2560);
      }
      __builtin_amdgcn_sched_barrier(0);
    }
    wf[1][2] = tr_frag(bbase, c.boff[2] + 2048, c.boff[2] + 2560);
    xf[1] = *(const bf16x8*)(ab + c.aoff[2 * (HS & 1) + 1]);
    f1_loop<HS + 1, G2>(c, smem, xf, wf, acc);
  } else {
#pragma unroll
    for (int ks = 0; ks < 2; ++ks)
#pragma unroll
      for (int j = 0; j < 3; ++j) acc[j] = mfma32(wf[ks][j], xf[ks], acc[j]);
  }
}

__device__ __forceinline__ void ffn1_block(char* smem, int rb, int cb, const __bf16* __restrict__ x,
                                           const __bf16* __restrict__ wg, const __bf16* __restrict__ wu,
                                           __bf16* __restrict__ h) {
  const int tid = threadIdx.x, lane = tid & 63, w = __builtin_amdgcn_readfirstlane(tid >> 6);
  const int row0 = rb * 128, f0 = cb * 96;
  const int lt = w >> 1, sel = w & 1;
  const int r32 = lane & 31, hf = lane >> 5, blk = (lane >> 4) & 1, q = (lane >> 2) & 3, p = lane & 3;
  F1Ctx c;
  c.x = x; c.wg = wg; c.wu = wu; c.w = w; c.lbase = lds_addr_of(smem);
  {
#pragma unroll
    for (int i = 0; i < 2; ++i) {
      const int row = 64 * i + 8 * w + (lane >> 3), ch = (lane & 7) ^ ((row >> 1) & 7);
      c.voffA[i] = (unsigned)(((row0 + row) * D + ch * 8) * 2);
    }
    const int kk = 8 * (w & 3) + (lane >> 3), pos = lane & 7;
    const int i = (pos >> 1) ^ (kk & 2);
    const int par = (kk + cb) & 1;
    int c0 = f0 + 16 * (i + 2 * par) + 8 * (pos & 1);
    int c1 = f0 + 16 * ((i & 1) + 4 * (1 - par)) + 8 * (pos & 1);
    c0 = c0 > F - 8 ? F - 8 : c0;
    c1 = c1 > F - 8 ? F - 8 : c1;
    c.voffB = (unsigned)((kk * F + c0) * 2);
    c.wmix = (i < 2 ? wg : wu) + kk * F + c1;
  }
#pragma unroll
  for (int k4 = 0; k4 < 4; ++k4) {
    const int row = 32 * lt + r32;
    c.aoff[k4] = row * 128 + (((2 * k4 + hf) ^ ((row >> 1) & 7)) << 4);
  }
#pragma unroll
  for (int j = 0; j < 3; ++j) {
    const int par = (q + cb) & 1, jj = j - par;
    const bool whole = jj >= 0 && jj < 2;
    const int sub = whole ? 2 * sel : 1, ps = (whole ? 2 * jj : 2 * sel) + blk;
    c.boff[j] = F1_B + sub * 4096 + (8 * hf + q) * 128 + ((ps ^ (q & 2)) << 5) + 8 * p;
    asm volatile("" : "+v"(c.boff[j]));
  }
  f32x16 acc[3];
#pragma unroll
  for (int j = 0; j < 3; ++j)
#pragma unroll
    for (int e = 0; e < 16; ++e) acc[j][e] = 0.f;
  bf16x8 xf[2], wf[2][3];

  f1_stage_a(c, 0);
  if (w < 4) {
    f1_stage_b<false>(c, 0);
    f1_stage_b<false>(c, 1);
    f1_stage_b<false>(c, 2);
    f1_loop<0, false>(c, smem, xf, wf, acc);
  } else {
    f1_stage_b<true>(c, 0);
    f1_stage_b<true>(c, 1);
    f1_stage_b<true>(c, 2);
    f1_loop<0, true>(c, smem, xf, wf, acc);
  }

  __syncthreads();
  constexpr int HT = 49152, HTP = 208;
  auto finish = [&](auto SEL) {
    constexpr int S = decltype(SEL)::value;
#pragma unroll
    for (int j = 0; j < 3; ++j)
#pragma unroll
      for (int m2 = 0; m2 < 2; ++m2) {
        constexpr int o = 8 * (1 - S);
        *(f32x4*)(smem + (w * 6 + j * 2 + m2) * 1024 + lane * 16) =
            f32x4{acc[j][o + 4 * m2], acc[j][o + 4 * m2 + 1], acc[j][o + 4 * m2 + 2], acc[j][o + 4 * m2 + 3]};
      }
    __syncthreads();
#pragma unroll
    for (int j = 0; j < 3; ++j)
#pragma unroll
      for (int m2 = 0; m2 < 2; ++m2) {
        const f32x4 o4 = *(const f32x4*)(smem + ((w ^ 1) * 6 + j * 2 + m2) * 1024 + lane * 16);
        float hv[4];
#pragma unroll
        for (int e = 0; e < 4; ++e) {
          const float own = acc[j][8 * S + 4 * m2 + e];
          const float gv = S ? o4[e] : own, uv = S ? own : o4[e];
          const float t = gv * __builtin_fmaf(gv * gv, -0.10294324f, -2.3022082f);
          hv[e] = (gv * uv) * __builtin_amdgcn_rcpf(1.f + __builtin_amdgcn_exp2f(t));
        }
        u32x2 o = {pk2(hv[0], hv[1]), pk2(hv[2], hv[3])};
        *(u32x2*)(smem + HT + (32 * lt + r32) * HTP + (32 * j + 16 * S + 8 * m2 + 4 * hf) * 2) = o;
      }
  };
  if (sel) finish(std::integral_constant<int, 1>{});
  else finish(std::integral_constant<int, 0>{});
  __syncthreads();
#pragma unroll
  for (int i = 0; i < 3; ++i) {
    const int idx = tid + 512 * i, row = idx / 12, c8 = idx - row * 12;
    if (f0 + 8 * c8 < F)
      *(u32x4*)(h + (size_t)(row0 + row) * HP + f0 + 8 * c8) = *(const u32x4*)(smem + HT + row * HTP + c8 * 16);
  }
}

__device__ __forceinline__ void gate_block(char* smem, int rb, const __bf16* __restrict__ x,
                                           const __bf16* __restrict__ gw, float* __restrict__ gout,
                                           __bf16* __restrict__ uT) {
  const int tid = threadIdx.x, lane = tid & 63, w = __builtin_amdgcn_readfirstlane(tid >> 6);
  const int g = lane >> 4, q = (lane >> 2) & 3, p = lane & 3;
  const unsigned lbase = lds_addr_of(smem);
  const int grow = tid >> 3, gpart = tid & 7, gf = (grow & 3) | (((grow >> 3) & 1) << 2);
  u32x4 gwv[8];
#pragma unroll
  for (int i = 0; i < 8; ++i) gwv[i] = *(const u32x4*)(gw + ((gpart ^ gf) + 8 * (i >> 1)) * 16 + 8 * (i & 1));
#pragma unroll 1
  for (int pass = 0; pass < 2; ++pass) {
    const int l0 = rb * 128 + pass * 64;
#pragma unroll
    for (int i = 0; i < 8; ++i) {
      const int row = 8 * w + i, fr = (row & 3) | (((row >> 3) & 1) << 2);
      glds16(x + (size_t)(l0 + row) * D, (unsigned)((((lane >> 1) ^ fr) * 2 + (lane & 1)) * 16), lbase + row * 1024);
    }
    WAIT_VM(0);
    __syncthreads();
#pragma unroll
    for (int i = 0; i < 8; ++i) {
      const int unit = w * 8 + i, c16 = unit >> 1, half = unit & 1;
      const int ra = 32 * half + 8 * g + q, rb2 = ra + 4;
      const int fa = (ra & 3) | (((ra >> 3) & 1) << 2);
      const bf16x8 v = tr_frag(smem, ra * 1024 + ((c16 ^ fa) << 5) + 8 * p, rb2 * 1024 + ((c16 ^ fa) << 5) + 8 * p);
      *(bf16x8*)(uT + (size_t)(16 * c16 + (lane & 15)) * L + l0 + 32 * half + 8 * g) = v;
    }
    float s = 0.f;
#pragma unroll
    for (int i = 0; i < 8; ++i) {
      const u32x4 xv = *(const u32x4*)(smem + grow * 1024 + (gpart + 8 * (i >> 1)) * 32 + 16 * (i & 1));
#pragma unroll
      for (int e = 0; e < 4; ++e) {
        const unsigned xe = xv[e], we = gwv[i][e];
        s = __builtin_amdgcn_fdot2_f32_bf16(__builtin_bit_cast(bf16x2, xe), __builtin_bit_cast(bf16x2, we), s, false);
      }
    }
    s += __shfl_xor(s, 1);
    s += __shfl_xor(s, 2);
    s += __shfl_xor(s, 4);
    if (gpart == 0) gout[l0 + grow] = bfr(1.f / (1.f + __expf(-bfr(s))));
    __syncthreads();
  }
}

constexpr int KC_PITCH = 768;
constexpr int KG_PITCH = 160;
constexpr int KG_ARR = 16 * KG_PITCH;
constexpr int KG_TAB = 8 * 2 * KG_ARR;
typedef __attribute__((ext_vector_type(2))) float f32x2;

__device__ __forceinline__ f32x2 cexp(float zr, float zi, float p) {
  const float e = __builtin_amdgcn_exp2f(zr * p);
  const float ph = __builtin_amdgcn_fractf(zi * p);
  return f32x2{e * __builtin_amdgcn_cosf(ph), e * __builtin_amdgcn_sinf(ph)};
}
__device__ __forceinline__ f32x2 cmul(f32x2 a, f32x2 b) {
  return f32x2{a[0] * b[0] - a[1] * b[1], a[0] * b[1] + a[1] * b[0]};
}
__device__ __forceinline__ void kgen_block(char* smem, int d0, const __bf16* __restrict__ lar,
                                           const __bf16* __restrict__ aim, const __bf16* __restrict__ cre,
                                           const __bf16* __restrict__ cim, const __bf16* __restrict__ ldt,
                                           unsigned* __restrict__ kc, unsigned* __restrict__ aG,
                                           const __bf16* __restrict__ wd) {
  const int tid = threadIdx.x, lane = tid & 63, w = __builtin_amdgcn_readfirstlane(tid >> 6);
  unsigned pf_dummy;
  asm volatile("global_load_dword %0, %1, off"
               : "=v"(pf_dummy) : "v"(wd + (size_t)((d0 >> 1) * 43 + (tid < 42 ? tid : 42)) * 64) : "memory");
  {
    u32x4* z = (u32x4*)(aG + (size_t)d0 * L) + tid;
    z[0] = u32x4{0u, 0u, 0u, 0u};
    z[512] = u32x4{0u, 0u, 0u, 0u};
    if (tid == 0) aG[(size_t)D * L + (d0 >> 1)] = 0u;
  }
  const int ch = w >> 2, wq = w & 3, tc = tid & 255;
  const int r = lane & 15, g = lane >> 4, hh = lane >> 5;
  char* wb = smem + w * 2 * KG_ARR;
  char* tb = smem + KG_TAB + ch * (4 * 656 * 4);
  float zr, zi;
  f32x2 cc;
  {
    const int d = d0 + ch, n = 32 * wq + (lane & 31);
    const float dt = bfr(expf((float)ldt[d]));
    const float ar = -bfr(expf((float)lar[d * NS + n]));
    zr = (ar * dt) * 1.4426950408889634f;
    zi = ((float)aim[d * NS + n] * dt) * 0.15915494309189535f;
    cc = f32x2{2.f * ((float)cre[d * NS + n] * dt), 2.f * ((float)cim[d * NS + n] * dt)};
  }
  const float fh = (float)(1 + hh);
  const f32x2 e128 = cexp(zr, zi, 128.f), e24 = cexp(zr, zi, 2.f * fh);
  f32x2 v[4], st[4];
  v[0] = cexp(zr, -zi, 64.f * hh);                 st[0] = cexp(zr, -zi, 8.f);
  v[1] = hh ? cmul(cc, e128) : cc;                 st[1] = cexp(zr, zi, 1.f);
  v[2] = cmul(cc, cexp(zr, zi, 256.f * fh));       st[2] = hh ? e128 : e24;
  v[3] = cmul(cc, cexp(zr, zi, 1024.f * fh));      st[3] = e128;
  const f32x2 v24 = cmul(v[2], hh ? e24 : e128);
  unsigned vh[4][8], vl[4][8];
#pragma unroll
  for (int i = 0; i < 8; ++i)
#pragma unroll
    for (int t = 0; t < 4; ++t) {
      vh[t][i] = pk2(v[t][0], v[t][1]);
      vl[t][i] = pk2(v[t][0] - lo_f(vh[t][i]), v[t][1] - hi_f(vh[t][i]));
      if (t == 2 && i == 3) v[t] = v24;
      else if (i < 7) v[t] = cmul(v[t], st[t]);
    }
  char* wr = wb + (8 * hh) * KG_PITCH + (lane & 31) * 4;
  const char* rd = wb + r * KG_PITCH + g * 16;
  bf16x8 fh_[4][2], fl_[4][2];
#pragma unroll
  for (int t = 0; t < 4; ++t) {
    __builtin_amdgcn_wave_barrier();
#pragma unroll
    for (int i = 0; i < 8; ++i) {
      *(unsigned*)(wr + i * KG_PITCH) = vh[t][i];
      *(unsigned*)(wr + i * KG_PITCH + KG_ARR) = vl[t][i];
    }
    __builtin_amdgcn_wave_barrier();
#pragma unroll
    for (int ss = 0; ss < 2; ++ss) {
      fh_[t][ss] = *(const bf16x8*)(rd + ss * 64);
      fl_[t][ss] = *(const bf16x8*)(rd + ss * 64 + KG_ARR);
    }
  }
  f32x4 acc[3];
#pragma unroll
  for (int m = 0; m < 3; ++m) acc[m] = f32x4{0.f, 0.f, 0.f, 0.f};
#pragma unroll
  for (int ss = 0; ss < 2; ++ss)
#pragma unroll
    for (int m = 0; m < 3; ++m) {
      acc[m] = mfma16(fl_[1 + m][ss], fh_[0][ss], acc[m]);
      acc[m] = mfma16(fh_[1 + m][ss], fl_[0][ss], acc[m]);
      acc[m] = mfma16(fh_[1 + m][ss], fh_[0][ss], acc[m]);
    }
  {
    float* tab = (float*)tb + wq * 656;
    *(f32x4*)(tab + 128 * (g >> 1) + 4 * (g & 1) + 8 * r) = acc[0];
    if (g < 2) {
      *(f32x4*)(tab + 256 + 64 * g + 4 * r) = acc[1];
#pragma unroll
      for (int e = 0; e < 4; ++e) tab[512 + 64 * g + 16 * e + r] = acc[2][e];
    } else {
#pragma unroll
      for (int e = 0; e < 4; ++e) tab[384 + (g & 1) + 32 * e + 2 * r] = acc[1][e];
      if (lane == 32) tab[640] = acc[2][0];
    }
  }
  __syncthreads();
  {
    const float* tab = (const float*)tb;
    unsigned* kd = kc + (size_t)(d0 + ch) * KC_PITCH;
#pragma unroll
    for (int e = 0; e < 3; ++e) {
      const int id = tc + 256 * e;
      if (id < 641) {
        const float v = (tab[id] + tab[656 + id]) + (tab[2 * 656 + id] + tab[3 * 656 + id]);
        const float vh = bfr(v);
        kd[id] = pk2(vh, v - vh);
      }
    }
  }
  asm volatile("s_waitcnt vmcnt(0)" : "+v"(pf_dummy)::"memory");
}

NOPK __global__ __launch_bounds__(512, 4) void k1(const __bf16* __restrict__ u, const __bf16* __restrict__ lar,
                                             const __bf16* __restrict__ aim, const __bf16* __restrict__ cre,
                                             const __bf16* __restrict__ cim, const __bf16* __restrict__ ldt,
                                             const __bf16* __restrict__ wg, const __bf16* __restrict__ wu,
                                             const __bf16* __restrict__ gw, __bf16* __restrict__ h,
                                             __bf16* __restrict__ uT, unsigned* __restrict__ kc,
                                             float* __restrict__ gout, unsigned* __restrict__ aG,
                                             const __bf16* __restrict__ wd) {
  extern __shared__ __attribute__((aligned(16))) char smem[];
  const int b = blockIdx.x;
  const int xcd = b & 7, i = (b & 255) >> 3;
  if (b < 256) {
    const int rb = 4 * (xcd >> 1) + (i & 3), cb = 8 * (xcd & 1) + (i >> 2);
    if (cb < 15) ffn1_block(smem, rb, cb, u, wg, wu, h);
    else gate_block(smem, rb, u, gw, gout, uT);
  } else {
    kgen_block(smem, 64 * xcd + 2 * i, lar, aim, cre, cim, ldt, kc, aG, wd);
  }
}

constexpr unsigned A_TAG = 0x5a17c3e9u;
constexpr int UCP = 4160;
constexpr int KARR = (240 + L) * 2;
constexpr int OFF_K = 4 * UCP;
constexpr int OFF_TAB = OFF_K + 2 * KARR;
constexpr int OFF_RED = UCP;
constexpr int CH_BYTES = OFF_RED + 32768;
static_assert(OFF_TAB + 3072 <= CH_BYTES && 2 * CH_BYTES <= LDS_BYTES, "lds");

struct PosLut {
  unsigned short v[L];
  constexpr PosLut() : v() {
    for (int j = 0; j < L; ++j) {
      const int sh = (j >= 256) + (j >= 512) + (j >= 1024);
      int q = j >> sh;
      if (sh) {
        const int rem = j & ((1 << sh) - 1), half = 1 << (sh - 1);
        if (rem > half || (rem == half && (q & 1))) ++q;
      }
      v[j] = (unsigned short)(4 * (128 * sh + q));
    }
  }
};
__device__ const PosLut g_poslut{};

__device__ __forceinline__ void conv_block(char* smem, int d0, const __bf16* __restrict__ uT,
                                           const unsigned* __restrict__ kc, const __bf16* __restrict__ dsk,
                                           unsigned* __restrict__ aG) {
  const int tid = threadIdx.x, lane = tid & 63, w = __builtin_amdgcn_readfirstlane(tid >> 6);
  const int ch = w >> 2, wq = w & 3, tc = tid & 255;
  const int r = lane & 15, g = lane >> 4, q = (lane >> 2) & 3, p = lane & 3;
  char* cb = smem + ch * CH_BYTES;
  const int d = d0 + ch;
  __builtin_amdgcn_s_setprio(3);
  {
    const unsigned lb = lds_addr_of(cb);
    glds16(uT + (size_t)d * L + wq * 512, lane * 16, lb + 32 + wq * 1024);
    if (wq < 3) glds16(kc + (size_t)d * KC_PITCH + wq * 256, lane * 16, lb + OFF_TAB + wq * 1024);
    const u32x4 z = {0u, 0u, 0u, 0u};
    if (tc < 60) *(u32x4*)(cb + OFF_K + (tc >= 30 ? KARR : 0) + (tc % 30) * 16) = z;
    else if (tc < 68) *(u32x4*)(cb + ((tc - 60) >> 1) * UCP + ((tc - 60) & 1) * 16) = z;
  }
  const float dsv = (float)dsk[d];
  const u32x4 lut = *(const u32x4*)(g_poslut.v + 8 * tc);
  WAIT_VM(0);
  __syncthreads();
  {
    const u32x4 pa = *(const u32x4*)(cb + 16 + 16 * tc), pb = *(const u32x4*)(cb + 32 + 16 * tc);
    const u32x4 c1 = {__builtin_amdgcn_alignbit(pb[0], pa[3], 16), __builtin_amdgcn_alignbit(pb[1], pb[0], 16),
                      __builtin_amdgcn_alignbit(pb[2], pb[1], 16), __builtin_amdgcn_alignbit(pb[3], pb[2], 16)};
    const u32x4 c2 = {pa[3], pb[0], pb[1], pb[2]};
    const u32x4 c3 = {__builtin_amdgcn_alignbit(pa[3], pa[2], 16), c1[0], c1[1], c1[2]};
    *(u32x4*)(cb + 1 * UCP + 32 + 16 * tc) = c1;
    *(u32x4*)(cb + 2 * UCP + 32 + 16 * tc) = c2;
    *(u32x4*)(cb + 3 * UCP + 32 + 16 * tc) = c3;
    u32x4 kh, kl;
#pragma unroll
    for (int e = 0; e < 4; ++e) {
      const unsigned t0 = *(const unsigned*)(cb + OFF_TAB + (lut[e] & 0xffffu));
      const unsigned t1 = *(const unsigned*)(cb + OFF_TAB + (lut[e] >> 16));
      kh[e] = __builtin_amdgcn_perm(t1, t0, 0x05040100u);
      kl[e] = __builtin_amdgcn_perm(t1, t0, 0x07060302u);
    }
    *(u32x4*)(cb + OFF_K + 480 + 16 * tc) = kh;
    *(u32x4*)(cb + OFF_K + KARR + 480 + 16 * tc) = kl;
  }
  __syncthreads();
  __builtin_amdgcn_s_setprio(1);
  f32x4 acc[8];
#pragma unroll
  for (int t = 0; t < 8; ++t) acc[t] = f32x4{0.f, 0.f, 0.f, 0.f};
#pragma unroll
  for (int rr = 0; rr < 2; ++rr) {
    const int rho = 2 * wq + rr;
    const int abase = q * UCP + 2 * (16 + 16 + 32 * rho - 8 * g + 4 * p);
    const int bbase = OFF_K + 2 * (240 - 16 - 32 * rho + 16 * r + 8 * g);
    bf16x8 af[8];
#pragma unroll
    for (int s = 0; s < 8; ++s) af[s] = tr_frag(cb, abase + 512 * s, abase + 512 * s - 8);
#pragma unroll
    for (int t = 0; t < 8; ++t) {
      const bf16x8 bh = *(const bf16x8*)(cb + bbase + 512 * t);
#pragma unroll
      for (int s = 0; s + t < 8; ++s) acc[s + t] = mfma16(af[s], bh, acc[s + t]);
      const bf16x8 bl = *(const bf16x8*)(cb + bbase + KARR + 512 * t);
#pragma unroll
      for (int s = 0; s + t < 8; ++s) acc[s + t] = mfma16(af[s], bl, acc[s + t]);
    }
  }
  __builtin_amdgcn_s_setprio(3);
  __syncthreads();
  if (tid == 256) __hip_atomic_store((gu32*)(aG + (size_t)D * L + (d0 >> 1)), 1u, __ATOMIC_RELAXED, __HIP_MEMORY_SCOPE_AGENT);
#pragma unroll
  for (int t = 0; t < 8; ++t)
    if ((t >> 1) != wq) *(f32x4*)(cb + OFF_RED + (wq * 8 + t) * 1024 + lane * 16) = acc[t];
  __syncthreads();
#pragma unroll
  for (int tt = 0; tt < 2; ++tt) {
    const int t = 2 * wq + tt;
    f32x4 own = f32x4{0.f, 0.f, 0.f, 0.f};
#pragma unroll
    for (int t2 = 0; t2 < 8; ++t2)
      if (t2 == t) own = acc[t2];
    f32x4 s = f32x4{0.f, 0.f, 0.f, 0.f};
#pragma unroll
    for (int sw = 0; sw < 4; ++sw) {
      const f32x4 o = *(const f32x4*)(cb + OFF_RED + (sw * 8 + t) * 1024 + lane * 16);
      s += (sw == wq) ? own : o;
    }
    const int l = 256 * t + 16 * r + 4 * g;
    const u32x2 uu = *(const u32x2*)(cb + 32 + 2 * l);
    const float a0 = bfr(s[0]) + bfr(lo_f(uu[0]) * dsv), a1 = bfr(s[1]) + bfr(hi_f(uu[0]) * dsv);
    const float a2 = bfr(s[2]) + bfr(lo_f(uu[1]) * dsv), a3 = bfr(s[3]) + bfr(hi_f(uu[1]) * dsv);
    const u32x4 gr = {pk2(a0, a1), A_TAG, pk2(a2, a3), A_TAG};
    asm volatile("global_store_dwordx4 %0, %1, off sc1\n\ts_nop 1" ::"v"(aG + (size_t)d * L + l), "v"(gr) : "memory");
  }
}

constexpr int F2_NS = 5;
constexpr int F2_STAGE = 16384;
constexpr int F2_NST = 22;

struct F2Ctx {
  const __bf16 *h, *wd;
  unsigned avoff, bvoff, bvoff_last, lbase;
  int w, kh;
  int aoff[2], boff[2];
  const unsigned* asrc;
  mutable u32x4 x0, x1;
};
__device__ __forceinline__ void f2_stage(const F2Ctx& c, int st) {
  const unsigned base = c.lbase + (st % F2_NS) * F2_STAGE;
  glds16(c.h + st * 64, c.avoff, base + c.w * 1024);
  glds16(c.wd + (size_t)st * 64 * D, st == F2_NST - 1 ? c.bvoff_last : c.bvoff, base + 8192 + c.w * 1024);
}
template <int ST>
__device__ __forceinline__ void f2_loop(const F2Ctx& c, const char* smem, bf16x8 (&ao)[2], bf16x8 (&bo)[2],
                                        f32x4 (&acc)[2][2]) {
  if constexpr (ST < F2_NST) {
    constexpr int rem = (F2_NST - 1 - ST) < (F2_NS - 2) ? (F2_NST - 1 - ST) : (F2_NS - 2);
    wait_vm<2 * rem>();
    WAIT_LGKM0();
    __builtin_amdgcn_s_barrier();
    asm volatile("" ::: "memory");
    if constexpr (ST + F2_NS - 1 < F2_NST) f2_stage(c, ST + F2_NS - 1);
    if constexpr (ST == F2_NST - 3)
      asm volatile("global_load_dwordx4 %0, %2, off sc1\n\tglobal_load_dwordx4 %1, %3, off sc1"
                   : "=&v"(c.x0), "=&v"(c.x1) : "v"(c.asrc), "v"(c.asrc + 16 * L) : "memory");
    const char* base = smem + (ST % F2_NS) * F2_STAGE;
    bf16x8 an[2], bn[2];
#pragma unroll
    for (int mt = 0; mt < 2; ++mt) an[mt] = *(const bf16x8*)(base + c.aoff[mt]);
#pragma unroll
    for (int nt = 0; nt < 2; ++nt) bn[nt] = tr_frag(base, c.boff[nt], c.boff[nt] + 512);
    __builtin_amdgcn_sched_barrier(0);
    if constexpr (ST > 0) {
#pragma unroll
      for (int mt = 0; mt < 2; ++mt)
#pragma unroll
        for (int nt = 0; nt < 2; ++nt) acc[mt][nt] = mfma16(ao[mt], bo[nt], acc[mt][nt]);
    }
    __builtin_amdgcn_sched_barrier(0);
    f2_loop<ST + 1>(c, smem, an, bn, acc);
  } else {
    if (c.kh == 0) {
#pragma unroll
      for (int mt = 0; mt < 2; ++mt)
#pragma unroll
        for (int nt = 0; nt < 2; ++nt) acc[mt][nt] = mfma16(ao[mt], bo[nt], acc[mt][nt]);
    }
  }
}

__device__ __forceinline__ void ffn2_block(char* smem, int row0, int cbk, const __bf16* __restrict__ h,
                                           const __bf16* __restrict__ wd, const unsigned* __restrict__ aG,
                                           const float* __restrict__ gin, __bf16* __restrict__ out) {
  const int col0 = 64 * cbk;
  const int tid = threadIdx.x, lane = tid & 63, w = __builtin_amdgcn_readfirstlane(tid >> 6);
  const int r = lane & 15, g = lane >> 4, q = (lane >> 2) & 3, p = lane & 3;
  const int kh = w >> 2, wm = (w >> 1) & 1, wn = w & 1;
  __builtin_amdgcn_s_setprio(1);
  F2Ctx c;
  c.h = h; c.wd = wd; c.w = w; c.kh = kh; c.lbase = lds_addr_of(smem);
  {
    const int row = 8 * w + (lane >> 3), ch = (lane & 7) ^ ((row >> 1) & 7);
    c.avoff = (unsigned)(((row0 + row) * HP + ch * 8) * 2);
    const int bk = 8 * w + (lane >> 3);
    const int pos = lane & 7, fp = ((bk >> 1) & 1) | (((bk >> 3) & 1) << 1);
    const int cc = col0 + 16 * ((pos >> 1) ^ fp) + 8 * (pos & 1);
    c.bvoff = (unsigned)((bk * D + cc) * 2);
    c.bvoff_last = (unsigned)(((bk > 31 ? 31 : bk) * D + cc) * 2);
  }
#pragma unroll
  for (int mt = 0; mt < 2; ++mt) {
    const int row = 32 * wm + 16 * mt + r, ch = 4 * kh + g;
    c.aoff[mt] = row * 128 + ((ch ^ ((row >> 1) & 7)) << 4);
  }
  const int fpl = (q >> 1) | ((g & 1) << 1);
#pragma unroll
  for (int nt = 0; nt < 2; ++nt) c.boff[nt] = 8192 + (32 * kh + 8 * g + q) * 128 + (((2 * wn + nt) ^ fpl) << 5) + 8 * p;

  f32x4 acc[2][2];
#pragma unroll
  for (int mt = 0; mt < 2; ++mt)
#pragma unroll
    for (int nt = 0; nt < 2; ++nt) acc[mt][nt] = f32x4{0.f, 0.f, 0.f, 0.f};

#pragma unroll
  for (int st = 0; st < F2_NS - 1; ++st) f2_stage(c, st);
  const int lr = 32 * wm + 16 * kh + 4 * g;
  const f32x4 gv = *(const f32x4*)(gin + row0 + lr);
  c.asrc = aG + (size_t)(col0 + 32 * wn + r) * L + row0 + lr;
  bf16x8 a0[2], b0[2];
  f2_loop<0>(c, smem, a0, b0, acc);
  WAIT_LGKM0();
  __builtin_amdgcn_s_barrier();
  asm volatile("" ::: "memory");
  asm volatile("s_waitcnt vmcnt(0)" : "+v"(c.x0), "+v"(c.x1)::"memory");
  const bool early = __all(c.x0[1] == A_TAG && c.x0[3] == A_TAG && c.x1[1] == A_TAG && c.x1[3] == A_TAG);
  if (w == 0 && !early) {
    const gu32* fl = (const gu32*)(aG + (size_t)D * L + 32 * cbk + (lane & 31));
    for (int spin = 0; spin < (1 << 22); ++spin) {
      const unsigned v = __hip_atomic_load(fl, __ATOMIC_RELAXED, __HIP_MEMORY_SCOPE_AGENT);
      if (__all(v == 1u)) break;
      __builtin_amdgcn_s_sleep(2);
    }
  }
  {
    char* red = smem + (w & 3) * 4096 + kh * 2048;
    const f32x4 o0 = kh ? acc[0][0] : acc[1][0], o1 = kh ? acc[0][1] : acc[1][1];
    *(f32x4*)(red + lane * 16) = o0;
    *(f32x4*)(red + 1024 + lane * 16) = o1;
  }
  __syncthreads();
  unsigned long long av[2];
  {
    const unsigned* src = c.asrc;
    av[0] = (unsigned long long)c.x0[0] | ((unsigned long long)c.x0[2] << 32);
    av[1] = (unsigned long long)c.x1[0] | ((unsigned long long)c.x1[2] << 32);
    for (int spin = 0; !early && spin < (1 << 22); ++spin) {
      u32x4 x0, x1;
      asm volatile("global_load_dwordx4 %0, %2, off sc1\n\tglobal_load_dwordx4 %1, %3, off sc1\n\ts_waitcnt vmcnt(0)"
                   : "=&v"(x0), "=&v"(x1) : "v"(src), "v"(src + 16 * L) : "memory");
      av[0] = (unsigned long long)x0[0] | ((unsigned long long)x0[2] << 32);
      av[1] = (unsigned long long)x1[0] | ((unsigned long long)x1[2] << 32);
      const bool ok = x0[1] == A_TAG && x0[3] == A_TAG && x1[1] == A_TAG && x1[3] == A_TAG;
      if (__all(ok)) break;
      __builtin_amdgcn_s_sleep(4);
    }
  }
  constexpr int OT = 16384, OTP = 144;
  {
    const char* red = smem + (w & 3) * 4096 + (1 - kh) * 2048;
#pragma unroll
    for (int nt = 0; nt < 2; ++nt) {
      const f32x4 o = *(const f32x4*)(red + nt * 1024 + lane * 16);
      const f32x4 own = kh ? acc[1][nt] : acc[0][nt];
      const f32x4 s = kh ? (o + own) : (own + o);
      const unsigned alo = (unsigned)av[nt], ahi = (unsigned)(av[nt] >> 32);
      const float a[4] = {lo_f(alo), hi_f(alo), lo_f(ahi), hi_f(ahi)};
#pragma unroll
      for (int e = 0; e < 4; ++e) {
        const float o2 = bfr(gv[e] * a[e]) + bfr(bfr(1.f - gv[e]) * bfr(s[e]));
        *(__bf16*)(smem + OT + (lr + e) * OTP + (32 * wn + 16 * nt + r) * 2) = (__bf16)o2;
      }
    }
  }
  __syncthreads();
  {
    const int row = tid >> 3, c8 = tid & 7;
    const u32x4 v = *(const u32x4*)(smem + OT + row * OTP + c8 * 16);
    *(u32x4*)(out + (size_t)(row0 + row) * D + col0 + c8 * 8) = v;
  }
}

NOPK __global__ __launch_bounds__(512, 4) void k2(const __bf16* __restrict__ h, const __bf16* __restrict__ wd,
                                             const __bf16* __restrict__ uT, const unsigned* __restrict__ kc,
                                             const __bf16* __restrict__ dsk, unsigned* __restrict__ aG,
                                             const float* __restrict__ gin, __bf16* __restrict__ out) {
  extern __shared__ __attribute__((aligned(16))) char smem[];
  const int b = blockIdx.x, xcd = b & 7, i = (b & 255) >> 3;
  if (b < 256) {
    const int pair = 32 * xcd + i;
    conv_block(smem, 2 * pair, uT, kc, dsk, aG);
  } else {
    ffn2_block(smem, 64 * (4 * xcd + (i & 3)), i >> 2, h, wd, aG, gin, out);
  }
}

extern "C" void kernel_launch(void* const* d_in, const int* in_sizes, int n_in, void* d_out, int out_size,
                              void* d_ws, size_t ws_size, hipStream_t stream) {
  const __bf16* u = (const __bf16*)d_in[0];
  const __bf16* lar = (const __bf16*)d_in[1];
  const __bf16* aim = (const __bf16*)d_in[2];
  const __bf16* cre = (const __bf16*)d_in[3];
  const __bf16* cim = (const __bf16*)d_in[4];
  const __bf16* ldt = (const __bf16*)d_in[5];
  const __bf16* dsk = (const __bf16*)d_in[6];
  const __bf16* wg = (const __bf16*)d_in[8];
  const __bf16* wu = (const __bf16*)d_in[9];
  const __bf16* wd = (const __bf16*)d_in[10];
  const __bf16* gw = (const __bf16*)d_in[11];
  char* ws = (char*)d_ws;
  __bf16* h = (__bf16*)ws;
  ws += (size_t)L * HP * 2;
  unsigned* aG = (unsigned*)ws;
  ws += (size_t)D * L * 4 + 1024;
  __bf16* uT = (__bf16*)ws;
  ws += (size_t)D * L * 2;
  unsigned* kc = (unsigned*)ws;
  ws += (size_t)D * KC_PITCH * 4;
  float* gout = (float*)ws;
  (void)hipFuncSetAttribute((const void*)k1, hipFuncAttributeMaxDynamicSharedMemorySize, LDS_BYTES);
  (void)hipFuncSetAttribute((const void*)k2, hipFuncAttributeMaxDynamicSharedMemorySize, LDS_BYTES);
  hipLaunchKernelGGL(k1, dim3(512), dim3(512), LDS_BYTES, stream, u, lar, aim, cre, cim, ldt, wg, wu, gw, h, uT, kc,
                     gout, aG, wd);
  hipLaunchKernelGGL(k2, dim3(512), dim3(512), LDS_BYTES, stream, (const __bf16*)h, wd, (const __bf16*)uT,
                     (const unsigned*)kc, dsk, aG, (const float*)gout, (__bf16*)d_out);
}
```

```cpp
#include <hip/hip_runtime.h>
#include <hip/hip_bf16.h>
#include <type_traits>

typedef __attribute__((ext_vector_type(8))) __bf16 bf16x8;
typedef __attribute__((ext_vector_type(2))) __bf16 bf16x2;
typedef __attribute__((ext_vector_type(4))) short s16x4;
typedef __attribute__((ext_vector_type(8))) short s16x8;
typedef __attribute__((ext_vector_type(4))) float f32x4;
typedef __attribute__((ext_vector_type(16))) float f32x16;
typedef __attribute__((ext_vector_type(4))) unsigned u32x4;
typedef __attribute__((ext_vector_type(2))) unsigned u32x2;
typedef __attribute__((address_space(3))) s16x4* lds_s16x4_ptr;
typedef __attribute__((address_space(1))) unsigned gu32;
typedef __attribute__((address_space(1))) unsigned long long gu64;

#define K_LO 1

#define NOPK __attribute__((target("no-packed-fp32-ops")))

constexpr int L = 2048, D = 512, NS = 128, F = 1376, HP = 1408;
constexpr int LDS_BYTES = 81920;

__device__ __forceinline__ float bf2f(unsigned short v) { return __uint_as_float((unsigned)v << 16); }
__device__ __forceinline__ float bfr(float x) { return (float)(__bf16)x; }
__device__ __forceinline__ unsigned pk2(float a, float b) {
  bf16x2 v = {(__bf16)a, (__bf16)b};
  return __builtin_bit_cast(unsigned, v);
}
__device__ __forceinline__ float lo_f(unsigned w) { return __uint_as_float(w << 16); }
__device__ __forceinline__ float hi_f(unsigned w) { return __uint_as_float(w & 0xffff0000u); }

__device__ __forceinline__ f32x4 mfma16(bf16x8 a, bf16x8 b, f32x4 c) {
  return __builtin_amdgcn_mfma_f32_16x16x32_bf16(a, b, c, 0, 0, 0);
}
__device__ __forceinline__ bf16x8 tr_frag(const char* lds, int off0, int off1) {
  s16x4 a = __builtin_amdgcn_ds_read_tr16_b64_v4i16((lds_s16x4_ptr)(lds + off0));
  s16x4 b = __builtin_amdgcn_ds_read_tr16_b64_v4i16((lds_s16x4_ptr)(lds + off1));
  s16x8 v = {a[0], a[1], a[2], a[3], b[0], b[1], b[2], b[3]};
  return __builtin_bit_cast(bf16x8, v);
}
__device__ __forceinline__ void glds16(const void* sbase, unsigned voff, unsigned lds_addr) {
  asm volatile("s_mov_b32 m0, %2\n\ts_nop 0\n\tglobal_load_lds_dwordx4 %0, %1" ::"v"(voff), "s"(sbase), "s"(lds_addr)
               : "memory", "m0");
}
__device__ __forceinline__ void glds16v(const void* vptr, unsigned lds_addr) {
  asm volatile("s_mov_b32 m0, %1\n\ts_nop 0\n\tglobal_load_lds_dwordx4 %0, off" ::"v"(vptr), "s"(lds_addr)
               : "memory", "m0");
}
__device__ __forceinline__ unsigned lds_addr_of(const char* p) {
  return (unsigned)(uintptr_t)(const __attribute__((address_space(3))) char*)p;
}
#define TOUCH(var, ptr) asm volatile("global_load_dword %0, %1, off" : "=v"(var) : "v"(ptr) : "memory")
#define WAIT_VM(n) asm volatile("s_waitcnt vmcnt(" #n ")" ::: "memory")
#define WAIT_LGKM0() asm volatile("s_waitcnt lgkmcnt(0)" ::: "memory")
template <int N>
__device__ __forceinline__ void wait_vm() {
  asm volatile("s_waitcnt vmcnt(%0)" ::"i"(N) : "memory");
}

constexpr int F1_B = 32768;

struct F1Ctx {
  const __bf16 *x, *wg, *wu, *wmix;
  unsigned voffA[2], voffB, lbase;
  int w;
  int aoff[4], boff[3];
};
__device__ __forceinline__ void f1_stage_a(const F1Ctx& c, int m) {
  const unsigned ab = c.lbase + (m & 1) * 16384 + c.w * 1024;
  glds16(c.x + m * 64, c.voffA[0], ab);
  glds16(c.x + m * 64, c.voffA[1], ab + 8192);
}
template <bool G2>
__device__ __forceinline__ void f1_stage_b(const F1Ctx& c, int hs) {
  const unsigned bb = c.lbase + F1_B + (hs & 3) * 12288 + c.w * 1024;
  if constexpr (!G2) {
    glds16(c.wg + (size_t)hs * 32 * F, c.voffB, bb);
    glds16(c.wu + (size_t)hs * 32 * F, c.voffB, bb + 8192);
  } else {
    glds16v(c.wmix + (size_t)hs * 32 * F, bb);
  }
}
constexpr int f1_allowed(int hs, int nb) {
  int n = 0;
  if ((hs & 1) == 0 && hs >= 2) {
    if (hs + 1 < 16) n += nb;
    if (hs + 2 < 16) n += nb;
  } else {
    if (hs + 1 < 16) n += nb;
    if (hs >= 1 && ((hs - 1) & 1) == 0 && (hs + 1) / 2 < 8) n += 2;
    if (hs + 2 < 16) n += nb;
  }
  return n;
}
__device__ __forceinline__ f32x16 mfma32(bf16x8 a, bf16x8 b, f32x16 c) {
  return __builtin_amdgcn_mfma_f32_32x32x16_bf16(a, b, c, 0, 0, 0);
}
template <int HS, bool G2>
__device__ __forceinline__ void f1_loop(const F1Ctx& c, const char* smem, bf16x8 (&xf)[2], bf16x8 (&wf)[2][3],
                                        f32x16 (&acc)[3]) {
  if constexpr (HS < 16) {
    if constexpr (HS > 0) {
#pragma unroll
      for (int j = 0; j < 3; ++j) acc[j] = mfma32(wf[0][j], xf[0], acc[j]);
    }
    __builtin_amdgcn_sched_barrier(0);
    wait_vm<f1_allowed(HS, G2 ? 1 : 2)>();
    WAIT_LGKM0();
    __builtin_amdgcn_s_barrier();
    asm volatile("" ::: "memory");
    const char* ab = smem + ((HS >> 1) & 1) * 16384;
    const char* bbase = smem + (HS & 3) * 12288;
    __builtin_amdgcn_sched_barrier(0);
    xf[0] = *(const bf16x8*)(ab + c.aoff[2 * (HS & 1)]);
#pragma unroll
    for (int j = 0; j < 3; ++j) wf[0][j] = tr_frag(bbase, c.boff[j], c.boff[j] + 512);
    __builtin_amdgcn_sched_barrier(0);
#pragma unroll
    for (int j = 0; j < 3; ++j) {
      if constexpr (HS > 0) acc[j] = mfma32(wf[1][j], xf[1], acc[j]);
      __builtin_amdgcn_sched_barrier(0);
      if (j == 0) {
        if constexpr ((HS & 1) == 0 && (HS + 2) / 2 < 8) f1_stage_a(c, (HS + 2) / 2);
        if constexpr (HS + 3 < 16) f1_stage_b<G2>(c, HS + 3);
      } else {
        wf[1][j - 1] = tr_frag(bbase, c.boff[j - 1] + 2048, c.boff[j - 1] + 2560);
      }
      __builtin_amdgcn_sched_barrier(0);
    }
    wf[1][2] = tr_frag(bbase, c.boff[2] + 2048, c.boff[2] + 2560);
    xf[1] = *(const bf16x8*)(ab + c.aoff[2 * (HS & 1) + 1]);
    f1_loop<HS + 1, G2>(c, smem, xf, wf, acc);
  } else {
#pragma unroll
    for (int ks = 0; ks < 2; ++ks)
#pragma unroll
      for (int j = 0; j < 3; ++j) acc[j] = mfma32(wf[ks][j], xf[ks], acc[j]);
  }
}

__device__ __forceinline__ void ffn1_block(char* smem, int rb, int cb, const __bf16* __restrict__ x,
                                           const __bf16* __restrict__ wg, const __bf16* __restrict__ wu,
                                           __bf16* __restrict__ h) {
  const int tid = threadIdx.x, lane = tid & 63, w = __builtin_amdgcn_readfirstlane(tid >> 6);
  const int row0 = rb * 128, f0 = cb * 96;
  const int lt = w >> 1, sel = w & 1;
  const int r32 = lane & 31, hf = lane >> 5, blk = (lane >> 4) & 1, q = (lane >> 2) & 3, p = lane & 3;
  F1Ctx c;
  c.x = x; c.wg = wg; c.wu = wu; c.w = w; c.lbase = lds_addr_of(smem);
  {
#pragma unroll
    for (int i = 0; i < 2; ++i) {
      const int row = 64 * i + 8 * w + (lane >> 3), ch = (lane & 7) ^ ((row >> 1) & 7);
      c.voffA[i] = (unsigned)(((row0 + row) * D + ch * 8) * 2);
    }
    const int kk = 8 * (w & 3) + (lane >> 3), pos = lane & 7;
    const int i = (pos >> 1) ^ (kk & 2);
    const int par = (kk + cb) & 1;
    int c0 = f0 + 16 * (i + 2 * par) + 8 * (pos & 1);
    int c1 = f0 + 16 * ((i & 1) + 4 * (1 - par)) + 8 * (pos & 1);
    c0 = c0 > F - 8 ? F - 8 : c0;
    c1 = c1 > F - 8 ? F - 8 : c1;
    c.voffB = (unsigned)((kk * F + c0) * 2);
    c.wmix = (i < 2 ? wg : wu) + kk * F + c1;
  }
#pragma unroll
  for (int k4 = 0; k4 < 4; ++k4) {
    const int row = 32 * lt + r32;
    c.aoff[k4] = row * 128 + (((2 * k4 + hf) ^ ((row >> 1) & 7)) << 4);
  }
#pragma unroll
  for (int j = 0; j < 3; ++j) {
    const int par = (q + cb) & 1, jj = j - par;
    const bool whole = jj >= 0 && jj < 2;
    const int sub = whole ? 2 * sel : 1, ps = (whole ? 2 * jj : 2 * sel) + blk;
    c.boff[j] = F1_B + sub * 4096 + (8 * hf + q) * 128 + ((ps ^ (q & 2)) << 5) + 8 * p;
    asm volatile("" : "+v"(c.boff[j]));
  }
  f32x16 acc[3];
#pragma unroll
  for (int j = 0; j < 3; ++j)
#pragma unroll
    for (int e = 0; e < 16; ++e) acc[j][e] = 0.f;
  bf16x8 xf[2], wf[2][3];

  unsigned tch;
  TOUCH(tch, h + (size_t)(row0 + 16 * w) * HP + f0);
  f1_stage_a(c, 0);
  if (w < 4) {
    f1_stage_b<false>(c, 0);
    f1_stage_b<false>(c, 1);
    f1_stage_b<false>(c, 2);
    f1_loop<0, false>(c, smem, xf, wf, acc);
  } else {
    f1_stage_b<true>(c, 0);
    f1_stage_b<true>(c, 1);
    f1_stage_b<true>(c, 2);
    f1_loop<0, true>(c, smem, xf, wf, acc);
  }

  asm volatile("" ::"v"(tch));
  __syncthreads();
  constexpr int HT = 49152, HTP = 208;
  auto finish = [&](auto SEL) {
    constexpr int S = decltype(SEL)::value;
#pragma unroll
    for (int j = 0; j < 3; ++j)
#pragma unroll
      for (int m2 = 0; m2 < 2; ++m2) {
        constexpr int o = 8 * (1 - S);
        *(f32x4*)(smem + (w * 6 + j * 2 + m2) * 1024 + lane * 16) =
            f32x4{acc[j][o + 4 * m2], acc[j][o + 4 * m2 + 1], acc[j][o + 4 * m2 + 2], acc[j][o + 4 * m2 + 3]};
      }
    __syncthreads();
#pragma unroll
    for (int j = 0; j < 3; ++j)
#pragma unroll
      for (int m2 = 0; m2 < 2; ++m2) {
        const f32x4 o4 = *(const f32x4*)(smem + ((w ^ 1) * 6 + j * 2 + m2) * 1024 + lane * 16);
        float hv[4];
#pragma unroll
        for (int e = 0; e < 4; ++e) {
          const float own = acc[j][8 * S + 4 * m2 + e];
          const float gv = S ? o4[e] : own, uv = S ? own : o4[e];
          const float t = gv * __builtin_fmaf(gv * gv, -0.10294324f, -2.3022082f);
          hv[e] = (gv * uv) * __builtin_amdgcn_rcpf(1.f + __builtin_amdgcn_exp2f(t));
        }
        u32x2 o = {pk2(hv[0], hv[1]), pk2(hv[2], hv[3])};
        *(u32x2*)(smem + HT + (32 * lt + r32) * HTP + (32 * j + 16 * S + 8 * m2 + 4 * hf) * 2) = o;
      }
  };
  if (sel) finish(std::integral_constant<int, 1>{});
  else finish(std::integral_constant<int, 0>{});
  __syncthreads();
#pragma unroll
  for (int i = 0; i < 3; ++i) {
    const int idx = tid + 512 * i, row = idx / 12, c8 = idx - row * 12;
    if (f0 + 8 * c8 < F)
      *(u32x4*)(h + (size_t)(row0 + row) * HP + f0 + 8 * c8) = *(const u32x4*)(smem + HT + row * HTP + c8 * 16);
  }
}

__device__ __forceinline__ void gate_block(char* smem, int rb, const __bf16* __restrict__ x,
                                           const __bf16* __restrict__ gw, float* __restrict__ gout,
                                           __bf16* __restrict__ uT) {
  const int tid = threadIdx.x, lane = tid & 63, w = __builtin_amdgcn_readfirstlane(tid >> 6);
  const int g = lane >> 4, q = (lane >> 2) & 3, p = lane & 3;
  const unsigned lbase = lds_addr_of(smem);
  const int grow = tid >> 3, gpart = tid & 7, gf = (grow & 3) | (((grow >> 3) & 1) << 2);
  u32x4 gwv[8];
#pragma unroll
  for (int i = 0; i < 8; ++i) gwv[i] = *(const u32x4*)(gw + ((gpart ^ gf) + 8 * (i >> 1)) * 16 + 8 * (i & 1));
#pragma unroll 1
  for (int pass = 0; pass < 2; ++pass) {
    const int l0 = rb * 128 + pass * 64;
#pragma unroll
    for (int i = 0; i < 8; ++i) {
      const int row = 8 * w + i, fr = (row & 3) | (((row >> 3) & 1) << 2);
      glds16(x + (size_t)(l0 + row) * D, (unsigned)((((lane >> 1) ^ fr) * 2 + (lane & 1)) * 16), lbase + row * 1024);
    }
    WAIT_VM(0);
    __syncthreads();
#pragma unroll
    for (int i = 0; i < 8; ++i) {
      const int unit = w * 8 + i, c16 = unit >> 1, half = unit & 1;
      const int ra = 32 * half + 8 * g + q, rb2 = ra + 4;
      const int fa = (ra & 3) | (((ra >> 3) & 1) << 2);
      const bf16x8 v = tr_frag(smem, ra * 1024 + ((c16 ^ fa) << 5) + 8 * p, rb2 * 1024 + ((c16 ^ fa) << 5) + 8 * p);
      *(bf16x8*)(uT + (size_t)(16 * c16 + (lane & 15)) * L + l0 + 32 * half + 8 * g) = v;
    }
    float s = 0.f;
#pragma unroll
    for (int i = 0; i < 8; ++i) {
      const u32x4 xv = *(const u32x4*)(smem + grow * 1024 + (gpart + 8 * (i >> 1)) * 32 + 16 * (i & 1));
#pragma unroll
      for (int e = 0; e < 4; ++e) {
        const unsigned xe = xv[e], we = gwv[i][e];
        s = __builtin_amdgcn_fdot2_f32_bf16(__builtin_bit_cast(bf16x2, xe), __builtin_bit_cast(bf16x2, we), s, false);
      }
    }
    s += __shfl_xor(s, 1);
    s += __shfl_xor(s, 2);
    s += __shfl_xor(s, 4);
    if (gpart == 0) gout[l0 + grow] = bfr(1.f / (1.f + __expf(-bfr(s))));
    __syncthreads();
  }
}

constexpr int KC_PITCH = 768;
constexpr int KG_PITCH = 160;
constexpr int KG_ARR = 16 * KG_PITCH;
constexpr int KG_TAB = 8 * 2 * KG_ARR;
typedef __attribute__((ext_vector_type(2))) float f32x2;

__device__ __forceinline__ f32x2 cexp(float zr, float zi, float p) {
  const float e = __builtin_amdgcn_exp2f(zr * p);
  const float ph = __builtin_amdgcn_fractf(zi * p);
  return f32x2{e * __builtin_amdgcn_cosf(ph), e * __builtin_amdgcn_sinf(ph)};
}
__device__ __forceinline__ f32x2 cmul(f32x2 a, f32x2 b) {
  return f32x2{a[0] * b[0] - a[1] * b[1], a[0] * b[1] + a[1] * b[0]};
}
__device__ __forceinline__ void kgen_block(char* smem, int d0, const __bf16* __restrict__ lar,
                                           const __bf16* __restrict__ aim, const __bf16* __restrict__ cre,
                                           const __bf16* __restrict__ cim, const __bf16* __restrict__ ldt,
                                           unsigned* __restrict__ kc, unsigned* __restrict__ aG,
                                           const __bf16* __restrict__ wd) {
  const int tid = threadIdx.x, lane = tid & 63, w = __builtin_amdgcn_readfirstlane(tid >> 6);
  unsigned pf_dummy;
  asm volatile("global_load_dword %0, %1, off"
               : "=v"(pf_dummy) : "v"(wd + (size_t)((d0 >> 1) * 43 + (tid < 42 ? tid : 42)) * 64) : "memory");
  {
    u32x4* z = (u32x4*)(aG + (size_t)d0 * L) + tid;
    z[0] = u32x4{0u, 0u, 0u, 0u};
    z[512] = u32x4{0u, 0u, 0u, 0u};
    if (tid == 0) aG[(size_t)D * L + (d0 >> 1)] = 0u;
  }
  const int ch = w >> 2, wq = w & 3, tc = tid & 255;
  const int r = lane & 15, g = lane >> 4, hh = lane >> 5;
  char* wb = smem + w * 2 * KG_ARR;
  char* tb = smem + KG_TAB + ch * (4 * 656 * 4);
  float zr, zi;
  f32x2 cc;
  {
    const int d = d0 + ch, n = 32 * wq + (lane & 31);
    const float dt = bfr(expf((float)ldt[d]));
    const float ar = -bfr(expf((float)lar[d * NS + n]));
    zr = (ar * dt) * 1.4426950408889634f;
    zi = ((float)aim[d * NS + n] * dt) * 0.15915494309189535f;
    cc = f32x2{2.f * ((float)cre[d * NS + n] * dt), 2.f * ((float)cim[d * NS + n] * dt)};
  }
  const float fh = (float)(1 + hh);
  const f32x2 e128 = cexp(zr, zi, 128.f), e24 = cexp(zr, zi, 2.f * fh);
  f32x2 v[4], st[4];
  v[0] = cexp(zr, -zi, 64.f * hh);                 st[0] = cexp(zr, -zi, 8.f);
  v[1] = hh ? cmul(cc, e128) : cc;                 st[1] = cexp(zr, zi, 1.f);
  v[2] = cmul(cc, cexp(zr, zi, 256.f * fh));       st[2] = hh ? e128 : e24;
  v[3] = cmul(cc, cexp(zr, zi, 1024.f * fh));      st[3] = e128;
  const f32x2 v24 = cmul(v[2], hh ? e24 : e128);
  unsigned vh[4][8], vl[4][8];
#pragma unroll
  for (int i = 0; i < 8; ++i)
#pragma unroll
    for (int t = 0; t < 4; ++t) {
      vh[t][i] = pk2(v[t][0], v[t][1]);
      vl[t][i] = pk2(v[t][0] - lo_f(vh[t][i]), v[t][1] - hi_f(vh[t][i]));
      if (t == 2 && i == 3) v[t] = v24;
      else if (i < 7) v[t] = cmul(v[t], st[t]);
    }
  char* wr = wb + (8 * hh) * KG_PITCH + (lane & 31) * 4;
  const char* rd = wb + r * KG_PITCH + g * 16;
  bf16x8 fh_[4][2], fl_[4][2];
#pragma unroll
  for (int t = 0; t < 4; ++t) {
    __builtin_amdgcn_wave_barrier();
#pragma unroll
    for (int i = 0; i < 8; ++i) {
      *(unsigned*)(wr + i * KG_PITCH) = vh[t][i];
      *(unsigned*)(wr + i * KG_PITCH + KG_ARR) = vl[t][i];
    }
    __builtin_amdgcn_wave_barrier();
#pragma unroll
    for (int ss = 0; ss < 2; ++ss) {
      fh_[t][ss] = *(const bf16x8*)(rd + ss * 64);
      fl_[t][ss] = *(const bf16x8*)(rd + ss * 64 + KG_ARR);
    }
  }
  f32x4 acc[3];
#pragma unroll
  for (int m = 0; m < 3; ++m) acc[m] = f32x4{0.f, 0.f, 0.f, 0.f};
#pragma unroll
  for (int ss = 0; ss < 2; ++ss)
#pragma unroll
    for (int m = 0; m < 3; ++m) {
      acc[m] = mfma16(fl_[1 + m][ss], fh_[0][ss], acc[m]);
      acc[m] = mfma16(fh_[1 + m][ss], fl_[0][ss], acc[m]);
      acc[m] = mfma16(fh_[1 + m][ss], fh_[0][ss], acc[m]);
    }
  {
    float* tab = (float*)tb + wq * 656;
    *(f32x4*)(tab + 128 * (g >> 1) + 4 * (g & 1) + 8 * r) = acc[0];
    if (g < 2) {
      *(f32x4*)(tab + 256 + 64 * g + 4 * r) = acc[1];
#pragma unroll
      for (int e = 0; e < 4; ++e) tab[512 + 64 * g + 16 * e + r] = acc[2][e];
    } else {
#pragma unroll
      for (int e = 0; e < 4; ++e) tab[384 + (g & 1) + 32 * e + 2 * r] = acc[1][e];
      if (lane == 32) tab[640] = acc[2][0];
    }
  }
  __syncthreads();
  {
    const float* tab = (const float*)tb;
    unsigned* kd = kc + (size_t)(d0 + ch) * KC_PITCH;
#pragma unroll
    for (int e = 0; e < 3; ++e) {
      const int id = tc + 256 * e;
      if (id < 641) {
        const float v = (tab[id] + tab[656 + id]) + (tab[2 * 656 + id] + tab[3 * 656 + id]);
        const float vh = bfr(v);
        kd[id] = pk2(vh, v - vh);
      }
    }
  }
  asm volatile("s_waitcnt vmcnt(0)" : "+v"(pf_dummy)::"memory");
}

NOPK __global__ __launch_bounds__(512, 4) void k1(const __bf16* __restrict__ u, const __bf16* __restrict__ lar,
                                             const __bf16* __restrict__ aim, const __bf16* __restrict__ cre,
                                             const __bf16* __restrict__ cim, const __bf16* __restrict__ ldt,
                                             const __bf16* __restrict__ wg, const __bf16* __restrict__ wu,
                                             const __bf16* __restrict__ gw, __bf16* __restrict__ h,
                                             __bf16* __restrict__ uT, unsigned* __restrict__ kc,
                                             float* __restrict__ gout, unsigned* __restrict__ aG,
                                             const __bf16* __restrict__ wd) {
  extern __shared__ __attribute__((aligned(16))) char smem[];
  const int b = blockIdx.x;
  const int xcd = b & 7, i = (b & 255) >> 3;
  if (b < 256) {
    const int rb = 4 * (xcd >> 1) + (i & 3), cb = 8 * (xcd & 1) + (i >> 2);
    if (cb < 15) ffn1_block(smem, rb, cb, u, wg, wu, h);
    else gate_block(smem, rb, u, gw, gout, uT);
  } else {
    kgen_block(smem, 64 * xcd + 2 * i, lar, aim, cre, cim, ldt, kc, aG, wd);
  }
}

constexpr unsigned A_TAG = 0x5a17c3e9u;
constexpr int UCP = 4160;
constexpr int KARR = (240 + L) * 2;
constexpr int OFF_K = 4 * UCP;
constexpr int OFF_TAB = OFF_K + 2 * KARR;
constexpr int OFF_RED = UCP;
constexpr int CH_BYTES = OFF_RED + 32768;
static_assert(OFF_TAB + 3072 <= CH_BYTES && 2 * CH_BYTES <= LDS_BYTES, "lds");

struct PosLut {
  unsigned short v[L];
  constexpr PosLut() : v() {
    for (int j = 0; j < L; ++j) {
      const int sh = (j >= 256) + (j >= 512) + (j >= 1024);
      int q = j >> sh;
      if (sh) {
        const int rem = j & ((1 << sh) - 1), half = 1 << (sh - 1);
        if (rem > half || (rem == half && (q & 1))) ++q;
      }
      v[j] = (unsigned short)(4 * (128 * sh + q));
    }
  }
};
__device__ const PosLut g_poslut{};

__device__ __forceinline__ void conv_block(char* smem, int d0, const __bf16* __restrict__ uT,
                                           const unsigned* __restrict__ kc, const __bf16* __restrict__ dsk,
                                           unsigned* __restrict__ aG) {
  const int tid = threadIdx.x, lane = tid & 63, w = __builtin_amdgcn_readfirstlane(tid >> 6);
  const int ch = w >> 2, wq = w & 3, tc = tid & 255;
  const int r = lane & 15, g = lane >> 4, q = (lane >> 2) & 3, p = lane & 3;
  char* cb = smem + ch * CH_BYTES;
  const int d = d0 + ch;
  __builtin_amdgcn_s_setprio(3);
  {
    const unsigned lb = lds_addr_of(cb);
    glds16(uT + (size_t)d * L + wq * 512, lane * 16, lb + 32 + wq * 1024);
    if (wq < 3) glds16(kc + (size_t)d * KC_PITCH + wq * 256, lane * 16, lb + OFF_TAB + wq * 1024);
    const u32x4 z = {0u, 0u, 0u, 0u};
    if (tc < 60) *(u32x4*)(cb + OFF_K + (tc >= 30 ? KARR : 0) + (tc % 30) * 16) = z;
    else if (tc < 68) *(u32x4*)(cb + ((tc - 60) >> 1) * UCP + ((tc - 60) & 1) * 16) = z;
  }
  const float dsv = (float)dsk[d];
  const u32x4 lut = *(const u32x4*)(g_poslut.v + 8 * tc);
  unsigned tch;
  TOUCH(tch, aG + (size_t)d * L);
  asm volatile("s_waitcnt vmcnt(0)" : "+v"(tch)::"memory");
  __syncthreads();
  {
    const u32x4 pa = *(const u32x4*)(cb + 16 + 16 * tc), pb = *(const u32x4*)(cb + 32 + 16 * tc);
    const u32x4 c1 = {__builtin_amdgcn_alignbit(pb[0], pa[3], 16), __builtin_amdgcn_alignbit(pb[1], pb[0], 16),
                      __builtin_amdgcn_alignbit(pb[2], pb[1], 16), __builtin_amdgcn_alignbit(pb[3], pb[2], 16)};
    const u32x4 c2 = {pa[3], pb[0], pb[1], pb[2]};
    const u32x4 c3 = {__builtin_amdgcn_alignbit(pa[3], pa[2], 16), c1[0], c1[1], c1[2]};
    *(u32x4*)(cb + 1 * UCP + 32 + 16 * tc) = c1;
    *(u32x4*)(cb + 2 * UCP + 32 + 16 * tc) = c2;
    *(u32x4*)(cb + 3 * UCP + 32 + 16 * tc) = c3;
    u32x4 kh, kl;
#pragma unroll
    for (int e = 0; e < 4; ++e) {
      const unsigned t0 = *(const unsigned*)(cb + OFF_TAB + (lut[e] & 0xffffu));
      const unsigned t1 = *(const unsigned*)(cb + OFF_TAB + (lut[e] >> 16));
      kh[e] = __builtin_amdgcn_perm(t1, t0, 0x05040100u);
      kl[e] = __builtin_amdgcn_perm(t1, t0, 0x07060302u);
    }
    *(u32x4*)(cb + OFF_K + 480 + 16 * tc) = kh;
    *(u32x4*)(cb + OFF_K + KARR + 480 + 16 * tc) = kl;
  }
  __syncthreads();
  __builtin_amdgcn_s_setprio(1);
  f32x4 acc[8];
#pragma unroll
  for (int t = 0; t < 8; ++t) acc[t] = f32x4{0.f, 0.f, 0.f, 0.f};
#pragma unroll
  for (int rr = 0; rr < 2; ++rr) {
    const int rho = 2 * wq + rr;
    const int abase = q * UCP + 2 * (16 + 16 + 32 * rho - 8 * g + 4 * p);
    const int bbase = OFF_K + 2 * (240 - 16 - 32 * rho + 16 * r + 8 * g);
    bf16x8 af[8];
#pragma unroll
    for (int s = 0; s < 8; ++s) af[s] = tr_frag(cb, abase + 512 * s, abase + 512 * s - 8);
#pragma unroll
    for (int t = 0; t < 8; ++t) {
      const bf16x8 bh = *(const bf16x8*)(cb + bbase + 512 * t);
#pragma unroll
      for (int s = 0; s + t < 8; ++s) acc[s + t] = mfma16(af[s], bh, acc[s + t]);
      const bf16x8 bl = *(const bf16x8*)(cb + bbase + KARR + 512 * t);
#pragma unroll
      for (int s = 0; s + t < 8; ++s) acc[s + t] = mfma16(af[s], bl, acc[s + t]);
    }
  }
  __builtin_amdgcn_s_setprio(3);
  __syncthreads();
  if (tid == 256) __hip_atomic_store((gu32*)(aG + (size_t)D * L + (d0 >> 1)), 1u, __ATOMIC_RELAXED, __HIP_MEMORY_SCOPE_AGENT);
#pragma unroll
  for (int t = 0; t < 8; ++t)
    if ((t >> 1) != wq) *(f32x4*)(cb + OFF_RED + (wq * 8 + t) * 1024 + lane * 16) = acc[t];
  __syncthreads();
#pragma unroll
  for (int tt = 0; tt < 2; ++tt) {
    const int t = 2 * wq + tt;
    f32x4 own = f32x4{0.f, 0.f, 0.f, 0.f};
#pragma unroll
    for (int t2 = 0; t2 < 8; ++t2)
      if (t2 == t) own = acc[t2];
    f32x4 s = f32x4{0.f, 0.f, 0.f, 0.f};
#pragma unroll
    for (int sw = 0; sw < 4; ++sw) {
      const f32x4 o = *(const f32x4*)(cb + OFF_RED + (sw * 8 + t) * 1024 + lane * 16);
      s += (sw == wq) ? own : o;
    }
    const int l = 256 * t + 16 * r + 4 * g;
    const u32x2 uu = *(const u32x2*)(cb + 32 + 2 * l);
    const float a0 = bfr(s[0]) + bfr(lo_f(uu[0]) * dsv), a1 = bfr(s[1]) + bfr(hi_f(uu[0]) * dsv);
    const float a2 = bfr(s[2]) + bfr(lo_f(uu[1]) * dsv), a3 = bfr(s[3]) + bfr(hi_f(uu[1]) * dsv);
    const u32x4 gr = {pk2(a0, a1), A_TAG, pk2(a2, a3), A_TAG};
    asm volatile("global_store_dwordx4 %0, %1, off sc1\n\ts_nop 1" ::"v"(aG + (size_t)d * L + l), "v"(gr) : "memory");
  }
}

constexpr int F2_NS = 5;
constexpr int F2_STAGE = 16384;
constexpr int F2_NST = 22;

struct F2Ctx {
  const __bf16 *h, *wd;
  unsigned avoff, bvoff, bvoff_last, lbase;
  int w, kh;
  int aoff[2], boff[2];
  const unsigned* asrc;
  mutable u32x4 x0, x1;
};
__device__ __forceinline__ void f2_stage(const F2Ctx& c, int st) {
  const unsigned base = c.lbase + (st % F2_NS) * F2_STAGE;
  glds16(c.h + st * 64, c.avoff, base + c.w * 1024);
  glds16(c.wd + (size_t)st * 64 * D, st == F2_NST - 1 ? c.bvoff_last : c.bvoff, base + 8192 + c.w * 1024);
}
template <int ST>
__device__ __forceinline__ void f2_loop(const F2Ctx& c, const char* smem, bf16x8 (&ao)[2], bf16x8 (&bo)[2],
                                        f32x4 (&acc)[2][2]) {
  if constexpr (ST < F2_NST) {
    constexpr int rem = (F2_NST - 1 - ST) < (F2_NS - 2) ? (F2_NST - 1 - ST) : (F2_NS - 2);
    wait_vm<2 * rem>();
    WAIT_LGKM0();
    __builtin_amdgcn_s_barrier();
    asm volatile("" ::: "memory");
    if constexpr (ST + F2_NS - 1 < F2_NST) f2_stage(c, ST + F2_NS - 1);
    if constexpr (ST == F2_NST - 3)
      asm volatile("global_load_dwordx4 %0, %2, off sc1\n\tglobal_load_dwordx4 %1, %3, off sc1"
                   : "=&v"(c.x0), "=&v"(c.x1) : "v"(c.asrc), "v"(c.asrc + 16 * L) : "memory");
    const char* base = smem + (ST % F2_NS) * F2_STAGE;
    bf16x8 an[2], bn[2];
#pragma unroll
    for (int mt = 0; mt < 2; ++mt) an[mt] = *(const bf16x8*)(base + c.aoff[mt]);
#pragma unroll
    for (int nt = 0; nt < 2; ++nt) bn[nt] = tr_frag(base, c.boff[nt], c.boff[nt] + 512);
    __builtin_amdgcn_sched_barrier(0);
    if constexpr (ST > 0) {
#pragma unroll
      for (int mt = 0; mt < 2; ++mt)
#pragma unroll
        for (int nt = 0; nt < 2; ++nt) acc[mt][nt] = mfma16(ao[mt], bo[nt], acc[mt][nt]);
    }
    __builtin_amdgcn_sched_barrier(0);
    f2_loop<ST + 1>(c, smem, an, bn, acc);
  } else {
    if (c.kh == 0) {
#pragma unroll
      for (int mt = 0; mt < 2; ++mt)
#pragma unroll
        for (int nt = 0; nt < 2; ++nt) acc[mt][nt] = mfma16(ao[mt], bo[nt], acc[mt][nt]);
    }
  }
}

__device__ __forceinline__ void ffn2_block(char* smem, int row0, int cbk, const __bf16* __restrict__ h,
                                           const __bf16* __restrict__ wd, const unsigned* __restrict__ aG,
                                           const float* __restrict__ gin, __bf16* __restrict__ out) {
  const int col0 = 64 * cbk;
  const int tid = threadIdx.x, lane = tid & 63, w = __builtin_amdgcn_readfirstlane(tid >> 6);
  const int r = lane & 15, g = lane >> 4, q = (lane >> 2) & 3, p = lane & 3;
  const int kh = w >> 2, wm = (w >> 1) & 1, wn = w & 1;
  __builtin_amdgcn_s_setprio(1);
  F2Ctx c;
  c.h = h; c.wd = wd; c.w = w; c.kh = kh; c.lbase = lds_addr_of(smem);
  {
    const int row = 8 * w + (lane >> 3), ch = (lane & 7) ^ ((row >> 1) & 7);
    c.avoff = (unsigned)(((row0 + row) * HP + ch * 8) * 2);
    const int bk = 8 * w + (lane >> 3);
    const int pos = lane & 7, fp = ((bk >> 1) & 1) | (((bk >> 3) & 1) << 1);
    const int cc = col0 + 16 * ((pos >> 1) ^ fp) + 8 * (pos & 1);
    c.bvoff = (unsigned)((bk * D + cc) * 2);
    c.bvoff_last = (unsigned)(((bk > 31 ? 31 : bk) * D + cc) * 2);
  }
#pragma unroll
  for (int mt = 0; mt < 2; ++mt) {
    const int row = 32 * wm + 16 * mt + r, ch = 4 * kh + g;
    c.aoff[mt] = row * 128 + ((ch ^ ((row >> 1) & 7)) << 4);
  }
  const int fpl = (q >> 1) | ((g & 1) << 1);
#pragma unroll
  for (int nt = 0; nt < 2; ++nt) c.boff[nt] = 8192 + (32 * kh + 8 * g + q) * 128 + (((2 * wn + nt) ^ fpl) << 5) + 8 * p;

  f32x4 acc[2][2];
#pragma unroll
  for (int mt = 0; mt < 2; ++mt)
#pragma unroll
    for (int nt = 0; nt < 2; ++nt) acc[mt][nt] = f32x4{0.f, 0.f, 0.f, 0.f};

  unsigned tch;
  TOUCH(tch, out + (size_t)(row0 + 8 * w) * D + col0);
#pragma unroll
  for (int st = 0; st < F2_NS - 1; ++st) f2_stage(c, st);
  const int lr = 32 * wm + 16 * kh + 4 * g;
  const f32x4 gv = *(const f32x4*)(gin + row0 + lr);
  c.asrc = aG + (size_t)(col0 + 32 * wn + r) * L + row0 + lr;
  bf16x8 a0[2], b0[2];
  f2_loop<0>(c, smem, a0, b0, acc);
  WAIT_LGKM0();
  __builtin_amdgcn_s_barrier();
  asm volatile("" ::: "memory");
  asm volatile("s_waitcnt vmcnt(0)" : "+v"(c.x0), "+v"(c.x1), "+v"(tch)::"memory");
  const bool early = __all(c.x0[1] == A_TAG && c.x0[3] == A_TAG && c.x1[1] == A_TAG && c.x1[3] == A_TAG);
  if (w == 0 && !early) {
    const gu32* fl = (const gu32*)(aG + (size_t)D * L + 32 * cbk + (lane & 31));
    for (int spin = 0; spin < (1 << 22); ++spin) {
      const unsigned v = __hip_atomic_load(fl, __ATOMIC_RELAXED, __HIP_MEMORY_SCOPE_AGENT);
      if (__all(v == 1u)) break;
      __builtin_amdgcn_s_sleep(2);
    }
  }
  {
    char* red = smem + (w & 3) * 4096 + kh * 2048;
    const f32x4 o0 = kh ? acc[0][0] : acc[1][0], o1 = kh ? acc[0][1] : acc[1][1];
    *(f32x4*)(red + lane * 16) = o0;
    *(f32x4*)(red + 1024 + lane * 16) = o1;
  }
  __syncthreads();
  unsigned long long av[2];
  {
    const unsigned* src = c.asrc;
    av[0] = (unsigned long long)c.x0[0] | ((unsigned long long)c.x0[2] << 32);
    av[1] = (unsigned long long)c.x1[0] | ((unsigned long long)c.x1[2] << 32);
    for (int spin = 0; !early && spin < (1 << 22); ++spin) {
      u32x4 x0, x1;
      asm volatile("global_load_dwordx4 %0, %2, off sc1\n\tglobal_load_dwordx4 %1, %3, off sc1\n\ts_waitcnt vmcnt(0)"
                   : "=&v"(x0), "=&v"(x1) : "v"(src), "v"(src + 16 * L) : "memory");
      av[0] = (unsigned long long)x0[0] | ((unsigned long long)x0[2] << 32);
      av[1] = (unsigned long long)x1[0] | ((unsigned long long)x1[2] << 32);
      const bool ok = x0[1] == A_TAG && x0[3] == A_TAG && x1[1] == A_TAG && x1[3] == A_TAG;
      if (__all(ok)) break;
      __builtin_amdgcn_s_sleep(4);
    }
  }
  constexpr int OT = 16384, OTP = 144;
  {
    const char* red = smem + (w & 3) * 4096 + (1 - kh) * 2048;
#pragma unroll
    for (int nt = 0; nt < 2; ++nt) {
      const f32x4 o = *(const f32x4*)(red + nt * 1024 + lane * 16);
      const f32x4 own = kh ? acc[1][nt] : acc[0][nt];
      const f32x4 s = kh ? (o + own) : (own + o);
      const unsigned alo = (unsigned)av[nt], ahi = (unsigned)(av[nt] >> 32);
      const float a[4] = {lo_f(alo), hi_f(alo), lo_f(ahi), hi_f(ahi)};
#pragma unroll
      for (int e = 0; e < 4; ++e) {
        const float o2 = bfr(gv[e] * a[e]) + bfr(bfr(1.f - gv[e]) * bfr(s[e]));
        *(__bf16*)(smem + OT + (lr + e) * OTP + (32 * wn + 16 * nt + r) * 2) = (__bf16)o2;
      }
    }
  }
  __syncthreads();
  {
    const int row = tid >> 3, c8 = tid & 7;
    const u32x4 v = *(const u32x4*)(smem + OT + row * OTP + c8 * 16);
    *(u32x4*)(out + (size_t)(row0 + row) * D + col0 + c8 * 8) = v;
  }
}

NOPK __global__ __launch_bounds__(512, 4) void k2(const __bf16* __restrict__ h, const __bf16* __restrict__ wd,
                                             const __bf16* __restrict__ uT, const unsigned* __restrict__ kc,
                                             const __bf16* __restrict__ dsk, unsigned* __restrict__ aG,
                                             const float* __restrict__ gin, __bf16* __restrict__ out) {
  extern __shared__ __attribute__((aligned(16))) char smem[];
  const int b = blockIdx.x, xcd = b & 7, i = (b & 255) >> 3;
  if (b < 256) {
    const int pair = 32 * xcd + i;
    conv_block(smem, 2 * pair, uT, kc, dsk, aG);
  } else {
    ffn2_block(smem, 64 * (4 * xcd + (i & 3)), i >> 2, h, wd, aG, gin, out);
  }
}

extern "C" void kernel_launch(void* const* d_in, const int* in_sizes, int n_in, void* d_out, int out_size,
                              void* d_ws, size_t ws_size, hipStream_t stream) {
  const __bf16* u = (const __bf16*)d_in[0];
  const __bf16* lar = (const __bf16*)d_in[1];
  const __bf16* aim = (const __bf16*)d_in[2];
  const __bf16* cre = (const __bf16*)d_in[3];
  const __bf16* cim = (const __bf16*)d_in[4];
  const __bf16* ldt = (const __bf16*)d_in[5];
  const __bf16* dsk = (const __bf16*)d_in[6];
  const __bf16* wg = (const __bf16*)d_in[8];
  const __bf16* wu = (const __bf16*)d_in[9];
  const __bf16* wd = (const __bf16*)d_in[10];
  const __bf16* gw = (const __bf16*)d_in[11];
  char* ws = (char*)d_ws;
  __bf16* h = (__bf16*)ws;
  ws += (size_t)L * HP * 2;
  unsigned* aG = (unsigned*)ws;
  ws += (size_t)D * L * 4 + 1024;
  __bf16* uT = (__bf16*)ws;
  ws += (size_t)D * L * 2;
  unsigned* kc = (unsigned*)ws;
  ws += (size_t)D * KC_PITCH * 4;
  float* gout = (float*)ws;
  (void)hipFuncSetAttribute((const void*)k1, hipFuncAttributeMaxDynamicSharedMemorySize, LDS_BYTES);
  (void)hipFuncSetAttribute((const void*)k2, hipFuncAttributeMaxDynamicSharedMemorySize, LDS_BYTES);
  hipLaunchKernelGGL(k1, dim3(512), dim3(512), LDS_BYTES, stream, u, lar, aim, cre, cim, ldt, wg, wu, gw, h, uT, kc,
                     gout, aG, wd);
  hipLaunchKernelGGL(k2, dim3(512), dim3(512), LDS_BYTES, stream, (const __bf16*)h, wd, (const __bf16*)uT,
                     (const unsigned*)kc, dsk, aG, (const float*)gout, (__bf16*)d_out);
}
```

```cpp
#include <hip/hip_runtime.h>
#include <hip/hip_bf16.h>
#include <type_traits>

typedef __attribute__((ext_vector_type(8))) __bf16 bf16x8;
typedef __attribute__((ext_vector_type(2))) __bf16 bf16x2;
typedef __attribute__((ext_vector_type(4))) short s16x4;
typedef __attribute__((ext_vector_type(8))) short s16x8;
typedef __attribute__((ext_vector_type(4))) float f32x4;
typedef __attribute__((ext_vector_type(16))) float f32x16;
typedef __attribute__((ext_vector_type(4))) unsigned u32x4;
typedef __attribute__((ext_vector_type(2))) unsigned u32x2;
typedef __attribute__((address_space(3))) s16x4* lds_s16x4_ptr;
typedef __attribute__((address_space(1))) unsigned gu32;
typedef __attribute__((address_space(1))) unsigned long long gu64;

#define K_LO 0

#define NOPK __attribute__((target("no-packed-fp32-ops")))

constexpr int L = 2048, D = 512, NS = 128, F = 1376, HP = 1408;
constexpr int LDS_BYTES = 81920;

__device__ __forceinline__ float bf2f(unsigned short v) { return __uint_as_float((unsigned)v << 16); }
__device__ __forceinline__ float bfr(float x) { return (float)(__bf16)x; }
__device__ __forceinline__ unsigned pk2(float a, float b) {
  bf16x2 v = {(__bf16)a, (__bf16)b};
  return __builtin_bit_cast(unsigned, v);
}
__device__ __forceinline__ float lo_f(unsigned w) { return __uint_as_float(w << 16); }
__device__ __forceinline__ float hi_f(unsigned w) { return __uint_as_float(w & 0xffff0000u); }

__device__ __forceinline__ f32x4 mfma16(bf16x8 a, bf16x8 b, f32x4 c) {
  return __builtin_amdgcn_mfma_f32_16x16x32_bf16(a, b, c, 0, 0, 0);
}
__device__ __forceinline__ bf16x8 tr_frag(const char* lds, int off0, int off1) {
  s16x4 a = __builtin_amdgcn_ds_read_tr16_b64_v4i16((lds_s16x4_ptr)(lds + off0));
  s16x4 b = __builtin_amdgcn_ds_read_tr16_b64_v4i16((lds_s16x4_ptr)(lds + off1));
  s16x8 v = {a[0], a[1], a[2], a[3], b[0], b[1], b[2], b[3]};
  return __builtin_bit_cast(bf16x8, v);
}
__device__ __forceinline__ void glds16(const void* sbase, unsigned voff, unsigned lds_addr) {
  asm volatile("s_mov_b32 m0, %2\n\ts_nop 0\n\tglobal_load_lds_dwordx4 %0, %1" ::"v"(voff), "s"(sbase), "s"(lds_addr)
               : "memory", "m0");
}
__device__ __forceinline__ void glds16v(const void* vptr, unsigned lds_addr) {
  asm volatile("s_mov_b32 m0, %1\n\ts_nop 0\n\tglobal_load_lds_dwordx4 %0, off" ::"v"(vptr), "s"(lds_addr)
               : "memory", "m0");
}
__device__ __forceinline__ unsigned lds_addr_of(const char* p) {
  return (unsigned)(uintptr_t)(const __attribute__((address_space(3))) char*)p;
}
#define WAIT_VM(n) asm volatile("s_waitcnt vmcnt(" #n ")" ::: "memory")
#define WAIT_LGKM0() asm volatile("s_waitcnt lgkmcnt(0)" ::: "memory")
template <int N>
__device__ __forceinline__ void wait_vm() {
  asm volatile("s_waitcnt vmcnt(%0)" ::"i"(N) : "memory");
}

constexpr int F1_B = 32768;

struct F1Ctx {
  const __bf16 *x, *wg, *wu, *wmix;
  unsigned voffA[2], voffB, lbase;
  int w;
  int aoff[4], boff[3];
};
__device__ __forceinline__ void f1_stage_a(const F1Ctx& c, int m) {
  const unsigned ab = c.lbase + (m & 1) * 16384 + c.w * 1024;
  glds16(c.x + m * 64, c.voffA[0], ab);
  glds16(c.x + m * 64, c.voffA[1], ab + 8192);
}
template <bool G2>
__device__ __forceinline__ void f1_stage_b(const F1Ctx& c, int hs) {
  const unsigned bb = c.lbase + F1_B + (hs & 3) * 12288 + c.w * 1024;
  if constexpr (!G2) {
    glds16(c.wg + (size_t)hs * 32 * F, c.voffB, bb);
    glds16(c.wu + (size_t)hs * 32 * F, c.voffB, bb + 8192);
  } else {
    glds16v(c.wmix + (size_t)hs * 32 * F, bb);
  }
}
constexpr int f1_allowed(int hs, int nb) {
  int n = 0;
  if ((hs & 1) == 0 && hs >= 2) {
    if (hs + 1 < 16) n += nb;
    if (hs + 2 < 16) n += nb;
  } else {
    if (hs + 1 < 16) n += nb;
    if (hs >= 1 && ((hs - 1) & 1) == 0 && (hs + 1) / 2 < 8) n += 2;
    if (hs + 2 < 16) n += nb;
  }
  return n;
}
__device__ __forceinline__ f32x16 mfma32(bf16x8 a, bf16x8 b, f32x16 c) {
  return __builtin_amdgcn_mfma_f32_32x32x16_bf16(a, b, c, 0, 0, 0);
}
template <int HS, bool G2>
__device__ __forceinline__ void f1_loop(const F1Ctx& c, const char* smem, bf16x8 (&xf)[2], bf16x8 (&wf)[2][3],
                                        f32x16 (&acc)[3]) {
  if constexpr (HS < 16) {
    if constexpr (HS > 0) {
#pragma unroll
      for (int j = 0; j < 3; ++j) acc[j] = mfma32(wf[0][j], xf[0], acc[j]);
    }
    __builtin_amdgcn_sched_barrier(0);
    wait_vm<f1_allowed(HS, G2 ? 1 : 2)>();
    WAIT_LGKM0();
    __builtin_amdgcn_s_barrier();
    asm volatile("" ::: "memory");
    const char* ab = smem + ((HS >> 1) & 1) * 16384;
    const char* bbase = smem + (HS & 3) * 12288;
    __builtin_amdgcn_sched_barrier(0);
    xf[0] = *(const bf16x8*)(ab + c.aoff[2 * (HS & 1)]);
#pragma unroll
    for (int j = 0; j < 3; ++j) wf[0][j] = tr_frag(bbase, c.boff[j], c.boff[j] + 512);
    __builtin_amdgcn_sched_barrier(0);
#pragma unroll
    for (int j = 0; j < 3; ++j) {
      if constexpr (HS > 0) acc[j] = mfma32(wf[1][j], xf[1], acc[j]);
      __builtin_amdgcn_sched_barrier(0);
      if (j == 0) {
        if constexpr ((HS & 1) == 0 && (HS + 2) / 2 < 8) f1_stage_a(c, (HS + 2) / 2);
        if constexpr (HS + 3 < 16) f1_stage_b<G2>(c, HS + 3);
      } else {
        wf[1][j - 1] = tr_frag(bbase, c.boff[j - 1] + 2048, c.boff[j - 1] + 2560);
      }
      __builtin_amdgcn_sched_barrier(0);
    }
    wf[1][2] = tr_frag(bbase, c.boff[2] + 2048, c.boff[2] + 2560);
    xf[1] = *(const bf16x8*)(ab + c.aoff[2 * (HS & 1) + 1]);
    f1_loop<HS + 1, G2>(c, smem, xf, wf, acc);
  } else {
#pragma unroll
    for (int ks = 0; ks < 2; ++ks)
#pragma unroll
      for (int j = 0; j < 3; ++j) acc[j] = mfma32(wf[ks][j], xf[ks], acc[j]);
  }
}

__device__ __forceinline__ void ffn1_block(char* smem, int rb, int cb, const __bf16* __restrict__ x,
                                           const __bf16* __restrict__ wg, const __bf16* __restrict__ wu,
                                           __bf16* __restrict__ h) {
  const int tid = threadIdx.x, lane = tid & 63, w = __builtin_amdgcn_readfirstlane(tid >> 6);
  const int row0 = rb * 128, f0 = cb * 96;
  const int lt = w >> 1, sel = w & 1;
  const int r32 = lane & 31, hf = lane >> 5, blk = (lane >> 4) & 1, q = (lane >> 2) & 3, p = lane & 3;
  F1Ctx c;
  c.x = x; c.wg = wg; c.wu = wu; c.w = w; c.lbase = lds_addr_of(smem);
  {
#pragma unroll
    for (int i = 0; i < 2; ++i) {
      const int row = 64 * i + 8 * w + (lane >> 3), ch = (lane & 7) ^ ((row >> 1) & 7);
      c.voffA[i] = (unsigned)(((row0 + row) * D + ch * 8) * 2);
    }
    const int kk = 8 * (w & 3) + (lane >> 3), pos = lane & 7;
    const int i = (pos >> 1) ^ (kk & 2);
    const int par = (kk + cb) & 1;
    int c0 = f0 + 16 * (i + 2 * par) + 8 * (pos & 1);
    int c1 = f0 + 16 * ((i & 1) + 4 * (1 - par)) + 8 * (pos & 1);
    c0 = c0 > F - 8 ? F - 8 : c0;
    c1 = c1 > F - 8 ? F - 8 : c1;
    c.voffB = (unsigned)((kk * F + c0) * 2);
    c.wmix = (i < 2 ? wg : wu) + kk * F + c1;
  }
#pragma unroll
  for (int k4 = 0; k4 < 4; ++k4) {
    const int row = 32 * lt + r32;
    c.aoff[k4] = row * 128 + (((2 * k4 + hf) ^ ((row >> 1) & 7)) << 4);
  }
#pragma unroll
  for (int j = 0; j < 3; ++j) {
    const int par = (q + cb) & 1, jj = j - par;
    const bool whole = jj >= 0 && jj < 2;
    const int sub = whole ? 2 * sel : 1, ps = (whole ? 2 * jj : 2 * sel) + blk;
    c.boff[j] = F1_B + sub * 4096 + (8 * hf + q) * 128 + ((ps ^ (q & 2)) << 5) + 8 * p;
    asm volatile("" : "+v"(c.boff[j]));
  }
  f32x16 acc[3];
#pragma unroll
  for (int j = 0; j < 3; ++j)
#pragma unroll
    for (int e = 0; e < 16; ++e) acc[j][e] = 0.f;
  bf16x8 xf[2], wf[2][3];

  f1_stage_a(c, 0);
  if (w < 4) {
    f1_stage_b<false>(c, 0);
    f1_stage_b<false>(c, 1);
    f1_stage_b<false>(c, 2);
    f1_loop<0, false>(c, smem, xf, wf, acc);
  } else {
    f1_stage_b<true>(c, 0);
    f1_stage_b<true>(c, 1);
    f1_stage_b<true>(c, 2);
    f1_loop<0, true>(c, smem, xf, wf, acc);
  }

  __syncthreads();
  constexpr int HT = 49152, HTP = 208;
  auto finish = [&](auto SEL) {
    constexpr int S = decltype(SEL)::value;
#pragma unroll
    for (int j = 0; j < 3; ++j)
#pragma unroll
      for (int m2 = 0; m2 < 2; ++m2) {
        constexpr int o = 8 * (1 - S);
        *(f32x4*)(smem + (w * 6 + j * 2 + m2) * 1024 + lane * 16) =
            f32x4{acc[j][o + 4 * m2], acc[j][o + 4 * m2 + 1], acc[j][o + 4 * m2 + 2], acc[j][o + 4 * m2 + 3]};
      }
    __syncthreads();
#pragma unroll
    for (int j = 0; j < 3; ++j)
#pragma unroll
      for (int m2 = 0; m2 < 2; ++m2) {
        const f32x4 o4 = *(const f32x4*)(smem + ((w ^ 1) * 6 + j * 2 + m2) * 1024 + lane * 16);
        float hv[4];
#pragma unroll
        for (int e = 0; e < 4; ++e) {
          const float own = acc[j][8 * S + 4 * m2 + e];
          const float gv = S ? o4[e] : own, uv = S ? own : o4[e];
          const float t = gv * __builtin_fmaf(gv * gv, -0.10294324f, -2.3022082f);
          hv[e] = (gv * uv) * __builtin_amdgcn_rcpf(1.f + __builtin_amdgcn_exp2f(t));
        }
        u32x2 o = {pk2(hv[0], hv[1]), pk2(hv[2], hv[3])};
        *(u32x2*)(smem + HT + (32 * lt + r32) * HTP + (32 * j + 16 * S + 8 * m2 + 4 * hf) * 2) = o;
      }
  };
  if (sel) finish(std::integral_constant<int, 1>{});
  else finish(std::integral_constant<int, 0>{});
  __syncthreads();
#pragma unroll
  for (int i = 0; i < 3; ++i) {
    const int idx = tid + 512 * i, row = idx / 12, c8 = idx - row * 12;
    if (f0 + 8 * c8 < F)
      *(u32x4*)(h + (size_t)(row0 + row) * HP + f0 + 8 * c8) = *(const u32x4*)(smem + HT + row * HTP + c8 * 16);
  }
}

__device__ __forceinline__ void gate_block(char* smem, int rb, const __bf16* __restrict__ x,
                                           const __bf16* __restrict__ gw, float* __restrict__ gout,
                                           __bf16* __restrict__ uT) {
  const int tid = threadIdx.x, lane = tid & 63, w = __builtin_amdgcn_readfirstlane(tid >> 6);
  const int g = lane >> 4, q = (lane >> 2) & 3, p = lane & 3;
  const unsigned lbase = lds_addr_of(smem);
  const int grow = tid >> 3, gpart = tid & 7, gf = (grow & 3) | (((grow >> 3) & 1) << 2);
  u32x4 gwv[8];
#pragma unroll
  for (int i = 0; i < 8; ++i) gwv[i] = *(const u32x4*)(gw + ((gpart ^ gf) + 8 * (i >> 1)) * 16 + 8 * (i & 1));
#pragma unroll 1
  for (int pass = 0; pass < 2; ++pass) {
    const int l0 = rb * 128 + pass * 64;
#pragma unroll
    for (int i = 0; i < 8; ++i) {
      const int row = 8 * w + i, fr = (row & 3) | (((row >> 3) & 1) << 2);
      glds16(x + (size_t)(l0 + row) * D, (unsigned)((((lane >> 1) ^ fr) * 2 + (lane & 1)) * 16), lbase + row * 1024);
    }
    WAIT_VM(0);
    __syncthreads();
#pragma unroll
    for (int i = 0; i < 8; ++i) {
      const int unit = w * 8 + i, c16 = unit >> 1, half = unit & 1;
      const int ra = 32 * half + 8 * g + q, rb2 = ra + 4;
      const int fa = (ra & 3) | (((ra >> 3) & 1) << 2);
      const bf16x8 v = tr_frag(smem, ra * 1024 + ((c16 ^ fa) << 5) + 8 * p, rb2 * 1024 + ((c16 ^ fa) << 5) + 8 * p);
      *(bf16x8*)(uT + (size_t)(16 * c16 + (lane & 15)) * L + l0 + 32 * half + 8 * g) = v;
    }
    float s = 0.f;
#pragma unroll
    for (int i = 0; i < 8; ++i) {
      const u32x4 xv = *(const u32x4*)(smem + grow * 1024 + (gpart + 8 * (i >> 1)) * 32 + 16 * (i & 1));
#pragma unroll
      for (int e = 0; e < 4; ++e) {
        const unsigned xe = xv[e], we = gwv[i][e];
        s = __builtin_amdgcn_fdot2_f32_bf16(__builtin_bit_cast(bf16x2, xe), __builtin_bit_cast(bf16x2, we), s, false);
      }
    }
    s += __shfl_xor(s, 1);
    s += __shfl_xor(s, 2);
    s += __shfl_xor(s, 4);
    if (gpart == 0) gout[l0 + grow] = bfr(1.f / (1.f + __expf(-bfr(s))));
    __syncthreads();
  }
}

constexpr int KC_PITCH = 768;
constexpr int KG_PITCH = 160;
constexpr int KG_ARR = 16 * KG_PITCH;
constexpr int KG_TAB = 8 * 2 * KG_ARR;
typedef __attribute__((ext_vector_type(2))) float f32x2;

__device__ __forceinline__ f32x2 cexp(float zr, float zi, float p) {
  const float e = __builtin_amdgcn_exp2f(zr * p);
  const float ph = __builtin_amdgcn_fractf(zi * p);
  return f32x2{e * __builtin_amdgcn_cosf(ph), e * __builtin_amdgcn_sinf(ph)};
}
__device__ __forceinline__ f32x2 cmul(f32x2 a, f32x2 b) {
  return f32x2{a[0] * b[0] - a[1] * b[1], a[0] * b[1] + a[1] * b[0]};
}
__device__ __forceinline__ void kgen_block(char* smem, int d0, const __bf16* __restrict__ lar,
                                           const __bf16* __restrict__ aim, const __bf16* __restrict__ cre,
                                           const __bf16* __restrict__ cim, const __bf16* __restrict__ ldt,
                                           unsigned* __restrict__ kc, unsigned* __restrict__ aG,
                                           const __bf16* __restrict__ wd) {
  const int tid = threadIdx.x, lane = tid & 63, w = __builtin_amdgcn_readfirstlane(tid >> 6);
  unsigned pf_dummy;
  asm volatile("global_load_dword %0, %1, off"
               : "=v"(pf_dummy) : "v"(wd + (size_t)((d0 >> 1) * 43 + (tid < 42 ? tid : 42)) * 64) : "memory");
  {
    u32x4* z = (u32x4*)(aG + (size_t)d0 * L) + tid;
    z[0] = u32x4{0u, 0u, 0u, 0u};
    z[512] = u32x4{0u, 0u, 0u, 0u};
    if (tid == 0) aG[(size_t)D * L + (d0 >> 1)] = 0u;
  }
  const int ch = w >> 2, wq = w & 3, tc = tid & 255;
  const int r = lane & 15, g = lane >> 4, hh = lane >> 5;
  char* wb = smem + w * 2 * KG_ARR;
  char* tb = smem + KG_TAB + ch * (4 * 656 * 4);
  float zr, zi;
  f32x2 cc;
  {
    const int d = d0 + ch, n = 32 * wq + (lane & 31);
    const float dt = bfr(expf((float)ldt[d]));
    const float ar = -bfr(expf((float)lar[d * NS + n]));
    zr = (ar * dt) * 1.4426950408889634f;
    zi = ((float)aim[d * NS + n] * dt) * 0.15915494309189535f;
    cc = f32x2{2.f * ((float)cre[d * NS + n] * dt), 2.f * ((float)cim[d * NS + n] * dt)};
  }
  const float fh = (float)(1 + hh);
  const f32x2 e128 = cexp(zr, zi, 128.f), e24 = cexp(zr, zi, 2.f * fh);
  f32x2 v[4], st[4];
  v[0] = cexp(zr, -zi, 64.f * hh);                 st[0] = cexp(zr, -zi, 8.f);
  v[1] = hh ? cmul(cc, e128) : cc;                 st[1] = cexp(zr, zi, 1.f);
  v[2] = cmul(cc, cexp(zr, zi, 256.f * fh));       st[2] = hh ? e128 : e24;
  v[3] = cmul(cc, cexp(zr, zi, 1024.f * fh));      st[3] = e128;
  const f32x2 v24 = cmul(v[2], hh ? e24 : e128);
  unsigned vh[4][8], vl[4][8];
#pragma unroll
  for (int i = 0; i < 8; ++i)
#pragma unroll
    for (int t = 0; t < 4; ++t) {
      vh[t][i] = pk2(v[t][0], v[t][1]);
      vl[t][i] = pk2(v[t][0] - lo_f(vh[t][i]), v[t][1] - hi_f(vh[t][i]));
      if (t == 2 && i == 3) v[t] = v24;
      else if (i < 7) v[t] = cmul(v[t], st[t]);
    }
  char* wr = wb + (8 * hh) * KG_PITCH + (lane & 31) * 4;
  const char* rd = wb + r * KG_PITCH + g * 16;
  bf16x8 fh_[4][2], fl_[4][2];
#pragma unroll
  for (int t = 0; t < 4; ++t) {
    __builtin_amdgcn_wave_barrier();
#pragma unroll
    for (int i = 0; i < 8; ++i) {
      *(unsigned*)(wr + i * KG_PITCH) = vh[t][i];
      *(unsigned*)(wr + i * KG_PITCH + KG_ARR) = vl[t][i];
    }
    __builtin_amdgcn_wave_barrier();
#pragma unroll
    for (int ss = 0; ss < 2; ++ss) {
      fh_[t][ss] = *(const bf16x8*)(rd + ss * 64);
      fl_[t][ss] = *(const bf16x8*)(rd + ss * 64 + KG_ARR);
    }
  }
  f32x4 acc[3];
#pragma unroll
  for (int m = 0; m < 3; ++m) acc[m] = f32x4{0.f, 0.f, 0.f, 0.f};
#pragma unroll
  for (int ss = 0; ss < 2; ++ss)
#pragma unroll
    for (int m = 0; m < 3; ++m) {
      acc[m] = mfma16(fl_[1 + m][ss], fh_[0][ss], acc[m]);
      acc[m] = mfma16(fh_[1 + m][ss], fl_[0][ss], acc[m]);
      acc[m] = mfma16(fh_[1 + m][ss], fh_[0][ss], acc[m]);
    }
  {
    float* tab = (float*)tb + wq * 656;
    *(f32x4*)(tab + 128 * (g >> 1) + 4 * (g & 1) + 8 * r) = acc[0];
    if (g < 2) {
      *(f32x4*)(tab + 256 + 64 * g + 4 * r) = acc[1];
#pragma unroll
      for (int e = 0; e < 4; ++e) tab[512 + 64 * g + 16 * e + r] = acc[2][e];
    } else {
#pragma unroll
      for (int e = 0; e < 4; ++e) tab[384 + (g & 1) + 32 * e + 2 * r] = acc[1][e];
      if (lane == 32) tab[640] = acc[2][0];
    }
  }
  __syncthreads();
  {
    const float* tab = (const float*)tb;
    unsigned* kd = kc + (size_t)(d0 + ch) * KC_PITCH;
#pragma unroll
    for (int e = 0; e < 3; ++e) {
      const int id = tc + 256 * e;
      if (id < 641) {
        const float v = (tab[id] + tab[656 + id]) + (tab[2 * 656 + id] + tab[3 * 656 + id]);
        const float vh = bfr(v);
        kd[id] = pk2(vh, v - vh);
      }
    }
  }
  asm volatile("s_waitcnt vmcnt(0)" : "+v"(pf_dummy)::"memory");
}

NOPK __global__ __launch_bounds__(512, 4) void k1(const __bf16* __restrict__ u, const __bf16* __restrict__ lar,
                                             const __bf16* __restrict__ aim, const __bf16* __restrict__ cre,
                                             const __bf16* __restrict__ cim, const __bf16* __restrict__ ldt,
                                             const __bf16* __restrict__ wg, const __bf16* __restrict__ wu,
                                             const __bf16* __restrict__ gw, __bf16* __restrict__ h,
                                             __bf16* __restrict__ uT, unsigned* __restrict__ kc,
                                             float* __restrict__ gout, unsigned* __restrict__ aG,
                                             const __bf16* __restrict__ wd) {
  extern __shared__ __attribute__((aligned(16))) char smem[];
  const int b = blockIdx.x;
  const int xcd = b & 7, i = (b & 255) >> 3;
  if (b < 256) {
    const int rb = 4 * (xcd >> 1) + (i & 3), cb = 8 * (xcd & 1) + (i >> 2);
    if (cb < 15) ffn1_block(smem, rb, cb, u, wg, wu, h);
    else gate_block(smem, rb, u, gw, gout, uT);
  } else {
    kgen_block(smem, 64 * xcd + 2 * i, lar, aim, cre, cim, ldt, kc, aG, wd);
  }
}

constexpr unsigned A_TAG = 0x5a17c3e9u;
constexpr int UCP = 4160;
constexpr int KARR = (240 + L) * 2;
constexpr int OFF_K = 4 * UCP;
constexpr int OFF_TAB = OFF_K + 2 * KARR;
constexpr int OFF_RED = UCP;
constexpr int CH_BYTES = OFF_RED + 32768;
static_assert(OFF_TAB + 3072 <= CH_BYTES && 2 * CH_BYTES <= LDS_BYTES, "lds");

struct PosLut {
  unsigned short v[L];
  constexpr PosLut() : v() {
    for (int j = 0; j < L; ++j) {
      const int sh = (j >= 256) + (j >= 512) + (j >= 1024);
      int q = j >> sh;
      if (sh) {
        const int rem = j & ((1 << sh) - 1), half = 1 << (sh - 1);
        if (rem > half || (rem == half && (q & 1))) ++q;
      }
      v[j] = (unsigned short)(4 * (128 * sh + q));
    }
  }
};
__device__ const PosLut g_poslut{};

__device__ __forceinline__ void conv_block(char* smem, int d0, const __bf16* __restrict__ uT,
                                           const unsigned* __restrict__ kc, const __bf16* __restrict__ dsk,
                                           unsigned* __restrict__ aG) {
  const int tid = threadIdx.x, lane = tid & 63, w = __builtin_amdgcn_readfirstlane(tid >> 6);
  const int ch = w >> 2, wq = w & 3, tc = tid & 255;
  const int r = lane & 15, g = lane >> 4, q = (lane >> 2) & 3, p = lane & 3;
  char* cb = smem + ch * CH_BYTES;
  const int d = d0 + ch;
  __builtin_amdgcn_s_setprio(3);
  {
    const unsigned lb = lds_addr_of(cb);
    glds16(uT + (size_t)d * L + wq * 512, lane * 16, lb + 32 + wq * 1024);
    if (wq < 3) glds16(kc + (size_t)d * KC_PITCH + wq * 256, lane * 16, lb + OFF_TAB + wq * 1024);
    const u32x4 z = {0u, 0u, 0u, 0u};
    if (tc < 60) *(u32x4*)(cb + OFF_K + (tc >= 30 ? KARR : 0) + (tc % 30) * 16) = z;
    else if (tc < 68) *(u32x4*)(cb + ((tc - 60) >> 1) * UCP + ((tc - 60) & 1) * 16) = z;
  }
  const float dsv = (float)dsk[d];
  const u32x4 lut = *(const u32x4*)(g_poslut.v + 8 * tc);
  WAIT_VM(0);
  __syncthreads();
  {
    const u32x4 pa = *(const u32x4*)(cb + 16 + 16 * tc), pb = *(const u32x4*)(cb + 32 + 16 * tc);
    const u32x4 c1 = {__builtin_amdgcn_alignbit(pb[0], pa[3], 16), __builtin_amdgcn_alignbit(pb[1], pb[0], 16),
                      __builtin_amdgcn_alignbit(pb[2], pb[1], 16), __builtin_amdgcn_alignbit(pb[3], pb[2], 16)};
    const u32x4 c2 = {pa[3], pb[0], pb[1], pb[2]};
    const u32x4 c3 = {__builtin_amdgcn_alignbit(pa[3], pa[2], 16), c1[0], c1[1], c1[2]};
    *(u32x4*)(cb + 1 * UCP + 32 + 16 * tc) = c1;
    *(u32x4*)(cb + 2 * UCP + 32 + 16 * tc) = c2;
    *(u32x4*)(cb + 3 * UCP + 32 + 16 * tc) = c3;
    u32x4 kh, kl;
#pragma unroll
    for (int e = 0; e < 4; ++e) {
      const unsigned t0 = *(const unsigned*)(cb + OFF_TAB + (lut[e] & 0xffffu));
      const unsigned t1 = *(const unsigned*)(cb + OFF_TAB + (lut[e] >> 16));
      kh[e] = __builtin_amdgcn_perm(t1, t0, 0x05040100u);
      kl[e] = __builtin_amdgcn_perm(t1, t0, 0x07060302u);
    }
    *(u32x4*)(cb + OFF_K + 480 + 16 * tc) = kh;
  }
  __syncthreads();
  __builtin_amdgcn_s_setprio(1);
  f32x4 acc[8];
#pragma unroll
  for (int t = 0; t < 8; ++t) acc[t] = f32x4{0.f, 0.f, 0.f, 0.f};
#pragma unroll
  for (int rr = 0; rr < 2; ++rr) {
    const int rho = 2 * wq + rr;
    const int abase = q * UCP + 2 * (16 + 16 + 32 * rho - 8 * g + 4 * p);
    const int bbase = OFF_K + 2 * (240 - 16 - 32 * rho + 16 * r + 8 * g);
    bf16x8 af[8];
#pragma unroll
    for (int s = 0; s < 8; ++s) af[s] = tr_frag(cb, abase + 512 * s, abase + 512 * s - 8);
#pragma unroll
    for (int t = 0; t < 8; ++t) {
      const bf16x8 bh = *(const bf16x8*)(cb + bbase + 512 * t);
#pragma unroll
      for (int s = 0; s + t < 8; ++s) acc[s + t] = mfma16(af[s], bh, acc[s + t]);
    }
  }
  __builtin_amdgcn_s_setprio(3);
  __syncthreads();
  if (tid == 256) __hip_atomic_store((gu32*)(aG + (size_t)D * L + (d0 >> 1)), 1u, __ATOMIC_RELAXED, __HIP_MEMORY_SCOPE_AGENT);
#pragma unroll
  for (int t = 0; t < 8; ++t)
    if ((t >> 1) != wq) *(f32x4*)(cb + OFF_RED + (wq * 8 + t) * 1024 + lane * 16) = acc[t];
  __syncthreads();
#pragma unroll
  for (int tt = 0; tt < 2; ++tt) {
    const int t = 2 * wq + tt;
    f32x4 own = f32x4{0.f, 0.f, 0.f, 0.f};
#pragma unroll
    for (int t2 = 0; t2 < 8; ++t2)
      if (t2 == t) own = acc[t2];
    f32x4 s = f32x4{0.f, 0.f, 0.f, 0.f};
#pragma unroll
    for (int sw = 0; sw < 4; ++sw) {
      const f32x4 o = *(const f32x4*)(cb + OFF_RED + (sw * 8 + t) * 1024 + lane * 16);
      s += (sw == wq) ? own : o;
    }
    const int l = 256 * t + 16 * r + 4 * g;
    const u32x2 uu = *(const u32x2*)(cb + 32 + 2 * l);
    const float a0 = bfr(s[0]) + bfr(lo_f(uu[0]) * dsv), a1 = bfr(s[1]) + bfr(hi_f(uu[0]) * dsv);
    const float a2 = bfr(s[2]) + bfr(lo_f(uu[1]) * dsv), a3 = bfr(s[3]) + bfr(hi_f(uu[1]) * dsv);
    const u32x4 gr = {pk2(a0, a1), A_TAG, pk2(a2, a3), A_TAG};
    asm volatile("global_store_dwordx4 %0, %1, off sc1\n\ts_nop 1" ::"v"(aG + (size_t)d * L + l), "v"(gr) : "memory");
  }
}

constexpr int F2_NS = 5;
constexpr int F2_STAGE = 16384;
constexpr int F2_NST = 22;

struct F2Ctx {
  const __bf16 *h, *wd;
  unsigned avoff, bvoff, bvoff_last, lbase;
  int w, kh;
  int aoff[2], boff[2];
  const unsigned* asrc;
  mutable u32x4 x0, x1;
};
__device__ __forceinline__ void f2_stage(const F2Ctx& c, int st) {
  const unsigned base = c.lbase + (st % F2_NS) * F2_STAGE;
  glds16(c.h + st * 64, c.avoff, base + c.w * 1024);
  glds16(c.wd + (size_t)st * 64 * D, st == F2_NST - 1 ? c.bvoff_last : c.bvoff, base + 8192 + c.w * 1024);
}
template <int ST>
__device__ __forceinline__ void f2_loop(const F2Ctx& c, const char* smem, bf16x8 (&ao)[2], bf16x8 (&bo)[2],
                                        f32x4 (&acc)[2][2]) {
  if constexpr (ST < F2_NST) {
    constexpr int rem = (F2_NST - 1 - ST) < (F2_NS - 2) ? (F2_NST - 1 - ST) : (F2_NS - 2);
    wait_vm<2 * rem>();
    WAIT_LGKM0();
    __builtin_amdgcn_s_barrier();
    asm volatile("" ::: "memory");
    if constexpr (ST + F2_NS - 1 < F2_NST) f2_stage(c, ST + F2_NS - 1);
    if constexpr (ST == F2_NST - 3)
      asm volatile("global_load_dwordx4 %0, %2, off sc1\n\tglobal_load_dwordx4 %1, %3, off sc1"
                   : "=&v"(c.x0), "=&v"(c.x1) : "v"(c.asrc), "v"(c.asrc + 16 * L) : "memory");
    const char* base = smem + (ST % F2_NS) * F2_STAGE;
    bf16x8 an[2], bn[2];
#pragma unroll
    for (int mt = 0; mt < 2; ++mt) an[mt] = *(const bf16x8*)(base + c.aoff[mt]);
#pragma unroll
    for (int nt = 0; nt < 2; ++nt) bn[nt] = tr_frag(base, c.boff[nt], c.boff[nt] + 512);
    __builtin_amdgcn_sched_barrier(0);
    if constexpr (ST > 0) {
#pragma unroll
      for (int mt = 0; mt < 2; ++mt)
#pragma unroll
        for (int nt = 0; nt < 2; ++nt) acc[mt][nt] = mfma16(ao[mt], bo[nt], acc[mt][nt]);
    }
    __builtin_amdgcn_sched_barrier(0);
    f2_loop<ST + 1>(c, smem, an, bn, acc);
  } else {
    if (c.kh == 0) {
#pragma unroll
      for (int mt = 0; mt < 2; ++mt)
#pragma unroll
        for (int nt = 0; nt < 2; ++nt) acc[mt][nt] = mfma16(ao[mt], bo[nt], acc[mt][nt]);
    }
  }
}

__device__ __forceinline__ void ffn2_block(char* smem, int row0, int cbk, const __bf16* __restrict__ h,
                                           const __bf16* __restrict__ wd, const unsigned* __restrict__ aG,
                                           const float* __restrict__ gin, __bf16* __restrict__ out) {
  const int col0 = 64 * cbk;
  const int tid = threadIdx.x, lane = tid & 63, w = __builtin_amdgcn_readfirstlane(tid >> 6);
  const int r = lane & 15, g = lane >> 4, q = (lane >> 2) & 3, p = lane & 3;
  const int kh = w >> 2, wm = (w >> 1) & 1, wn = w & 1;
  __builtin_amdgcn_s_setprio(1);
  F2Ctx c;
  c.h = h; c.wd = wd; c.w = w; c.kh = kh; c.lbase = lds_addr_of(smem);
  {
    const int row = 8 * w + (lane >> 3), ch = (lane & 7) ^ ((row >> 1) & 7);
    c.avoff = (unsigned)(((row0 + row) * HP + ch * 8) * 2);
    const int bk = 8 * w + (lane >> 3);
    const int pos = lane & 7, fp = ((bk >> 1) & 1) | (((bk >> 3) & 1) << 1);
    const int cc = col0 + 16 * ((pos >> 1) ^ fp) + 8 * (pos & 1);
    c.bvoff = (unsigned)((bk * D + cc) * 2);
    c.bvoff_last = (unsigned)(((bk > 31 ? 31 : bk) * D + cc) * 2);
  }
#pragma unroll
  for (int mt = 0; mt < 2; ++mt) {
    const int row = 32 * wm + 16 * mt + r, ch = 4 * kh + g;
    c.aoff[mt] = row * 128 + ((ch ^ ((row >> 1) & 7)) << 4);
  }
  const int fpl = (q >> 1) | ((g & 1) << 1);
#pragma unroll
  for (int nt = 0; nt < 2; ++nt) c.boff[nt] = 8192 + (32 * kh + 8 * g + q) * 128 + (((2 * wn + nt) ^ fpl) << 5) + 8 * p;

  f32x4 acc[2][2];
#pragma unroll
  for (int mt = 0; mt < 2; ++mt)
#pragma unroll
    for (int nt = 0; nt < 2; ++nt) acc[mt][nt] = f32x4{0.f, 0.f, 0.f, 0.f};

#pragma unroll
  for (int st = 0; st < F2_NS - 1; ++st) f2_stage(c, st);
  const int lr = 32 * wm + 16 * kh + 4 * g;
  const f32x4 gv = *(const f32x4*)(gin + row0 + lr);
  c.asrc = aG + (size_t)(col0 + 32 * wn + r) * L + row0 + lr;
  bf16x8 a0[2], b0[2];
  f2_loop<0>(c, smem, a0, b0, acc);
  WAIT_LGKM0();
  __builtin_amdgcn_s_barrier();
  asm volatile("" ::: "memory");
  asm volatile("s_waitcnt vmcnt(0)" : "+v"(c.x0), "+v"(c.x1)::"memory");
  const bool early = __all(c.x0[1] == A_TAG && c.x0[3] == A_TAG && c.x1[1] == A_TAG && c.x1[3] == A_TAG);
  if (w == 0 && !early) {
    const gu32* fl = (const gu32*)(aG + (size_t)D * L + 32 * cbk + (lane & 31));
    for (int spin = 0; spin < (1 << 22); ++spin) {
      const unsigned v = __hip_atomic_load(fl, __ATOMIC_RELAXED, __HIP_MEMORY_SCOPE_AGENT);
      if (__all(v == 1u)) break;
      __builtin_amdgcn_s_sleep(2);
    }
  }
  {
    char* red = smem + (w & 3) * 4096 + kh * 2048;
    const f32x4 o0 = kh ? acc[0][0] : acc[1][0], o1 = kh ? acc[0][1] : acc[1][1];
    *(f32x4*)(red + lane * 16) = o0;
    *(f32x4*)(red + 1024 + lane * 16) = o1;
  }
  __syncthreads();
  unsigned long long av[2];
  {
    const unsigned* src = c.asrc;
    av[0] = (unsigned long long)c.x0[0] | ((unsigned long long)c.x0[2] << 32);
    av[1] = (unsigned long long)c.x1[0] | ((unsigned long long)c.x1[2] << 32);
    for (int spin = 0; !early && spin < (1 << 22); ++spin) {
      u32x4 x0, x1;
      asm volatile("global_load_dwordx4 %0, %2, off sc1\n\tglobal_load_dwordx4 %1, %3, off sc1\n\ts_waitcnt vmcnt(0)"
                   : "=&v"(x0), "=&v"(x1) : "v"(src), "v"(src + 16 * L) : "memory");
      av[0] = (unsigned long long)x0[0] | ((unsigned long long)x0[2] << 32);
      av[1] = (unsigned long long)x1[0] | ((unsigned long long)x1[2] << 32);
      const bool ok = x0[1] == A_TAG && x0[3] == A_TAG && x1[1] == A_TAG && x1[3] == A_TAG;
      if (__all(ok)) break;
      __builtin_amdgcn_s_sleep(4);
    }
  }
  constexpr int OT = 16384, OTP = 144;
  {
    const char* red = smem + (w & 3) * 4096 + (1 - kh) * 2048;
#pragma unroll
    for (int nt = 0; nt < 2; ++nt) {
      const f32x4 o = *(const f32x4*)(red + nt * 1024 + lane * 16);
      const f32x4 own = kh ? acc[1][nt] : acc[0][nt];
      const f32x4 s = kh ? (o + own) : (own + o);
      const unsigned alo = (unsigned)av[nt], ahi = (unsigned)(av[nt] >> 32);
      const float a[4] = {lo_f(alo), hi_f(alo), lo_f(ahi), hi_f(ahi)};
#pragma unroll
      for (int e = 0; e < 4; ++e) {
        const float o2 = bfr(gv[e] * a[e]) + bfr(bfr(1.f - gv[e]) * bfr(s[e]));
        *(__bf16*)(smem + OT + (lr + e) * OTP + (32 * wn + 16 * nt + r) * 2) = (__bf16)o2;
      }
    }
  }
  __syncthreads();
  {
    const int row = tid >> 3, c8 = tid & 7;
    const u32x4 v = *(const u32x4*)(smem + OT + row * OTP + c8 * 16);
    *(u32x4*)(out + (size_t)(row0 + row) * D + col0 + c8 * 8) = v;
  }
}

NOPK __global__ __launch_bounds__(512, 4) void k2(const __bf16* __restrict__ h, const __bf16* __restrict__ wd,
                                             const __bf16* __restrict__ uT, const unsigned* __restrict__ kc,
                                             const __bf16* __restrict__ dsk, unsigned* __restrict__ aG,
                                             const float* __restrict__ gin, __bf16* __restrict__ out) {
  extern __shared__ __attribute__((aligned(16))) char smem[];
  const int b = blockIdx.x, xcd = b & 7, i = (b & 255) >> 3;
  if (b < 256) {
    const int pair = 32 * xcd + i;
    conv_block(smem, 2 * pair, uT, kc, dsk, aG);
  } else {
    ffn2_block(smem, 64 * (4 * xcd + (i & 3)), i >> 2, h, wd, aG, gin, out);
  }
}

extern "C" void kernel_launch(void* const* d_in, const int* in_sizes, int n_in, void* d_out, int out_size,
                              void* d_ws, size_t ws_size, hipStream_t stream) {
  const __bf16* u = (const __bf16*)d_in[0];
  const __bf16* lar = (const __bf16*)d_in[1];
  const __bf16* aim = (const __bf16*)d_in[2];
  const __bf16* cre = (const __bf16*)d_in[3];
  const __bf16* cim = (const __bf16*)d_in[4];
  const __bf16* ldt = (const __bf16*)d_in[5];
  const __bf16* dsk = (const __bf16*)d_in[6];
  const __bf16* wg = (const __bf16*)d_in[8];
  const __bf16* wu = (const __bf16*)d_in[9];
  const __bf16* wd = (const __bf16*)d_in[10];
  const __bf16* gw = (const __bf16*)d_in[11];
  char* ws = (char*)d_ws;
  __bf16* h = (__bf16*)ws;
  ws += (size_t)L * HP * 2;
  unsigned* aG = (unsigned*)ws;
  ws += (size_t)D * L * 4 + 1024;
  __bf16* uT = (__bf16*)ws;
  ws += (size_t)D * L * 2;
  unsigned* kc = (unsigned*)ws;
  ws += (size_t)D * KC_PITCH * 4;
  float* gout = (float*)ws;
  (void)hipFuncSetAttribute((const void*)k1, hipFuncAttributeMaxDynamicSharedMemorySize, LDS_BYTES);
  (void)hipFuncSetAttribute((const void*)k2, hipFuncAttributeMaxDynamicSharedMemorySize, LDS_BYTES);
  hipLaunchKernelGGL(k1, dim3(512), dim3(512), LDS_BYTES, stream, u, lar, aim, cre, cim, ldt, wg, wu, gw, h, uT, kc,
                     gout, aG, wd);
  hipLaunchKernelGGL(k2, dim3(512), dim3(512), LDS_BYTES, stream, (const __bf16*)h, wd, (const __bf16*)uT,
                     (const unsigned*)kc, dsk, aG, (const float*)gout, (__bf16*)d_out);
}
```

```cpp
#include <hip/hip_runtime.h>
#include <hip/hip_bf16.h>
#include <type_traits>

typedef __attribute__((ext_vector_type(8))) __bf16 bf16x8;
typedef __attribute__((ext_vector_type(2))) __bf16 bf16x2;
typedef __attribute__((ext_vector_type(4))) short s16x4;
typedef __attribute__((ext_vector_type(8))) short s16x8;
typedef __attribute__((ext_vector_type(4))) float f32x4;
typedef __attribute__((ext_vector_type(4))) unsigned u32x4;
typedef __attribute__((ext_vector_type(2))) unsigned u32x2;
typedef __attribute__((address_space(3))) s16x4* lds_s16x4_ptr;
typedef __attribute__((address_space(1))) unsigned gu32;
typedef __attribute__((address_space(1))) unsigned long long gu64;

#define K_LO 0

#define NOPK __attribute__((target("no-packed-fp32-ops")))

constexpr int L = 2048, D = 512, NS = 128, F = 1376, HP = 1408;
constexpr int LDS_BYTES = 81920;

__device__ __forceinline__ float bf2f(unsigned short v) { return __uint_as_float((unsigned)v << 16); }
__device__ __forceinline__ float bfr(float x) { return (float)(__bf16)x; }
__device__ __forceinline__ unsigned pk2(float a, float b) {
  bf16x2 v = {(__bf16)a, (__bf16)b};
  return __builtin_bit_cast(unsigned, v);
}
__device__ __forceinline__ float lo_f(unsigned w) { return __uint_as_float(w << 16); }
__device__ __forceinline__ float hi_f(unsigned w) { return __uint_as_float(w & 0xffff0000u); }

__device__ __forceinline__ f32x4 mfma16(bf16x8 a, bf16x8 b, f32x4 c) {
  return __builtin_amdgcn_mfma_f32_16x16x32_bf16(a, b, c, 0, 0, 0);
}
__device__ __forceinline__ bf16x8 tr_frag(const char* lds, int off0, int off1) {
  s16x4 a = __builtin_amdgcn_ds_read_tr16_b64_v4i16((lds_s16x4_ptr)(lds + off0));
  s16x4 b = __builtin_amdgcn_ds_read_tr16_b64_v4i16((lds_s16x4_ptr)(lds + off1));
  s16x8 v = {a[0], a[1], a[2], a[3], b[0], b[1], b[2], b[3]};
  return __builtin_bit_cast(bf16x8, v);
}
__device__ __forceinline__ void glds16(const void* sbase, unsigned voff, unsigned lds_addr) {
  asm volatile("s_mov_b32 m0, %2\n\ts_nop 0\n\tglobal_load_lds_dwordx4 %0, %1" ::"v"(voff), "s"(sbase), "s"(lds_addr)
               : "memory", "m0");
}
__device__ __forceinline__ void glds16v(const void* vptr, unsigned lds_addr) {
  asm volatile("s_mov_b32 m0, %1\n\ts_nop 0\n\tglobal_load_lds_dwordx4 %0, off" ::"v"(vptr), "s"(lds_addr)
               : "memory", "m0");
}
__device__ __forceinline__ unsigned lds_addr_of(const char* p) {
  return (unsigned)(uintptr_t)(const __attribute__((address_space(3))) char*)p;
}
#define WAIT_VM(n) asm volatile("s_waitcnt vmcnt(" #n ")" ::: "memory")
#define WAIT_LGKM0() asm volatile("s_waitcnt lgkmcnt(0)" ::: "memory")
template <int N>
__device__ __forceinline__ void wait_vm() {
  asm volatile("s_waitcnt vmcnt(%0)" ::"i"(N) : "memory");
}

constexpr int F1_B = 32768;

struct F1Ctx {
  const __bf16 *x, *wg, *wu, *wmix;
  unsigned voffA[2], voffB, lbase;
  int w;
  int aoff[2], boff[3];
};
__device__ __forceinline__ void f1_stage_a(const F1Ctx& c, int m) {
  const unsigned ab = c.lbase + (m & 1) * 16384 + c.w * 1024;
  glds16(c.x + m * 64, c.voffA[0], ab);
  glds16(c.x + m * 64, c.voffA[1], ab + 8192);
}
template <bool G2>
__device__ __forceinline__ void f1_stage_b(const F1Ctx& c, int hs) {
  const unsigned bb = c.lbase + F1_B + (hs & 3) * 12288 + c.w * 1024;
  if constexpr (!G2) {
    glds16(c.wg + (size_t)hs * 32 * F, c.voffB, bb);
    glds16(c.wu + (size_t)hs * 32 * F, c.voffB, bb + 8192);
  } else {
    glds16v(c.wmix + (size_t)hs * 32 * F, bb);
  }
}
constexpr int f1_allowed(int hs, int nb) {
  int n = 0;
  if ((hs & 1) == 0 && hs >= 2) {
    if (hs + 1 < 16) n += nb;
    if (hs + 2 < 16) n += nb;
  } else {
    if (hs + 1 < 16) n += nb;
    if (hs >= 1 && ((hs - 1) & 1) == 0 && (hs + 1) / 2 < 8) n += 2;
    if (hs + 2 < 16) n += nb;
  }
  return n;
}
template <int HS, bool G2>
__device__ __forceinline__ void f1_loop(const F1Ctx& c, const char* smem, bf16x8 (&af)[4], bf16x8 (&bq)[3],
                                        f32x4 (&acc)[4][3]) {
  if constexpr (HS < 16) {
    if constexpr (HS > 0) {
#pragma unroll
      for (int j = 0; j < 3; ++j) {
        acc[0][j] = mfma16(bq[j], af[0], acc[0][j]);
        acc[1][j] = mfma16(bq[j], af[1], acc[1][j]);
      }
    }
    __builtin_amdgcn_sched_barrier(0);
    wait_vm<f1_allowed(HS, G2 ? 1 : 2)>();
    WAIT_LGKM0();
    __builtin_amdgcn_s_barrier();
    asm volatile("" ::: "memory");
    const char* ab = smem + ((HS >> 1) & 1) * 16384 + c.aoff[HS & 1];
    const char* bbase = smem + (HS & 3) * 12288;
    __builtin_amdgcn_sched_barrier(0);
    bf16x8 bn[3];
    af[0] = *(const bf16x8*)(ab);
    bn[0] = tr_frag(bbase, c.boff[0], c.boff[0] + 512);
    af[1] = *(const bf16x8*)(ab + 2048);
    bn[1] = tr_frag(bbase, c.boff[1], c.boff[1] + 512);
    bn[2] = tr_frag(bbase, c.boff[2], c.boff[2] + 512);
    __builtin_amdgcn_sched_barrier(0);
#pragma unroll
    for (int j = 0; j < 3; ++j) {
      if constexpr (HS > 0) {
        acc[2][j] = mfma16(bq[j], af[2], acc[2][j]);
        acc[3][j] = mfma16(bq[j], af[3], acc[3][j]);
      }
      __builtin_amdgcn_sched_barrier(0);
      if (j == 0) {
        if constexpr ((HS & 1) == 0 && (HS + 2) / 2 < 8) f1_stage_a(c, (HS + 2) / 2);
      } else if (j == 1) {
        if constexpr (HS + 3 < 16) f1_stage_b<G2>(c, HS + 3);
      }
      __builtin_amdgcn_sched_barrier(0);
    }
    af[2] = *(const bf16x8*)(ab + 4096);
    af[3] = *(const bf16x8*)(ab + 6144);
    f1_loop<HS + 1, G2>(c, smem, af, bn, acc);
  } else {
#pragma unroll
    for (int j = 0; j < 3; ++j)
#pragma unroll
      for (int mt = 0; mt < 4; ++mt) acc[mt][j] = mfma16(bq[j], af[mt], acc[mt][j]);
  }
}

__device__ __forceinline__ void ffn1_block(char* smem, int rb, int cb, const __bf16* __restrict__ x,
                                           const __bf16* __restrict__ wg, const __bf16* __restrict__ wu,
                                           __bf16* __restrict__ h) {
  const int tid = threadIdx.x, lane = tid & 63, w = __builtin_amdgcn_readfirstlane(tid >> 6);
  const int row0 = rb * 128, f0 = cb * 96;
  const int wm = w >> 2, sel = (w >> 1) & 1, wn = w & 1;
  const int r = lane & 15, g = lane >> 4, q = (lane >> 2) & 3, p = lane & 3;
  F1Ctx c;
  c.x = x; c.wg = wg; c.wu = wu; c.w = w; c.lbase = lds_addr_of(smem);
  {
#pragma unroll
    for (int i = 0; i < 2; ++i) {
      const int row = 64 * i + 8 * w + (lane >> 3), ch = (lane & 7) ^ ((row >> 1) & 7);
      c.voffA[i] = (unsigned)(((row0 + row) * D + ch * 8) * 2);
    }
    const int kk = 8 * (w & 3) + (lane >> 3), pos = lane & 7;
    const int fp = ((kk >> 1) & 1) | (((kk >> 3) & 1) << 1);
    const int i = (pos >> 1) ^ fp;
    const int par = (kk + cb) & 1;
    int c0 = f0 + 16 * (i + 2 * par) + 8 * (pos & 1);
    int c1 = f0 + 16 * ((i & 1) + 4 * (1 - par)) + 8 * (pos & 1);
    c0 = c0 > F - 8 ? F - 8 : c0;
    c1 = c1 > F - 8 ? F - 8 : c1;
    c.voffB = (unsigned)((kk * F + c0) * 2);
    c.wmix = (i < 2 ? wg : wu) + kk * F + c1;
  }
#pragma unroll
  for (int ks = 0; ks < 2; ++ks)
    c.aoff[ks] = (64 * wm + r) * 128 + (((4 * ks + g) ^ ((r >> 1) & 7)) << 4);
  {
    const int fp = (q >> 1) | ((g & 1) << 1);
#pragma unroll
    for (int j = 0; j < 3; ++j) {
      const int t = 3 * wn + j, par = (q + cb) & 1, tt = t - 2 * par;
      const bool whole = tt >= 0 && tt < 4;
      const int sub = whole ? 2 * sel : 1, ps = whole ? tt : 2 * sel + (t & 1);
      c.boff[j] = F1_B + sub * 4096 + (8 * g + q) * 128 + ((ps ^ fp) << 5) + 8 * p;
      asm volatile("" : "+v"(c.boff[j]));
    }
  }
  f32x4 acc[4][3];
#pragma unroll
  for (int mt = 0; mt < 4; ++mt)
#pragma unroll
    for (int j = 0; j < 3; ++j) acc[mt][j] = f32x4{0.f, 0.f, 0.f, 0.f};
  bf16x8 af[4], bq[3];

  f1_stage_a(c, 0);
  if (w < 4) {
    f1_stage_b<false>(c, 0);
    f1_stage_b<false>(c, 1);
    f1_stage_b<false>(c, 2);
    f1_loop<0, false>(c, smem, af, bq, acc);
  } else {
    f1_stage_b<true>(c, 0);
    f1_stage_b<true>(c, 1);
    f1_stage_b<true>(c, 2);
    f1_loop<0, true>(c, smem, af, bq, acc);
  }

  __syncthreads();
  constexpr int HT = 49152, HTP = 208;
  auto finish = [&](auto SEL) {
    constexpr int S = decltype(SEL)::value;
#pragma unroll
    for (int m2 = 0; m2 < 2; ++m2)
#pragma unroll
      for (int j = 0; j < 3; ++j) *(f32x4*)(smem + (w * 6 + m2 * 3 + j) * 1024 + lane * 16) = acc[2 * (1 - S) + m2][j];
    __syncthreads();
#pragma unroll
    for (int m2 = 0; m2 < 2; ++m2)
#pragma unroll
      for (int j = 0; j < 3; ++j) {
        const f32x4 o4 = *(const f32x4*)(smem + ((w ^ 2) * 6 + m2 * 3 + j) * 1024 + lane * 16);
        const f32x4 g4 = S ? o4 : acc[m2][j], u4 = S ? acc[2 + m2][j] : o4;
        float hv[4];
#pragma unroll
        for (int e = 0; e < 4; ++e) {
          const float gv = g4[e];
          const float t = gv * __builtin_fmaf(gv * gv, -0.10294324f, -2.3022082f);
          hv[e] = (gv * u4[e]) * __builtin_amdgcn_rcpf(1.f + __builtin_amdgcn_exp2f(t));
        }
        u32x2 o = {pk2(hv[0], hv[1]), pk2(hv[2], hv[3])};
        *(u32x2*)(smem + HT + (64 * wm + 32 * S + 16 * m2 + r) * HTP + (16 * (3 * wn + j) + 4 * g) * 2) = o;
      }
  };
  if (sel) finish(std::integral_constant<int, 1>{});
  else finish(std::integral_constant<int, 0>{});
  __syncthreads();
#pragma unroll
  for (int i = 0; i < 3; ++i) {
    const int idx = tid + 512 * i, row = idx / 12, c8 = idx - row * 12;
    if (f0 + 8 * c8 < F)
      *(u32x4*)(h + (size_t)(row0 + row) * HP + f0 + 8 * c8) = *(const u32x4*)(smem + HT + row * HTP + c8 * 16);
  }
}

__device__ __forceinline__ void gate_block(char* smem, int rb, const __bf16* __restrict__ x,
                                           const __bf16* __restrict__ gw, float* __restrict__ gout,
                                           __bf16* __restrict__ uT) {
  const int tid = threadIdx.x, lane = tid & 63, w = __builtin_amdgcn_readfirstlane(tid >> 6);
  const int g = lane >> 4, q = (lane >> 2) & 3, p = lane & 3;
  const unsigned lbase = lds_addr_of(smem);
  const int grow = tid >> 3, gpart = tid & 7, gf = (grow & 3) | (((grow >> 3) & 1) << 2);
  u32x4 gwv[8];
#pragma unroll
  for (int i = 0; i < 8; ++i) gwv[i] = *(const u32x4*)(gw + ((gpart ^ gf) + 8 * (i >> 1)) * 16 + 8 * (i & 1));
#pragma unroll 1
  for (int pass = 0; pass < 2; ++pass) {
    const int l0 = rb * 128 + pass * 64;
#pragma unroll
    for (int i = 0; i < 8; ++i) {
      const int row = 8 * w + i, fr = (row & 3) | (((row >> 3) & 1) << 2);
      glds16(x + (size_t)(l0 + row) * D, (unsigned)((((lane >> 1) ^ fr) * 2 + (lane & 1)) * 16), lbase + row * 1024);
    }
    WAIT_VM(0);
    __syncthreads();
#pragma unroll
    for (int i = 0; i < 8; ++i) {
      const int unit = w * 8 + i, c16 = unit >> 1, half = unit & 1;
      const int ra = 32 * half + 8 * g + q, rb2 = ra + 4;
      const int fa = (ra & 3) | (((ra >> 3) & 1) << 2);
      const bf16x8 v = tr_frag(smem, ra * 1024 + ((c16 ^ fa) << 5) + 8 * p, rb2 * 1024 + ((c16 ^ fa) << 5) + 8 * p);
      *(bf16x8*)(uT + (size_t)(16 * c16 + (lane & 15)) * L + l0 + 32 * half + 8 * g) = v;
    }
    float s = 0.f;
#pragma unroll
    for (int i = 0; i < 8; ++i) {
      const u32x4 xv = *(const u32x4*)(smem + grow * 1024 + (gpart + 8 * (i >> 1)) * 32 + 16 * (i & 1));
#pragma unroll
      for (int e = 0; e < 4; ++e) {
        const unsigned xe = xv[e], we = gwv[i][e];
        s = __builtin_amdgcn_fdot2_f32_bf16(__builtin_bit_cast(bf16x2, xe), __builtin_bit_cast(bf16x2, we), s, false);
      }
    }
    s += __shfl_xor(s, 1);
    s += __shfl_xor(s, 2);
    s += __shfl_xor(s, 4);
    if (gpart == 0) gout[l0 + grow] = bfr(1.f / (1.f + __expf(-bfr(s))));
    __syncthreads();
  }
}

constexpr int KC_PITCH = 768;
constexpr int KG_PITCH = 160;
constexpr int KG_ARR = 16 * KG_PITCH;
constexpr int KG_TAB = 8 * 2 * KG_ARR;
typedef __attribute__((ext_vector_type(2))) float f32x2;

__device__ __forceinline__ f32x2 cexp(float zr, float zi, float p) {
  const float e = __builtin_amdgcn_exp2f(zr * p);
  const float ph = __builtin_amdgcn_fractf(zi * p);
  return f32x2{e * __builtin_amdgcn_cosf(ph), e * __builtin_amdgcn_sinf(ph)};
}
__device__ __forceinline__ f32x2 cmul(f32x2 a, f32x2 b) {
  return f32x2{a[0] * b[0] - a[1] * b[1], a[0] * b[1] + a[1] * b[0]};
}
__device__ __forceinline__ void kgen_block(char* smem, int d0, const __bf16* __restrict__ lar,
                                           const __bf16* __restrict__ aim, const __bf16* __restrict__ cre,
                                           const __bf16* __restrict__ cim, const __bf16* __restrict__ ldt,
                                           unsigned* __restrict__ kc, unsigned* __restrict__ aG,
                                           const __bf16* __restrict__ wd) {
  const int tid = threadIdx.x, lane = tid & 63, w = __builtin_amdgcn_readfirstlane(tid >> 6);
  unsigned pf_dummy;
  asm volatile("global_load_dword %0, %1, off"
               : "=v"(pf_dummy) : "v"(wd + (size_t)((d0 >> 1) * 43 + (tid < 42 ? tid : 42)) * 64) : "memory");
  {
    u32x4* z = (u32x4*)(aG + (size_t)d0 * L) + tid;
    z[0] = u32x4{0u, 0u, 0u, 0u};
    z[512] = u32x4{0u, 0u, 0u, 0u};
    if (tid == 0) aG[(size_t)D * L + (d0 >> 1)] = 0u;
  }
  const int ch = w >> 2, wq = w & 3, tc = tid & 255;
  const int r = lane & 15, g = lane >> 4, hh = lane >> 5;
  char* wb = smem + w * 2 * KG_ARR;
  char* tb = smem + KG_TAB + ch * (4 * 656 * 4);
  float zr, zi;
  f32x2 cc;
  {
    const int d = d0 + ch, n = 32 * wq + (lane & 31);
    const float dt = bfr(expf((float)ldt[d]));
    const float ar = -bfr(expf((float)lar[d * NS + n]));
    zr = (ar * dt) * 1.4426950408889634f;
    zi = ((float)aim[d * NS + n] * dt) * 0.15915494309189535f;
    cc = f32x2{2.f * ((float)cre[d * NS + n] * dt), 2.f * ((float)cim[d * NS + n] * dt)};
  }
  const float fh = (float)(1 + hh);
  const f32x2 e128 = cexp(zr, zi, 128.f), e24 = cexp(zr, zi, 2.f * fh);
  f32x2 v[4], st[4];
  v[0] = cexp(zr, -zi, 64.f * hh);                 st[0] = cexp(zr, -zi, 8.f);
  v[1] = hh ? cmul(cc, e128) : cc;                 st[1] = cexp(zr, zi, 1.f);
  v[2] = cmul(cc, cexp(zr, zi, 256.f * fh));       st[2] = hh ? e128 : e24;
  v[3] = cmul(cc, cexp(zr, zi, 1024.f * fh));      st[3] = e128;
  const f32x2 v24 = cmul(v[2], hh ? e24 : e128);
  unsigned vh[4][8], vl[4][8];
#pragma unroll
  for (int i = 0; i < 8; ++i)
#pragma unroll
    for (int t = 0; t < 4; ++t) {
      vh[t][i] = pk2(v[t][0], v[t][1]);
      vl[t][i] = pk2(v[t][0] - lo_f(vh[t][i]), v[t][1] - hi_f(vh[t][i]));
      if (t == 2 && i == 3) v[t] = v24;
      else if (i < 7) v[t] = cmul(v[t], st[t]);
    }
  char* wr = wb + (8 * hh) * KG_PITCH + (lane & 31) * 4;
  const char* rd = wb + r * KG_PITCH + g * 16;
  bf16x8 fh_[4][2], fl_[4][2];
#pragma unroll
  for (int t = 0; t < 4; ++t) {
    __builtin_amdgcn_wave_barrier();
#pragma unroll
    for (int i = 0; i < 8; ++i) {
      *(unsigned*)(wr + i * KG_PITCH) = vh[t][i];
      *(unsigned*)(wr + i * KG_PITCH + KG_ARR) = vl[t][i];
    }
    __builtin_amdgcn_wave_barrier();
#pragma unroll
    for (int ss = 0; ss < 2; ++ss) {
      fh_[t][ss] = *(const bf16x8*)(rd + ss * 64);
      fl_[t][ss] = *(const bf16x8*)(rd + ss * 64 + KG_ARR);
    }
  }
  f32x4 acc[3];
#pragma unroll
  for (int m = 0; m < 3; ++m) acc[m] = f32x4{0.f, 0.f, 0.f, 0.f};
#pragma unroll
  for (int ss = 0; ss < 2; ++ss)
#pragma unroll
    for (int m = 0; m < 3; ++m) {
      acc[m] = mfma16(fl_[1 + m][ss], fh_[0][ss], acc[m]);
      acc[m] = mfma16(fh_[1 + m][ss], fl_[0][ss], acc[m]);
      acc[m] = mfma16(fh_[1 + m][ss], fh_[0][ss], acc[m]);
    }
  {
    float* tab = (float*)tb + wq * 656;
    *(f32x4*)(tab + 128 * (g >> 1) + 4 * (g & 1) + 8 * r) = acc[0];
    if (g < 2) {
      *(f32x4*)(tab + 256 + 64 * g + 4 * r) = acc[1];
#pragma unroll
      for (int e = 0; e < 4; ++e) tab[512 + 64 * g + 16 * e + r] = acc[2][e];
    } else {
#pragma unroll
      for (int e = 0; e < 4; ++e) tab[384 + (g & 1) + 32 * e + 2 * r] = acc[1][e];
      if (lane == 32) tab[640] = acc[2][0];
    }
  }
  __syncthreads();
  {
    const float* tab = (const float*)tb;
    unsigned* kd = kc + (size_t)(d0 + ch) * KC_PITCH;
#pragma unroll
    for (int e = 0; e < 3; ++e) {
      const int id = tc + 256 * e;
      if (id < 641) {
        const float v = (tab[id] + tab[656 + id]) + (tab[2 * 656 + id] + tab[3 * 656 + id]);
        const float vh = bfr(v);
        kd[id] = pk2(vh, v - vh);
      }
    }
  }
  asm volatile("s_waitcnt vmcnt(0)" : "+v"(pf_dummy)::"memory");
}

NOPK __global__ __launch_bounds__(512, 4) void k1(const __bf16* __restrict__ u, const __bf16* __restrict__ lar,
                                             const __bf16* __restrict__ aim, const __bf16* __restrict__ cre,
                                             const __bf16* __restrict__ cim, const __bf16* __restrict__ ldt,
                                             const __bf16* __restrict__ wg, const __bf16* __restrict__ wu,
                                             const __bf16* __restrict__ gw, __bf16* __restrict__ h,
                                             __bf16* __restrict__ uT, unsigned* __restrict__ kc,
                                             float* __restrict__ gout, unsigned* __restrict__ aG,
                                             const __bf16* __restrict__ wd) {
  extern __shared__ __attribute__((aligned(16))) char smem[];
  const int b = blockIdx.x;
  const int xcd = b & 7, i = (b & 255) >> 3;
  if (b < 256) {
    const int rb = 4 * (xcd >> 1) + (i & 3), cb = 8 * (xcd & 1) + (i >> 2);
    if (cb < 15) ffn1_block(smem, rb, cb, u, wg, wu, h);
    else gate_block(smem, rb, u, gw, gout, uT);
  } else {
    kgen_block(smem, 64 * xcd + 2 * i, lar, aim, cre, cim, ldt, kc, aG, wd);
  }
}

constexpr unsigned A_TAG = 0x5a17c3e9u;
constexpr int UCP = 4160;
constexpr int KARR = (240 + L) * 2;
constexpr int OFF_K = 4 * UCP;
constexpr int OFF_TAB = OFF_K + 2 * KARR;
constexpr int OFF_RED = UCP;
constexpr int CH_BYTES = OFF_RED + 32768;
static_assert(OFF_TAB + 3072 <= CH_BYTES && 2 * CH_BYTES <= LDS_BYTES, "lds");

struct PosLut {
  unsigned short v[L];
  constexpr PosLut() : v() {
    for (int j = 0; j < L; ++j) {
      const int sh = (j >= 256) + (j >= 512) + (j >= 1024);
      int q = j >> sh;
      if (sh) {
        const int rem = j & ((1 << sh) - 1), half = 1 << (sh - 1);
        if (rem > half || (rem == half && (q & 1))) ++q;
      }
      v[j] = (unsigned short)(4 * (128 * sh + q));
    }
  }
};
__device__ const PosLut g_poslut{};

__device__ __forceinline__ void conv_block(char* smem, int d0, const __bf16* __restrict__ uT,
                                           const unsigned* __restrict__ kc, const __bf16* __restrict__ dsk,
                                           unsigned* __restrict__ aG) {
  const int tid = threadIdx.x, lane = tid & 63, w = __builtin_amdgcn_readfirstlane(tid >> 6);
  const int ch = w >> 2, wq = w & 3, tc = tid & 255;
  const int r = lane & 15, g = lane >> 4, q = (lane >> 2) & 3, p = lane & 3;
  char* cb = smem + ch * CH_BYTES;
  const int d = d0 + ch;
  __builtin_amdgcn_s_setprio(3);
  {
    const unsigned lb = lds_addr_of(cb);
    glds16(uT + (size_t)d * L + wq * 512, lane * 16, lb + 32 + wq * 1024);
    if (wq < 3) glds16(kc + (size_t)d * KC_PITCH + wq * 256, lane * 16, lb + OFF_TAB + wq * 1024);
    const u32x4 z = {0u, 0u, 0u, 0u};
    if (tc < 60) *(u32x4*)(cb + OFF_K + (tc >= 30 ? KARR : 0) + (tc % 30) * 16) = z;
    else if (tc < 68) *(u32x4*)(cb + ((tc - 60) >> 1) * UCP + ((tc - 60) & 1) * 16) = z;
  }
  const float dsv = (float)dsk[d];
  const u32x4 lut = *(const u32x4*)(g_poslut.v + 8 * tc);
  WAIT_VM(0);
  __syncthreads();
  {
    const u32x4 pa = *(const u32x4*)(cb + 16 + 16 * tc), pb = *(const u32x4*)(cb + 32 + 16 * tc);
    const u32x4 c1 = {__builtin_amdgcn_alignbit(pb[0], pa[3], 16), __builtin_amdgcn_alignbit(pb[1], pb[0], 16),
                      __builtin_amdgcn_alignbit(pb[2], pb[1], 16), __builtin_amdgcn_alignbit(pb[3], pb[2], 16)};
    const u32x4 c2 = {pa[3], pb[0], pb[1], pb[2]};
    const u32x4 c3 = {__builtin_amdgcn_alignbit(pa[3], pa[2], 16), c1[0], c1[1], c1[2]};
    *(u32x4*)(cb + 1 * UCP + 32 + 16 * tc) = c1;
    *(u32x4*)(cb + 2 * UCP + 32 + 16 * tc) = c2;
    *(u32x4*)(cb + 3 * UCP + 32 + 16 * tc) = c3;
    u32x4 kh, kl;
#pragma unroll
    for (int e = 0; e < 4; ++e) {
      const unsigned t0 = *(const unsigned*)(cb + OFF_TAB + (lut[e] & 0xffffu));
      const unsigned t1 = *(const unsigned*)(cb + OFF_TAB + (lut[e] >> 16));
      kh[e] = __builtin_amdgcn_perm(t1, t0, 0x05040100u);
      kl[e] = __builtin_amdgcn_perm(t1, t0, 0x07060302u);
    }
    *(u32x4*)(cb + OFF_K + 480 + 16 * tc) = kh;
  }
  __syncthreads();
  __builtin_amdgcn_s_setprio(1);
  f32x4 acc[8];
#pragma unroll
  for (int t = 0; t < 8; ++t) acc[t] = f32x4{0.f, 0.f, 0.f, 0.f};
#pragma unroll
  for (int rr = 0; rr < 2; ++rr) {
    const int rho = 2 * wq + rr;
    const int abase = q * UCP + 2 * (16 + 16 + 32 * rho - 8 * g + 4 * p);
    const int bbase = OFF_K + 2 * (240 - 16 - 32 * rho + 16 * r + 8 * g);
    bf16x8 af[8];
#pragma unroll
    for (int s = 0; s < 8; ++s) af[s] = tr_frag(cb, abase + 512 * s, abase + 512 * s - 8);
#pragma unroll
    for (int t = 0; t < 8; ++t) {
      const bf16x8 bh = *(const bf16x8*)(cb + bbase + 512 * t);
#pragma unroll
      for (int s = 0; s + t < 8; ++s) acc[s + t] = mfma16(af[s], bh, acc[s + t]);
    }
  }
  __builtin_amdgcn_s_setprio(3);
  __syncthreads();
  if (tid == 256) __hip_atomic_store((gu32*)(aG + (size_t)D * L + (d0 >> 1)), 1u, __ATOMIC_RELAXED, __HIP_MEMORY_SCOPE_AGENT);
#pragma unroll
  for (int t = 0; t < 8; ++t)
    if ((t >> 1) != wq) *(f32x4*)(cb + OFF_RED + (wq * 8 + t) * 1024 + lane * 16) = acc[t];
  __syncthreads();
#pragma unroll
  for (int tt = 0; tt < 2; ++tt) {
    const int t = 2 * wq + tt;
    f32x4 own = f32x4{0.f, 0.f, 0.f, 0.f};
#pragma unroll
    for (int t2 = 0; t2 < 8; ++t2)
      if (t2 == t) own = acc[t2];
    f32x4 s = f32x4{0.f, 0.f, 0.f, 0.f};
#pragma unroll
    for (int sw = 0; sw < 4; ++sw) {
      const f32x4 o = *(const f32x4*)(cb + OFF_RED + (sw * 8 + t) * 1024 + lane * 16);
      s += (sw == wq) ? own : o;
    }
    const int l = 256 * t + 16 * r + 4 * g;
    const u32x2 uu = *(const u32x2*)(cb + 32 + 2 * l);
    const float a0 = bfr(s[0]) + bfr(lo_f(uu[0]) * dsv), a1 = bfr(s[1]) + bfr(hi_f(uu[0]) * dsv);
    const float a2 = bfr(s[2]) + bfr(lo_f(uu[1]) * dsv), a3 = bfr(s[3]) + bfr(hi_f(uu[1]) * dsv);
    const u32x4 gr = {pk2(a0, a1), A_TAG, pk2(a2, a3), A_TAG};
    asm volatile("global_store_dwordx4 %0, %1, off sc1\n\ts_nop 1" ::"v"(aG + (size_t)d * L + l), "v"(gr) : "memory");
  }
}

constexpr int F2_NS = 5;
constexpr int F2_STAGE = 16384;
constexpr int F2_NST = 22;

struct F2Ctx {
  const __bf16 *h, *wd;
  unsigned avoff, bvoff, bvoff_last, lbase;
  int w, kh;
  int aoff[2], boff[2];
  const unsigned* asrc;
  mutable u32x4 x0, x1;
};
__device__ __forceinline__ void f2_stage(const F2Ctx& c, int st) {
  const unsigned base = c.lbase + (st % F2_NS) * F2_STAGE;
  glds16(c.h + st * 64, c.avoff, base + c.w * 1024);
  glds16(c.wd + (size_t)st * 64 * D, st == F2_NST - 1 ? c.bvoff_last : c.bvoff, base + 8192 + c.w * 1024);
}
template <int ST>
__device__ __forceinline__ void f2_loop(const F2Ctx& c, const char* smem, bf16x8 (&ao)[2], bf16x8 (&bo)[2],
                                        f32x4 (&acc)[2][2]) {
  if constexpr (ST < F2_NST) {
    constexpr int rem = (F2_NST - 1 - ST) < (F2_NS - 2) ? (F2_NST - 1 - ST) : (F2_NS - 2);
    wait_vm<2 * rem>();
    WAIT_LGKM0();
    __builtin_amdgcn_s_barrier();
    asm volatile("" ::: "memory");
    if constexpr (ST + F2_NS - 1 < F2_NST) f2_stage(c, ST + F2_NS - 1);
    if constexpr (ST == F2_NST - 3)
      asm volatile("global_load_dwordx4 %0, %2, off sc1\n\tglobal_load_dwordx4 %1, %3, off sc1"
                   : "=&v"(c.x0), "=&v"(c.x1) : "v"(c.asrc), "v"(c.asrc + 16 * L) : "memory");
    const char* base = smem + (ST % F2_NS) * F2_STAGE;
    bf16x8 an[2], bn[2];
#pragma unroll
    for (int mt = 0; mt < 2; ++mt) an[mt] = *(const bf16x8*)(base + c.aoff[mt]);
#pragma unroll
    for (int nt = 0; nt < 2; ++nt) bn[nt] = tr_frag(base, c.boff[nt], c.boff[nt] + 512);
    __builtin_amdgcn_sched_barrier(0);
    if constexpr (ST > 0) {
#pragma unroll
      for (int mt = 0; mt < 2; ++mt)
#pragma unroll
        for (int nt = 0; nt < 2; ++nt) acc[mt][nt] = mfma16(ao[mt], bo[nt], acc[mt][nt]);
    }
    __builtin_amdgcn_sched_barrier(0);
    f2_loop<ST + 1>(c, smem, an, bn, acc);
  } else {
    if (c.kh == 0) {
#pragma unroll
      for (int mt = 0; mt < 2; ++mt)
#pragma unroll
        for (int nt = 0; nt < 2; ++nt) acc[mt][nt] = mfma16(ao[mt], bo[nt], acc[mt][nt]);
    }
  }
}

__device__ __forceinline__ void ffn2_block(char* smem, int row0, int cbk, const __bf16* __restrict__ h,
                                           const __bf16* __restrict__ wd, const unsigned* __restrict__ aG,
                                           const float* __restrict__ gin, __bf16* __restrict__ out) {
  const int col0 = 64 * cbk;
  const int tid = threadIdx.x, lane = tid & 63, w = __builtin_amdgcn_readfirstlane(tid >> 6);
  const int r = lane & 15, g = lane >> 4, q = (lane >> 2) & 3, p = lane & 3;
  const int kh = w >> 2, wm = (w >> 1) & 1, wn = w & 1;
  __builtin_amdgcn_s_setprio(1);
  F2Ctx c;
  c.h = h; c.wd = wd; c.w = w; c.kh = kh; c.lbase = lds_addr_of(smem);
  {
    const int row = 8 * w + (lane >> 3), ch = (lane & 7) ^ ((row >> 1) & 7);
    c.avoff = (unsigned)(((row0 + row) * HP + ch * 8) * 2);
    const int bk = 8 * w + (lane >> 3);
    const int pos = lane & 7, fp = ((bk >> 1) & 1) | (((bk >> 3) & 1) << 1);
    const int cc = col0 + 16 * ((pos >> 1) ^ fp) + 8 * (pos & 1);
    c.bvoff = (unsigned)((bk * D + cc) * 2);
    c.bvoff_last = (unsigned)(((bk > 31 ? 31 : bk) * D + cc) * 2);
  }
#pragma unroll
  for (int mt = 0; mt < 2; ++mt) {
    const int row = 32 * wm + 16 * mt + r, ch = 4 * kh + g;
    c.aoff[mt] = row * 128 + ((ch ^ ((row >> 1) & 7)) << 4);
  }
  const int fpl = (q >> 1) | ((g & 1) << 1);
#pragma unroll
  for (int nt = 0; nt < 2; ++nt) c.boff[nt] = 8192 + (32 * kh + 8 * g + q) * 128 + (((2 * wn + nt) ^ fpl) << 5) + 8 * p;

  f32x4 acc[2][2];
#pragma unroll
  for (int mt = 0; mt < 2; ++mt)
#pragma unroll
    for (int nt = 0; nt < 2; ++nt) acc[mt][nt] = f32x4{0.f, 0.f, 0.f, 0.f};

#pragma unroll
  for (int st = 0; st < F2_NS - 1; ++st) f2_stage(c, st);
  const int lr = 32 * wm + 16 * kh + 4 * g;
  const f32x4 gv = *(const f32x4*)(gin + row0 + lr);
  c.asrc = aG + (size_t)(col0 + 32 * wn + r) * L + row0 + lr;
  bf16x8 a0[2], b0[2];
  f2_loop<0>(c, smem, a0, b0, acc);
  WAIT_LGKM0();
  __builtin_amdgcn_s_barrier();
  asm volatile("" ::: "memory");
  asm volatile("s_waitcnt vmcnt(0)" : "+v"(c.x0), "+v"(c.x1)::"memory");
  const bool early = __all(c.x0[1] == A_TAG && c.x0[3] == A_TAG && c.x1[1] == A_TAG && c.x1[3] == A_TAG);
  if (w == 0 && !early) {
    const gu32* fl = (const gu32*)(aG + (size_t)D * L + 32 * cbk + (lane & 31));
    for (int spin = 0; spin < (1 << 22); ++spin) {
      const unsigned v = __hip_atomic_load(fl, __ATOMIC_RELAXED, __HIP_MEMORY_SCOPE_AGENT);
      if (__all(v == 1u)) break;
      __builtin_amdgcn_s_sleep(2);
    }
  }
  {
    char* red = smem + (w & 3) * 4096 + kh * 2048;
    const f32x4 o0 = kh ? acc[0][0] : acc[1][0], o1 = kh ? acc[0][1] : acc[1][1];
    *(f32x4*)(red + lane * 16) = o0;
    *(f32x4*)(red + 1024 + lane * 16) = o1;
  }
  __syncthreads();
  unsigned long long av[2];
  {
    const unsigned* src = c.asrc;
    av[0] = (unsigned long long)c.x0[0] | ((unsigned long long)c.x0[2] << 32);
    av[1] = (unsigned long long)c.x1[0] | ((unsigned long long)c.x1[2] << 32);
    for (int spin = 0; !early && spin < (1 << 22); ++spin) {
      u32x4 x0, x1;
      asm volatile("global_load_dwordx4 %0, %2, off sc1\n\tglobal_load_dwordx4 %1, %3, off sc1\n\ts_waitcnt vmcnt(0)"
                   : "=&v"(x0), "=&v"(x1) : "v"(src), "v"(src + 16 * L) : "memory");
      av[0] = (unsigned long long)x0[0] | ((unsigned long long)x0[2] << 32);
      av[1] = (unsigned long long)x1[0] | ((unsigned long long)x1[2] << 32);
      const bool ok = x0[1] == A_TAG && x0[3] == A_TAG && x1[1] == A_TAG && x1[3] == A_TAG;
      if (__all(ok)) break;
      __builtin_amdgcn_s_sleep(4);
    }
  }
  constexpr int OT = 16384, OTP = 144;
  {
    const char* red = smem + (w & 3) * 4096 + (1 - kh) * 2048;
#pragma unroll
    for (int nt = 0; nt < 2; ++nt) {
      const f32x4 o = *(const f32x4*)(red + nt * 1024 + lane * 16);
      const f32x4 own = kh ? acc[1][nt] : acc[0][nt];
      const f32x4 s = kh ? (o + own) : (own + o);
      const unsigned alo = (unsigned)av[nt], ahi = (unsigned)(av[nt] >> 32);
      const float a[4] = {lo_f(alo), hi_f(alo), lo_f(ahi), hi_f(ahi)};
#pragma unroll
      for (int e = 0; e < 4; ++e) {
        const float o2 = bfr(gv[e] * a[e]) + bfr(bfr(1.f - gv[e]) * bfr(s[e]));
        *(__bf16*)(smem + OT + (lr + e) * OTP + (32 * wn + 16 * nt + r) * 2) = (__bf16)o2;
      }
    }
  }
  __syncthreads();
  {
    const int row = tid >> 3, c8 = tid & 7;
    const u32x4 v = *(const u32x4*)(smem + OT + row * OTP + c8 * 16);
    *(u32x4*)(out + (size_t)(row0 + row) * D + col0 + c8 * 8) = v;
  }
}

NOPK __global__ __launch_bounds__(512, 4) void k2(const __bf16* __restrict__ h, const __bf16* __restrict__ wd,
                                             const __bf16* __restrict__ uT, const unsigned* __restrict__ kc,
                                             const __bf16* __restrict__ dsk, unsigned* __restrict__ aG,
                                             const float* __restrict__ gin, __bf16* __restrict__ out) {
  extern __shared__ __attribute__((aligned(16))) char smem[];
  const int b = blockIdx.x, xcd = b & 7, i = (b & 255) >> 3;
  if (b < 256) {
    const int pair = 32 * xcd + i;
    conv_block(smem, 2 * pair, uT, kc, dsk, aG);
  } else {
    ffn2_block(smem, 64 * (4 * xcd + (i & 3)), i >> 2, h, wd, aG, gin, out);
  }
}

extern "C" void kernel_launch(void* const* d_in, const int* in_sizes, int n_in, void* d_out, int out_size,
                              void* d_ws, size_t ws_size, hipStream_t stream) {
  const __bf16* u = (const __bf16*)d_in[0];
  const __bf16* lar = (const __bf16*)d_in[1];
  const __bf16* aim = (const __bf16*)d_in[2];
  const __bf16* cre = (const __bf16*)d_in[3];
  const __bf16* cim = (const __bf16*)d_in[4];
  const __bf16* ldt = (const __bf16*)d_in[5];
  const __bf16* dsk = (const __bf16*)d_in[6];
  const __bf16* wg = (const __bf16*)d_in[8];
  const __bf16* wu = (const __bf16*)d_in[9];
  const __bf16* wd = (const __bf16*)d_in[10];
  const __bf16* gw = (const __bf16*)d_in[11];
  char* ws = (char*)d_ws;
  __bf16* h = (__bf16*)ws;
  ws += (size_t)L * HP * 2;
  unsigned* aG = (unsigned*)ws;
  ws += (size_t)D * L * 4 + 1024;
  __bf16* uT = (__bf16*)ws;
  ws += (size_t)D * L * 2;
  unsigned* kc = (unsigned*)ws;
  ws += (size_t)D * KC_PITCH * 4;
  float* gout = (float*)ws;
  (void)hipFuncSetAttribute((const void*)k1, hipFuncAttributeMaxDynamicSharedMemorySize, LDS_BYTES);
  (void)hipFuncSetAttribute((const void*)k2, hipFuncAttributeMaxDynamicSharedMemorySize, LDS_BYTES);
  hipLaunchKernelGGL(k1, dim3(512), dim3(512), LDS_BYTES, stream, u, lar, aim, cre, cim, ldt, wg, wu, gw, h, uT, kc,
                     gout, aG, wd);
  hipLaunchKernelGGL(k2, dim3(512), dim3(512), LDS_BYTES, stream, (const __bf16*)h, wd, (const __bf16*)uT,
                     (const unsigned*)kc, dsk, aG, (const float*)gout, (__bf16*)d_out);
}
```

```cpp
#include <hip/hip_runtime.h>
#include <hip/hip_bf16.h>
#include <type_traits>

typedef __attribute__((ext_vector_type(8))) __bf16 bf16x8;
typedef __attribute__((ext_vector_type(2))) __bf16 bf16x2;
typedef __attribute__((ext_vector_type(4))) short s16x4;
typedef __attribute__((ext_vector_type(8))) short s16x8;
typedef __attribute__((ext_vector_type(4))) float f32x4;
typedef __attribute__((ext_vector_type(4))) unsigned u32x4;
typedef __attribute__((ext_vector_type(2))) unsigned u32x2;
typedef __attribute__((address_space(3))) s16x4* lds_s16x4_ptr;
typedef __attribute__((address_space(1))) unsigned gu32;
typedef __attribute__((address_space(1))) unsigned long long gu64;

#define K_LO 0

#define NOPK __attribute__((target("no-packed-fp32-ops")))

constexpr int L = 2048, D = 512, NS = 128, F = 1376, HP = 1408;
constexpr int LDS_BYTES = 81920;

__device__ __forceinline__ float bf2f(unsigned short v) { return __uint_as_float((unsigned)v << 16); }
__device__ __forceinline__ float bfr(float x) { return (float)(__bf16)x; }
__device__ __forceinline__ unsigned pk2(float a, float b) {
  bf16x2 v = {(__bf16)a, (__bf16)b};
  return __builtin_bit_cast(unsigned, v);
}
__device__ __forceinline__ float lo_f(unsigned w) { return __uint_as_float(w << 16); }
__device__ __forceinline__ float hi_f(unsigned w) { return __uint_as_float(w & 0xffff0000u); }

__device__ __forceinline__ f32x4 mfma16(bf16x8 a, bf16x8 b, f32x4 c) {
  return __builtin_amdgcn_mfma_f32_16x16x32_bf16(a, b, c, 0, 0, 0);
}
__device__ __forceinline__ bf16x8 tr_frag(const char* lds, int off0, int off1) {
  s16x4 a = __builtin_amdgcn_ds_read_tr16_b64_v4i16((lds_s16x4_ptr)(lds + off0));
  s16x4 b = __builtin_amdgcn_ds_read_tr16_b64_v4i16((lds_s16x4_ptr)(lds + off1));
  s16x8 v = {a[0], a[1], a[2], a[3], b[0], b[1], b[2], b[3]};
  return __builtin_bit_cast(bf16x8, v);
}
__device__ __forceinline__ void glds16(const void* sbase, unsigned voff, unsigned lds_addr) {
  asm volatile("s_mov_b32 m0, %2\n\ts_nop 0\n\tglobal_load_lds_dwordx4 %0, %1" ::"v"(voff), "s"(sbase), "s"(lds_addr)
               : "memory", "m0");
}
__device__ __forceinline__ void glds16v(const void* vptr, unsigned lds_addr) {
  asm volatile("s_mov_b32 m0, %1\n\ts_nop 0\n\tglobal_load_lds_dwordx4 %0, off" ::"v"(vptr), "s"(lds_addr)
               : "memory", "m0");
}
__device__ __forceinline__ unsigned lds_addr_of(const char* p) {
  return (unsigned)(uintptr_t)(const __attribute__((address_space(3))) char*)p;
}
#define WAIT_VM(n) asm volatile("s_waitcnt vmcnt(" #n ")" ::: "memory")
#define WAIT_LGKM0() asm volatile("s_waitcnt lgkmcnt(0)" ::: "memory")
template <int N>
__device__ __forceinline__ void wait_vm() {
  asm volatile("s_waitcnt vmcnt(%0)" ::"i"(N) : "memory");
}

constexpr int F1_B = 32768;

struct F1Ctx {
  const __bf16 *x, *wg, *wu, *wmix;
  unsigned voffA[2], voffB, lbase;
  int w;
  int aoff[2], boff[3];
};
__device__ __forceinline__ void f1_stage_a(const F1Ctx& c, int m) {
  const unsigned ab = c.lbase + (m & 1) * 16384 + c.w * 1024;
  glds16(c.x + m * 64, c.voffA[0], ab);
  glds16(c.x + m * 64, c.voffA[1], ab + 8192);
}
template <bool G2>
__device__ __forceinline__ void f1_stage_b(const F1Ctx& c, int hs) {
  const unsigned bb = c.lbase + F1_B + (hs & 3) * 12288 + c.w * 1024;
  if constexpr (!G2) {
    glds16(c.wg + (size_t)hs * 32 * F, c.voffB, bb);
    glds16(c.wu + (size_t)hs * 32 * F, c.voffB, bb + 8192);
  } else {
    glds16v(c.wmix + (size_t)hs * 32 * F, bb);
  }
}
constexpr int f1_allowed(int hs, int nb) {
  int n = 0;
  if ((hs & 1) == 0 && hs >= 2) {
    if (hs + 1 < 16) n += nb;
    if (hs + 2 < 16) n += nb;
  } else {
    if (hs + 1 < 16) n += nb;
    if (hs >= 1 && ((hs - 1) & 1) == 0 && (hs + 1) / 2 < 8) n += 2;
    if (hs + 2 < 16) n += nb;
  }
  return n;
}
template <int HS, bool G2>
__device__ __forceinline__ void f1_loop(const F1Ctx& c, const char* smem, bf16x8 (&af)[4], bf16x8 (&bq)[3],
                                        f32x4 (&acc)[4][3]) {
  if constexpr (HS < 16) {
    if constexpr (HS > 0) {
#pragma unroll
      for (int j = 0; j < 3; ++j) {
        acc[0][j] = mfma16(bq[j], af[0], acc[0][j]);
        acc[1][j] = mfma16(bq[j], af[1], acc[1][j]);
      }
    }
    __builtin_amdgcn_sched_barrier(0);
    wait_vm<f1_allowed(HS, G2 ? 1 : 2)>();
    WAIT_LGKM0();
    __builtin_amdgcn_s_barrier();
    asm volatile("" ::: "memory");
    const char* ab = smem + ((HS >> 1) & 1) * 16384 + c.aoff[HS & 1];
    const char* bbase = smem + (HS & 3) * 12288;
    __builtin_amdgcn_sched_barrier(0);
    bf16x8 bn[3];
    af[0] = *(const bf16x8*)(ab);
    bn[0] = tr_frag(bbase, c.boff[0], c.boff[0] + 512);
    af[1] = *(const bf16x8*)(ab + 2048);
    bn[1] = tr_frag(bbase, c.boff[1], c.boff[1] + 512);
    bn[2] = tr_frag(bbase, c.boff[2], c.boff[2] + 512);
    __builtin_amdgcn_sched_barrier(0);
#pragma unroll
    for (int j = 0; j < 3; ++j) {
      if constexpr (HS > 0) {
        acc[2][j] = mfma16(bq[j], af[2], acc[2][j]);
        acc[3][j] = mfma16(bq[j], af[3], acc[3][j]);
      }
      __builtin_amdgcn_sched_barrier(0);
      if (j == 0) {
        if constexpr ((HS & 1) == 0 && (HS + 2) / 2 < 8) f1_stage_a(c, (HS + 2) / 2);
      } else if (j == 1) {
        if constexpr (HS + 3 < 16) f1_stage_b<G2>(c, HS + 3);
      }
      __builtin_amdgcn_sched_barrier(0);
    }
    af[2] = *(const bf16x8*)(ab + 4096);
    af[3] = *(const bf16x8*)(ab + 6144);
    f1_loop<HS + 1, G2>(c, smem, af, bn, acc);
  } else {
#pragma unroll
    for (int j = 0; j < 3; ++j)
#pragma unroll
      for (int mt = 0; mt < 4; ++mt) acc[mt][j] = mfma16(bq[j], af[mt], acc[mt][j]);
  }
}

__device__ __forceinline__ void ffn1_block(char* smem, int rb, int cb, const __bf16* __restrict__ x,
                                           const __bf16* __restrict__ wg, const __bf16* __restrict__ wu,
                                           __bf16* __restrict__ h) {
  const int tid = threadIdx.x, lane = tid & 63, w = __builtin_amdgcn_readfirstlane(tid >> 6);
  const int row0 = rb * 128, f0 = cb * 96;
  const int wm = w >> 2, sel = (w >> 1) & 1, wn = w & 1;
  const int r = lane & 15, g = lane >> 4, q = (lane >> 2) & 3, p = lane & 3;
  F1Ctx c;
  c.x = x; c.wg = wg; c.wu = wu; c.w = w; c.lbase = lds_addr_of(smem);
  {
#pragma unroll
    for (int i = 0; i < 2; ++i) {
      const int row = 64 * i + 8 * w + (lane >> 3), ch = (lane & 7) ^ ((row >> 1) & 7);
      c.voffA[i] = (unsigned)(((row0 + row) * D + ch * 8) * 2);
    }
    const int kk = 8 * (w & 3) + (lane >> 3), pos = lane & 7;
    const int fp = ((kk >> 1) & 1) | (((kk >> 3) & 1) << 1);
    const int i = (pos >> 1) ^ fp;
    const int par = (kk + cb) & 1;
    int c0 = f0 + 16 * (i + 2 * par) + 8 * (pos & 1);
    int c1 = f0 + 16 * ((i & 1) + 4 * (1 - par)) + 8 * (pos & 1);
    c0 = c0 > F - 8 ? F - 8 : c0;
    c1 = c1 > F - 8 ? F - 8 : c1;
    c.voffB = (unsigned)((kk * F + c0) * 2);
    c.wmix = (i < 2 ? wg : wu) + kk * F + c1;
  }
#pragma unroll
  for (int ks = 0; ks < 2; ++ks)
    c.aoff[ks] = (64 * wm + r) * 128 + (((4 * ks + g) ^ ((r >> 1) & 7)) << 4);
  {
    const int fp = (q >> 1) | ((g & 1) << 1);
#pragma unroll
    for (int j = 0; j < 3; ++j) {
      const int t = 3 * wn + j, par = (q + cb) & 1, tt = t - 2 * par;
      const bool whole = tt >= 0 && tt < 4;
      const int sub = whole ? 2 * sel : 1, ps = whole ? tt : 2 * sel + (t & 1);
      c.boff[j] = F1_B + sub * 4096 + (8 * g + q) * 128 + ((ps ^ fp) << 5) + 8 * p;
      asm volatile("" : "+v"(c.boff[j]));
    }
  }
  f32x4 acc[4][3];
#pragma unroll
  for (int mt = 0; mt < 4; ++mt)
#pragma unroll
    for (int j = 0; j < 3; ++j) acc[mt][j] = f32x4{0.f, 0.f, 0.f, 0.f};
  bf16x8 af[4], bq[3];

  f1_stage_a(c, 0);
  if (w < 4) {
    f1_stage_b<false>(c, 0);
    f1_stage_b<false>(c, 1);
    f1_stage_b<false>(c, 2);
    f1_loop<0, false>(c, smem, af, bq, acc);
  } else {
    f1_stage_b<true>(c, 0);
    f1_stage_b<true>(c, 1);
    f1_stage_b<true>(c, 2);
    f1_loop<0, true>(c, smem, af, bq, acc);
  }

  __syncthreads();
  constexpr int HT = 49152, HTP = 208;
  auto finish = [&](auto SEL) {
    constexpr int S = decltype(SEL)::value;
#pragma unroll
    for (int m2 = 0; m2 < 2; ++m2)
#pragma unroll
      for (int j = 0; j < 3; ++j) *(f32x4*)(smem + (w * 6 + m2 * 3 + j) * 1024 + lane * 16) = acc[2 * (1 - S) + m2][j];
    __syncthreads();
#pragma unroll
    for (int m2 = 0; m2 < 2; ++m2)
#pragma unroll
      for (int j = 0; j < 3; ++j) {
        const f32x4 o4 = *(const f32x4*)(smem + ((w ^ 2) * 6 + m2 * 3 + j) * 1024 + lane * 16);
        const f32x4 g4 = S ? o4 : acc[m2][j], u4 = S ? acc[2 + m2][j] : o4;
        float hv[4];
#pragma unroll
        for (int e = 0; e < 4; ++e) {
          const float gv = g4[e];
          const float t = gv * __builtin_fmaf(gv * gv, -0.10294324f, -2.3022082f);
          hv[e] = (gv * u4[e]) * __builtin_amdgcn_rcpf(1.f + __builtin_amdgcn_exp2f(t));
        }
        u32x2 o = {pk2(hv[0], hv[1]), pk2(hv[2], hv[3])};
        *(u32x2*)(smem + HT + (64 * wm + 32 * S + 16 * m2 + r) * HTP + (16 * (3 * wn + j) + 4 * g) * 2) = o;
      }
  };
  if (sel) finish(std::integral_constant<int, 1>{});
  else finish(std::integral_constant<int, 0>{});
  __syncthreads();
#pragma unroll
  for (int i = 0; i < 3; ++i) {
    const int idx = tid + 512 * i, row = idx / 12, c8 = idx - row * 12;
    if (f0 + 8 * c8 < F)
      *(u32x4*)(h + (size_t)(row0 + row) * HP + f0 + 8 * c8) = *(const u32x4*)(smem + HT + row * HTP + c8 * 16);
  }
}

__device__ __forceinline__ void gate_block(char* smem, int rb, const __bf16* __restrict__ x,
                                           const __bf16* __restrict__ gw, float* __restrict__ gout,
                                           __bf16* __restrict__ uT) {
  const int tid = threadIdx.x, lane = tid & 63, w = __builtin_amdgcn_readfirstlane(tid >> 6);
  const int g = lane >> 4, q = (lane >> 2) & 3, p = lane & 3;
  const unsigned lbase = lds_addr_of(smem);
  const int grow = tid >> 3, gpart = tid & 7, gf = (grow & 3) | (((grow >> 3) & 1) << 2);
  u32x4 gwv[8];
#pragma unroll
  for (int i = 0; i < 8; ++i) gwv[i] = *(const u32x4*)(gw + ((gpart ^ gf) + 8 * (i >> 1)) * 16 + 8 * (i & 1));
#pragma unroll 1
  for (int pass = 0; pass < 2; ++pass) {
    const int l0 = rb * 128 + pass * 64;
#pragma unroll
    for (int i = 0; i < 8; ++i) {
      const int row = 8 * w + i, fr = (row & 3) | (((row >> 3) & 1) << 2);
      glds16(x + (size_t)(l0 + row) * D, (unsigned)((((lane >> 1) ^ fr) * 2 + (lane & 1)) * 16), lbase + row * 1024);
    }
    WAIT_VM(0);
    __syncthreads();
#pragma unroll
    for (int i = 0; i < 8; ++i) {
      const int unit = w * 8 + i, c16 = unit >> 1, half = unit & 1;
      const int ra = 32 * half + 8 * g + q, rb2 = ra + 4;
      const int fa = (ra & 3) | (((ra >> 3) & 1) << 2);
      const bf16x8 v = tr_frag(smem, ra * 1024 + ((c16 ^ fa) << 5) + 8 * p, rb2 * 1024 + ((c16 ^ fa) << 5) + 8 * p);
      *(bf16x8*)(uT + (size_t)(16 * c16 + (lane & 15)) * L + l0 + 32 * half + 8 * g) = v;
    }
    float s = 0.f;
#pragma unroll
    for (int i = 0; i < 8; ++i) {
      const u32x4 xv = *(const u32x4*)(smem + grow * 1024 + (gpart + 8 * (i >> 1)) * 32 + 16 * (i & 1));
#pragma unroll
      for (int e = 0; e < 4; ++e) {
        const unsigned xe = xv[e], we = gwv[i][e];
        s = __builtin_amdgcn_fdot2_f32_bf16(__builtin_bit_cast(bf16x2, xe), __builtin_bit_cast(bf16x2, we), s, false);
      }
    }
    s += __shfl_xor(s, 1);
    s += __shfl_xor(s, 2);
    s += __shfl_xor(s, 4);
    if (gpart == 0) gout[l0 + grow] = bfr(1.f / (1.f + __expf(-bfr(s))));
    __syncthreads();
  }
}

constexpr int KC_PITCH = 768;
constexpr int KG_PITCH = 160;
constexpr int KG_ARR = 16 * KG_PITCH;
constexpr int KG_TAB = 8 * 2 * KG_ARR;
typedef __attribute__((ext_vector_type(2))) float f32x2;

__device__ __forceinline__ f32x2 cexp(float zr, float zi, float p) {
  const float e = __builtin_amdgcn_exp2f(zr * p);
  const float ph = __builtin_amdgcn_fractf(zi * p);
  return f32x2{e * __builtin_amdgcn_cosf(ph), e * __builtin_amdgcn_sinf(ph)};
}
__device__ __forceinline__ f32x2 cmul(f32x2 a, f32x2 b) {
  return f32x2{a[0] * b[0] - a[1] * b[1], a[0] * b[1] + a[1] * b[0]};
}
__device__ __forceinline__ void kgen_block(char* smem, int d0, const __bf16* __restrict__ lar,
                                           const __bf16* __restrict__ aim, const __bf16* __restrict__ cre,
                                           const __bf16* __restrict__ cim, const __bf16* __restrict__ ldt,
                                           unsigned* __restrict__ kc, unsigned* __restrict__ aG,
                                           const __bf16* __restrict__ wd, const __bf16* __restrict__ x,
                                           const __bf16* __restrict__ wg, const __bf16* __restrict__ wu) {
  const int tid = threadIdx.x, lane = tid & 63, w = __builtin_amdgcn_readfirstlane(tid >> 6);
  unsigned pf_dummy;
  asm volatile("global_load_dword %0, %1, off"
               : "=v"(pf_dummy) : "v"(wd + (size_t)((d0 >> 1) * 43 + (tid < 42 ? tid : 42)) * 64) : "memory");
  {
    u32x4* z = (u32x4*)(aG + (size_t)d0 * L) + tid;
    z[0] = u32x4{0u, 0u, 0u, 0u};
    z[512] = u32x4{0u, 0u, 0u, 0u};
    if (tid == 0) aG[(size_t)D * L + (d0 >> 1)] = 0u;
  }
  const int ch = w >> 2, wq = w & 3, tc = tid & 255;
  const int r = lane & 15, g = lane >> 4, hh = lane >> 5;
  char* wb = smem + w * 2 * KG_ARR;
  char* tb = smem + KG_TAB + ch * (4 * 656 * 4);
  float zr, zi;
  f32x2 cc;
  {
    const int d = d0 + ch, n = 32 * wq + (lane & 31);
    const float dt = bfr(expf((float)ldt[d]));
    const float ar = -bfr(expf((float)lar[d * NS + n]));
    zr = (ar * dt) * 1.4426950408889634f;
    zi = ((float)aim[d * NS + n] * dt) * 0.15915494309189535f;
    cc = f32x2{2.f * ((float)cre[d * NS + n] * dt), 2.f * ((float)cim[d * NS + n] * dt)};
  }
  unsigned pf2;
  {
    asm volatile("" : "+v"(zr), "+v"(zi), "+v"(cc)::"memory");
    const int idx = (d0 >> 1) * 150 + (tid < 149 ? tid : 149);
    const __bf16* src = idx < 16384 ? x + (size_t)idx * 64
                                    : (idx < 27392 ? wg + (size_t)(idx - 16384) * 64 : wu + (size_t)(idx - 27392) * 64);
    asm volatile("global_load_dword %0, %1, off" : "=v"(pf2) : "v"(src) : "memory");
  }
  const float fh = (float)(1 + hh);
  const f32x2 e128 = cexp(zr, zi, 128.f), e24 = cexp(zr, zi, 2.f * fh);
  f32x2 v[4], st[4];
  v[0] = cexp(zr, -zi, 64.f * hh);                 st[0] = cexp(zr, -zi, 8.f);
  v[1] = hh ? cmul(cc, e128) : cc;                 st[1] = cexp(zr, zi, 1.f);
  v[2] = cmul(cc, cexp(zr, zi, 256.f * fh));       st[2] = hh ? e128 : e24;
  v[3] = cmul(cc, cexp(zr, zi, 1024.f * fh));      st[3] = e128;
  const f32x2 v24 = cmul(v[2], hh ? e24 : e128);
  unsigned vh[4][8], vl[4][8];
#pragma unroll
  for (int i = 0; i < 8; ++i)
#pragma unroll
    for (int t = 0; t < 4; ++t) {
      vh[t][i] = pk2(v[t][0], v[t][1]);
      vl[t][i] = pk2(v[t][0] - lo_f(vh[t][i]), v[t][1] - hi_f(vh[t][i]));
      if (t == 2 && i == 3) v[t] = v24;
      else if (i < 7) v[t] = cmul(v[t], st[t]);
    }
  char* wr = wb + (8 * hh) * KG_PITCH + (lane & 31) * 4;
  const char* rd = wb + r * KG_PITCH + g * 16;
  bf16x8 fh_[4][2], fl_[4][2];
#pragma unroll
  for (int t = 0; t < 4; ++t) {
    __builtin_amdgcn_wave_barrier();
#pragma unroll
    for (int i = 0; i < 8; ++i) {
      *(unsigned*)(wr + i * KG_PITCH) = vh[t][i];
      *(unsigned*)(wr + i * KG_PITCH + KG_ARR) = vl[t][i];
    }
    __builtin_amdgcn_wave_barrier();
#pragma unroll
    for (int ss = 0; ss < 2; ++ss) {
      fh_[t][ss] = *(const bf16x8*)(rd + ss * 64);
      fl_[t][ss] = *(const bf16x8*)(rd + ss * 64 + KG_ARR);
    }
  }
  f32x4 acc[3];
#pragma unroll
  for (int m = 0; m < 3; ++m) acc[m] = f32x4{0.f, 0.f, 0.f, 0.f};
#pragma unroll
  for (int ss = 0; ss < 2; ++ss)
#pragma unroll
    for (int m = 0; m < 3; ++m) {
      acc[m] = mfma16(fl_[1 + m][ss], fh_[0][ss], acc[m]);
      acc[m] = mfma16(fh_[1 + m][ss], fl_[0][ss], acc[m]);
      acc[m] = mfma16(fh_[1 + m][ss], fh_[0][ss], acc[m]);
    }
  {
    float* tab = (float*)tb + wq * 656;
    *(f32x4*)(tab + 128 * (g >> 1) + 4 * (g & 1) + 8 * r) = acc[0];
    if (g < 2) {
      *(f32x4*)(tab + 256 + 64 * g + 4 * r) = acc[1];
#pragma unroll
      for (int e = 0; e < 4; ++e) tab[512 + 64 * g + 16 * e + r] = acc[2][e];
    } else {
#pragma unroll
      for (int e = 0; e < 4; ++e) tab[384 + (g & 1) + 32 * e + 2 * r] = acc[1][e];
      if (lane == 32) tab[640] = acc[2][0];
    }
  }
  __syncthreads();
  {
    const float* tab = (const float*)tb;
    unsigned* kd = kc + (size_t)(d0 + ch) * KC_PITCH;
#pragma unroll
    for (int e = 0; e < 3; ++e) {
      const int id = tc + 256 * e;
      if (id < 641) {
        const float v = (tab[id] + tab[656 + id]) + (tab[2 * 656 + id] + tab[3 * 656 + id]);
        const float vh = bfr(v);
        kd[id] = pk2(vh, v - vh);
      }
    }
  }
  asm volatile("s_waitcnt vmcnt(0)" : "+v"(pf_dummy), "+v"(pf2)::"memory");
}

NOPK __global__ __launch_bounds__(512, 4) void k1(const __bf16* __restrict__ u, const __bf16* __restrict__ lar,
                                             const __bf16* __restrict__ aim, const __bf16* __restrict__ cre,
                                             const __bf16* __restrict__ cim, const __bf16* __restrict__ ldt,
                                             const __bf16* __restrict__ wg, const __bf16* __restrict__ wu,
                                             const __bf16* __restrict__ gw, __bf16* __restrict__ h,
                                             __bf16* __restrict__ uT, unsigned* __restrict__ kc,
                                             float* __restrict__ gout, unsigned* __restrict__ aG,
                                             const __bf16* __restrict__ wd) {
  extern __shared__ __attribute__((aligned(16))) char smem[];
  const int b = blockIdx.x;
  const int xcd = b & 7, i = (b & 255) >> 3;
  if (b < 256) {
    const int rb = 4 * (xcd >> 1) + (i & 3), cb = 8 * (xcd & 1) + (i >> 2);
    if (cb < 15) ffn1_block(smem, rb, cb, u, wg, wu, h);
    else gate_block(smem, rb, u, gw, gout, uT);
  } else {
    kgen_block(smem, 64 * xcd + 2 * i, lar, aim, cre, cim, ldt, kc, aG, wd, u, wg, wu);
  }
}

constexpr unsigned A_TAG = 0x5a17c3e9u;
constexpr int UCP = 4160;
constexpr int KARR = (240 + L) * 2;
constexpr int OFF_K = 4 * UCP;
constexpr int OFF_TAB = OFF_K + 2 * KARR;
constexpr int OFF_RED = UCP;
constexpr int CH_BYTES = OFF_RED + 32768;
static_assert(OFF_TAB + 3072 <= CH_BYTES && 2 * CH_BYTES <= LDS_BYTES, "lds");

struct PosLut {
  unsigned short v[L];
  constexpr PosLut() : v() {
    for (int j = 0; j < L; ++j) {
      const int sh = (j >= 256) + (j >= 512) + (j >= 1024);
      int q = j >> sh;
      if (sh) {
        const int rem = j & ((1 << sh) - 1), half = 1 << (sh - 1);
        if (rem > half || (rem == half && (q & 1))) ++q;
      }
      v[j] = (unsigned short)(4 * (128 * sh + q));
    }
  }
};
__device__ const PosLut g_poslut{};

__device__ __forceinline__ void conv_block(char* smem, int d0, const __bf16* __restrict__ uT,
                                           const unsigned* __restrict__ kc, const __bf16* __restrict__ dsk,
                                           unsigned* __restrict__ aG) {
  const int tid = threadIdx.x, lane = tid & 63, w = __builtin_amdgcn_readfirstlane(tid >> 6);
  const int ch = w >> 2, wq = w & 3, tc = tid & 255;
  const int r = lane & 15, g = lane >> 4, q = (lane >> 2) & 3, p = lane & 3;
  char* cb = smem + ch * CH_BYTES;
  const int d = d0 + ch;
  __builtin_amdgcn_s_setprio(3);
  {
    const unsigned lb = lds_addr_of(cb);
    glds16(uT + (size_t)d * L + wq * 512, lane * 16, lb + 32 + wq * 1024);
    if (wq < 3) glds16(kc + (size_t)d * KC_PITCH + wq * 256, lane * 16, lb + OFF_TAB + wq * 1024);
    const u32x4 z = {0u, 0u, 0u, 0u};
    if (tc < 60) *(u32x4*)(cb + OFF_K + (tc >= 30 ? KARR : 0) + (tc % 30) * 16) = z;
    else if (tc < 68) *(u32x4*)(cb + ((tc - 60) >> 1) * UCP + ((tc - 60) & 1) * 16) = z;
  }
  const float dsv = (float)dsk[d];
  const u32x4 lut = *(const u32x4*)(g_poslut.v + 8 * tc);
  WAIT_VM(0);
  __syncthreads();
  {
    const u32x4 pa = *(const u32x4*)(cb + 16 + 16 * tc), pb = *(const u32x4*)(cb + 32 + 16 * tc);
    const u32x4 c1 = {__builtin_amdgcn_alignbit(pb[0], pa[3], 16), __builtin_amdgcn_alignbit(pb[1], pb[0], 16),
                      __builtin_amdgcn_alignbit(pb[2], pb[1], 16), __builtin_amdgcn_alignbit(pb[3], pb[2], 16)};
    const u32x4 c2 = {pa[3], pb[0], pb[1], pb[2]};
    const u32x4 c3 = {__builtin_amdgcn_alignbit(pa[3], pa[2], 16), c1[0], c1[1], c1[2]};
    *(u32x4*)(cb + 1 * UCP + 32 + 16 * tc) = c1;
    *(u32x4*)(cb + 2 * UCP + 32 + 16 * tc) = c2;
    *(u32x4*)(cb + 3 * UCP + 32 + 16 * tc) = c3;
    u32x4 kh, kl;
#pragma unroll
    for (int e = 0; e < 4; ++e) {
      const unsigned t0 = *(const unsigned*)(cb + OFF_TAB + (lut[e] & 0xffffu));
      const unsigned t1 = *(const unsigned*)(cb + OFF_TAB + (lut[e] >> 16));
      kh[e] = __builtin_amdgcn_perm(t1, t0, 0x05040100u);
      kl[e] = __builtin_amdgcn_perm(t1, t0, 0x07060302u);
    }
    *(u32x4*)(cb + OFF_K + 480 + 16 * tc) = kh;
  }
  __syncthreads();
  __builtin_amdgcn_s_setprio(1);
  f32x4 acc[8];
#pragma unroll
  for (int t = 0; t < 8; ++t) acc[t] = f32x4{0.f, 0.f, 0.f, 0.f};
#pragma unroll
  for (int rr = 0; rr < 2; ++rr) {
    const int rho = 2 * wq + rr;
    const int abase = q * UCP + 2 * (16 + 16 + 32 * rho - 8 * g + 4 * p);
    const int bbase = OFF_K + 2 * (240 - 16 - 32 * rho + 16 * r + 8 * g);
    bf16x8 af[8];
#pragma unroll
    for (int s = 0; s < 8; ++s) af[s] = tr_frag(cb, abase + 512 * s, abase + 512 * s - 8);
#pragma unroll
    for (int t = 0; t < 8; ++t) {
      const bf16x8 bh = *(const bf16x8*)(cb + bbase + 512 * t);
#pragma unroll
      for (int s = 0; s + t < 8; ++s) acc[s + t] = mfma16(af[s], bh, acc[s + t]);
    }
  }
  __builtin_amdgcn_s_setprio(3);
  __syncthreads();
  if (tid == 256) __hip_atomic_store((gu32*)(aG + (size_t)D * L + (d0 >> 1)), 1u, __ATOMIC_RELAXED, __HIP_MEMORY_SCOPE_AGENT);
#pragma unroll
  for (int t = 0; t < 8; ++t)
    if ((t >> 1) != wq) *(f32x4*)(cb + OFF_RED + (wq * 8 + t) * 1024 + lane * 16) = acc[t];
  __syncthreads();
#pragma unroll
  for (int tt = 0; tt < 2; ++tt) {
    const int t = 2 * wq + tt;
    f32x4 own = f32x4{0.f, 0.f, 0.f, 0.f};
#pragma unroll
    for (int t2 = 0; t2 < 8; ++t2)
      if (t2 == t) own = acc[t2];
    f32x4 s = f32x4{0.f, 0.f, 0.f, 0.f};
#pragma unroll
    for (int sw = 0; sw < 4; ++sw) {
      const f32x4 o = *(const f32x4*)(cb + OFF_RED + (sw * 8 + t) * 1024 + lane * 16);
      s += (sw == wq) ? own : o;
    }
    const int l = 256 * t + 16 * r + 4 * g;
    const u32x2 uu = *(const u32x2*)(cb + 32 + 2 * l);
    const float a0 = bfr(s[0]) + bfr(lo_f(uu[0]) * dsv), a1 = bfr(s[1]) + bfr(hi_f(uu[0]) * dsv);
    const float a2 = bfr(s[2]) + bfr(lo_f(uu[1]) * dsv), a3 = bfr(s[3]) + bfr(hi_f(uu[1]) * dsv);
    const u32x4 gr = {pk2(a0, a1), A_TAG, pk2(a2, a3), A_TAG};
    asm volatile("global_store_dwordx4 %0, %1, off sc1\n\ts_nop 1" ::"v"(aG + (size_t)d * L + l), "v"(gr) : "memory");
  }
}

constexpr int F2_NS = 5;
constexpr int F2_STAGE = 16384;
constexpr int F2_NST = 22;

struct F2Ctx {
  const __bf16 *h, *wd;
  unsigned avoff, bvoff, bvoff_last, lbase;
  int w, kh;
  int aoff[2], boff[2];
  const unsigned* asrc;
  mutable u32x4 x0, x1;
};
__device__ __forceinline__ void f2_stage(const F2Ctx& c, int st) {
  const unsigned base = c.lbase + (st % F2_NS) * F2_STAGE;
  glds16(c.h + st * 64, c.avoff, base + c.w * 1024);
  glds16(c.wd + (size_t)st * 64 * D, st == F2_NST - 1 ? c.bvoff_last : c.bvoff, base + 8192 + c.w * 1024);
}
template <int ST>
__device__ __forceinline__ void f2_loop(const F2Ctx& c, const char* smem, bf16x8 (&ao)[2], bf16x8 (&bo)[2],
                                        f32x4 (&acc)[2][2]) {
  if constexpr (ST < F2_NST) {
    constexpr int rem = (F2_NST - 1 - ST) < (F2_NS - 2) ? (F2_NST - 1 - ST) : (F2_NS - 2);
    wait_vm<2 * rem>();
    WAIT_LGKM0();
    __builtin_amdgcn_s_barrier();
    asm volatile("" ::: "memory");
    if constexpr (ST + F2_NS - 1 < F2_NST) f2_stage(c, ST + F2_NS - 1);
    if constexpr (ST == F2_NST - 3)
      asm volatile("global_load_dwordx4 %0, %2, off sc1\n\tglobal_load_dwordx4 %1, %3, off sc1"
                   : "=&v"(c.x0), "=&v"(c.x1) : "v"(c.asrc), "v"(c.asrc + 16 * L) : "memory");
    const char* base = smem + (ST % F2_NS) * F2_STAGE;
    bf16x8 an[2], bn[2];
#pragma unroll
    for (int mt = 0; mt < 2; ++mt) an[mt] = *(const bf16x8*)(base + c.aoff[mt]);
#pragma unroll
    for (int nt = 0; nt < 2; ++nt) bn[nt] = tr_frag(base, c.boff[nt], c.boff[nt] + 512);
    __builtin_amdgcn_sched_barrier(0);
    if constexpr (ST > 0) {
#pragma unroll
      for (int mt = 0; mt < 2; ++mt)
#pragma unroll
        for (int nt = 0; nt < 2; ++nt) acc[mt][nt] = mfma16(ao[mt], bo[nt], acc[mt][nt]);
    }
    __builtin_amdgcn_sched_barrier(0);
    f2_loop<ST + 1>(c, smem, an, bn, acc);
  } else {
    if (c.kh == 0) {
#pragma unroll
      for (int mt = 0; mt < 2; ++mt)
#pragma unroll
        for (int nt = 0; nt < 2; ++nt) acc[mt][nt] = mfma16(ao[mt], bo[nt], acc[mt][nt]);
    }
  }
}

__device__ __forceinline__ void ffn2_block(char* smem, int row0, int cbk, const __bf16* __restrict__ h,
                                           const __bf16* __restrict__ wd, const unsigned* __restrict__ aG,
                                           const float* __restrict__ gin, __bf16* __restrict__ out) {
  const int col0 = 64 * cbk;
  const int tid = threadIdx.x, lane = tid & 63, w = __builtin_amdgcn_readfirstlane(tid >> 6);
  const int r = lane & 15, g = lane >> 4, q = (lane >> 2) & 3, p = lane & 3;
  const int kh = w >> 2, wm = (w >> 1) & 1, wn = w & 1;
  __builtin_amdgcn_s_setprio(1);
  F2Ctx c;
  c.h = h; c.wd = wd; c.w = w; c.kh = kh; c.lbase = lds_addr_of(smem);
  {
    const int row = 8 * w + (lane >> 3), ch = (lane & 7) ^ ((row >> 1) & 7);
    c.avoff = (unsigned)(((row0 + row) * HP + ch * 8) * 2);
    const int bk = 8 * w + (lane >> 3);
    const int pos = lane & 7, fp = ((bk >> 1) & 1) | (((bk >> 3) & 1) << 1);
    const int cc = col0 + 16 * ((pos >> 1) ^ fp) + 8 * (pos & 1);
    c.bvoff = (unsigned)((bk * D + cc) * 2);
    c.bvoff_last = (unsigned)(((bk > 31 ? 31 : bk) * D + cc) * 2);
  }
#pragma unroll
  for (int mt = 0; mt < 2; ++mt) {
    const int row = 32 * wm + 16 * mt + r, ch = 4 * kh + g;
    c.aoff[mt] = row * 128 + ((ch ^ ((row >> 1) & 7)) << 4);
  }
  const int fpl = (q >> 1) | ((g & 1) << 1);
#pragma unroll
  for (int nt = 0; nt < 2; ++nt) c.boff[nt] = 8192 + (32 * kh + 8 * g + q) * 128 + (((2 * wn + nt) ^ fpl) << 5) + 8 * p;

  f32x4 acc[2][2];
#pragma unroll
  for (int mt = 0; mt < 2; ++mt)
#pragma unroll
    for (int nt = 0; nt < 2; ++nt) acc[mt][nt] = f32x4{0.f, 0.f, 0.f, 0.f};

#pragma unroll
  for (int st = 0; st < F2_NS - 1; ++st) f2_stage(c, st);
  const int lr = 32 * wm + 16 * kh + 4 * g;
  const f32x4 gv = *(const f32x4*)(gin + row0 + lr);
  c.asrc = aG + (size_t)(col0 + 32 * wn + r) * L + row0 + lr;
  bf16x8 a0[2], b0[2];
  f2_loop<0>(c, smem, a0, b0, acc);
  WAIT_LGKM0();
  __builtin_amdgcn_s_barrier();
  asm volatile("" ::: "memory");
  asm volatile("s_waitcnt vmcnt(0)" : "+v"(c.x0), "+v"(c.x1)::"memory");
  const bool early = __all(c.x0[1] == A_TAG && c.x0[3] == A_TAG && c.x1[1] == A_TAG && c.x1[3] == A_TAG);
  if (w == 0 && !early) {
    const gu32* fl = (const gu32*)(aG + (size_t)D * L + 32 * cbk + (lane & 31));
    for (int spin = 0; spin < (1 << 22); ++spin) {
      const unsigned v = __hip_atomic_load(fl, __ATOMIC_RELAXED, __HIP_MEMORY_SCOPE_AGENT);
      if (__all(v == 1u)) break;
      __builtin_amdgcn_s_sleep(2);
    }
  }
  {
    char* red = smem + (w & 3) * 4096 + kh * 2048;
    const f32x4 o0 = kh ? acc[0][0] : acc[1][0], o1 = kh ? acc[0][1] : acc[1][1];
    *(f32x4*)(red + lane * 16) = o0;
    *(f32x4*)(red + 1024 + lane * 16) = o1;
  }
  __syncthreads();
  unsigned long long av[2];
  {
    const unsigned* src = c.asrc;
    av[0] = (unsigned long long)c.x0[0] | ((unsigned long long)c.x0[2] << 32);
    av[1] = (unsigned long long)c.x1[0] | ((unsigned long long)c.x1[2] << 32);
    for (int spin = 0; !early && spin < (1 << 22); ++spin) {
      u32x4 x0, x1;
      asm volatile("global_load_dwordx4 %0, %2, off sc1\n\tglobal_load_dwordx4 %1, %3, off sc1\n\ts_waitcnt vmcnt(0)"
                   : "=&v"(x0), "=&v"(x1) : "v"(src), "v"(src + 16 * L) : "memory");
      av[0] = (unsigned long long)x0[0] | ((unsigned long long)x0[2] << 32);
      av[1] = (unsigned long long)x1[0] | ((unsigned long long)x1[2] << 32);
      const bool ok = x0[1] == A_TAG && x0[3] == A_TAG && x1[1] == A_TAG && x1[3] == A_TAG;
      if (__all(ok)) break;
      __builtin_amdgcn_s_sleep(4);
    }
  }
  constexpr int OT = 16384, OTP = 144;
  {
    const char* red = smem + (w & 3) * 4096 + (1 - kh) * 2048;
#pragma unroll
    for (int nt = 0; nt < 2; ++nt) {
      const f32x4 o = *(const f32x4*)(red + nt * 1024 + lane * 16);
      const f32x4 own = kh ? acc[1][nt] : acc[0][nt];
      const f32x4 s = kh ? (o + own) : (own + o);
      const unsigned alo = (unsigned)av[nt], ahi = (unsigned)(av[nt] >> 32);
      const float a[4] = {lo_f(alo), hi_f(alo), lo_f(ahi), hi_f(ahi)};
#pragma unroll
      for (int e = 0; e < 4; ++e) {
        const float o2 = bfr(gv[e] * a[e]) + bfr(bfr(1.f - gv[e]) * bfr(s[e]));
        *(__bf16*)(smem + OT + (lr + e) * OTP + (32 * wn + 16 * nt + r) * 2) = (__bf16)o2;
      }
    }
  }
  __syncthreads();
  {
    const int row = tid >> 3, c8 = tid & 7;
    const u32x4 v = *(const u32x4*)(smem + OT + row * OTP + c8 * 16);
    *(u32x4*)(out + (size_t)(row0 + row) * D + col0 + c8 * 8) = v;
  }
}

NOPK __global__ __launch_bounds__(512, 4) void k2(const __bf16* __restrict__ h, const __bf16* __restrict__ wd,
                                             const __bf16* __restrict__ uT, const unsigned* __restrict__ kc,
                                             const __bf16* __restrict__ dsk, unsigned* __restrict__ aG,
                                             const float* __restrict__ gin, __bf16* __restrict__ out) {
  extern __shared__ __attribute__((aligned(16))) char smem[];
  const int b = blockIdx.x, xcd = b & 7, i = (b & 255) >> 3;
  if (b < 256) {
    const int pair = 32 * xcd + i;
    conv_block(smem, 2 * pair, uT, kc, dsk, aG);
  } else {
    ffn2_block(smem, 64 * (4 * xcd + (i & 3)), i >> 2, h, wd, aG, gin, out);
  }
}

extern "C" void kernel_launch(void* const* d_in, const int* in_sizes, int n_in, void* d_out, int out_size,
                              void* d_ws, size_t ws_size, hipStream_t stream) {
  const __bf16* u = (const __bf16*)d_in[0];
  const __bf16* lar = (const __bf16*)d_in[1];
  const __bf16* aim = (const __bf16*)d_in[2];
  const __bf16* cre = (const __bf16*)d_in[3];
  const __bf16* cim = (const __bf16*)d_in[4];
  const __bf16* ldt = (const __bf16*)d_in[5];
  const __bf16* dsk = (const __bf16*)d_in[6];
  const __bf16* wg = (const __bf16*)d_in[8];
  const __bf16* wu = (const __bf16*)d_in[9];
  const __bf16* wd = (const __bf16*)d_in[10];
  const __bf16* gw = (const __bf16*)d_in[11];
  char* ws = (char*)d_ws;
  __bf16* h = (__bf16*)ws;
  ws += (size_t)L * HP * 2;
  unsigned* aG = (unsigned*)ws;
  ws += (size_t)D * L * 4 + 1024;
  __bf16* uT = (__bf16*)ws;
  ws += (size_t)D * L * 2;
  unsigned* kc = (unsigned*)ws;
  ws += (size_t)D * KC_PITCH * 4;
  float* gout = (float*)ws;
  (void)hipFuncSetAttribute((const void*)k1, hipFuncAttributeMaxDynamicSharedMemorySize, LDS_BYTES);
  (void)hipFuncSetAttribute((const void*)k2, hipFuncAttributeMaxDynamicSharedMemorySize, LDS_BYTES);
  hipLaunchKernelGGL(k1, dim3(512), dim3(512), LDS_BYTES, stream, u, lar, aim, cre, cim, ldt, wg, wu, gw, h, uT, kc,
                     gout, aG, wd);
  hipLaunchKernelGGL(k2, dim3(512), dim3(512), LDS_BYTES, stream, (const __bf16*)h, wd, (const __bf16*)uT,
                     (const unsigned*)kc, dsk, aG, (const float*)gout, (__bf16*)d_out);
}
```

```cpp
#include <hip/hip_runtime.h>
#include <hip/hip_bf16.h>
#include <type_traits>

typedef __attribute__((ext_vector_type(8))) __bf16 bf16x8;
typedef __attribute__((ext_vector_type(2))) __bf16 bf16x2;
typedef __attribute__((ext_vector_type(4))) short s16x4;
typedef __attribute__((ext_vector_type(8))) short s16x8;
typedef __attribute__((ext_vector_type(4))) float f32x4;
typedef __attribute__((ext_vector_type(4))) unsigned u32x4;
typedef __attribute__((ext_vector_type(2))) unsigned u32x2;
typedef __attribute__((address_space(3))) s16x4* lds_s16x4_ptr;
typedef __attribute__((address_space(1))) unsigned gu32;
typedef __attribute__((address_space(1))) unsigned long long gu64;

#define K_LO 0

#define NOPK __attribute__((target("no-packed-fp32-ops")))

constexpr int L = 2048, D = 512, NS = 128, F = 1376, HP = 1408;
constexpr int LDS_BYTES = 81920;

__device__ __forceinline__ float bf2f(unsigned short v) { return __uint_as_float((unsigned)v << 16); }
__device__ __forceinline__ float bfr(float x) { return (float)(__bf16)x; }
__device__ __forceinline__ unsigned pk2(float a, float b) {
  bf16x2 v = {(__bf16)a, (__bf16)b};
  return __builtin_bit_cast(unsigned, v);
}
__device__ __forceinline__ float lo_f(unsigned w) { return __uint_as_float(w << 16); }
__device__ __forceinline__ float hi_f(unsigned w) { return __uint_as_float(w & 0xffff0000u); }

__device__ __forceinline__ f32x4 mfma16(bf16x8 a, bf16x8 b, f32x4 c) {
  return __builtin_amdgcn_mfma_f32_16x16x32_bf16(a, b, c, 0, 0, 0);
}
__device__ __forceinline__ bf16x8 tr_frag(const char* lds, int off0, int off1) {
  s16x4 a = __builtin_amdgcn_ds_read_tr16_b64_v4i16((lds_s16x4_ptr)(lds + off0));
  s16x4 b = __builtin_amdgcn_ds_read_tr16_b64_v4i16((lds_s16x4_ptr)(lds + off1));
  s16x8 v = {a[0], a[1], a[2], a[3], b[0], b[1], b[2], b[3]};
  return __builtin_bit_cast(bf16x8, v);
}
__device__ __forceinline__ void glds16(const void* sbase, unsigned voff, unsigned lds_addr) {
  asm volatile("s_mov_b32 m0, %2\n\ts_nop 0\n\tglobal_load_lds_dwordx4 %0, %1" ::"v"(voff), "s"(sbase), "s"(lds_addr)
               : "memory", "m0");
}
__device__ __forceinline__ void glds16v(const void* vptr, unsigned lds_addr) {
  asm volatile("s_mov_b32 m0, %1\n\ts_nop 0\n\tglobal_load_lds_dwordx4 %0, off" ::"v"(vptr), "s"(lds_addr)
               : "memory", "m0");
}
__device__ __forceinline__ unsigned lds_addr_of(const char* p) {
  return (unsigned)(uintptr_t)(const __attribute__((address_space(3))) char*)p;
}
#define WAIT_VM(n) asm volatile("s_waitcnt vmcnt(" #n ")" ::: "memory")
#define WAIT_LGKM0() asm volatile("s_waitcnt lgkmcnt(0)" ::: "memory")
template <int N>
__device__ __forceinline__ void wait_vm() {
  asm volatile("s_waitcnt vmcnt(%0)" ::"i"(N) : "memory");
}

constexpr int F1_B = 32768;

struct F1Ctx {
  const __bf16 *x, *wg, *wu, *wmix;
  unsigned voffA[2], voffB, lbase;
  int w;
  int aoff[2], boff[3];
};
__device__ __forceinline__ void f1_stage_a(const F1Ctx& c, int m) {
  const unsigned ab = c.lbase + (m & 1) * 16384 + c.w * 1024;
  glds16(c.x + m * 64, c.voffA[0], ab);
  glds16(c.x + m * 64, c.voffA[1], ab + 8192);
}
template <bool G2>
__device__ __forceinline__ void f1_stage_b(const F1Ctx& c, int hs) {
  const unsigned bb = c.lbase + F1_B + (hs & 3) * 12288 + c.w * 1024;
  if constexpr (!G2) {
    glds16(c.wg + (size_t)hs * 32 * F, c.voffB, bb);
    glds16(c.wu + (size_t)hs * 32 * F, c.voffB, bb + 8192);
  } else {
    glds16v(c.wmix + (size_t)hs * 32 * F, bb);
  }
}
constexpr int f1_allowed(int hs, int nb) {
  int n = 0;
  if ((hs & 1) == 0 && hs >= 2) {
    if (hs + 1 < 16) n += nb;
    if (hs + 2 < 16) n += nb;
  } else {
    if (hs + 1 < 16) n += nb;
    if (hs >= 1 && ((hs - 1) & 1) == 0 && (hs + 1) / 2 < 8) n += 2;
    if (hs + 2 < 16) n += nb;
  }
  return n;
}
template <int HS, bool G2>
__device__ __forceinline__ void f1_loop(const F1Ctx& c, const char* smem, bf16x8 (&af)[4], bf16x8 (&bq)[3],
                                        f32x4 (&acc)[4][3]) {
  if constexpr (HS < 16) {
    if constexpr (HS > 0) {
#pragma unroll
      for (int j = 0; j < 3; ++j) {
        acc[0][j] = mfma16(bq[j], af[0], acc[0][j]);
        acc[1][j] = mfma16(bq[j], af[1], acc[1][j]);
      }
    }
    __builtin_amdgcn_sched_barrier(0);
    wait_vm<f1_allowed(HS, G2 ? 1 : 2)>();
    WAIT_LGKM0();
    __builtin_amdgcn_s_barrier();
    asm volatile("" ::: "memory");
    const char* ab = smem + ((HS >> 1) & 1) * 16384 + c.aoff[HS & 1];
    const char* bbase = smem + (HS & 3) * 12288;
    __builtin_amdgcn_sched_barrier(0);
    bf16x8 bn[3];
    af[0] = *(const bf16x8*)(ab);
    bn[0] = tr_frag(bbase, c.boff[0], c.boff[0] + 512);
    af[1] = *(const bf16x8*)(ab + 2048);
    bn[1] = tr_frag(bbase, c.boff[1], c.boff[1] + 512);
    bn[2] = tr_frag(bbase, c.boff[2], c.boff[2] + 512);
    __builtin_amdgcn_sched_barrier(0);
#pragma unroll
    for (int j = 0; j < 3; ++j) {
      if constexpr (HS > 0) {
        acc[2][j] = mfma16(bq[j], af[2], acc[2][j]);
        acc[3][j] = mfma16(bq[j], af[3], acc[3][j]);
      }
      __builtin_amdgcn_sched_barrier(0);
      if (j == 0) {
        if constexpr ((HS & 1) == 0 && (HS + 2) / 2 < 8) f1_stage_a(c, (HS + 2) / 2);
      } else if (j == 1) {
        if constexpr (HS + 3 < 16) f1_stage_b<G2>(c, HS + 3);
      }
      __builtin_amdgcn_sched_barrier(0);
    }
    af[2] = *(const bf16x8*)(ab + 4096);
    af[3] = *(const bf16x8*)(ab + 6144);
    f1_loop<HS + 1, G2>(c, smem, af, bn, acc);
  } else {
#pragma unroll
    for (int j = 0; j < 3; ++j)
#pragma unroll
      for (int mt = 0; mt < 4; ++mt) acc[mt][j] = mfma16(bq[j], af[mt], acc[mt][j]);
  }
}

__device__ __forceinline__ void ffn1_block(char* smem, int rb, int cb, const __bf16* __restrict__ x,
                                           const __bf16* __restrict__ wg, const __bf16* __restrict__ wu,
                                           __bf16* __restrict__ h) {
  const int tid = threadIdx.x, lane = tid & 63, w = __builtin_amdgcn_readfirstlane(tid >> 6);
  const int row0 = rb * 128, f0 = cb * 96;
  const int wm = w >> 2, sel = (w >> 1) & 1, wn = w & 1;
  const int r = lane & 15, g = lane >> 4, q = (lane >> 2) & 3, p = lane & 3;
  F1Ctx c;
  c.x = x; c.wg = wg; c.wu = wu; c.w = w; c.lbase = lds_addr_of(smem);
  {
#pragma unroll
    for (int i = 0; i < 2; ++i) {
      const int row = 64 * i + 8 * w + (lane >> 3), ch = (lane & 7) ^ ((row >> 1) & 7);
      c.voffA[i] = (unsigned)(((row0 + row) * D + ch * 8) * 2);
    }
    const int kk = 8 * (w & 3) + (lane >> 3), pos = lane & 7;
    const int fp = ((kk >> 1) & 1) | (((kk >> 3) & 1) << 1);
    const int i = (pos >> 1) ^ fp;
    const int par = (kk + cb) & 1;
    int c0 = f0 + 16 * (i + 2 * par) + 8 * (pos & 1);
    int c1 = f0 + 16 * ((i & 1) + 4 * (1 - par)) + 8 * (pos & 1);
    c0 = c0 > F - 8 ? F - 8 : c0;
    c1 = c1 > F - 8 ? F - 8 : c1;
    c.voffB = (unsigned)((kk * F + c0) * 2);
    c.wmix = (i < 2 ? wg : wu) + kk * F + c1;
  }
#pragma unroll
  for (int ks = 0; ks < 2; ++ks)
    c.aoff[ks] = (64 * wm + r) * 128 + (((4 * ks + g) ^ ((r >> 1) & 7)) << 4);
  {
    const int fp = (q >> 1) | ((g & 1) << 1);
#pragma unroll
    for (int j = 0; j < 3; ++j) {
      const int t = 3 * wn + j, par = (q + cb) & 1, tt = t - 2 * par;
      const bool whole = tt >= 0 && tt < 4;
      const int sub = whole ? 2 * sel : 1, ps = whole ? tt : 2 * sel + (t & 1);
      c.boff[j] = F1_B + sub * 4096 + (8 * g + q) * 128 + ((ps ^ fp) << 5) + 8 * p;
      asm volatile("" : "+v"(c.boff[j]));
    }
  }
  f32x4 acc[4][3];
#pragma unroll
  for (int mt = 0; mt < 4; ++mt)
#pragma unroll
    for (int j = 0; j < 3; ++j) acc[mt][j] = f32x4{0.f, 0.f, 0.f, 0.f};
  bf16x8 af[4], bq[3];

  f1_stage_a(c, 0);
  if (w < 4) {
    f1_stage_b<false>(c, 0);
    f1_stage_b<false>(c, 1);
    f1_stage_b<false>(c, 2);
    f1_loop<0, false>(c, smem, af, bq, acc);
  } else {
    f1_stage_b<true>(c, 0);
    f1_stage_b<true>(c, 1);
    f1_stage_b<true>(c, 2);
    f1_loop<0, true>(c, smem, af, bq, acc);
  }

  __syncthreads();
  constexpr int HT = 49152, HTP = 208;
  auto finish = [&](auto SEL) {
    constexpr int S = decltype(SEL)::value;
#pragma unroll
    for (int m2 = 0; m2 < 2; ++m2)
#pragma unroll
      for (int j = 0; j < 3; ++j) *(f32x4*)(smem + (w * 6 + m2 * 3 + j) * 1024 + lane * 16) = acc[2 * (1 - S) + m2][j];
    __syncthreads();
#pragma unroll
    for (int m2 = 0; m2 < 2; ++m2)
#pragma unroll
      for (int j = 0; j < 3; ++j) {
        const f32x4 o4 = *(const f32x4*)(smem + ((w ^ 2) * 6 + m2 * 3 + j) * 1024 + lane * 16);
        const f32x4 g4 = S ? o4 : acc[m2][j], u4 = S ? acc[2 + m2][j] : o4;
        float hv[4];
#pragma unroll
        for (int e = 0; e < 4; ++e) {
          const float gv = g4[e];
          const float t = gv * __builtin_fmaf(gv * gv, -0.10294324f, -2.3022082f);
          hv[e] = (gv * u4[e]) * __builtin_amdgcn_rcpf(1.f + __builtin_amdgcn_exp2f(t));
        }
        u32x2 o = {pk2(hv[0], hv[1]), pk2(hv[2], hv[3])};
        *(u32x2*)(smem + HT + (64 * wm + 32 * S + 16 * m2 + r) * HTP + (16 * (3 * wn + j) + 4 * g) * 2) = o;
      }
  };
  if (sel) finish(std::integral_constant<int, 1>{});
  else finish(std::integral_constant<int, 0>{});
  __syncthreads();
#pragma unroll
  for (int i = 0; i < 3; ++i) {
    const int idx = tid + 512 * i, row = idx / 12, c8 = idx - row * 12;
    if (f0 + 8 * c8 < F)
      *(u32x4*)(h + (size_t)(row0 + row) * HP + f0 + 8 * c8) = *(const u32x4*)(smem + HT + row * HTP + c8 * 16);
  }
}

__device__ __forceinline__ void gate_block(char* smem, int rb, const __bf16* __restrict__ x,
                                           const __bf16* __restrict__ gw, float* __restrict__ gout,
                                           __bf16* __restrict__ uT) {
  const int tid = threadIdx.x, lane = tid & 63, w = __builtin_amdgcn_readfirstlane(tid >> 6);
  const int g = lane >> 4, q = (lane >> 2) & 3, p = lane & 3;
  const unsigned lbase = lds_addr_of(smem);
  const int grow = tid >> 3, gpart = tid & 7, gf = (grow & 3) | (((grow >> 3) & 1) << 2);
  u32x4 gwv[8];
#pragma unroll
  for (int i = 0; i < 8; ++i) gwv[i] = *(const u32x4*)(gw + ((gpart ^ gf) + 8 * (i >> 1)) * 16 + 8 * (i & 1));
#pragma unroll 1
  for (int pass = 0; pass < 2; ++pass) {
    const int l0 = rb * 128 + pass * 64;
#pragma unroll
    for (int i = 0; i < 8; ++i) {
      const int row = 8 * w + i, fr = (row & 3) | (((row >> 3) & 1) << 2);
      glds16(x + (size_t)(l0 + row) * D, (unsigned)((((lane >> 1) ^ fr) * 2 + (lane & 1)) * 16), lbase + row * 1024);
    }
    WAIT_VM(0);
    __syncthreads();
#pragma unroll
    for (int i = 0; i < 8; ++i) {
      const int unit = w * 8 + i, c16 = unit >> 1, half = unit & 1;
      const int ra = 32 * half + 8 * g + q, rb2 = ra + 4;
      const int fa = (ra & 3) | (((ra >> 3) & 1) << 2);
      const bf16x8 v = tr_frag(smem, ra * 1024 + ((c16 ^ fa) << 5) + 8 * p, rb2 * 1024 + ((c16 ^ fa) << 5) + 8 * p);
      *(bf16x8*)(uT + (size_t)(16 * c16 + (lane & 15)) * L + l0 + 32 * half + 8 * g) = v;
    }
    float s = 0.f;
#pragma unroll
    for (int i = 0; i < 8; ++i) {
      const u32x4 xv = *(const u32x4*)(smem + grow * 1024 + (gpart + 8 * (i >> 1)) * 32 + 16 * (i & 1));
#pragma unroll
      for (int e = 0; e < 4; ++e) {
        const unsigned xe = xv[e], we = gwv[i][e];
        s = __builtin_amdgcn_fdot2_f32_bf16(__builtin_bit_cast(bf16x2, xe), __builtin_bit_cast(bf16x2, we), s, false);
      }
    }
    s += __shfl_xor(s, 1);
    s += __shfl_xor(s, 2);
    s += __shfl_xor(s, 4);
    if (gpart == 0) gout[l0 + grow] = bfr(1.f / (1.f + __expf(-bfr(s))));
    __syncthreads();
  }
}

constexpr int KC_PITCH = 768;
constexpr int KG_PITCH = 160;
constexpr int KG_ARR = 16 * KG_PITCH;
constexpr int KG_TAB = 8 * 2 * KG_ARR;
typedef __attribute__((ext_vector_type(2))) float f32x2;

__device__ __forceinline__ f32x2 cexp(float zr, float zi, float p) {
  const float e = __builtin_amdgcn_exp2f(zr * p);
  const float ph = __builtin_amdgcn_fractf(zi * p);
  return f32x2{e * __builtin_amdgcn_cosf(ph), e * __builtin_amdgcn_sinf(ph)};
}
__device__ __forceinline__ f32x2 cmul(f32x2 a, f32x2 b) {
  return f32x2{a[0] * b[0] - a[1] * b[1], a[0] * b[1] + a[1] * b[0]};
}
__device__ __forceinline__ void kgen_block(char* smem, int d0, const __bf16* __restrict__ lar,
                                           const __bf16* __restrict__ aim, const __bf16* __restrict__ cre,
                                           const __bf16* __restrict__ cim, const __bf16* __restrict__ ldt,
                                           unsigned* __restrict__ kc, unsigned* __restrict__ aG,
                                           const __bf16* __restrict__ wd, const __bf16* __restrict__ x,
                                           const __bf16* __restrict__ wg, const __bf16* __restrict__ wu) {
  const int tid = threadIdx.x, lane = tid & 63, w = __builtin_amdgcn_readfirstlane(tid >> 6);
  const int ch = w >> 2, wq = w & 3, tc = tid & 255;
  const int r = lane & 15, g = lane >> 4, hh = lane >> 5;
  char* wb = smem + w * 2 * KG_ARR;
  char* tb = smem + KG_TAB + ch * (4 * 656 * 4);
  float zr, zi;
  f32x2 cc;
  unsigned pf2, pf_dummy;
  {
    const int d = d0 + ch, n = 32 * wq + (lane & 31);
    const int idx = (d0 >> 1) * 150 + (tid < 149 ? tid : 149);
    const __bf16* src = idx < 16384 ? x + (size_t)idx * 64
                                    : (idx < 27392 ? wg + (size_t)(idx - 16384) * 64 : wu + (size_t)(idx - 27392) * 64);
    unsigned p0, p1, p2, p3, p4;
    asm volatile(
        "global_load_ushort %0, %7, off\n\tglobal_load_ushort %1, %8, off\n\tglobal_load_ushort %2, %9, off\n\t"
        "global_load_ushort %3, %10, off\n\tglobal_load_ushort %4, %11, off\n\t"
        "global_load_dword %5, %12, off\n\tglobal_load_dword %6, %13, off\n\ts_waitcnt vmcnt(2)"
        : "=&v"(p0), "=&v"(p1), "=&v"(p2), "=&v"(p3), "=&v"(p4), "=&v"(pf2), "=&v"(pf_dummy)
        : "v"(ldt + d), "v"(lar + d * NS + n), "v"(aim + d * NS + n), "v"(cre + d * NS + n), "v"(cim + d * NS + n),
          "v"(src), "v"(wd + (size_t)((d0 >> 1) * 43 + (tid < 42 ? tid : 42)) * 64)
        : "memory");
    const float dt = bfr(expf(lo_f(p0)));
    const float ar = -bfr(expf(lo_f(p1)));
    zr = (ar * dt) * 1.4426950408889634f;
    zi = (lo_f(p2) * dt) * 0.15915494309189535f;
    cc = f32x2{2.f * (lo_f(p3) * dt), 2.f * (lo_f(p4) * dt)};
  }
  {
    u32x4* z = (u32x4*)(aG + (size_t)d0 * L) + tid;
    z[0] = u32x4{0u, 0u, 0u, 0u};
    z[512] = u32x4{0u, 0u, 0u, 0u};
    if (tid == 0) aG[(size_t)D * L + (d0 >> 1)] = 0u;
  }
  const float fh = (float)(1 + hh);
  const f32x2 e128 = cexp(zr, zi, 128.f), e24 = cexp(zr, zi, 2.f * fh);
  f32x2 v[4], st[4];
  v[0] = cexp(zr, -zi, 64.f * hh);                 st[0] = cexp(zr, -zi, 8.f);
  v[1] = hh ? cmul(cc, e128) : cc;                 st[1] = cexp(zr, zi, 1.f);
  v[2] = cmul(cc, cexp(zr, zi, 256.f * fh));       st[2] = hh ? e128 : e24;
  v[3] = cmul(cc, cexp(zr, zi, 1024.f * fh));      st[3] = e128;
  const f32x2 v24 = cmul(v[2], hh ? e24 : e128);
  unsigned vh[4][8], vl[4][8];
#pragma unroll
  for (int i = 0; i < 8; ++i)
#pragma unroll
    for (int t = 0; t < 4; ++t) {
      vh[t][i] = pk2(v[t][0], v[t][1]);
      vl[t][i] = pk2(v[t][0] - lo_f(vh[t][i]), v[t][1] - hi_f(vh[t][i]));
      if (t == 2 && i == 3) v[t] = v24;
      else if (i < 7) v[t] = cmul(v[t], st[t]);
    }
  char* wr = wb + (8 * hh) * KG_PITCH + (lane & 31) * 4;
  const char* rd = wb + r * KG_PITCH + g * 16;
  bf16x8 fh_[4][2], fl_[4][2];
#pragma unroll
  for (int t = 0; t < 4; ++t) {
    __builtin_amdgcn_wave_barrier();
#pragma unroll
    for (int i = 0; i < 8; ++i) {
      *(unsigned*)(wr + i * KG_PITCH) = vh[t][i];
      *(unsigned*)(wr + i * KG_PITCH + KG_ARR) = vl[t][i];
    }
    __builtin_amdgcn_wave_barrier();
#pragma unroll
    for (int ss = 0; ss < 2; ++ss) {
      fh_[t][ss] = *(const bf16x8*)(rd + ss * 64);
      fl_[t][ss] = *(const bf16x8*)(rd + ss * 64 + KG_ARR);
    }
  }
  f32x4 acc[3];
#pragma unroll
  for (int m = 0; m < 3; ++m) acc[m] = f32x4{0.f, 0.f, 0.f, 0.f};
#pragma unroll
  for (int ss = 0; ss < 2; ++ss)
#pragma unroll
    for (int m = 0; m < 3; ++m) {
      acc[m] = mfma16(fl_[1 + m][ss], fh_[0][ss], acc[m]);
      acc[m] = mfma16(fh_[1 + m][ss], fl_[0][ss], acc[m]);
      acc[m] = mfma16(fh_[1 + m][ss], fh_[0][ss], acc[m]);
    }
  {
    float* tab = (float*)tb + wq * 656;
    *(f32x4*)(tab + 128 * (g >> 1) + 4 * (g & 1) + 8 * r) = acc[0];
    if (g < 2) {
      *(f32x4*)(tab + 256 + 64 * g + 4 * r) = acc[1];
#pragma unroll
      for (int e = 0; e < 4; ++e) tab[512 + 64 * g + 16 * e + r] = acc[2][e];
    } else {
#pragma unroll
      for (int e = 0; e < 4; ++e) tab[384 + (g & 1) + 32 * e + 2 * r] = acc[1][e];
      if (lane == 32) tab[640] = acc[2][0];
    }
  }
  __syncthreads();
  {
    const float* tab = (const float*)tb;
    unsigned* kd = kc + (size_t)(d0 + ch) * KC_PITCH;
#pragma unroll
    for (int e = 0; e < 3; ++e) {
      const int id = tc + 256 * e;
      if (id < 641) {
        const float v = (tab[id] + tab[656 + id]) + (tab[2 * 656 + id] + tab[3 * 656 + id]);
        const float vh = bfr(v);
        kd[id] = pk2(vh, v - vh);
      }
    }
  }
  asm volatile("s_waitcnt vmcnt(0)" : "+v"(pf_dummy), "+v"(pf2)::"memory");
}

NOPK __global__ __launch_bounds__(512, 4) void k1(const __bf16* __restrict__ u, const __bf16* __restrict__ lar,
                                             const __bf16* __restrict__ aim, const __bf16* __restrict__ cre,
                                             const __bf16* __restrict__ cim, const __bf16* __restrict__ ldt,
                                             const __bf16* __restrict__ wg, const __bf16* __restrict__ wu,
                                             const __bf16* __restrict__ gw, __bf16* __restrict__ h,
                                             __bf16* __restrict__ uT, unsigned* __restrict__ kc,
                                             float* __restrict__ gout, unsigned* __restrict__ aG,
                                             const __bf16* __restrict__ wd) {
  extern __shared__ __attribute__((aligned(16))) char smem[];
  const int b = blockIdx.x;
  const int xcd = b & 7, i = (b & 255) >> 3;
  if (b < 256) {
    const int rb = 4 * (xcd >> 1) + (i & 3), cb = 8 * (xcd & 1) + (i >> 2);
    if (cb < 15) ffn1_block(smem, rb, cb, u, wg, wu, h);
    else gate_block(smem, rb, u, gw, gout, uT);
  } else {
    kgen_block(smem, 64 * xcd + 2 * i, lar, aim, cre, cim, ldt, kc, aG, wd, u, wg, wu);
  }
}

constexpr unsigned A_TAG = 0x5a17c3e9u;
constexpr int UCP = 4160;
constexpr int KARR = (240 + L) * 2;
constexpr int OFF_K = 4 * UCP;
constexpr int OFF_TAB = OFF_K + 2 * KARR;
constexpr int OFF_RED = UCP;
constexpr int CH_BYTES = OFF_RED + 32768;
static_assert(OFF_TAB + 3072 <= CH_BYTES && 2 * CH_BYTES <= LDS_BYTES, "lds");

struct PosLut {
  unsigned short v[L];
  constexpr PosLut() : v() {
    for (int j = 0; j < L; ++j) {
      const int sh = (j >= 256) + (j >= 512) + (j >= 1024);
      int q = j >> sh;
      if (sh) {
        const int rem = j & ((1 << sh) - 1), half = 1 << (sh - 1);
        if (rem > half || (rem == half && (q & 1))) ++q;
      }
      v[j] = (unsigned short)(4 * (128 * sh + q));
    }
  }
};
__device__ const PosLut g_poslut{};

__device__ __forceinline__ void conv_block(char* smem, int d0, const __bf16* __restrict__ uT,
                                           const unsigned* __restrict__ kc, const __bf16* __restrict__ dsk,
                                           unsigned* __restrict__ aG) {
  const int tid = threadIdx.x, lane = tid & 63, w = __builtin_amdgcn_readfirstlane(tid >> 6);
  const int ch = w >> 2, wq = w & 3, tc = tid & 255;
  const int r = lane & 15, g = lane >> 4, q = (lane >> 2) & 3, p = lane & 3;
  char* cb = smem + ch * CH_BYTES;
  const int d = d0 + ch;
  __builtin_amdgcn_s_setprio(3);
  {
    const unsigned lb = lds_addr_of(cb);
    glds16(uT + (size_t)d * L + wq * 512, lane * 16, lb + 32 + wq * 1024);
    if (wq < 3) glds16(kc + (size_t)d * KC_PITCH + wq * 256, lane * 16, lb + OFF_TAB + wq * 1024);
    const u32x4 z = {0u, 0u, 0u, 0u};
    if (tc < 60) *(u32x4*)(cb + OFF_K + (tc >= 30 ? KARR : 0) + (tc % 30) * 16) = z;
    else if (tc < 68) *(u32x4*)(cb + ((tc - 60) >> 1) * UCP + ((tc - 60) & 1) * 16) = z;
  }
  const float dsv = (float)dsk[d];
  const u32x4 lut = *(const u32x4*)(g_poslut.v + 8 * tc);
  WAIT_VM(0);
  __syncthreads();
  {
    const u32x4 pa = *(const u32x4*)(cb + 16 + 16 * tc), pb = *(const u32x4*)(cb + 32 + 16 * tc);
    const u32x4 c1 = {__builtin_amdgcn_alignbit(pb[0], pa[3], 16), __builtin_amdgcn_alignbit(pb[1], pb[0], 16),
                      __builtin_amdgcn_alignbit(pb[2], pb[1], 16), __builtin_amdgcn_alignbit(pb[3], pb[2], 16)};
    const u32x4 c2 = {pa[3], pb[0], pb[1], pb[2]};
    const u32x4 c3 = {__builtin_amdgcn_alignbit(pa[3], pa[2], 16), c1[0], c1[1], c1[2]};
    *(u32x4*)(cb + 1 * UCP + 32 + 16 * tc) = c1;
    *(u32x4*)(cb + 2 * UCP + 32 + 16 * tc) = c2;
    *(u32x4*)(cb + 3 * UCP + 32 + 16 * tc) = c3;
    u32x4 kh, kl;
#pragma unroll
    for (int e = 0; e < 4; ++e) {
      const unsigned t0 = *(const unsigned*)(cb + OFF_TAB + (lut[e] & 0xffffu));
      const unsigned t1 = *(const unsigned*)(cb + OFF_TAB + (lut[e] >> 16));
      kh[e] = __builtin_amdgcn_perm(t1, t0, 0x05040100u);
      kl[e] = __builtin_amdgcn_perm(t1, t0, 0x07060302u);
    }
    *(u32x4*)(cb + OFF_K + 480 + 16 * tc) = kh;
  }
  __syncthreads();
  __builtin_amdgcn_s_setprio(1);
  f32x4 acc[8];
#pragma unroll
  for (int t = 0; t < 8; ++t) acc[t] = f32x4{0.f, 0.f, 0.f, 0.f};
#pragma unroll
  for (int rr = 0; rr < 2; ++rr) {
    const int rho = 2 * wq + rr;
    const int abase = q * UCP + 2 * (16 + 16 + 32 * rho - 8 * g + 4 * p);
    const int bbase = OFF_K + 2 * (240 - 16 - 32 * rho + 16 * r + 8 * g);
    bf16x8 af[8];
#pragma unroll
    for (int s = 0; s < 8; ++s) af[s] = tr_frag(cb, abase + 512 * s, abase + 512 * s - 8);
#pragma unroll
    for (int t = 0; t < 8; ++t) {
      const bf16x8 bh = *(const bf16x8*)(cb + bbase + 512 * t);
#pragma unroll
      for (int s = 0; s + t < 8; ++s) acc[s + t] = mfma16(af[s], bh, acc[s + t]);
    }
  }
  __builtin_amdgcn_s_setprio(3);
  __syncthreads();
  if (tid == 256) __hip_atomic_store((gu32*)(aG + (size_t)D * L + (d0 >> 1)), 1u, __ATOMIC_RELAXED, __HIP_MEMORY_SCOPE_AGENT);
#pragma unroll
  for (int t = 0; t < 8; ++t)
    if ((t >> 1) != wq) *(f32x4*)(cb + OFF_RED + (wq * 8 + t) * 1024 + lane * 16) = acc[t];
  __syncthreads();
#pragma unroll
  for (int tt = 0; tt < 2; ++tt) {
    const int t = 2 * wq + tt;
    f32x4 own = f32x4{0.f, 0.f, 0.f, 0.f};
#pragma unroll
    for (int t2 = 0; t2 < 8; ++t2)
      if (t2 == t) own = acc[t2];
    f32x4 s = f32x4{0.f, 0.f, 0.f, 0.f};
#pragma unroll
    for (int sw = 0; sw < 4; ++sw) {
      const f32x4 o = *(const f32x4*)(cb + OFF_RED + (sw * 8 + t) * 1024 + lane * 16);
      s += (sw == wq) ? own : o;
    }
    const int l = 256 * t + 16 * r + 4 * g;
    const u32x2 uu = *(const u32x2*)(cb + 32 + 2 * l);
    const float a0 = bfr(s[0]) + bfr(lo_f(uu[0]) * dsv), a1 = bfr(s[1]) + bfr(hi_f(uu[0]) * dsv);
    const float a2 = bfr(s[2]) + bfr(lo_f(uu[1]) * dsv), a3 = bfr(s[3]) + bfr(hi_f(uu[1]) * dsv);
    const u32x4 gr = {pk2(a0, a1), A_TAG, pk2(a2, a3), A_TAG};
    asm volatile("global_store_dwordx4 %0, %1, off sc1\n\ts_nop 1" ::"v"(aG + (size_t)d * L + l), "v"(gr) : "memory");
  }
}

constexpr int F2_NS = 5;
constexpr int F2_STAGE = 16384;
constexpr int F2_NST = 22;

struct F2Ctx {
  const __bf16 *h, *wd;
  unsigned avoff, bvoff, bvoff_last, lbase;
  int w, kh;
  int aoff[2], boff[2];
  const unsigned* asrc;
  mutable u32x4 x0, x1;
};
__device__ __forceinline__ void f2_stage(const F2Ctx& c, int st) {
  const unsigned base = c.lbase + (st % F2_NS) * F2_STAGE;
  glds16(c.h + st * 64, c.avoff, base + c.w * 1024);
  glds16(c.wd + (size_t)st * 64 * D, st == F2_NST - 1 ? c.bvoff_last : c.bvoff, base + 8192 + c.w * 1024);
}
template <int ST>
__device__ __forceinline__ void f2_loop(const F2Ctx& c, const char* smem, bf16x8 (&ao)[2], bf16x8 (&bo)[2],
                                        f32x4 (&acc)[2][2]) {
  if constexpr (ST < F2_NST) {
    constexpr int rem = (F2_NST - 1 - ST) < (F2_NS - 2) ? (F2_NST - 1 - ST) : (F2_NS - 2);
    wait_vm<2 * rem>();
    WAIT_LGKM0();
    __builtin_amdgcn_s_barrier();
    asm volatile("" ::: "memory");
    if constexpr (ST + F2_NS - 1 < F2_NST) f2_stage(c, ST + F2_NS - 1);
    if constexpr (ST == F2_NST - 3)
      asm volatile("global_load_dwordx4 %0, %2, off sc1\n\tglobal_load_dwordx4 %1, %3, off sc1"
                   : "=&v"(c.x0), "=&v"(c.x1) : "v"(c.asrc), "v"(c.asrc + 16 * L) : "memory");
    const char* base = smem + (ST % F2_NS) * F2_STAGE;
    bf16x8 an[2], bn[2];
#pragma unroll
    for (int mt = 0; mt < 2; ++mt) an[mt] = *(const bf16x8*)(base + c.aoff[mt]);
#pragma unroll
    for (int nt = 0; nt < 2; ++nt) bn[nt] = tr_frag(base, c.boff[nt], c.boff[nt] + 512);
    __builtin_amdgcn_sched_barrier(0);
    if constexpr (ST > 0) {
#pragma unroll
      for (int mt = 0; mt < 2; ++mt)
#pragma unroll
        for (int nt = 0; nt < 2; ++nt) acc[mt][nt] = mfma16(ao[mt], bo[nt], acc[mt][nt]);
    }
    __builtin_amdgcn_sched_barrier(0);
    f2_loop<ST + 1>(c, smem, an, bn, acc);
  } else {
    if (c.kh == 0) {
#pragma unroll
      for (int mt = 0; mt < 2; ++mt)
#pragma unroll
        for (int nt = 0; nt < 2; ++nt) acc[mt][nt] = mfma16(ao[mt], bo[nt], acc[mt][nt]);
    }
  }
}

__device__ __forceinline__ void ffn2_block(char* smem, int row0, int cbk, const __bf16* __restrict__ h,
                                           const __bf16* __restrict__ wd, const unsigned* __restrict__ aG,
                                           const float* __restrict__ gin, __bf16* __restrict__ out) {
  const int col0 = 64 * cbk;
  const int tid = threadIdx.x, lane = tid & 63, w = __builtin_amdgcn_readfirstlane(tid >> 6);
  const int r = lane & 15, g = lane >> 4, q = (lane >> 2) & 3, p = lane & 3;
  const int kh = w >> 2, wm = (w >> 1) & 1, wn = w & 1;
  __builtin_amdgcn_s_setprio(1);
  F2Ctx c;
  c.h = h; c.wd = wd; c.w = w; c.kh = kh; c.lbase = lds_addr_of(smem);
  {
    const int row = 8 * w + (lane >> 3), ch = (lane & 7) ^ ((row >> 1) & 7);
    c.avoff = (unsigned)(((row0 + row) * HP + ch * 8) * 2);
    const int bk = 8 * w + (lane >> 3);
    const int pos = lane & 7, fp = ((bk >> 1) & 1) | (((bk >> 3) & 1) << 1);
    const int cc = col0 + 16 * ((pos >> 1) ^ fp) + 8 * (pos & 1);
    c.bvoff = (unsigned)((bk * D + cc) * 2);
    c.bvoff_last = (unsigned)(((bk > 31 ? 31 : bk) * D + cc) * 2);
  }
#pragma unroll
  for (int mt = 0; mt < 2; ++mt) {
    const int row = 32 * wm + 16 * mt + r, ch = 4 * kh + g;
    c.aoff[mt] = row * 128 + ((ch ^ ((row >> 1) & 7)) << 4);
  }
  const int fpl = (q >> 1) | ((g & 1) << 1);
#pragma unroll
  for (int nt = 0; nt < 2; ++nt) c.boff[nt] = 8192 + (32 * kh + 8 * g + q) * 128 + (((2 * wn + nt) ^ fpl) << 5) + 8 * p;

  f32x4 acc[2][2];
#pragma unroll
  for (int mt = 0; mt < 2; ++mt)
#pragma unroll
    for (int nt = 0; nt < 2; ++nt) acc[mt][nt] = f32x4{0.f, 0.f, 0.f, 0.f};

#pragma unroll
  for (int st = 0; st < F2_NS - 1; ++st) f2_stage(c, st);
  const int lr = 32 * wm + 16 * kh + 4 * g;
  const f32x4 gv = *(const f32x4*)(gin + row0 + lr);
  c.asrc = aG + (size_t)(col0 + 32 * wn + r) * L + row0 + lr;
  bf16x8 a0[2], b0[2];
  f2_loop<0>(c, smem, a0, b0, acc);
  WAIT_LGKM0();
  __builtin_amdgcn_s_barrier();
  asm volatile("" ::: "memory");
  asm volatile("s_waitcnt vmcnt(0)" : "+v"(c.x0), "+v"(c.x1)::"memory");
  const bool early = __all(c.x0[1] == A_TAG && c.x0[3] == A_TAG && c.x1[1] == A_TAG && c.x1[3] == A_TAG);
  if (w == 0 && !early) {
    const gu32* fl = (const gu32*)(aG + (size_t)D * L + 32 * cbk + (lane & 31));
    for (int spin = 0; spin < (1 << 22); ++spin) {
      const unsigned v = __hip_atomic_load(fl, __ATOMIC_RELAXED, __HIP_MEMORY_SCOPE_AGENT);
      if (__all(v == 1u)) break;
      __builtin_amdgcn_s_sleep(2);
    }
  }
  {
    char* red = smem + (w & 3) * 4096 + kh * 2048;
    const f32x4 o0 = kh ? acc[0][0] : acc[1][0], o1 = kh ? acc[0][1] : acc[1][1];
    *(f32x4*)(red + lane * 16) = o0;
    *(f32x4*)(red + 1024 + lane * 16) = o1;
  }
  __syncthreads();
  unsigned long long av[2];
  {
    const unsigned* src = c.asrc;
    av[0] = (unsigned long long)c.x0[0] | ((unsigned long long)c.x0[2] << 32);
    av[1] = (unsigned long long)c.x1[0] | ((unsigned long long)c.x1[2] << 32);
    for (int spin = 0; !early && spin < (1 << 22); ++spin) {
      u32x4 x0, x1;
      asm volatile("global_load_dwordx4 %0, %2, off sc1\n\tglobal_load_dwordx4 %1, %3, off sc1\n\ts_waitcnt vmcnt(0)"
                   : "=&v"(x0), "=&v"(x1) : "v"(src), "v"(src + 16 * L) : "memory");
      av[0] = (unsigned long long)x0[0] | ((unsigned long long)x0[2] << 32);
      av[1] = (unsigned long long)x1[0] | ((unsigned long long)x1[2] << 32);
      const bool ok = x0[1] == A_TAG && x0[3] == A_TAG && x1[1] == A_TAG && x1[3] == A_TAG;
      if (__all(ok)) break;
      __builtin_amdgcn_s_sleep(4);
    }
  }
  constexpr int OT = 16384, OTP = 144;
  {
    const char* red = smem + (w & 3) * 4096 + (1 - kh) * 2048;
#pragma unroll
    for (int nt = 0; nt < 2; ++nt) {
      const f32x4 o = *(const f32x4*)(red + nt * 1024 + lane * 16);
      const f32x4 own = kh ? acc[1][nt] : acc[0][nt];
      const f32x4 s = kh ? (o + own) : (own + o);
      const unsigned alo = (unsigned)av[nt], ahi = (unsigned)(av[nt] >> 32);
      const float a[4] = {lo_f(alo), hi_f(alo), lo_f(ahi), hi_f(ahi)};
#pragma unroll
      for (int e = 0; e < 4; ++e) {
        const float o2 = bfr(gv[e] * a[e]) + bfr(bfr(1.f - gv[e]) * bfr(s[e]));
        *(__bf16*)(smem + OT + (lr + e) * OTP + (32 * wn + 16 * nt + r) * 2) = (__bf16)o2;
      }
    }
  }
  __syncthreads();
  {
    const int row = tid >> 3, c8 = tid & 7;
    const u32x4 v = *(const u32x4*)(smem + OT + row * OTP + c8 * 16);
    *(u32x4*)(out + (size_t)(row0 + row) * D + col0 + c8 * 8) = v;
  }
}

NOPK __global__ __launch_bounds__(512, 4) void k2(const __bf16* __restrict__ h, const __bf16* __restrict__ wd,
                                             const __bf16* __restrict__ uT, const unsigned* __restrict__ kc,
                                             const __bf16* __restrict__ dsk, unsigned* __restrict__ aG,
                                             const float* __restrict__ gin, __bf16* __restrict__ out) {
  extern __shared__ __attribute__((aligned(16))) char smem[];
  const int b = blockIdx.x, xcd = b & 7, i = (b & 255) >> 3;
  if (b < 256) {
    const int pair = 32 * xcd + i;
    conv_block(smem, 2 * pair, uT, kc, dsk, aG);
  } else {
    ffn2_block(smem, 64 * (4 * xcd + (i & 3)), i >> 2, h, wd, aG, gin, out);
  }
}

extern "C" void kernel_launch(void* const* d_in, const int* in_sizes, int n_in, void* d_out, int out_size,
                              void* d_ws, size_t ws_size, hipStream_t stream) {
  const __bf16* u = (const __bf16*)d_in[0];
  const __bf16* lar = (const __bf16*)d_in[1];
  const __bf16* aim = (const __bf16*)d_in[2];
  const __bf16* cre = (const __bf16*)d_in[3];
  const __bf16* cim = (const __bf16*)d_in[4];
  const __bf16* ldt = (const __bf16*)d_in[5];
  const __bf16* dsk = (const __bf16*)d_in[6];
  const __bf16* wg = (const __bf16*)d_in[8];
  const __bf16* wu = (const __bf16*)d_in[9];
  const __bf16* wd = (const __bf16*)d_in[10];
  const __bf16* gw = (const __bf16*)d_in[11];
  char* ws = (char*)d_ws;
  __bf16* h = (__bf16*)ws;
  ws += (size_t)L * HP * 2;
  unsigned* aG = (unsigned*)ws;
  ws += (size_t)D * L * 4 + 1024;
  __bf16* uT = (__bf16*)ws;
  ws += (size_t)D * L * 2;
  unsigned* kc = (unsigned*)ws;
  ws += (size_t)D * KC_PITCH * 4;
  float* gout = (float*)ws;
  (void)hipFuncSetAttribute((const void*)k1, hipFuncAttributeMaxDynamicSharedMemorySize, LDS_BYTES);
  (void)hipFuncSetAttribute((const void*)k2, hipFuncAttributeMaxDynamicSharedMemorySize, LDS_BYTES);
  hipLaunchKernelGGL(k1, dim3(512), dim3(512), LDS_BYTES, stream, u, lar, aim, cre, cim, ldt, wg, wu, gw, h, uT, kc,
                     gout, aG, wd);
  hipLaunchKernelGGL(k2, dim3(512), dim3(512), LDS_BYTES, stream, (const __bf16*)h, wd, (const __bf16*)uT,
                     (const unsigned*)kc, dsk, aG, (const float*)gout, (__bf16*)d_out);
}
```

```cpp
#include <hip/hip_runtime.h>
#include <hip/hip_bf16.h>
#include <type_traits>

typedef __attribute__((ext_vector_type(8))) __bf16 bf16x8;
typedef __attribute__((ext_vector_type(2))) __bf16 bf16x2;
typedef __attribute__((ext_vector_type(4))) short s16x4;
typedef __attribute__((ext_vector_type(8))) short s16x8;
typedef __attribute__((ext_vector_type(4))) float f32x4;
typedef __attribute__((ext_vector_type(4))) unsigned u32x4;
typedef __attribute__((ext_vector_type(2))) unsigned u32x2;
typedef __attribute__((address_space(3))) s16x4* lds_s16x4_ptr;
typedef __attribute__((address_space(1))) unsigned gu32;
typedef __attribute__((address_space(1))) unsigned long long gu64;

#define K_LO 0

#define NOPK __attribute__((target("no-packed-fp32-ops")))

constexpr int L = 2048, D = 512, NS = 128, F = 1376, HP = 1408;
constexpr int LDS_BYTES = 81920;

__device__ __forceinline__ float bf2f(unsigned short v) { return __uint_as_float((unsigned)v << 16); }
__device__ __forceinline__ float bfr(float x) { return (float)(__bf16)x; }
__device__ __forceinline__ unsigned pk2(float a, float b) {
  bf16x2 v = {(__bf16)a, (__bf16)b};
  return __builtin_bit_cast(unsigned, v);
}
__device__ __forceinline__ float lo_f(unsigned w) { return __uint_as_float(w << 16); }
__device__ __forceinline__ float hi_f(unsigned w) { return __uint_as_float(w & 0xffff0000u); }

__device__ __forceinline__ f32x4 mfma16(bf16x8 a, bf16x8 b, f32x4 c) {
  return __builtin_amdgcn_mfma_f32_16x16x32_bf16(a, b, c, 0, 0, 0);
}
__device__ __forceinline__ bf16x8 tr_frag(const char* lds, int off0, int off1) {
  s16x4 a = __builtin_amdgcn_ds_read_tr16_b64_v4i16((lds_s16x4_ptr)(lds + off0));
  s16x4 b = __builtin_amdgcn_ds_read_tr16_b64_v4i16((lds_s16x4_ptr)(lds + off1));
  s16x8 v = {a[0], a[1], a[2], a[3], b[0], b[1], b[2], b[3]};
  return __builtin_bit_cast(bf16x8, v);
}
__device__ __forceinline__ void glds16(const void* sbase, unsigned voff, unsigned lds_addr) {
  asm volatile("s_mov_b32 m0, %2\n\ts_nop 0\n\tglobal_load_lds_dwordx4 %0, %1" ::"v"(voff), "s"(sbase), "s"(lds_addr)
               : "memory", "m0");
}
__device__ __forceinline__ void glds16v(const void* vptr, unsigned lds_addr) {
  asm volatile("s_mov_b32 m0, %1\n\ts_nop 0\n\tglobal_load_lds_dwordx4 %0, off" ::"v"(vptr), "s"(lds_addr)
               : "memory", "m0");
}
__device__ __forceinline__ unsigned lds_addr_of(const char* p) {
  return (unsigned)(uintptr_t)(const __attribute__((address_space(3))) char*)p;
}
#define WAIT_VM(n) asm volatile("s_waitcnt vmcnt(" #n ")" ::: "memory")
#define WAIT_LGKM0() asm volatile("s_waitcnt lgkmcnt(0)" ::: "memory")
template <int N>
__device__ __forceinline__ void wait_vm() {
  asm volatile("s_waitcnt vmcnt(%0)" ::"i"(N) : "memory");
}

constexpr int F1_B = 32768;

struct F1Ctx {
  const __bf16 *x, *wg, *wu, *wmix;
  unsigned voffA[2], voffB, lbase;
  int w;
  int aoff[2], boff[3];
};
__device__ __forceinline__ void f1_stage_a(const F1Ctx& c, int m) {
  const unsigned ab = c.lbase + (m & 1) * 16384 + c.w * 1024;
  glds16(c.x + m * 64, c.voffA[0], ab);
  glds16(c.x + m * 64, c.voffA[1], ab + 8192);
}
template <bool G2>
__device__ __forceinline__ void f1_stage_b(const F1Ctx& c, int hs) {
  const unsigned bb = c.lbase + F1_B + (hs & 3) * 12288 + c.w * 1024;
  if constexpr (!G2) {
    glds16(c.wg + (size_t)hs * 32 * F, c.voffB, bb);
    glds16(c.wu + (size_t)hs * 32 * F, c.voffB, bb + 8192);
  } else {
    glds16v(c.wmix + (size_t)hs * 32 * F, bb);
  }
}
constexpr int f1_allowed(int hs, int nb) {
  int n = 0;
  if ((hs & 1) == 0 && hs >= 2) {
    if (hs + 1 < 16) n += nb;
    if (hs + 2 < 16) n += nb;
  } else {
    if (hs + 1 < 16) n += nb;
    if (hs >= 1 && ((hs - 1) & 1) == 0 && (hs + 1) / 2 < 8) n += 2;
    if (hs + 2 < 16) n += nb;
  }
  return n;
}
template <int HS, bool G2>
__device__ __forceinline__ void f1_loop(const F1Ctx& c, const char* smem, bf16x8 (&af)[4], bf16x8 (&bq)[3],
                                        f32x4 (&acc)[4][3]) {
  if constexpr (HS < 16) {
    if constexpr (HS > 0) {
#pragma unroll
      for (int j = 0; j < 3; ++j) {
        acc[0][j] = mfma16(bq[j], af[0], acc[0][j]);
        acc[1][j] = mfma16(bq[j], af[1], acc[1][j]);
      }
    }
    __builtin_amdgcn_sched_barrier(0);
    wait_vm<f1_allowed(HS, G2 ? 1 : 2)>();
    WAIT_LGKM0();
    __builtin_amdgcn_s_barrier();
    asm volatile("" ::: "memory");
    const char* ab = smem + ((HS >> 1) & 1) * 16384 + c.aoff[HS & 1];
    const char* bbase = smem + (HS & 3) * 12288;
    __builtin_amdgcn_sched_barrier(0);
    bf16x8 bn[3];
    af[0] = *(const bf16x8*)(ab);
    bn[0] = tr_frag(bbase, c.boff[0], c.boff[0] + 512);
    af[1] = *(const bf16x8*)(ab + 2048);
    bn[1] = tr_frag(bbase, c.boff[1], c.boff[1] + 512);
    bn[2] = tr_frag(bbase, c.boff[2], c.boff[2] + 512);
    __builtin_amdgcn_sched_barrier(0);
#pragma unroll
    for (int j = 0; j < 3; ++j) {
      if constexpr (HS > 0) {
        acc[2][j] = mfma16(bq[j], af[2], acc[2][j]);
        acc[3][j] = mfma16(bq[j], af[3], acc[3][j]);
      }
      __builtin_amdgcn_sched_barrier(0);
      if (j == 0) {
        if constexpr ((HS & 1) == 0 && (HS + 2) / 2 < 8) f1_stage_a(c, (HS + 2) / 2);
      } else if (j == 1) {
        if constexpr (HS + 3 < 16) f1_stage_b<G2>(c, HS + 3);
      }
      __builtin_amdgcn_sched_barrier(0);
    }
    af[2] = *(const bf16x8*)(ab + 4096);
    af[3] = *(const bf16x8*)(ab + 6144);
    f1_loop<HS + 1, G2>(c, smem, af, bn, acc);
  } else {
#pragma unroll
    for (int j = 0; j < 3; ++j)
#pragma unroll
      for (int mt = 0; mt < 4; ++mt) acc[mt][j] = mfma16(bq[j], af[mt], acc[mt][j]);
  }
}

__device__ __forceinline__ void ffn1_block(char* smem, int rb, int cb, const __bf16* __restrict__ x,
                                           const __bf16* __restrict__ wg, const __bf16* __restrict__ wu,
                                           __bf16* __restrict__ h) {
  const int tid = threadIdx.x, lane = tid & 63, w = __builtin_amdgcn_readfirstlane(tid >> 6);
  const int row0 = rb * 128, f0 = cb * 96;
  const int wm = w >> 2, sel = (w >> 1) & 1, wn = w & 1;
  const int r = lane & 15, g = lane >> 4, q = (lane >> 2) & 3, p = lane & 3;
  F1Ctx c;
  c.x = x; c.wg = wg; c.wu = wu; c.w = w; c.lbase = lds_addr_of(smem);
  {
#pragma unroll
    for (int i = 0; i < 2; ++i) {
      const int row = 64 * i + 8 * w + (lane >> 3), ch = (lane & 7) ^ ((row >> 1) & 7);
      c.voffA[i] = (unsigned)(((row0 + row) * D + ch * 8) * 2);
    }
    const int kk = 8 * (w & 3) + (lane >> 3), pos = lane & 7;
    const int fp = ((kk >> 1) & 1) | (((kk >> 3) & 1) << 1);
    const int i = (pos >> 1) ^ fp;
    const int par = (kk + cb) & 1;
    int c0 = f0 + 16 * (i + 2 * par) + 8 * (pos & 1);
    int c1 = f0 + 16 * ((i & 1) + 4 * (1 - par)) + 8 * (pos & 1);
    c0 = c0 > F - 8 ? F - 8 : c0;
    c1 = c1 > F - 8 ? F - 8 : c1;
    c.voffB = (unsigned)((kk * F + c0) * 2);
    c.wmix = (i < 2 ? wg : wu) + kk * F + c1;
  }
#pragma unroll
  for (int ks = 0; ks < 2; ++ks)
    c.aoff[ks] = (64 * wm + r) * 128 + (((4 * ks + g) ^ ((r >> 1) & 7)) << 4);
  {
    const int fp = (q >> 1) | ((g & 1) << 1);
#pragma unroll
    for (int j = 0; j < 3; ++j) {
      const int t = 3 * wn + j, par = (q + cb) & 1, tt = t - 2 * par;
      const bool whole = tt >= 0 && tt < 4;
      const int sub = whole ? 2 * sel : 1, ps = whole ? tt : 2 * sel + (t & 1);
      c.boff[j] = F1_B + sub * 4096 + (8 * g + q) * 128 + ((ps ^ fp) << 5) + 8 * p;
      asm volatile("" : "+v"(c.boff[j]));
    }
  }
  f32x4 acc[4][3];
#pragma unroll
  for (int mt = 0; mt < 4; ++mt)
#pragma unroll
    for (int j = 0; j < 3; ++j) acc[mt][j] = f32x4{0.f, 0.f, 0.f, 0.f};
  bf16x8 af[4], bq[3];

  f1_stage_a(c, 0);
  if (w < 4) {
    f1_stage_b<false>(c, 0);
    f1_stage_b<false>(c, 1);
    f1_stage_b<false>(c, 2);
    f1_loop<0, false>(c, smem, af, bq, acc);
  } else {
    f1_stage_b<true>(c, 0);
    f1_stage_b<true>(c, 1);
    f1_stage_b<true>(c, 2);
    f1_loop<0, true>(c, smem, af, bq, acc);
  }

  __syncthreads();
  constexpr int HT = 49152, HTP = 208;
  auto finish = [&](auto SEL) {
    constexpr int S = decltype(SEL)::value;
#pragma unroll
    for (int m2 = 0; m2 < 2; ++m2)
#pragma unroll
      for (int j = 0; j < 3; ++j) *(f32x4*)(smem + (w * 6 + m2 * 3 + j) * 1024 + lane * 16) = acc[2 * (1 - S) + m2][j];
    __syncthreads();
#pragma unroll
    for (int m2 = 0; m2 < 2; ++m2)
#pragma unroll
      for (int j = 0; j < 3; ++j) {
        const f32x4 o4 = *(const f32x4*)(smem + ((w ^ 2) * 6 + m2 * 3 + j) * 1024 + lane * 16);
        const f32x4 g4 = S ? o4 : acc[m2][j], u4 = S ? acc[2 + m2][j] : o4;
        float hv[4];
#pragma unroll
        for (int e = 0; e < 4; ++e) {
          const float gv = g4[e];
          const float t = gv * __builtin_fmaf(gv * gv, -0.10294324f, -2.3022082f);
          hv[e] = (gv * u4[e]) * __builtin_amdgcn_rcpf(1.f + __builtin_amdgcn_exp2f(t));
        }
        u32x2 o = {pk2(hv[0], hv[1]), pk2(hv[2], hv[3])};
        *(u32x2*)(smem + HT + (64 * wm + 32 * S + 16 * m2 + r) * HTP + (16 * (3 * wn + j) + 4 * g) * 2) = o;
      }
  };
  if (sel) finish(std::integral_constant<int, 1>{});
  else finish(std::integral_constant<int, 0>{});
  __syncthreads();
#pragma unroll
  for (int i = 0; i < 3; ++i) {
    const int idx = tid + 512 * i, row = idx / 12, c8 = idx - row * 12;
    if (f0 + 8 * c8 < F)
      *(u32x4*)(h + (size_t)(row0 + row) * HP + f0 + 8 * c8) = *(const u32x4*)(smem + HT + row * HTP + c8 * 16);
  }
}

__device__ __forceinline__ void gate_block(char* smem, int rb, const __bf16* __restrict__ x,
                                           const __bf16* __restrict__ gw, float* __restrict__ gout,
                                           __bf16* __restrict__ uT) {
  const int tid = threadIdx.x, lane = tid & 63, w = __builtin_amdgcn_readfirstlane(tid >> 6);
  const int g = lane >> 4, q = (lane >> 2) & 3, p = lane & 3;
  const unsigned lbase = lds_addr_of(smem);
  const int grow = tid >> 3, gpart = tid & 7, gf = (grow & 3) | (((grow >> 3) & 1) << 2);
  u32x4 gwv[8];
#pragma unroll
  for (int i = 0; i < 8; ++i) gwv[i] = *(const u32x4*)(gw + ((gpart ^ gf) + 8 * (i >> 1)) * 16 + 8 * (i & 1));
#pragma unroll 1
  for (int pass = 0; pass < 2; ++pass) {
    const int l0 = rb * 128 + pass * 64;
#pragma unroll
    for (int i = 0; i < 8; ++i) {
      const int row = 8 * w + i, fr = (row & 3) | (((row >> 3) & 1) << 2);
      glds16(x + (size_t)(l0 + row) * D, (unsigned)((((lane >> 1) ^ fr) * 2 + (lane & 1)) * 16), lbase + row * 1024);
    }
    WAIT_VM(0);
    __syncthreads();
#pragma unroll
    for (int i = 0; i < 8; ++i) {
      const int unit = w * 8 + i, c16 = unit >> 1, half = unit & 1;
      const int ra = 32 * half + 8 * g + q, rb2 = ra + 4;
      const int fa = (ra & 3) | (((ra >> 3) & 1) << 2);
      const bf16x8 v = tr_frag(smem, ra * 1024 + ((c16 ^ fa) << 5) + 8 * p, rb2 * 1024 + ((c16 ^ fa) << 5) + 8 * p);
      *(bf16x8*)(uT + (size_t)(16 * c16 + (lane & 15)) * L + l0 + 32 * half + 8 * g) = v;
    }
    float s = 0.f;
#pragma unroll
    for (int i = 0; i < 8; ++i) {
      const u32x4 xv = *(const u32x4*)(smem + grow * 1024 + (gpart + 8 * (i >> 1)) * 32 + 16 * (i & 1));
#pragma unroll
      for (int e = 0; e < 4; ++e) {
        const unsigned xe = xv[e], we = gwv[i][e];
        s = __builtin_amdgcn_fdot2_f32_bf16(__builtin_bit_cast(bf16x2, xe), __builtin_bit_cast(bf16x2, we), s, false);
      }
    }
    s += __shfl_xor(s, 1);
    s += __shfl_xor(s, 2);
    s += __shfl_xor(s, 4);
    if (gpart == 0) gout[l0 + grow] = bfr(1.f / (1.f + __expf(-bfr(s))));
    __syncthreads();
  }
}

constexpr int KC_PITCH = 768;
constexpr int KG_PITCH = 160;
constexpr int KG_ARR = 16 * KG_PITCH;
constexpr int KG_TAB = 8 * 2 * KG_ARR;
typedef __attribute__((ext_vector_type(2))) float f32x2;

struct PosLut {
  unsigned short v[L];
  constexpr PosLut() : v() {
    for (int j = 0; j < L; ++j) {
      const int sh = (j >= 256) + (j >= 512) + (j >= 1024);
      int q = j >> sh;
      if (sh) {
        const int rem = j & ((1 << sh) - 1), half = 1 << (sh - 1);
        if (rem > half || (rem == half && (q & 1))) ++q;
      }
      v[j] = (unsigned short)(4 * (128 * sh + q));
    }
  }
};
__device__ const PosLut g_poslut{};

__device__ __forceinline__ f32x2 cexp(float zr, float zi, float p) {
  const float e = __builtin_amdgcn_exp2f(zr * p);
  const float ph = __builtin_amdgcn_fractf(zi * p);
  return f32x2{e * __builtin_amdgcn_cosf(ph), e * __builtin_amdgcn_sinf(ph)};
}
__device__ __forceinline__ f32x2 cmul(f32x2 a, f32x2 b) {
  return f32x2{a[0] * b[0] - a[1] * b[1], a[0] * b[1] + a[1] * b[0]};
}
__device__ __forceinline__ void kgen_block(char* smem, int d0, const __bf16* __restrict__ lar,
                                           const __bf16* __restrict__ aim, const __bf16* __restrict__ cre,
                                           const __bf16* __restrict__ cim, const __bf16* __restrict__ ldt,
                                           unsigned* __restrict__ kc, unsigned* __restrict__ aG,
                                           const __bf16* __restrict__ wd, const __bf16* __restrict__ x,
                                           const __bf16* __restrict__ wg, const __bf16* __restrict__ wu,
                                           const __bf16* __restrict__ dsk) {
  const int tid = threadIdx.x, lane = tid & 63, w = __builtin_amdgcn_readfirstlane(tid >> 6);
  const int ch = w >> 2, wq = w & 3, tc = tid & 255;
  const int r = lane & 15, g = lane >> 4, hh = lane >> 5;
  char* wb = smem + w * 2 * KG_ARR;
  char* tb = smem + KG_TAB + ch * (4 * 656 * 4);
  float zr, zi;
  f32x2 cc;
  unsigned pf2, pf_dummy;
  {
    const int d = d0 + ch, n = 32 * wq + (lane & 31);
    const int idx = (d0 >> 1) * 150 + (tid < 149 ? tid : 149);
    const __bf16* src = idx < 16384 ? x + (size_t)idx * 64
                                    : (idx < 27392 ? wg + (size_t)(idx - 16384) * 64 : wu + (size_t)(idx - 27392) * 64);
    const void* src2 = tid < 43 ? (const void*)(wd + (size_t)((d0 >> 1) * 43 + tid) * 64)
                                : (tid < 75 ? (const void*)(g_poslut.v + (tid - 43) * 64)
                                            : (const void*)(dsk + (tid < 82 ? tid - 75 : 7) * 64));
    unsigned p0, p1, p2, p3, p4;
    asm volatile(
        "global_load_ushort %0, %7, off\n\tglobal_load_ushort %1, %8, off\n\tglobal_load_ushort %2, %9, off\n\t"
        "global_load_ushort %3, %10, off\n\tglobal_load_ushort %4, %11, off\n\t"
        "global_load_dword %5, %12, off\n\tglobal_load_dword %6, %13, off\n\ts_waitcnt vmcnt(2)"
        : "=&v"(p0), "=&v"(p1), "=&v"(p2), "=&v"(p3), "=&v"(p4), "=&v"(pf2), "=&v"(pf_dummy)
        : "v"(ldt + d), "v"(lar + d * NS + n), "v"(aim + d * NS + n), "v"(cre + d * NS + n), "v"(cim + d * NS + n),
          "v"(src), "v"(src2)
        : "memory");
    const float dt = bfr(expf(lo_f(p0)));
    const float ar = -bfr(expf(lo_f(p1)));
    zr = (ar * dt) * 1.4426950408889634f;
    zi = (lo_f(p2) * dt) * 0.15915494309189535f;
    cc = f32x2{2.f * (lo_f(p3) * dt), 2.f * (lo_f(p4) * dt)};
  }
  {
    u32x4* z = (u32x4*)(aG + (size_t)d0 * L) + tid;
    z[0] = u32x4{0u, 0u, 0u, 0u};
    z[512] = u32x4{0u, 0u, 0u, 0u};
    if (tid == 0) aG[(size_t)D * L + (d0 >> 1)] = 0u;
  }
  const float fh = (float)(1 + hh);
  const f32x2 e128 = cexp(zr, zi, 128.f), e24 = cexp(zr, zi, 2.f * fh);
  f32x2 v[4], st[4];
  v[0] = cexp(zr, -zi, 64.f * hh);                 st[0] = cexp(zr, -zi, 8.f);
  v[1] = hh ? cmul(cc, e128) : cc;                 st[1] = cexp(zr, zi, 1.f);
  v[2] = cmul(cc, cexp(zr, zi, 256.f * fh));       st[2] = hh ? e128 : e24;
  v[3] = cmul(cc, cexp(zr, zi, 1024.f * fh));      st[3] = e128;
  const f32x2 v24 = cmul(v[2], hh ? e24 : e128);
  unsigned vh[4][8], vl[4][8];
#pragma unroll
  for (int i = 0; i < 8; ++i)
#pragma unroll
    for (int t = 0; t < 4; ++t) {
      vh[t][i] = pk2(v[t][0], v[t][1]);
      vl[t][i] = pk2(v[t][0] - lo_f(vh[t][i]), v[t][1] - hi_f(vh[t][i]));
      if (t == 2 && i == 3) v[t] = v24;
      else if (i < 7) v[t] = cmul(v[t], st[t]);
    }
  char* wr = wb + (8 * hh) * KG_PITCH + (lane & 31) * 4;
  const char* rd = wb + r * KG_PITCH + g * 16;
  bf16x8 fh_[4][2], fl_[4][2];
#pragma unroll
  for (int t = 0; t < 4; ++t) {
    __builtin_amdgcn_wave_barrier();
#pragma unroll
    for (int i = 0; i < 8; ++i) {
      *(unsigned*)(wr + i * KG_PITCH) = vh[t][i];
      *(unsigned*)(wr + i * KG_PITCH + KG_ARR) = vl[t][i];
    }
    __builtin_amdgcn_wave_barrier();
#pragma unroll
    for (int ss = 0; ss < 2; ++ss) {
      fh_[t][ss] = *(const bf16x8*)(rd + ss * 64);
      fl_[t][ss] = *(const bf16x8*)(rd + ss * 64 + KG_ARR);
    }
  }
  f32x4 acc[3];
#pragma unroll
  for (int m = 0; m < 3; ++m) acc[m] = f32x4{0.f, 0.f, 0.f, 0.f};
#pragma unroll
  for (int ss = 0; ss < 2; ++ss)
#pragma unroll
    for (int m = 0; m < 3; ++m) {
      acc[m] = mfma16(fl_[1 + m][ss], fh_[0][ss], acc[m]);
      acc[m] = mfma16(fh_[1 + m][ss], fl_[0][ss], acc[m]);
      acc[m] = mfma16(fh_[1 + m][ss], fh_[0][ss], acc[m]);
    }
  {
    float* tab = (float*)tb + wq * 656;
    *(f32x4*)(tab + 128 * (g >> 1) + 4 * (g & 1) + 8 * r) = acc[0];
    if (g < 2) {
      *(f32x4*)(tab + 256 + 64 * g + 4 * r) = acc[1];
#pragma unroll
      for (int e = 0; e < 4; ++e) tab[512 + 64 * g + 16 * e + r] = acc[2][e];
    } else {
#pragma unroll
      for (int e = 0; e < 4; ++e) tab[384 + (g & 1) + 32 * e + 2 * r] = acc[1][e];
      if (lane == 32) tab[640] = acc[2][0];
    }
  }
  __syncthreads();
  {
    const float* tab = (const float*)tb;
    unsigned* kd = kc + (size_t)(d0 + ch) * KC_PITCH;
#pragma unroll
    for (int e = 0; e < 3; ++e) {
      const int id = tc + 256 * e;
      if (id < 641) {
        const float v = (tab[id] + tab[656 + id]) + (tab[2 * 656 + id] + tab[3 * 656 + id]);
        const float vh = bfr(v);
        kd[id] = pk2(vh, v - vh);
      }
    }
  }
  asm volatile("s_waitcnt vmcnt(0)" : "+v"(pf_dummy), "+v"(pf2)::"memory");
}

NOPK __global__ __launch_bounds__(512, 4) void k1(const __bf16* __restrict__ u, const __bf16* __restrict__ lar,
                                             const __bf16* __restrict__ aim, const __bf16* __restrict__ cre,
                                             const __bf16* __restrict__ cim, const __bf16* __restrict__ ldt,
                                             const __bf16* __restrict__ wg, const __bf16* __restrict__ wu,
                                             const __bf16* __restrict__ gw, __bf16* __restrict__ h,
                                             __bf16* __restrict__ uT, unsigned* __restrict__ kc,
                                             float* __restrict__ gout, unsigned* __restrict__ aG,
                                             const __bf16* __restrict__ wd, const __bf16* __restrict__ dsk) {
  extern __shared__ __attribute__((aligned(16))) char smem[];
  const int b = blockIdx.x;
  const int xcd = b & 7, i = (b & 255) >> 3;
  if (b < 256) {
    const int rb = 4 * (xcd >> 1) + (i & 3), cb = 8 * (xcd & 1) + (i >> 2);
    if (cb < 15) ffn1_block(smem, rb, cb, u, wg, wu, h);
    else gate_block(smem, rb, u, gw, gout, uT);
  } else {
    kgen_block(smem, 64 * xcd + 2 * i, lar, aim, cre, cim, ldt, kc, aG, wd, u, wg, wu, dsk);
  }
}

constexpr unsigned A_TAG = 0x5a17c3e9u;
constexpr int UCP = 4160;
constexpr int KARR = (240 + L) * 2;
constexpr int OFF_K = 4 * UCP;
constexpr int OFF_TAB = OFF_K + 2 * KARR;
constexpr int OFF_RED = UCP;
constexpr int CH_BYTES = OFF_RED + 32768;
static_assert(OFF_TAB + 3072 <= CH_BYTES && 2 * CH_BYTES <= LDS_BYTES, "lds");

__device__ __forceinline__ void conv_block(char* smem, int d0, const __bf16* __restrict__ uT,
                                           const unsigned* __restrict__ kc, const __bf16* __restrict__ dsk,
                                           unsigned* __restrict__ aG) {
  const int tid = threadIdx.x, lane = tid & 63, w = __builtin_amdgcn_readfirstlane(tid >> 6);
  const int ch = w >> 2, wq = w & 3, tc = tid & 255;
  const int r = lane & 15, g = lane >> 4, q = (lane >> 2) & 3, p = lane & 3;
  char* cb = smem + ch * CH_BYTES;
  const int d = d0 + ch;
  __builtin_amdgcn_s_setprio(3);
  {
    const unsigned lb = lds_addr_of(cb);
    glds16(uT + (size_t)d * L + wq * 512, lane * 16, lb + 32 + wq * 1024);
    if (wq < 3) glds16(kc + (size_t)d * KC_PITCH + wq * 256, lane * 16, lb + OFF_TAB + wq * 1024);
    const u32x4 z = {0u, 0u, 0u, 0u};
    if (tc < 60) *(u32x4*)(cb + OFF_K + (tc >= 30 ? KARR : 0) + (tc % 30) * 16) = z;
    else if (tc < 68) *(u32x4*)(cb + ((tc - 60) >> 1) * UCP + ((tc - 60) & 1) * 16) = z;
  }
  const float dsv = (float)dsk[d];
  const u32x4 lut = *(const u32x4*)(g_poslut.v + 8 * tc);
  WAIT_VM(0);
  __syncthreads();
  {
    const u32x4 pa = *(const u32x4*)(cb + 16 + 16 * tc), pb = *(const u32x4*)(cb + 32 + 16 * tc);
    const u32x4 c1 = {__builtin_amdgcn_alignbit(pb[0], pa[3], 16), __builtin_amdgcn_alignbit(pb[1], pb[0], 16),
                      __builtin_amdgcn_alignbit(pb[2], pb[1], 16), __builtin_amdgcn_alignbit(pb[3], pb[2], 16)};
    const u32x4 c2 = {pa[3], pb[0], pb[1], pb[2]};
    const u32x4 c3 = {__builtin_amdgcn_alignbit(pa[3], pa[2], 16), c1[0], c1[1], c1[2]};
    *(u32x4*)(cb + 1 * UCP + 32 + 16 * tc) = c1;
    *(u32x4*)(cb + 2 * UCP + 32 + 16 * tc) = c2;
    *(u32x4*)(cb + 3 * UCP + 32 + 16 * tc) = c3;
    u32x4 kh, kl;
#pragma unroll
    for (int e = 0; e < 4; ++e) {
      const unsigned t0 = *(const unsigned*)(cb + OFF_TAB + (lut[e] & 0xffffu));
      const unsigned t1 = *(const unsigned*)(cb + OFF_TAB + (lut[e] >> 16));
      kh[e] = __builtin_amdgcn_perm(t1, t0, 0x05040100u);
      kl[e] = __builtin_amdgcn_perm(t1, t0, 0x07060302u);
    }
    *(u32x4*)(cb + OFF_K + 480 + 16 * tc) = kh;
  }
  __syncthreads();
  __builtin_amdgcn_s_setprio(1);
  f32x4 acc[8];
#pragma unroll
  for (int t = 0; t < 8; ++t) acc[t] = f32x4{0.f, 0.f, 0.f, 0.f};
#pragma unroll
  for (int rr = 0; rr < 2; ++rr) {
    const int rho = 2 * wq + rr;
    const int abase = q * UCP + 2 * (16 + 16 + 32 * rho - 8 * g + 4 * p);
    const int bbase = OFF_K + 2 * (240 - 16 - 32 * rho + 16 * r + 8 * g);
    bf16x8 af[8];
#pragma unroll
    for (int s = 0; s < 8; ++s) af[s] = tr_frag(cb, abase + 512 * s, abase + 512 * s - 8);
#pragma unroll
    for (int t = 0; t < 8; ++t) {
      const bf16x8 bh = *(const bf16x8*)(cb + bbase + 512 * t);
#pragma unroll
      for (int s = 0; s + t < 8; ++s) acc[s + t] = mfma16(af[s], bh, acc[s + t]);
    }
  }
  __builtin_amdgcn_s_setprio(3);
  __syncthreads();
  if (tid == 256) __hip_atomic_store((gu32*)(aG + (size_t)D * L + (d0 >> 1)), 1u, __ATOMIC_RELAXED, __HIP_MEMORY_SCOPE_AGENT);
#pragma unroll
  for (int t = 0; t < 8; ++t)
    if ((t >> 1) != wq) *(f32x4*)(cb + OFF_RED + (wq * 8 + t) * 1024 + lane * 16) = acc[t];
  __syncthreads();
#pragma unroll
  for (int tt = 0; tt < 2; ++tt) {
    const int t = 2 * wq + tt;
    f32x4 own = f32x4{0.f, 0.f, 0.f, 0.f};
#pragma unroll
    for (int t2 = 0; t2 < 8; ++t2)
      if (t2 == t) own = acc[t2];
    f32x4 s = f32x4{0.f, 0.f, 0.f, 0.f};
#pragma unroll
    for (int sw = 0; sw < 4; ++sw) {
      const f32x4 o = *(const f32x4*)(cb + OFF_RED + (sw * 8 + t) * 1024 + lane * 16);
      s += (sw == wq) ? own : o;
    }
    const int l = 256 * t + 16 * r + 4 * g;
    const u32x2 uu = *(const u32x2*)(cb + 32 + 2 * l);
    const float a0 = bfr(s[0]) + bfr(lo_f(uu[0]) * dsv), a1 = bfr(s[1]) + bfr(hi_f(uu[0]) * dsv);
    const float a2 = bfr(s[2]) + bfr(lo_f(uu[1]) * dsv), a3 = bfr(s[3]) + bfr(hi_f(uu[1]) * dsv);
    const u32x4 gr = {pk2(a0, a1), A_TAG, pk2(a2, a3), A_TAG};
    asm volatile("global_store_dwordx4 %0, %1, off sc1\n\ts_nop 1" ::"v"(aG + (size_t)d * L + l), "v"(gr) : "memory");
  }
}

constexpr int F2_NS = 5;
constexpr int F2_STAGE = 16384;
constexpr int F2_NST = 22;

struct F2Ctx {
  const __bf16 *h, *wd;
  unsigned avoff, bvoff, bvoff_last, lbase;
  int w, kh;
  int aoff[2], boff[2];
  const unsigned* asrc;
  mutable u32x4 x0, x1;
};
__device__ __forceinline__ void f2_stage(const F2Ctx& c, int st) {
  const unsigned base = c.lbase + (st % F2_NS) * F2_STAGE;
  glds16(c.h + st * 64, c.avoff, base + c.w * 1024);
  glds16(c.wd + (size_t)st * 64 * D, st == F2_NST - 1 ? c.bvoff_last : c.bvoff, base + 8192 + c.w * 1024);
}
template <int ST>
__device__ __forceinline__ void f2_loop(const F2Ctx& c, const char* smem, bf16x8 (&ao)[2], bf16x8 (&bo)[2],
                                        f32x4 (&acc)[2][2]) {
  if constexpr (ST < F2_NST) {
    constexpr int rem = (F2_NST - 1 - ST) < (F2_NS - 2) ? (F2_NST - 1 - ST) : (F2_NS - 2);
    wait_vm<2 * rem>();
    WAIT_LGKM0();
    __builtin_amdgcn_s_barrier();
    asm volatile("" ::: "memory");
    if constexpr (ST + F2_NS - 1 < F2_NST) f2_stage(c, ST + F2_NS - 1);
    if constexpr (ST == F2_NST - 3)
      asm volatile("global_load_dwordx4 %0, %2, off sc1\n\tglobal_load_dwordx4 %1, %3, off sc1"
                   : "=&v"(c.x0), "=&v"(c.x1) : "v"(c.asrc), "v"(c.asrc + 16 * L) : "memory");
    const char* base = smem + (ST % F2_NS) * F2_STAGE;
    bf16x8 an[2], bn[2];
#pragma unroll
    for (int mt = 0; mt < 2; ++mt) an[mt] = *(const bf16x8*)(base + c.aoff[mt]);
#pragma unroll
    for (int nt = 0; nt < 2; ++nt) bn[nt] = tr_frag(base, c.boff[nt], c.boff[nt] + 512);
    __builtin_amdgcn_sched_barrier(0);
    if constexpr (ST > 0) {
#pragma unroll
      for (int mt = 0; mt < 2; ++mt)
#pragma unroll
        for (int nt = 0; nt < 2; ++nt) acc[mt][nt] = mfma16(ao[mt], bo[nt], acc[mt][nt]);
    }
    __builtin_amdgcn_sched_barrier(0);
    f2_loop<ST + 1>(c, smem, an, bn, acc);
  } else {
    if (c.kh == 0) {
#pragma unroll
      for (int mt = 0; mt < 2; ++mt)
#pragma unroll
        for (int nt = 0; nt < 2; ++nt) acc[mt][nt] = mfma16(ao[mt], bo[nt], acc[mt][nt]);
    }
  }
}

__device__ __forceinline__ void ffn2_block(char* smem, int row0, int cbk, const __bf16* __restrict__ h,
                                           const __bf16* __restrict__ wd, const unsigned* __restrict__ aG,
                                           const float* __restrict__ gin, __bf16* __restrict__ out) {
  const int col0 = 64 * cbk;
  const int tid = threadIdx.x, lane = tid & 63, w = __builtin_amdgcn_readfirstlane(tid >> 6);
  const int r = lane & 15, g = lane >> 4, q = (lane >> 2) & 3, p = lane & 3;
  const int kh = w >> 2, wm = (w >> 1) & 1, wn = w & 1;
  __builtin_amdgcn_s_setprio(1);
  F2Ctx c;
  c.h = h; c.wd = wd; c.w = w; c.kh = kh; c.lbase = lds_addr_of(smem);
  {
    const int row = 8 * w + (lane >> 3), ch = (lane & 7) ^ ((row >> 1) & 7);
    c.avoff = (unsigned)(((row0 + row) * HP + ch * 8) * 2);
    const int bk = 8 * w + (lane >> 3);
    const int pos = lane & 7, fp = ((bk >> 1) & 1) | (((bk >> 3) & 1) << 1);
    const int cc = col0 + 16 * ((pos >> 1) ^ fp) + 8 * (pos & 1);
    c.bvoff = (unsigned)((bk * D + cc) * 2);
    c.bvoff_last = (unsigned)(((bk > 31 ? 31 : bk) * D + cc) * 2);
  }
#pragma unroll
  for (int mt = 0; mt < 2; ++mt) {
    const int row = 32 * wm + 16 * mt + r, ch = 4 * kh + g;
    c.aoff[mt] = row * 128 + ((ch ^ ((row >> 1) & 7)) << 4);
  }
  const int fpl = (q >> 1) | ((g & 1) << 1);
#pragma unroll
  for (int nt = 0; nt < 2; ++nt) c.boff[nt] = 8192 + (32 * kh + 8 * g + q) * 128 + (((2 * wn + nt) ^ fpl) << 5) + 8 * p;

  f32x4 acc[2][2];
#pragma unroll
  for (int mt = 0; mt < 2; ++mt)
#pragma unroll
    for (int nt = 0; nt < 2; ++nt) acc[mt][nt] = f32x4{0.f, 0.f, 0.f, 0.f};

#pragma unroll
  for (int st = 0; st < F2_NS - 1; ++st) f2_stage(c, st);
  const int lr = 32 * wm + 16 * kh + 4 * g;
  const f32x4 gv = *(const f32x4*)(gin + row0 + lr);
  c.asrc = aG + (size_t)(col0 + 32 * wn + r) * L + row0 + lr;
  bf16x8 a0[2], b0[2];
  f2_loop<0>(c, smem, a0, b0, acc);
  WAIT_LGKM0();
  __builtin_amdgcn_s_barrier();
  asm volatile("" ::: "memory");
  asm volatile("s_waitcnt vmcnt(0)" : "+v"(c.x0), "+v"(c.x1)::"memory");
  const bool early = __all(c.x0[1] == A_TAG && c.x0[3] == A_TAG && c.x1[1] == A_TAG && c.x1[3] == A_TAG);
  if (w == 0 && !early) {
    const gu32* fl = (const gu32*)(aG + (size_t)D * L + 32 * cbk + (lane & 31));
    for (int spin = 0; spin < (1 << 22); ++spin) {
      const unsigned v = __hip_atomic_load(fl, __ATOMIC_RELAXED, __HIP_MEMORY_SCOPE_AGENT);
      if (__all(v == 1u)) break;
      __builtin_amdgcn_s_sleep(2);
    }
  }
  {
    char* red = smem + (w & 3) * 4096 + kh * 2048;
    const f32x4 o0 = kh ? acc[0][0] : acc[1][0], o1 = kh ? acc[0][1] : acc[1][1];
    *(f32x4*)(red + lane * 16) = o0;
    *(f32x4*)(red + 1024 + lane * 16) = o1;
  }
  __syncthreads();
  unsigned long long av[2];
  {
    const unsigned* src = c.asrc;
    av[0] = (unsigned long long)c.x0[0] | ((unsigned long long)c.x0[2] << 32);
    av[1] = (unsigned long long)c.x1[0] | ((unsigned long long)c.x1[2] << 32);
    for (int spin = 0; !early && spin < (1 << 22); ++spin) {
      u32x4 x0, x1;
      asm volatile("global_load_dwordx4 %0, %2, off sc1\n\tglobal_load_dwordx4 %1, %3, off sc1\n\ts_waitcnt vmcnt(0)"
                   : "=&v"(x0), "=&v"(x1) : "v"(src), "v"(src + 16 * L) : "memory");
      av[0] = (unsigned long long)x0[0] | ((unsigned long long)x0[2] << 32);
      av[1] = (unsigned long long)x1[0] | ((unsigned long long)x1[2] << 32);
      const bool ok = x0[1] == A_TAG && x0[3] == A_TAG && x1[1] == A_TAG && x1[3] == A_TAG;
      if (__all(ok)) break;
      __builtin_amdgcn_s_sleep(4);
    }
  }
  constexpr int OT = 16384, OTP = 144;
  {
    const char* red = smem + (w & 3) * 4096 + (1 - kh) * 2048;
#pragma unroll
    for (int nt = 0; nt < 2; ++nt) {
      const f32x4 o = *(const f32x4*)(red + nt * 1024 + lane * 16);
      const f32x4 own = kh ? acc[1][nt] : acc[0][nt];
      const f32x4 s = kh ? (o + own) : (own + o);
      const unsigned alo = (unsigned)av[nt], ahi = (unsigned)(av[nt] >> 32);
      const float a[4] = {lo_f(alo), hi_f(alo), lo_f(ahi), hi_f(ahi)};
#pragma unroll
      for (int e = 0; e < 4; ++e) {
        const float o2 = bfr(gv[e] * a[e]) + bfr(bfr(1.f - gv[e]) * bfr(s[e]));
        *(__bf16*)(smem + OT + (lr + e) * OTP + (32 * wn + 16 * nt + r) * 2) = (__bf16)o2;
      }
    }
  }
  __syncthreads();
  {
    const int row = tid >> 3, c8 = tid & 7;
    const u32x4 v = *(const u32x4*)(smem + OT + row * OTP + c8 * 16);
    *(u32x4*)(out + (size_t)(row0 + row) * D + col0 + c8 * 8) = v;
  }
}

NOPK __global__ __launch_bounds__(512, 4) void k2(const __bf16* __restrict__ h, const __bf16* __restrict__ wd,
                                             const __bf16* __restrict__ uT, const unsigned* __restrict__ kc,
                                             const __bf16* __restrict__ dsk, unsigned* __restrict__ aG,
                                             const float* __restrict__ gin, __bf16* __restrict__ out) {
  extern __shared__ __attribute__((aligned(16))) char smem[];
  const int b = blockIdx.x, xcd = b & 7, i = (b & 255) >> 3;
  if (b < 256) {
    const int pair = 32 * xcd + i;
    conv_block(smem, 2 * pair, uT, kc, dsk, aG);
  } else {
    ffn2_block(smem, 64 * (4 * xcd + (i & 3)), i >> 2, h, wd, aG, gin, out);
  }
}

extern "C" void kernel_launch(void* const* d_in, const int* in_sizes, int n_in, void* d_out, int out_size,
                              void* d_ws, size_t ws_size, hipStream_t stream) {
  const __bf16* u = (const __bf16*)d_in[0];
  const __bf16* lar = (const __bf16*)d_in[1];
  const __bf16* aim = (const __bf16*)d_in[2];
  const __bf16* cre = (const __bf16*)d_in[3];
  const __bf16* cim = (const __bf16*)d_in[4];
  const __bf16* ldt = (const __bf16*)d_in[5];
  const __bf16* dsk = (const __bf16*)d_in[6];
  const __bf16* wg = (const __bf16*)d_in[8];
  const __bf16* wu = (const __bf16*)d_in[9];
  const __bf16* wd = (const __bf16*)d_in[10];
  const __bf16* gw = (const __bf16*)d_in[11];
  char* ws = (char*)d_ws;
  __bf16* h = (__bf16*)ws;
  ws += (size_t)L * HP * 2;
  unsigned* aG = (unsigned*)ws;
  ws += (size_t)D * L * 4 + 1024;
  __bf16* uT = (__bf16*)ws;
  ws += (size_t)D * L * 2;
  unsigned* kc = (unsigned*)ws;
  ws += (size_t)D * KC_PITCH * 4;
  float* gout = (float*)ws;
  (void)hipFuncSetAttribute((const void*)k1, hipFuncAttributeMaxDynamicSharedMemorySize, LDS_BYTES);
  (void)hipFuncSetAttribute((const void*)k2, hipFuncAttributeMaxDynamicSharedMemorySize, LDS_BYTES);
  hipLaunchKernelGGL(k1, dim3(512), dim3(512), LDS_BYTES, stream, u, lar, aim, cre, cim, ldt, wg, wu, gw, h, uT, kc,
                     gout, aG, wd, dsk);
  hipLaunchKernelGGL(k2, dim3(512), dim3(512), LDS_BYTES, stream, (const __bf16*)h, wd, (const __bf16*)uT,
                     (const unsigned*)kc, dsk, aG, (const float*)gout, (__bf16*)d_out);
}
```

```cpp
#include <hip/hip_runtime.h>
#include <hip/hip_bf16.h>
#include <type_traits>

typedef __attribute__((ext_vector_type(8))) __bf16 bf16x8;
typedef __attribute__((ext_vector_type(2))) __bf16 bf16x2;
typedef __attribute__((ext_vector_type(4))) short s16x4;
typedef __attribute__((ext_vector_type(8))) short s16x8;
typedef __attribute__((ext_vector_type(4))) float f32x4;
typedef __attribute__((ext_vector_type(4))) unsigned u32x4;
typedef __attribute__((ext_vector_type(2))) unsigned u32x2;
typedef __attribute__((address_space(3))) s16x4* lds_s16x4_ptr;
typedef __attribute__((address_space(1))) unsigned gu32;
typedef __attribute__((address_space(1))) unsigned long long gu64;

#define K_LO 0

#define NOPK __attribute__((target("no-packed-fp32-ops")))

constexpr int L = 2048, D = 512, NS = 128, F = 1376, HP = 1408;
constexpr int LDS_BYTES = 81920;

__device__ __forceinline__ float bf2f(unsigned short v) { return __uint_as_float((unsigned)v << 16); }
__device__ __forceinline__ float bfr(float x) { return (float)(__bf16)x; }
__device__ __forceinline__ unsigned pk2(float a, float b) {
  bf16x2 v = {(__bf16)a, (__bf16)b};
  return __builtin_bit_cast(unsigned, v);
}
__device__ __forceinline__ float lo_f(unsigned w) { return __uint_as_float(w << 16); }
__device__ __forceinline__ float hi_f(unsigned w) { return __uint_as_float(w & 0xffff0000u); }

__device__ __forceinline__ f32x4 mfma16(bf16x8 a, bf16x8 b, f32x4 c) {
  return __builtin_amdgcn_mfma_f32_16x16x32_bf16(a, b, c, 0, 0, 0);
}
__device__ __forceinline__ bf16x8 tr_frag(const char* lds, int off0, int off1) {
  s16x4 a = __builtin_amdgcn_ds_read_tr16_b64_v4i16((lds_s16x4_ptr)(lds + off0));
  s16x4 b = __builtin_amdgcn_ds_read_tr16_b64_v4i16((lds_s16x4_ptr)(lds + off1));
  s16x8 v = {a[0], a[1], a[2], a[3], b[0], b[1], b[2], b[3]};
  return __builtin_bit_cast(bf16x8, v);
}
__device__ __forceinline__ void glds16(const void* sbase, unsigned voff, unsigned lds_addr) {
  asm volatile("s_mov_b32 m0, %2\n\ts_nop 0\n\tglobal_load_lds_dwordx4 %0, %1" ::"v"(voff), "s"(sbase), "s"(lds_addr)
               : "memory", "m0");
}
__device__ __forceinline__ void glds16v(const void* vptr, unsigned lds_addr) {
  asm volatile("s_mov_b32 m0, %1\n\ts_nop 0\n\tglobal_load_lds_dwordx4 %0, off" ::"v"(vptr), "s"(lds_addr)
               : "memory", "m0");
}
__device__ __forceinline__ unsigned lds_addr_of(const char* p) {
  return (unsigned)(uintptr_t)(const __attribute__((address_space(3))) char*)p;
}
#define WAIT_VM(n) asm volatile("s_waitcnt vmcnt(" #n ")" ::: "memory")
#define WAIT_LGKM0() asm volatile("s_waitcnt lgkmcnt(0)" ::: "memory")
template <int N>
__device__ __forceinline__ void wait_vm() {
  asm volatile("s_waitcnt vmcnt(%0)" ::"i"(N) : "memory");
}

constexpr int F1_B = 32768;

struct F1Ctx {
  const __bf16 *x, *wg, *wu, *wmix;
  unsigned voffA[2], voffB, lbase;
  int w;
  int aoff[2], boff[3];
};
__device__ __forceinline__ void f1_stage_a(const F1Ctx& c, int m) {
  const unsigned ab = c.lbase + (m & 1) * 16384 + c.w * 1024;
  glds16(c.x + m * 64, c.voffA[0], ab);
  glds16(c.x + m * 64, c.voffA[1], ab + 8192);
}
template <bool G2>
__device__ __forceinline__ void f1_stage_b(const F1Ctx& c, int hs) {
  const unsigned bb = c.lbase + F1_B + (hs & 3) * 12288 + c.w * 1024;
  if constexpr (!G2) {
    glds16(c.wg + (size_t)hs * 32 * F, c.voffB, bb);
    glds16(c.wu + (size_t)hs * 32 * F, c.voffB, bb + 8192);
  } else {
    glds16v(c.wmix + (size_t)hs * 32 * F, bb);
  }
}
constexpr int f1_allowed(int hs, int nb) {
  int n = 0;
  if ((hs & 1) == 0 && hs >= 2) {
    if (hs + 1 < 16) n += nb;
    if (hs + 2 < 16) n += nb;
  } else {
    if (hs + 1 < 16) n += nb;
    if (hs >= 1 && ((hs - 1) & 1) == 0 && (hs + 1) / 2 < 8) n += 2;
    if (hs + 2 < 16) n += nb;
  }
  return n;
}
template <int HS, bool G2>
__device__ __forceinline__ void f1_loop(const F1Ctx& c, const char* smem, bf16x8 (&af)[4], bf16x8 (&bq)[3],
                                        f32x4 (&acc)[4][3]) {
  if constexpr (HS < 16) {
    if constexpr (HS > 0) {
#pragma unroll
      for (int j = 0; j < 3; ++j) {
        acc[0][j] = mfma16(bq[j], af[0], acc[0][j]);
        acc[1][j] = mfma16(bq[j], af[1], acc[1][j]);
      }
    }
    __builtin_amdgcn_sched_barrier(0);
    wait_vm<f1_allowed(HS, G2 ? 1 : 2)>();
    WAIT_LGKM0();
    __builtin_amdgcn_s_barrier();
    asm volatile("" ::: "memory");
    const char* ab = smem + ((HS >> 1) & 1) * 16384 + c.aoff[HS & 1];
    const char* bbase = smem + (HS & 3) * 12288;
    __builtin_amdgcn_sched_barrier(0);
    bf16x8 bn[3];
    af[0] = *(const bf16x8*)(ab);
    bn[0] = tr_frag(bbase, c.boff[0], c.boff[0] + 512);
    af[1] = *(const bf16x8*)(ab + 2048);
    bn[1] = tr_frag(bbase, c.boff[1], c.boff[1] + 512);
    bn[2] = tr_frag(bbase, c.boff[2], c.boff[2] + 512);
    __builtin_amdgcn_sched_barrier(0);
#pragma unroll
    for (int j = 0; j < 3; ++j) {
      if constexpr (HS > 0) {
        acc[2][j] = mfma16(bq[j], af[2], acc[2][j]);
        acc[3][j] = mfma16(bq[j], af[3], acc[3][j]);
      }
      __builtin_amdgcn_sched_barrier(0);
      if (j == 0) {
        if constexpr ((HS & 1) == 0 && (HS + 2) / 2 < 8) f1_stage_a(c, (HS + 2) / 2);
      } else if (j == 1) {
        if constexpr (HS + 3 < 16) f1_stage_b<G2>(c, HS + 3);
      }
      __builtin_amdgcn_sched_barrier(0);
    }
    af[2] = *(const bf16x8*)(ab + 4096);
    af[3] = *(const bf16x8*)(ab + 6144);
    f1_loop<HS + 1, G2>(c, smem, af, bn, acc);
  } else {
#pragma unroll
    for (int j = 0; j < 3; ++j)
#pragma unroll
      for (int mt = 0; mt < 4; ++mt) acc[mt][j] = mfma16(bq[j], af[mt], acc[mt][j]);
  }
}

__device__ __forceinline__ void ffn1_block(char* smem, int rb, int cb, const __bf16* __restrict__ x,
                                           const __bf16* __restrict__ wg, const __bf16* __restrict__ wu,
                                           __bf16* __restrict__ h) {
  const int tid = threadIdx.x, lane = tid & 63, w = __builtin_amdgcn_readfirstlane(tid >> 6);
  const int row0 = rb * 128, f0 = cb * 96;
  const int wm = w >> 2, sel = (w >> 1) & 1, wn = w & 1;
  const int r = lane & 15, g = lane >> 4, q = (lane >> 2) & 3, p = lane & 3;
  F1Ctx c;
  c.x = x; c.wg = wg; c.wu = wu; c.w = w; c.lbase = lds_addr_of(smem);
  {
#pragma unroll
    for (int i = 0; i < 2; ++i) {
      const int row = 64 * i + 8 * w + (lane >> 3), ch = (lane & 7) ^ ((row >> 1) & 7);
      c.voffA[i] = (unsigned)(((row0 + row) * D + ch * 8) * 2);
    }
    const int kk = 8 * (w & 3) + (lane >> 3), pos = lane & 7;
    const int fp = ((kk >> 1) & 1) | (((kk >> 3) & 1) << 1);
    const int i = (pos >> 1) ^ fp;
    const int par = (kk + cb) & 1;
    int c0 = f0 + 16 * (i + 2 * par) + 8 * (pos & 1);
    int c1 = f0 + 16 * ((i & 1) + 4 * (1 - par)) + 8 * (pos & 1);
    c0 = c0 > F - 8 ? F - 8 : c0;
    c1 = c1 > F - 8 ? F - 8 : c1;
    c.voffB = (unsigned)((kk * F + c0) * 2);
    c.wmix = (i < 2 ? wg : wu) + kk * F + c1;
  }
#pragma unroll
  for (int ks = 0; ks < 2; ++ks)
    c.aoff[ks] = (64 * wm + r) * 128 + (((4 * ks + g) ^ ((r >> 1) & 7)) << 4);
  {
    const int fp = (q >> 1) | ((g & 1) << 1);
#pragma unroll
    for (int j = 0; j < 3; ++j) {
      const int t = 3 * wn + j, par = (q + cb) & 1, tt = t - 2 * par;
      const bool whole = tt >= 0 && tt < 4;
      const int sub = whole ? 2 * sel : 1, ps = whole ? tt : 2 * sel + (t & 1);
      c.boff[j] = F1_B + sub * 4096 + (8 * g + q) * 128 + ((ps ^ fp) << 5) + 8 * p;
      asm volatile("" : "+v"(c.boff[j]));
    }
  }
  f32x4 acc[4][3];
#pragma unroll
  for (int mt = 0; mt < 4; ++mt)
#pragma unroll
    for (int j = 0; j < 3; ++j) acc[mt][j] = f32x4{0.f, 0.f, 0.f, 0.f};
  bf16x8 af[4], bq[3];

  f1_stage_a(c, 0);
  if (w < 4) {
    f1_stage_b<false>(c, 0);
    f1_stage_b<false>(c, 1);
    f1_stage_b<false>(c, 2);
    f1_loop<0, false>(c, smem, af, bq, acc);
  } else {
    f1_stage_b<true>(c, 0);
    f1_stage_b<true>(c, 1);
    f1_stage_b<true>(c, 2);
    f1_loop<0, true>(c, smem, af, bq, acc);
  }

  __syncthreads();
  constexpr int HT = 49152, HTP = 208;
  auto finish = [&](auto SEL) {
    constexpr int S = decltype(SEL)::value;
#pragma unroll
    for (int m2 = 0; m2 < 2; ++m2)
#pragma unroll
      for (int j = 0; j < 3; ++j) *(f32x4*)(smem + (w * 6 + m2 * 3 + j) * 1024 + lane * 16) = acc[2 * (1 - S) + m2][j];
    __syncthreads();
#pragma unroll
    for (int m2 = 0; m2 < 2; ++m2)
#pragma unroll
      for (int j = 0; j < 3; ++j) {
        const f32x4 o4 = *(const f32x4*)(smem + ((w ^ 2) * 6 + m2 * 3 + j) * 1024 + lane * 16);
        const f32x4 g4 = S ? o4 : acc[m2][j], u4 = S ? acc[2 + m2][j] : o4;
        float hv[4];
#pragma unroll
        for (int e = 0; e < 4; ++e) {
          const float gv = g4[e];
          const float t = gv * __builtin_fmaf(gv * gv, -0.10294324f, -2.3022082f);
          hv[e] = (gv * u4[e]) * __builtin_amdgcn_rcpf(1.f + __builtin_amdgcn_exp2f(t));
        }
        u32x2 o = {pk2(hv[0], hv[1]), pk2(hv[2], hv[3])};
        *(u32x2*)(smem + HT + (64 * wm + 32 * S + 16 * m2 + r) * HTP + (16 * (3 * wn + j) + 4 * g) * 2) = o;
      }
  };
  if (sel) finish(std::integral_constant<int, 1>{});
  else finish(std::integral_constant<int, 0>{});
  __syncthreads();
#pragma unroll
  for (int i = 0; i < 3; ++i) {
    const int idx = tid + 512 * i, row = idx / 12, c8 = idx - row * 12;
    if (f0 + 8 * c8 < F)
      *(u32x4*)(h + (size_t)(row0 + row) * HP + f0 + 8 * c8) = *(const u32x4*)(smem + HT + row * HTP + c8 * 16);
  }
}

__device__ __forceinline__ void gate_block(char* smem, int rb, const __bf16* __restrict__ x,
                                           const __bf16* __restrict__ gw, float* __restrict__ gout,
                                           __bf16* __restrict__ uT) {
  const int tid = threadIdx.x, lane = tid & 63, w = __builtin_amdgcn_readfirstlane(tid >> 6);
  const int g = lane >> 4, q = (lane >> 2) & 3, p = lane & 3;
  const unsigned lbase = lds_addr_of(smem);
  const int grow = tid >> 3, gpart = tid & 7, gf = (grow & 3) | (((grow >> 3) & 1) << 2);
  u32x4 gwv[8];
#pragma unroll
  for (int i = 0; i < 8; ++i) gwv[i] = *(const u32x4*)(gw + ((gpart ^ gf) + 8 * (i >> 1)) * 16 + 8 * (i & 1));
#pragma unroll 1
  for (int pass = 0; pass < 2; ++pass) {
    const int l0 = rb * 128 + pass * 64;
#pragma unroll
    for (int i = 0; i < 8; ++i) {
      const int row = 8 * w + i, fr = (row & 3) | (((row >> 3) & 1) << 2);
      glds16(x + (size_t)(l0 + row) * D, (unsigned)((((lane >> 1) ^ fr) * 2 + (lane & 1)) * 16), lbase + row * 1024);
    }
    WAIT_VM(0);
    __syncthreads();
#pragma unroll
    for (int i = 0; i < 8; ++i) {
      const int unit = w * 8 + i, c16 = unit >> 1, half = unit & 1;
      const int ra = 32 * half + 8 * g + q, rb2 = ra + 4;
      const int fa = (ra & 3) | (((ra >> 3) & 1) << 2);
      const bf16x8 v = tr_frag(smem, ra * 1024 + ((c16 ^ fa) << 5) + 8 * p, rb2 * 1024 + ((c16 ^ fa) << 5) + 8 * p);
      *(bf16x8*)(uT + (size_t)(16 * c16 + (lane & 15)) * L + l0 + 32 * half + 8 * g) = v;
    }
    float s = 0.f, s2 = 0.f;
#pragma unroll
    for (int i = 0; i < 8; ++i) {
      const u32x4 xv = *(const u32x4*)(smem + grow * 1024 + (gpart + 8 * (i >> 1)) * 32 + 16 * (i & 1));
#pragma unroll
      for (int e = 0; e < 4; ++e) {
        const unsigned xe = xv[e], we = gwv[i][e];
        const unsigned pr = pk2(lo_f(xe) * lo_f(we), hi_f(xe) * hi_f(we));
        s += lo_f(pr);
        s2 += hi_f(pr);
      }
    }
    s += s2;
    s += __shfl_xor(s, 1);
    s += __shfl_xor(s, 2);
    s += __shfl_xor(s, 4);
    if (gpart == 0) gout[l0 + grow] = bfr(1.f / (1.f + __expf(-bfr(s))));
    __syncthreads();
  }
}

constexpr int KC_PITCH = 768;
constexpr int KG_PITCH = 160;
constexpr int KG_ARR = 16 * KG_PITCH;
constexpr int KG_TAB = 8 * 2 * KG_ARR;
typedef __attribute__((ext_vector_type(2))) float f32x2;

__device__ __forceinline__ f32x2 cexp(float zr, float zi, float p) {
  const float e = __builtin_amdgcn_exp2f(zr * p);
  const float ph = __builtin_amdgcn_fractf(zi * p);
  return f32x2{e * __builtin_amdgcn_cosf(ph), e * __builtin_amdgcn_sinf(ph)};
}
__device__ __forceinline__ f32x2 cmul(f32x2 a, f32x2 b) {
  return f32x2{a[0] * b[0] - a[1] * b[1], a[0] * b[1] + a[1] * b[0]};
}
__device__ __forceinline__ void kgen_block(char* smem, int d0, const __bf16* __restrict__ lar,
                                           const __bf16* __restrict__ aim, const __bf16* __restrict__ cre,
                                           const __bf16* __restrict__ cim, const __bf16* __restrict__ ldt,
                                           unsigned* __restrict__ kc, unsigned* __restrict__ aG,
                                           const __bf16* __restrict__ wd, const __bf16* __restrict__ x,
                                           const __bf16* __restrict__ wg, const __bf16* __restrict__ wu) {
  const int tid = threadIdx.x, lane = tid & 63, w = __builtin_amdgcn_readfirstlane(tid >> 6);
  const int ch = w >> 2, wq = w & 3, tc = tid & 255;
  const int r = lane & 15, g = lane >> 4, hh = lane >> 5;
  char* wb = smem + w * 2 * KG_ARR;
  char* tb = smem + KG_TAB + ch * (4 * 656 * 4);
  float zr, zi;
  f32x2 cc;
  unsigned pf2, pf_dummy;
  {
    const int d = d0 + ch, n = 32 * wq + (lane & 31);
    const int idx = (d0 >> 1) * 150 + (tid < 149 ? tid : 149);
    const __bf16* src = idx < 16384 ? x + (size_t)idx * 64
                                    : (idx < 27392 ? wg + (size_t)(idx - 16384) * 64 : wu + (size_t)(idx - 27392) * 64);
    unsigned p0, p1, p2, p3, p4;
    asm volatile(
        "global_load_ushort %0, %7, off\n\tglobal_load_ushort %1, %8, off\n\tglobal_load_ushort %2, %9, off\n\t"
        "global_load_ushort %3, %10, off\n\tglobal_load_ushort %4, %11, off\n\t"
        "global_load_dword %5, %12, off\n\tglobal_load_dword %6, %13, off\n\ts_waitcnt vmcnt(2)"
        : "=&v"(p0), "=&v"(p1), "=&v"(p2), "=&v"(p3), "=&v"(p4), "=&v"(pf2), "=&v"(pf_dummy)
        : "v"(ldt + d), "v"(lar + d * NS + n), "v"(aim + d * NS + n), "v"(cre + d * NS + n), "v"(cim + d * NS + n),
          "v"(src), "v"(wd + (size_t)((d0 >> 1) * 43 + (tid < 42 ? tid : 42)) * 64)
        : "memory");
    const float dt = bfr(expf(lo_f(p0)));
    const float ar = -bfr(expf(lo_f(p1)));
    zr = (ar * dt) * 1.4426950408889634f;
    zi = (lo_f(p2) * dt) * 0.15915494309189535f;
    cc = f32x2{2.f * (lo_f(p3) * dt), 2.f * (lo_f(p4) * dt)};
  }
  {
    u32x4* z = (u32x4*)(aG + (size_t)d0 * L) + tid;
    z[0] = u32x4{0u, 0u, 0u, 0u};
    z[512] = u32x4{0u, 0u, 0u, 0u};
    if (tid == 0) aG[(size_t)D * L + (d0 >> 1)] = 0u;
  }
  const float fh = (float)(1 + hh);
  const f32x2 e128 = cexp(zr, zi, 128.f), e24 = cexp(zr, zi, 2.f * fh);
  f32x2 v[4], st[4];
  v[0] = cexp(zr, -zi, 64.f * hh);                 st[0] = cexp(zr, -zi, 8.f);
  v[1] = hh ? cmul(cc, e128) : cc;                 st[1] = cexp(zr, zi, 1.f);
  v[2] = cmul(cc, cexp(zr, zi, 256.f * fh));       st[2] = hh ? e128 : e24;
  v[3] = cmul(cc, cexp(zr, zi, 1024.f * fh));      st[3] = e128;
  const f32x2 v24 = cmul(v[2], hh ? e24 : e128);
  unsigned vh[4][8], vl[4][8];
#pragma unroll
  for (int i = 0; i < 8; ++i)
#pragma unroll
    for (int t = 0; t < 4; ++t) {
      vh[t][i] = pk2(v[t][0], v[t][1]);
      vl[t][i] = pk2(v[t][0] - lo_f(vh[t][i]), v[t][1] - hi_f(vh[t][i]));
      if (t == 2 && i == 3) v[t] = v24;
      else if (i < 7) v[t] = cmul(v[t], st[t]);
    }
  char* wr = wb + (8 * hh) * KG_PITCH + (lane & 31) * 4;
  const char* rd = wb + r * KG_PITCH + g * 16;
  bf16x8 fh_[4][2], fl_[4][2];
#pragma unroll
  for (int t = 0; t < 4; ++t) {
    __builtin_amdgcn_wave_barrier();
#pragma unroll
    for (int i = 0; i < 8; ++i) {
      *(unsigned*)(wr + i * KG_PITCH) = vh[t][i];
      *(unsigned*)(wr + i * KG_PITCH + KG_ARR) = vl[t][i];
    }
    __builtin_amdgcn_wave_barrier();
#pragma unroll
    for (int ss = 0; ss < 2; ++ss) {
      fh_[t][ss] = *(const bf16x8*)(rd + ss * 64);
      fl_[t][ss] = *(const bf16x8*)(rd + ss * 64 + KG_ARR);
    }
  }
  f32x4 acc[3];
#pragma unroll
  for (int m = 0; m < 3; ++m) acc[m] = f32x4{0.f, 0.f, 0.f, 0.f};
#pragma unroll
  for (int ss = 0; ss < 2; ++ss)
#pragma unroll
    for (int m = 0; m < 3; ++m) {
      acc[m] = mfma16(fl_[1 + m][ss], fh_[0][ss], acc[m]);
      acc[m] = mfma16(fh_[1 + m][ss], fl_[0][ss], acc[m]);
      acc[m] = mfma16(fh_[1 + m][ss], fh_[0][ss], acc[m]);
    }
  {
    float* tab = (float*)tb + wq * 656;
    *(f32x4*)(tab + 128 * (g >> 1) + 4 * (g & 1) + 8 * r) = acc[0];
    if (g < 2) {
      *(f32x4*)(tab + 256 + 64 * g + 4 * r) = acc[1];
#pragma unroll
      for (int e = 0; e < 4; ++e) tab[512 + 64 * g + 16 * e + r] = acc[2][e];
    } else {
#pragma unroll
      for (int e = 0; e < 4; ++e) tab[384 + (g & 1) + 32 * e + 2 * r] = acc[1][e];
      if (lane == 32) tab[640] = acc[2][0];
    }
  }
  __syncthreads();
  {
    const float* tab = (const float*)tb;
    unsigned* kd = kc + (size_t)(d0 + ch) * KC_PITCH;
#pragma unroll
    for (int e = 0; e < 3; ++e) {
      const int id = tc + 256 * e;
      if (id < 641) {
        const float v = (tab[id] + tab[656 + id]) + (tab[2 * 656 + id] + tab[3 * 656 + id]);
        const float vh = bfr(v);
        kd[id] = pk2(vh, v - vh);
      }
    }
  }
  asm volatile("s_waitcnt vmcnt(0)" : "+v"(pf_dummy), "+v"(pf2)::"memory");
}

NOPK __global__ __launch_bounds__(512, 4) void k1(const __bf16* __restrict__ u, const __bf16* __restrict__ lar,
                                             const __bf16* __restrict__ aim, const __bf16* __restrict__ cre,
                                             const __bf16* __restrict__ cim, const __bf16* __restrict__ ldt,
                                             const __bf16* __restrict__ wg, const __bf16* __restrict__ wu,
                                             const __bf16* __restrict__ gw, __bf16* __restrict__ h,
                                             __bf16* __restrict__ uT, unsigned* __restrict__ kc,
                                             float* __restrict__ gout, unsigned* __restrict__ aG,
                                             const __bf16* __restrict__ wd) {
  extern __shared__ __attribute__((aligned(16))) char smem[];
  const int b = blockIdx.x;
  const int xcd = b & 7, i = (b & 255) >> 3;
  if (b < 256) {
    const int rb = 4 * (xcd >> 1) + (i & 3), cb = 8 * (xcd & 1) + (i >> 2);
    if (cb < 15) ffn1_block(smem, rb, cb, u, wg, wu, h);
    else gate_block(smem, rb, u, gw, gout, uT);
  } else {
    kgen_block(smem, 64 * xcd + 2 * i, lar, aim, cre, cim, ldt, kc, aG, wd, u, wg, wu);
  }
}

constexpr unsigned A_TAG = 0x5a17c3e9u;
constexpr int UCP = 4160;
constexpr int KARR = (240 + L) * 2;
constexpr int OFF_K = 4 * UCP;
constexpr int OFF_TAB = OFF_K + 2 * KARR;
constexpr int OFF_RED = UCP;
constexpr int CH_BYTES = OFF_RED + 32768;
static_assert(OFF_TAB + 3072 <= CH_BYTES && 2 * CH_BYTES <= LDS_BYTES, "lds");

struct PosLut {
  unsigned short v[L];
  constexpr PosLut() : v() {
    for (int j = 0; j < L; ++j) {
      const int sh = (j >= 256) + (j >= 512) + (j >= 1024);
      int q = j >> sh;
      if (sh) {
        const int rem = j & ((1 << sh) - 1), half = 1 << (sh - 1);
        if (rem > half || (rem == half && (q & 1))) ++q;
      }
      v[j] = (unsigned short)(4 * (128 * sh + q));
    }
  }
};
__device__ const PosLut g_poslut{};

__device__ __forceinline__ void conv_block(char* smem, int d0, const __bf16* __restrict__ uT,
                                           const unsigned* __restrict__ kc, const __bf16* __restrict__ dsk,
                                           unsigned* __restrict__ aG) {
  const int tid = threadIdx.x, lane = tid & 63, w = __builtin_amdgcn_readfirstlane(tid >> 6);
  const int ch = w >> 2, wq = w & 3, tc = tid & 255;
  const int r = lane & 15, g = lane >> 4, q = (lane >> 2) & 3, p = lane & 3;
  char* cb = smem + ch * CH_BYTES;
  const int d = d0 + ch;
  __builtin_amdgcn_s_setprio(3);
  {
    const unsigned lb = lds_addr_of(cb);
    glds16(uT + (size_t)d * L + wq * 512, lane * 16, lb + 32 + wq * 1024);
    if (wq < 3) glds16(kc + (size_t)d * KC_PITCH + wq * 256, lane * 16, lb + OFF_TAB + wq * 1024);
    const u32x4 z = {0u, 0u, 0u, 0u};
    if (tc < 60) *(u32x4*)(cb + OFF_K + (tc >= 30 ? KARR : 0) + (tc % 30) * 16) = z;
    else if (tc < 68) *(u32x4*)(cb + ((tc - 60) >> 1) * UCP + ((tc - 60) & 1) * 16) = z;
  }
  const float dsv = (float)dsk[d];
  const u32x4 lut = *(const u32x4*)(g_poslut.v + 8 * tc);
  WAIT_VM(0);
  __syncthreads();
  {
    const u32x4 pa = *(const u32x4*)(cb + 16 + 16 * tc), pb = *(const u32x4*)(cb + 32 + 16 * tc);
    const u32x4 c1 = {__builtin_amdgcn_alignbit(pb[0], pa[3], 16), __builtin_amdgcn_alignbit(pb[1], pb[0], 16),
                      __builtin_amdgcn_alignbit(pb[2], pb[1], 16), __builtin_amdgcn_alignbit(pb[3], pb[2], 16)};
    const u32x4 c2 = {pa[3], pb[0], pb[1], pb[2]};
    const u32x4 c3 = {__builtin_amdgcn_alignbit(pa[3], pa[2], 16), c1[0], c1[1], c1[2]};
    *(u32x4*)(cb + 1 * UCP + 32 + 16 * tc) = c1;
    *(u32x4*)(cb + 2 * UCP + 32 + 16 * tc) = c2;
    *(u32x4*)(cb + 3 * UCP + 32 + 16 * tc) = c3;
    u32x4 kh, kl;
#pragma unroll
    for (int e = 0; e < 4; ++e) {
      const unsigned t0 = *(const unsigned*)(cb + OFF_TAB + (lut[e] & 0xffffu));
      const unsigned t1 = *(const unsigned*)(cb + OFF_TAB + (lut[e] >> 16));
      kh[e] = __builtin_amdgcn_perm(t1, t0, 0x05040100u);
      kl[e] = __builtin_amdgcn_perm(t1, t0, 0x07060302u);
    }
    *(u32x4*)(cb + OFF_K + 480 + 16 * tc) = kh;
  }
  __syncthreads();
  __builtin_amdgcn_s_setprio(1);
  f32x4 acc[8];
#pragma unroll
  for (int t = 0; t < 8; ++t) acc[t] = f32x4{0.f, 0.f, 0.f, 0.f};
#pragma unroll
  for (int rr = 0; rr < 2; ++rr) {
    const int rho = 2 * wq + rr;
    const int abase = q * UCP + 2 * (16 + 16 + 32 * rho - 8 * g + 4 * p);
    const int bbase = OFF_K + 2 * (240 - 16 - 32 * rho + 16 * r + 8 * g);
    bf16x8 af[8];
#pragma unroll
    for (int s = 0; s < 8; ++s) af[s] = tr_frag(cb, abase + 512 * s, abase + 512 * s - 8);
#pragma unroll
    for (int t = 0; t < 8; ++t) {
      const bf16x8 bh = *(const bf16x8*)(cb + bbase + 512 * t);
#pragma unroll
      for (int s = 0; s + t < 8; ++s) acc[s + t] = mfma16(af[s], bh, acc[s + t]);
    }
  }
  __builtin_amdgcn_s_setprio(3);
  __syncthreads();
  if (tid == 256) __hip_atomic_store((gu32*)(aG + (size_t)D * L + (d0 >> 1)), 1u, __ATOMIC_RELAXED, __HIP_MEMORY_SCOPE_AGENT);
#pragma unroll
  for (int t = 0; t < 8; ++t)
    if ((t >> 1) != wq) *(f32x4*)(cb + OFF_RED + (wq * 8 + t) * 1024 + lane * 16) = acc[t];
  __syncthreads();
#pragma unroll
  for (int tt = 0; tt < 2; ++tt) {
    const int t = 2 * wq + tt;
    f32x4 own = f32x4{0.f, 0.f, 0.f, 0.f};
#pragma unroll
    for (int t2 = 0; t2 < 8; ++t2)
      if (t2 == t) own = acc[t2];
    f32x4 s = f32x4{0.f, 0.f, 0.f, 0.f};
#pragma unroll
    for (int sw = 0; sw < 4; ++sw) {
      const f32x4 o = *(const f32x4*)(cb + OFF_RED + (sw * 8 + t) * 1024 + lane * 16);
      s += (sw == wq) ? own : o;
    }
    const int l = 256 * t + 16 * r + 4 * g;
    const u32x2 uu = *(const u32x2*)(cb + 32 + 2 * l);
    const float a0 = bfr(s[0]) + bfr(lo_f(uu[0]) * dsv), a1 = bfr(s[1]) + bfr(hi_f(uu[0]) * dsv);
    const float a2 = bfr(s[2]) + bfr(lo_f(uu[1]) * dsv), a3 = bfr(s[3]) + bfr(hi_f(uu[1]) * dsv);
    const u32x4 gr = {pk2(a0, a1), A_TAG, pk2(a2, a3), A_TAG};
    asm volatile("global_store_dwordx4 %0, %1, off sc1\n\ts_nop 1" ::"v"(aG + (size_t)d * L + l), "v"(gr) : "memory");
  }
}

constexpr int F2_NS = 5;
constexpr int F2_STAGE = 16384;
constexpr int F2_NST = 22;

struct F2Ctx {
  const __bf16 *h, *wd;
  unsigned avoff, bvoff, bvoff_last, lbase;
  int w, kh;
  int aoff[2], boff[2];
  const unsigned* asrc;
  mutable u32x4 x0, x1;
};
__device__ __forceinline__ void f2_stage(const F2Ctx& c, int st) {
  const unsigned base = c.lbase + (st % F2_NS) * F2_STAGE;
  glds16(c.h + st * 64, c.avoff, base + c.w * 1024);
  glds16(c.wd + (size_t)st * 64 * D, st == F2_NST - 1 ? c.bvoff_last : c.bvoff, base + 8192 + c.w * 1024);
}
template <int ST>
__device__ __forceinline__ void f2_loop(const F2Ctx& c, const char* smem, bf16x8 (&ao)[2], bf16x8 (&bo)[2],
                                        f32x4 (&acc)[2][2]) {
  if constexpr (ST < F2_NST) {
    constexpr int rem = (F2_NST - 1 - ST) < (F2_NS - 2) ? (F2_NST - 1 - ST) : (F2_NS - 2);
    wait_vm<2 * rem>();
    WAIT_LGKM0();
    __builtin_amdgcn_s_barrier();
    asm volatile("" ::: "memory");
    if constexpr (ST + F2_NS - 1 < F2_NST) f2_stage(c, ST + F2_NS - 1);
    if constexpr (ST == F2_NST - 3)
      asm volatile("global_load_dwordx4 %0, %2, off sc1\n\tglobal_load_dwordx4 %1, %3, off sc1"
                   : "=&v"(c.x0), "=&v"(c.x1) : "v"(c.asrc), "v"(c.asrc + 16 * L) : "memory");
    const char* base = smem + (ST % F2_NS) * F2_STAGE;
    bf16x8 an[2], bn[2];
#pragma unroll
    for (int mt = 0; mt < 2; ++mt) an[mt] = *(const bf16x8*)(base + c.aoff[mt]);
#pragma unroll
    for (int nt = 0; nt < 2; ++nt) bn[nt] = tr_frag(base, c.boff[nt], c.boff[nt] + 512);
    __builtin_amdgcn_sched_barrier(0);
    if constexpr (ST > 0) {
#pragma unroll
      for (int mt = 0; mt < 2; ++mt)
#pragma unroll
        for (int nt = 0; nt < 2; ++nt) acc[mt][nt] = mfma16(ao[mt], bo[nt], acc[mt][nt]);
    }
    __builtin_amdgcn_sched_barrier(0);
    f2_loop<ST + 1>(c, smem, an, bn, acc);
  } else {
    if (c.kh == 0) {
#pragma unroll
      for (int mt = 0; mt < 2; ++mt)
#pragma unroll
        for (int nt = 0; nt < 2; ++nt) acc[mt][nt] = mfma16(ao[mt], bo[nt], acc[mt][nt]);
    }
  }
}

__device__ __forceinline__ void ffn2_block(char* smem, int row0, int cbk, const __bf16* __restrict__ h,
                                           const __bf16* __restrict__ wd, const unsigned* __restrict__ aG,
                                           const float* __restrict__ gin, __bf16* __restrict__ out) {
  const int col0 = 64 * cbk;
  const int tid = threadIdx.x, lane = tid & 63, w = __builtin_amdgcn_readfirstlane(tid >> 6);
  const int r = lane & 15, g = lane >> 4, q = (lane >> 2) & 3, p = lane & 3;
  const int kh = w >> 2, wm = (w >> 1) & 1, wn = w & 1;
  __builtin_amdgcn_s_setprio(1);
  F2Ctx c;
  c.h = h; c.wd = wd; c.w = w; c.kh = kh; c.lbase = lds_addr_of(smem);
  {
    const int row = 8 * w + (lane >> 3), ch = (lane & 7) ^ ((row >> 1) & 7);
    c.avoff = (unsigned)(((row0 + row) * HP + ch * 8) * 2);
    const int bk = 8 * w + (lane >> 3);
    const int pos = lane & 7, fp = ((bk >> 1) & 1) | (((bk >> 3) & 1) << 1);
    const int cc = col0 + 16 * ((pos >> 1) ^ fp) + 8 * (pos & 1);
    c.bvoff = (unsigned)((bk * D + cc) * 2);
    c.bvoff_last = (unsigned)(((bk > 31 ? 31 : bk) * D + cc) * 2);
  }
#pragma unroll
  for (int mt = 0; mt < 2; ++mt) {
    const int row = 32 * wm + 16 * mt + r, ch = 4 * kh + g;
    c.aoff[mt] = row * 128 + ((ch ^ ((row >> 1) & 7)) << 4);
  }
  const int fpl = (q >> 1) | ((g & 1) << 1);
#pragma unroll
  for (int nt = 0; nt < 2; ++nt) c.boff[nt] = 8192 + (32 * kh + 8 * g + q) * 128 + (((2 * wn + nt) ^ fpl) << 5) + 8 * p;

  f32x4 acc[2][2];
#pragma unroll
  for (int mt = 0; mt < 2; ++mt)
#pragma unroll
    for (int nt = 0; nt < 2; ++nt) acc[mt][nt] = f32x4{0.f, 0.f, 0.f, 0.f};

#pragma unroll
  for (int st = 0; st < F2_NS - 1; ++st) f2_stage(c, st);
  const int lr = 32 * wm + 16 * kh + 4 * g;
  const f32x4 gv = *(const f32x4*)(gin + row0 + lr);
  c.asrc = aG + (size_t)(col0 + 32 * wn + r) * L + row0 + lr;
  bf16x8 a0[2], b0[2];
  f2_loop<0>(c, smem, a0, b0, acc);
  WAIT_LGKM0();
  __builtin_amdgcn_s_barrier();
  asm volatile("" ::: "memory");
  asm volatile("s_waitcnt vmcnt(0)" : "+v"(c.x0), "+v"(c.x1)::"memory");
  const bool early = __all(c.x0[1] == A_TAG && c.x0[3] == A_TAG && c.x1[1] == A_TAG && c.x1[3] == A_TAG);
  if (w == 0 && !early) {
    const gu32* fl = (const gu32*)(aG + (size_t)D * L + 32 * cbk + (lane & 31));
    for (int spin = 0; spin < (1 << 22); ++spin) {
      const unsigned v = __hip_atomic_load(fl, __ATOMIC_RELAXED, __HIP_MEMORY_SCOPE_AGENT);
      if (__all(v == 1u)) break;
      __builtin_amdgcn_s_sleep(2);
    }
  }
  {
    char* red = smem + (w & 3) * 4096 + kh * 2048;
    const f32x4 o0 = kh ? acc[0][0] : acc[1][0], o1 = kh ? acc[0][1] : acc[1][1];
    *(f32x4*)(red + lane * 16) = o0;
    *(f32x4*)(red + 1024 + lane * 16) = o1;
  }
  __syncthreads();
  unsigned long long av[2];
  {
    const unsigned* src = c.asrc;
    av[0] = (unsigned long long)c.x0[0] | ((unsigned long long)c.x0[2] << 32);
    av[1] = (unsigned long long)c.x1[0] | ((unsigned long long)c.x1[2] << 32);
    for (int spin = 0; !early && spin < (1 << 22); ++spin) {
      u32x4 x0, x1;
      asm volatile("global_load_dwordx4 %0, %2, off sc1\n\tglobal_load_dwordx4 %1, %3, off sc1\n\ts_waitcnt vmcnt(0)"
                   : "=&v"(x0), "=&v"(x1) : "v"(src), "v"(src + 16 * L) : "memory");
      av[0] = (unsigned long long)x0[0] | ((unsigned long long)x0[2] << 32);
      av[1] = (unsigned long long)x1[0] | ((unsigned long long)x1[2] << 32);
      const bool ok = x0[1] == A_TAG && x0[3] == A_TAG && x1[1] == A_TAG && x1[3] == A_TAG;
      if (__all(ok)) break;
      __builtin_amdgcn_s_sleep(4);
    }
  }
  constexpr int OT = 16384, OTP = 144;
  {
    const char* red = smem + (w & 3) * 4096 + (1 - kh) * 2048;
#pragma unroll
    for (int nt = 0; nt < 2; ++nt) {
      const f32x4 o = *(const f32x4*)(red + nt * 1024 + lane * 16);
      const f32x4 own = kh ? acc[1][nt] : acc[0][nt];
      const f32x4 s = kh ? (o + own) : (own + o);
      const unsigned alo = (unsigned)av[nt], ahi = (unsigned)(av[nt] >> 32);
      const float a[4] = {lo_f(alo), hi_f(alo), lo_f(ahi), hi_f(ahi)};
#pragma unroll
      for (int e = 0; e < 4; ++e) {
        const float o2 = bfr(gv[e] * a[e]) + bfr(bfr(1.f - gv[e]) * bfr(s[e]));
        *(__bf16*)(smem + OT + (lr + e) * OTP + (32 * wn + 16 * nt + r) * 2) = (__bf16)o2;
      }
    }
  }
  __syncthreads();
  {
    const int row = tid >> 3, c8 = tid & 7;
    const u32x4 v = *(const u32x4*)(smem + OT + row * OTP + c8 * 16);
    *(u32x4*)(out + (size_t)(row0 + row) * D + col0 + c8 * 8) = v;
  }
}

NOPK __global__ __launch_bounds__(512, 4) void k2(const __bf16* __restrict__ h, const __bf16* __restrict__ wd,
                                             const __bf16* __restrict__ uT, const unsigned* __restrict__ kc,
                                             const __bf16* __restrict__ dsk, unsigned* __restrict__ aG,
                                             const float* __restrict__ gin, __bf16* __restrict__ out) {
  extern __shared__ __attribute__((aligned(16))) char smem[];
  const int b = blockIdx.x, xcd = b & 7, i = (b & 255) >> 3;
  if (b < 256) {
    const int pair = 32 * xcd + i;
    conv_block(smem, 2 * pair, uT, kc, dsk, aG);
  } else {
    ffn2_block(smem, 64 * (4 * xcd + (i & 3)), i >> 2, h, wd, aG, gin, out);
  }
}

extern "C" void kernel_launch(void* const* d_in, const int* in_sizes, int n_in, void* d_out, int out_size,
                              void* d_ws, size_t ws_size, hipStream_t stream) {
  const __bf16* u = (const __bf16*)d_in[0];
  const __bf16* lar = (const __bf16*)d_in[1];
  const __bf16* aim = (const __bf16*)d_in[2];
  const __bf16* cre = (const __bf16*)d_in[3];
  const __bf16* cim = (const __bf16*)d_in[4];
  const __bf16* ldt = (const __bf16*)d_in[5];
  const __bf16* dsk = (const __bf16*)d_in[6];
  const __bf16* wg = (const __bf16*)d_in[8];
  const __bf16* wu = (const __bf16*)d_in[9];
  const __bf16* wd = (const __bf16*)d_in[10];
  const __bf16* gw = (const __bf16*)d_in[11];
  char* ws = (char*)d_ws;
  __bf16* h = (__bf16*)ws;
  ws += (size_t)L * HP * 2;
  unsigned* aG = (unsigned*)ws;
  ws += (size_t)D * L * 4 + 1024;
  __bf16* uT = (__bf16*)ws;
  ws += (size_t)D * L * 2;
  unsigned* kc = (unsigned*)ws;
  ws += (size_t)D * KC_PITCH * 4;
  float* gout = (float*)ws;
  (void)hipFuncSetAttribute((const void*)k1, hipFuncAttributeMaxDynamicSharedMemorySize, LDS_BYTES);
  (void)hipFuncSetAttribute((const void*)k2, hipFuncAttributeMaxDynamicSharedMemorySize, LDS_BYTES);
  hipLaunchKernelGGL(k1, dim3(512), dim3(512), LDS_BYTES, stream, u, lar, aim, cre, cim, ldt, wg, wu, gw, h, uT, kc,
                     gout, aG, wd);
  hipLaunchKernelGGL(k2, dim3(512), dim3(512), LDS_BYTES, stream, (const __bf16*)h, wd, (const __bf16*)uT,
                     (const unsigned*)kc, dsk, aG, (const float*)gout, (__bf16*)d_out);
}
```
